```python
import torch

def _softmax(x, axis=-1):
    return torch.softmax(x, dim=axis)
def _gelu(x):
    return torch.nn.functional.gelu(x, approximate="tanh")
def _silu(x):
    return torch.nn.functional.silu(x)
def _sigmoid(x):
    return torch.sigmoid(x)
def _layernorm(x, axis=-1, eps=1e-5):
    m = x.mean(dim=axis, keepdim=True)
    v = x.var(dim=axis, keepdim=True, unbiased=False)
    return (x - m) / torch.sqrt(v + eps)
def _scatter_add(tgt, idx, src, axis=0):
    return tgt.clone().index_add_(axis, idx, src)
def _segment_sum(src, seg, n):
    out = torch.zeros((n,) + src.shape[1:], dtype=src.dtype)
    return out.index_add_(0, seg, src)
def _pad(x, pw):
    flat = tuple(v for pair in reversed(pw) for v in pair)
    return torch.nn.functional.pad(x, flat)

kernel_name = 'interleave_layers__linear_kernel_hedgehog__sliding_local_dilated__prompt'

def setup_inputs():

    _w = (lambda a: (lambda t: t.to(torch.bfloat16) if t.is_floating_point() else t)(torch.as_tensor(a)))
    def _A_setup():
        B, S, D, H = 1, 2048, 512, 32
        Dh = D // H
        return {
            "x": _w((lambda *s: torch.randn(*s, dtype=torch.bfloat16) * 0.05)(B, S, D)),
            "wqkv": _w((lambda *s: torch.randn(*s, dtype=torch.bfloat16) * 0.4)(D, 3 * D)),
            "w_phi": _w((lambda *s: torch.randn(*s, dtype=torch.bfloat16) * 0.4)(H, Dh, Dh) * (Dh ** -0.5)),
            "wo": _w((lambda *s: torch.randn(*s, dtype=torch.bfloat16) * 0.05)(D, D)),
        }
    _A_in = _A_setup()

    def _B_setup():
        B, S, D = 1, 2048, 512
        return {
            "x": _w((lambda *s: torch.randn(*s, dtype=torch.bfloat16) * 0.05)(B, S, D)),
            "wqkv": _w((lambda *s: torch.randn(*s, dtype=torch.bfloat16) * 0.1)(D, 3 * D)),
            "wo": _w((lambda *s: torch.randn(*s, dtype=torch.bfloat16) * 0.05)(D, D)),
        }
    _B_in = _B_setup()

    return {
        "A_x": _A_in["x"],
        "A_wqkv": _A_in["wqkv"],
        "A_w_phi": _A_in["w_phi"],
        "A_wo": _A_in["wo"],
        "B_x": _B_in["x"],
        "B_wqkv": _B_in["wqkv"],
        "B_wo": _B_in["wo"],
    }

def reference(A_x, A_wqkv, A_w_phi, A_wo, B_x, B_wqkv, B_wo):
    def _A_ref(x, wqkv, w_phi, wo):
        B, S, D = x.shape
        H = 32; Dh = D // H
        qkv = (lambda x, *d: x.permute(*d))((x @ wqkv).reshape(B, S, 3, H, Dh), 2, 0, 3, 1, 4)
        q, k, v = qkv[0], qkv[1], qkv[2]
        pq = torch.einsum("bhsd,hdf->bhsf", q, w_phi)
        pk = torch.einsum("bhsd,hdf->bhsf", k, w_phi)
        phi_q = torch.concatenate([_softmax(pq, axis=-1), _softmax(-pq, axis=-1)], axis=-1)
        phi_k = torch.concatenate([_softmax(pk, axis=-1), _softmax(-pk, axis=-1)], axis=-1)
        kv = torch.einsum("bhsf,bhsd->bhfd", phi_k, v)
        z  = torch.einsum("bhsf->bhf", phi_k)
        num = torch.einsum("bhqf,bhfd->bhqd", phi_q, kv)
        den = torch.einsum("bhqf,bhf->bhq", phi_q, z)[..., None] + 1e-6
        o = num / den
        return (lambda x, *d: x.permute(*d))(o, 0, 2, 1, 3).reshape(B, S, D) @ wo

    def _B_ref(x, wqkv, wo):
        B, S, D = x.shape
        H = 32; Dh = D // H
        W, dil = 8, 2
        qkv = (lambda x, *d: x.permute(*d))((x @ wqkv).reshape(B, S, 3, H, Dh), 2, 0, 3, 1, 4)
        q, k, v = qkv[0], qkv[1], qkv[2]
        scale = Dh ** -0.5
        pos = torch.arange(S)
        off = torch.arange(W) * dil
        idx = pos[:, None] - off[None, :]
        mask = idx >= 0
        idx_c = torch.where(mask, idx, 0).reshape(-1)
        kg = k[:, :, idx_c, :].reshape(B, H, S, W, Dh)
        vg = v[:, :, idx_c, :].reshape(B, H, S, W, Dh)
        logits = torch.einsum("bhsd,bhswd->bhsw", q, kg) * scale
        logits = torch.where(mask[None, None, :, :], logits, -1e9)
        attn = _softmax(logits, axis=-1)
        o = torch.einsum("bhsw,bhswd->bhsd", attn, vg)
        return (lambda x, *d: x.permute(*d))(o, 0, 2, 1, 3).reshape(B, S, D) @ wo

    x = A_x
    x = x + _A_ref(_layernorm(x, axis=-1), A_wqkv, A_w_phi, A_wo)
    x = x + _B_ref(_layernorm(x, axis=-1), B_wqkv, B_wo)
    x = x + _A_ref(_layernorm(x, axis=-1), A_wqkv, A_w_phi, A_wo)
    x = x + _B_ref(_layernorm(x, axis=-1), B_wqkv, B_wo)
    return x
```

```cpp
#include <hip/hip_runtime.h>
#include <hip/hip_bf16.h>

constexpr int S = 2048, D = 512, H = 32, DH = 16;

__device__ __forceinline__ float bf2f(unsigned short b) { return __uint_as_float(((unsigned)b) << 16); }
__device__ __forceinline__ unsigned short f2bf(float f) { __hip_bfloat16 h = __float2bfloat16(f); return *reinterpret_cast<unsigned short*>(&h); }
__device__ __forceinline__ float rbf(float f) { return bf2f(f2bf(f)); }

__global__ void k_init(const unsigned short* x, float* xf) {
  int i = blockIdx.x * 256 + threadIdx.x;
  xf[i] = bf2f(x[i]);
}
__global__ void k_ln(const float* x, float* xn) {
  int row = blockIdx.x; int t = threadIdx.x;
  float v[8]; float s = 0;
  for (int j = 0; j < 8; ++j) { v[j] = x[row * D + t + 64 * j]; s += v[j]; }
  for (int o = 32; o > 0; o >>= 1) s += __shfl_xor(s, o);
  float m = s / D; float q = 0;
  for (int j = 0; j < 8; ++j) { float d = v[j] - m; q += d * d; }
  for (int o = 32; o > 0; o >>= 1) q += __shfl_xor(q, o);
  float r = 1.0f / sqrtf(q / D + 1e-5f);
  for (int j = 0; j < 8; ++j) xn[row * D + t + 64 * j] = rbf((v[j] - m) * r);
}
__global__ void k_gemm(const float* A, const unsigned short* B, float* C, const float* res, int N, int K) {
  __shared__ float As[16][17]; __shared__ float Bs[16][17];
  int tx = threadIdx.x & 15, ty = threadIdx.x >> 4;
  int row = blockIdx.y * 16 + ty, col = blockIdx.x * 16 + tx;
  float acc = 0;
  for (int k0 = 0; k0 < K; k0 += 16) {
    As[ty][tx] = A[row * K + k0 + tx];
    Bs[ty][tx] = bf2f(B[(k0 + ty) * N + col]);
    __syncthreads();
    for (int k = 0; k < 16; ++k) acc += As[ty][k] * Bs[k][tx];
    __syncthreads();
  }
  acc = rbf(acc);
  if (res) acc = rbf(acc + res[row * N + col]);
  C[row * N + col] = acc;
}
__global__ void k_phi(const float* qkv, const unsigned short* wphi, float* phi  ) {
  int i = blockIdx.x * 256 + threadIdx.x;
  int h = i % H, s = (i / H) % S, w = i / (H * S);
  float q[16]; for (int d = 0; d < 16; ++d) q[d] = qkv[s * 3 * D + w * D + h * 16 + d];
  float p[16]; float mx = -1e30f, mn = 1e30f;
  for (int f = 0; f < 16; ++f) { float a = 0; for (int d = 0; d < 16; ++d) a += q[d] * bf2f(wphi[(h * 16 + d) * 16 + f]); p[f] = a; mx = fmaxf(mx, a); mn = fminf(mn, a); }
  float sp = 0, sn = 0; float ep[16], en[16];
  for (int f = 0; f < 16; ++f) { ep[f] = __expf(p[f] - mx); sp += ep[f]; en[f] = __expf(mn - p[f]); sn += en[f]; }
  float* o = phi + ((size_t)(w * S + s) * H + h) * 32;
  for (int f = 0; f < 16; ++f) { o[f] = rbf(ep[f] / sp); o[16 + f] = rbf(en[f] / sn); }
}
__global__ void k_kv(const float* qkv, const float* phi, float* kv  ) {
  int h = blockIdx.x; int t = threadIdx.x; if (t >= 32 * 17) return;
  int f = t / 17, d = t % 17; float a = 0;
  const float* pk = phi + (size_t)S * H * 32;
  for (int s = 0; s < S; ++s) { float p = pk[((size_t)s * H + h) * 32 + f]; float v = d < 16 ? qkv[s * 3 * D + 2 * D + h * 16 + d] : 1.0f; a += p * v; }
  kv[(h * 32 + f) * 17 + d] = a;
}
__global__ void k_lino(const float* phi, const float* kv, float* o) {
  int i = blockIdx.x * 256 + threadIdx.x;
  int s = i / D, c = i % D, h = c / 16, d = c % 16;
  float num = 0, den = 0;
  for (int f = 0; f < 32; ++f) { float p = phi[((size_t)s * H + h) * 32 + f]; num += p * kv[(h * 32 + f) * 17 + d]; den += p * kv[(h * 32 + f) * 17 + 16]; }
  o[i] = rbf(num / (den + 1e-6f));
}
__global__ void k_local(const float* qkv, float* o) {
  int i = blockIdx.x * 256 + threadIdx.x;
  int h = i % H, s = i / H;
  float l[8]; float mx = -1e30f;
  for (int w = 0; w < 8; ++w) { int j = s - 2 * w; if (j < 0) { l[w] = -1e9f; } else { float a = 0; for (int d = 0; d < 16; ++d) a += qkv[s * 3 * D + h * 16 + d] * qkv[j * 3 * D + D + h * 16 + d]; l[w] = a * 0.25f; } mx = fmaxf(mx, l[w]); }
  float sum = 0; for (int w = 0; w < 8; ++w) { l[w] = __expf(l[w] - mx); sum += l[w]; }
  for (int d = 0; d < 16; ++d) { float a = 0; for (int w = 0; w < 8; ++w) { int j = s - 2 * w; if (j < 0) j = 0; a += l[w] / sum * qkv[j * 3 * D + 2 * D + h * 16 + d]; } o[s * D + h * 16 + d] = rbf(a); }
}
__global__ void k_out(const float* xf, unsigned short* out) {
  int i = blockIdx.x * 256 + threadIdx.x; out[i] = f2bf(xf[i]);
}

extern "C" void kernel_launch(void* const* d_in, const int* in_sizes, int n_in, void* d_out, int out_size, void* d_ws, size_t ws_size, hipStream_t stream) {
  const unsigned short* Ax = (const unsigned short*)d_in[0];
  const unsigned short* Awqkv = (const unsigned short*)d_in[1];
  const unsigned short* Awphi = (const unsigned short*)d_in[2];
  const unsigned short* Awo = (const unsigned short*)d_in[3];
  const unsigned short* Bwqkv = (const unsigned short*)d_in[5];
  const unsigned short* Bwo = (const unsigned short*)d_in[6];
  float* ws = (float*)d_ws;
  float* x0 = ws; float* x1 = x0 + S * D; float* xn = x1 + S * D; float* qkv = xn + S * D; float* phi = qkv + S * 3 * D; float* kv = phi + 2 * S * H * 32; float* o = kv + H * 32 * 17 + 64;
  k_init<<<S * D / 256, 256, 0, stream>>>(Ax, x0);
  float* cur = x0; float* nxt = x1;
  for (int l = 0; l < 4; ++l) {
    k_ln<<<S, 64, 0, stream>>>(cur, xn);
    k_gemm<<<dim3(3 * D / 16, S / 16), 256, 0, stream>>>(xn, (l & 1) ? Bwqkv : Awqkv, qkv, nullptr, 3 * D, D);
    if ((l & 1) == 0) {
      k_phi<<<2 * S * H / 256, 256, 0, stream>>>(qkv, Awphi, phi);
      k_kv<<<H, 576, 0, stream>>>(qkv, phi, kv);
      k_lino<<<S * D / 256, 256, 0, stream>>>(phi, kv, o);
    } else {
      k_local<<<S * H / 256, 256, 0, stream>>>(qkv, o);
    }
    k_gemm<<<dim3(D / 16, S / 16), 256, 0, stream>>>(o, (l & 1) ? Bwo : Awo, nxt, cur, D, D);
    float* t = cur; cur = nxt; nxt = t;
  }
  k_out<<<S * D / 256, 256, 0, stream>>>(cur, (unsigned short*)d_out);
}
```

```cpp
#include <hip/hip_runtime.h>
#include <hip/hip_bf16.h>
#include <type_traits>

#define GAS __attribute__((address_space(1)))
#define LAS __attribute__((address_space(3)))
#define DEV __device__ __forceinline__
typedef short s4 __attribute__((ext_vector_type(4)));
typedef short s8 __attribute__((ext_vector_type(8)));
typedef float f4 __attribute__((ext_vector_type(4)));
typedef float f2 __attribute__((ext_vector_type(2)));
typedef __bf16 bf2 __attribute__((ext_vector_type(2)));
typedef unsigned v4u __attribute__((ext_vector_type(4)));
typedef unsigned v2u __attribute__((ext_vector_type(2)));
typedef GAS unsigned gu32;
typedef LAS unsigned char lds_t;
#define RLX_AGENT __ATOMIC_RELAXED, __HIP_MEMORY_SCOPE_AGENT

namespace {
constexpr int NWG = 256, NT = 256;
constexpr int WS_EPOCH = 0;
constexpr int WS_FX = 4096;
constexpr int WS_FO = WS_FX + 3 * 32 * 128;
constexpr int WS_STAT = 1 << 17;
constexpr int STAT_L = 32 * 8 * 64 * 8;
constexpr int WS_KV2 = 1 << 19;
constexpr int KV2_L = 8 * 64 * 256;
constexpr int WS_STAMP = 3 << 18;
constexpr int WS_X = 1 << 20;
constexpr int ACT_L = 2 << 20;
constexpr int WS_O = WS_X + 3 * ACT_L;
constexpr int WS_KV1 = WS_O + 4 * ACT_L;
constexpr int KV1_L = 8 * 64 * 32 * 384;
constexpr int WS_END = WS_KV1 + 2 * KV1_L;
constexpr int L_XT = 0;
constexpr int L_RQ = 81920;
constexpr int L_MEAN = 147456;
constexpr int L_RSTD = L_MEAN + 320;
constexpr int L_CS = L_RSTD + 320;
constexpr int L_END = L_CS + 4 * 2 * 3 * 64;
constexpr int LDS_BYTES = 150 * 1024;
static_assert(L_END <= LDS_BYTES, "lds");
constexpr unsigned SPIN_CAP = 1u << 17;
constexpr float LOG2E = 1.4426950408889634f;
}

DEV unsigned pk2(float a, float b) { return __builtin_bit_cast(unsigned, __builtin_convertvector(f2{a, b}, bf2)); }
DEV f4 mfma32(v4u a, v4u b, f4 c) { return __builtin_amdgcn_mfma_f32_16x16x32_bf16(__builtin_bit_cast(s8, a), __builtin_bit_cast(s8, b), c, 0, 0, 0); }
DEV f4 mfma16(v2u a, v2u b, f4 c) { return __builtin_amdgcn_mfma_f32_16x16x16bf16_1k(__builtin_bit_cast(s4, a), __builtin_bit_cast(s4, b), c, 0, 0, 0); }
DEV float asf(unsigned u) { return __builtin_bit_cast(float, u); }
DEV unsigned asu(float f) { return __builtin_bit_cast(unsigned, f); }
DEV float xg_max(float v) {
  auto r = __builtin_amdgcn_permlane16_swap(asu(v), asu(v), false, false);
  v = fmaxf(asf(r[0]), asf(r[1]));
  auto q = __builtin_amdgcn_permlane32_swap(asu(v), asu(v), false, false);
  return fmaxf(asf(q[0]), asf(q[1]));
}
DEV float xg_sum(float v) {
  auto r = __builtin_amdgcn_permlane16_swap(asu(v), asu(v), false, false);
  v = asf(r[0]) + asf(r[1]);
  auto q = __builtin_amdgcn_permlane32_swap(asu(v), asu(v), false, false);
  return asf(q[0]) + asf(q[1]);
}
DEV float row_sum(float v) {
  v += asf(__builtin_amdgcn_update_dpp(0u, asu(v), 0x128, 0xf, 0xf, false));
  v += asf(__builtin_amdgcn_update_dpp(0u, asu(v), 0x124, 0xf, 0xf, false));
  v += asf(__builtin_amdgcn_update_dpp(0u, asu(v), 0x122, 0xf, 0xf, false));
  v += asf(__builtin_amdgcn_update_dpp(0u, asu(v), 0x121, 0xf, 0xf, false));
  return v;
}
DEV float sum32(float v) {
  v = row_sum(v);
  auto r = __builtin_amdgcn_permlane16_swap(asu(v), asu(v), false, false);
  return asf(r[0]) + asf(r[1]);
}
#define BAR() do { asm volatile("s_waitcnt lgkmcnt(0)" ::: "memory"); __builtin_amdgcn_s_barrier(); asm volatile("" ::: "memory"); } while (0)
#define DRAIN() asm volatile("s_waitcnt vmcnt(0)" ::: "memory")
#define WAIT_VM(n) asm volatile("s_waitcnt vmcnt(%0)" :: "n"(n) : "memory")
#define SC1 16
#define ONES4 (v4u{0x3f803f80u, 0x3f803f80u, 0x3f803f80u, 0x3f803f80u})
#define ZERO4 (f4{0.f, 0.f, 0.f, 0.f})

DEV int opq(int v) { asm volatile("" : "+v"(v)); return v; }
DEV v4u lds_ld128(lds_t* L, int off) { return *(LAS v4u*)(L + off); }
DEV void lds_st128(lds_t* L, int off, v4u v) { *(LAS v4u*)(L + off) = v; }
DEV v4u tr_frag(lds_t* L, int off) {
  s4 a = __builtin_amdgcn_ds_read_tr16_b64_v4i16((LAS s4*)(L + off));
  s4 b = __builtin_amdgcn_ds_read_tr16_b64_v4i16((LAS s4*)(L + off + 512));
  v2u ua = __builtin_bit_cast(v2u, a), ub = __builtin_bit_cast(v2u, b);
  return v4u{ua.x, ua.y, ub.x, ub.y};
}

constexpr int P_AK = 0, P_AV = 16, P_BQ = 32, P_BK = 48, P_BV = 64;
template <int F, bool WA, bool FIRST>
DEV void wmfma(f4& acc, v4u x) {
  constexpr int R = 96 + 4 * (F < 40 ? F : F - 40);
  if constexpr (F < 40) {
    if constexpr (WA) {
      if constexpr (FIRST) asm volatile("v_mfma_f32_16x16x32_bf16 %0, v[%c2:%c3], %1, 0" : "=&v"(acc) : "v"(x), "n"(R), "n"(R + 3));
      else asm volatile("v_mfma_f32_16x16x32_bf16 %0, v[%c2:%c3], %1, %0" : "+v"(acc) : "v"(x), "n"(R), "n"(R + 3));
    } else {
      if constexpr (FIRST) asm volatile("v_mfma_f32_16x16x32_bf16 %0, %1, v[%c2:%c3], 0" : "=&v"(acc) : "v"(x), "n"(R), "n"(R + 3));
      else asm volatile("v_mfma_f32_16x16x32_bf16 %0, %1, v[%c2:%c3], %0" : "+v"(acc) : "v"(x), "n"(R), "n"(R + 3));
    }
  } else {
    if constexpr (WA) {
      if constexpr (FIRST) asm volatile("v_mfma_f32_16x16x32_bf16 %0, a[%c2:%c3], %1, 0" : "=&v"(acc) : "v"(x), "n"(R), "n"(R + 3));
      else asm volatile("v_mfma_f32_16x16x32_bf16 %0, a[%c2:%c3], %1, %0" : "+v"(acc) : "v"(x), "n"(R), "n"(R + 3));
    } else {
      if constexpr (FIRST) asm volatile("v_mfma_f32_16x16x32_bf16 %0, %1, a[%c2:%c3], 0" : "=&v"(acc) : "v"(x), "n"(R), "n"(R + 3));
      else asm volatile("v_mfma_f32_16x16x32_bf16 %0, %1, a[%c2:%c3], %0" : "+v"(acc) : "v"(x), "n"(R), "n"(R + 3));
    }
  }
}
template <bool FIRST>
DEV void vamfma(f4& acc, v4u a, v4u b) {
  if constexpr (FIRST) asm volatile("v_mfma_f32_16x16x32_bf16 %0, %1, %2, 0" : "=&v"(acc) : "a"(a), "v"(b));
  else asm volatile("v_mfma_f32_16x16x32_bf16 %0, %1, %2, %0" : "+v"(acc) : "a"(a), "v"(b));
}
DEV void vbmfma(f4& acc, v4u a, v4u b) {
  asm volatile("v_mfma_f32_16x16x32_bf16 %0, %1, %2, %0" : "+v"(acc) : "v"(a), "a"(b));
}
template <int F, int OFF>
DEV void wtr(unsigned addr) {
  constexpr int R = 96 + 4 * (F < 40 ? F : F - 40);
  if constexpr (F < 40)
    asm volatile("ds_read_b64_tr_b16 v[%c1:%c2], %0 offset:%c5\n\tds_read_b64_tr_b16 v[%c3:%c4], %0 offset:%c6"
                 :: "v"(addr), "n"(R), "n"(R + 1), "n"(R + 2), "n"(R + 3), "n"(OFF), "n"(OFF + 512) : "memory");
  else
    asm volatile("ds_read_b64_tr_b16 a[%c1:%c2], %0 offset:%c5\n\tds_read_b64_tr_b16 a[%c3:%c4], %0 offset:%c6"
                 :: "v"(addr), "n"(R), "n"(R + 1), "n"(R + 2), "n"(R + 3), "n"(OFF), "n"(OFF + 512) : "memory");
}
#define FENCE1(a) asm volatile("s_nop 7" : "+v"(a))
#define FENCE2(a, b) asm volatile("s_nop 7" : "+v"(a), "+v"(b))
#define FENCE3(a, b, c) asm volatile("s_nop 7" : "+v"(a), "+v"(b), "+v"(c))
#define FENCE4(a, b, c, d) asm volatile("s_nop 7" : "+v"(a), "+v"(b), "+v"(c), "+v"(d))
template <int I, int N, class F> DEV void sfor(F&& f) { if constexpr (I < N) { f(std::integral_constant<int, I>{}); sfor<I + 1, N>(f); } }

#define STAMP(i) do {} while (0)

struct Ctx {
  GAS unsigned char* ws;
  __amdgpu_buffer_rsrc_t rs;
  lds_t* L;
  int c, m, w;
  int lane, tok, g;
  unsigned T;
  bool fail;
};
DEV void fresh(Ctx& ctx) {
  int l;
  asm volatile("v_mbcnt_lo_u32_b32 %0, -1, 0\n\tv_mbcnt_hi_u32_b32 %0, -1, %0" : "=v"(l));
  ctx.lane = l; ctx.tok = l & 15; ctx.g = l >> 4;
}
DEV v4u ld128(Ctx& ctx, int voff, int soff) { return __builtin_amdgcn_raw_buffer_load_b128(ctx.rs, voff, soff, SC1); }
DEV void st128(Ctx& ctx, v4u v, int voff, int soff) { __builtin_amdgcn_raw_buffer_store_b128(v, ctx.rs, voff, soff, SC1); }
DEV void st64(Ctx& ctx, v2u v, int voff, int soff) { __builtin_amdgcn_raw_buffer_store_b64(v, ctx.rs, voff, soff, SC1); }

DEV void wait_flags(Ctx& ctx, int off0, int off1, bool two) {
  if (ctx.w == 0 && !ctx.fail) {
    const int lane = ctx.lane;
    const bool act = lane < (two ? 16 : 8);
    gu32* p = (gu32*)(ctx.ws + (lane < 8 ? off0 + lane * 16 : off1 + (lane & 7) * 16));
    unsigned spins = 0;
    for (;;) {
      unsigned v = ctx.T;
      if (act) v = __hip_atomic_load(p, RLX_AGENT);
      if (__all(v == ctx.T)) break;
      if (++spins > SPIN_CAP) { ctx.fail = true; break; }
    }
    __builtin_amdgcn_fence(__ATOMIC_ACQUIRE, "wavefront");
  }
  BAR();
}
DEV void set_flag(Ctx& ctx, int off) {
  DRAIN();
  BAR();
  if (ctx.w == 0 && ctx.lane == 0) __hip_atomic_store((gu32*)(ctx.ws + off + ctx.m * 16), ctx.T, RLX_AGENT);
}

DEV int tr_base(int lane, int j) {
  const int g = lane >> 4, q = (lane & 15) >> 2, p = lane & 3;
  const int s = ((q >> 1) & 1) | ((g & 1) << 1);
  return (8 * g + q) * 128 + ((j ^ s) << 5) + p * 8;
}
template <int F>
DEV void raw_ld1(unsigned voff, __amdgpu_buffer_rsrc_t rs, int soff) {
  constexpr int R = 96 + 4 * (F < 40 ? F : F - 40);
  if constexpr (F < 40) asm volatile("buffer_load_dwordx4 v[%c3:%c4], %0, %1, %2 offen" :: "v"(voff), "s"(rs), "s"(soff), "n"(R), "n"(R + 3) : "memory");
  else asm volatile("buffer_load_dwordx4 a[%c3:%c4], %0, %1, %2 offen" :: "v"(voff), "s"(rs), "s"(soff), "n"(R), "n"(R + 3) : "memory");
}
template <int P>
DEV void raw_load(Ctx& ctx, __amdgpu_buffer_rsrc_t rs, int colbyte) {
  const unsigned vo = (unsigned)((ctx.lane >> 3) * 3072 + (ctx.lane & 7) * 16);
  const int sb = colbyte + ctx.m * 128 + 32 * ctx.w * 3072;
  asm volatile("s_nop 4" ::: "memory");
  sfor<0, 16>([&](auto i) { raw_ld1<P + i>(vo, rs, sb + (128 * (i >> 2) + 8 * (i & 3)) * 3072); });
}
template <int F, int OFF>
DEV void raw_st1(unsigned addr) {
  constexpr int R = 96 + 4 * (F < 40 ? F : F - 40);
  if constexpr (F < 40) asm volatile("ds_write_b128 %0, v[%c1:%c2] offset:%c3" :: "v"(addr), "n"(R), "n"(R + 3), "n"(OFF) : "memory");
  else asm volatile("ds_write_b128 %0, a[%c1:%c2] offset:%c3" :: "v"(addr), "n"(R), "n"(R + 3), "n"(OFF) : "memory");
}
template <int P, int I0, int I1>
DEV void raw_store(Ctx& ctx) {
  const int lane = ctx.lane, ch = lane & 7, rb1 = (lane >> 4) & 1;
  const unsigned base = (unsigned)(size_t)ctx.L + L_RQ + (32 * ctx.w + (lane >> 3)) * 128 + (ch & 1) * 16;
  const unsigned p0 = base + (((ch >> 1) ^ rb1) << 5), p1 = base + (((ch >> 1) ^ (rb1 | 2)) << 5);
  sfor<I0, I1>([&](auto i) { raw_st1<P + i, (i >> 2) * 16384 + (i & 3) * 1024>((i & 1) ? p1 : p0); });
}
template <int P, int K0, int K1>
DEV void part_tr(Ctx& ctx) {
  const unsigned tb = (unsigned)(size_t)ctx.L + L_RQ + tr_base(ctx.lane, ctx.w);
  sfor<K0, K1>([&](auto ks) { wtr<P + ks, ks * 4096>(tb); });
}
template <int P>
DEV void part_cs(Ctx& ctx, int csoff) {
  v4u ones = ONES4;
  asm volatile("s_waitcnt lgkmcnt(0)\n\ts_nop 1" : "+v"(ones) :: "memory");
  f4 cs;
  sfor<0, 16>([&](auto ks) { wmfma<P + ks, false, ks == 0>(cs, ones); });
  FENCE1(cs);
  if (ctx.g == 0) *(LAS float*)(ctx.L + csoff + ctx.tok * 4) = cs[0];
}
template <int P>
DEV void part_step(Ctx& ctx, int csoff) {
  raw_store<P, 0, 8>(ctx);
  BAR();
  part_tr<P, 0, 8>(ctx);
  raw_store<P, 8, 16>(ctx);
  BAR();
  part_tr<P, 8, 16>(ctx);
  part_cs<P>(ctx, csoff);
}

template <int ROWB>
DEV void img_chunk(Ctx& ctx, const GAS unsigned char* wcol, int ldsoff, int j) {
  const int lane = ctx.lane, w = ctx.w;
  const int cp = lane & 7, rb1 = (lane >> 4) & 1;
  const unsigned vb = (unsigned)((lane >> 3) * ROWB + (cp & 1) * 16);
  const unsigned v0 = vb + (((cp >> 1) ^ rb1) << 5), v1 = vb + 8 * ROWB + (((cp >> 1) ^ (rb1 | 2)) << 5);
  const GAS unsigned char* sb = wcol + (size_t)(128 * j + 32 * w) * ROWB + ctx.m * 128;
  const unsigned ldsbase = (unsigned)(size_t)(ctx.L) + ldsoff + j * 16384 + w * 4096;
  unsigned keep;
  asm volatile("s_mov_b32 %0, m0\n\ts_mov_b32 m0, %5\n\ts_nop 2\n\t"
               "global_load_lds_dwordx4 %1, %3\n\ts_add_u32 m0, m0, 0x400\n\ts_nop 0\n\t"
               "global_load_lds_dwordx4 %2, %3\n\ts_add_u32 m0, m0, 0x400\n\ts_nop 0\n\t"
               "global_load_lds_dwordx4 %1, %4\n\ts_add_u32 m0, m0, 0x400\n\ts_nop 0\n\t"
               "global_load_lds_dwordx4 %2, %4\n\ts_mov_b32 m0, %0"
               : "=&s"(keep) : "v"(v0), "v"(v1), "s"(sb), "s"(sb + 16 * ROWB), "s"(ldsbase) : "memory", "scc");
}
DEV void wo_chunk(Ctx& ctx, const GAS unsigned char* wo, int j) { img_chunk<1024>(ctx, wo, L_XT, j); }

DEV void put_stats(Ctx& ctx, int slot, float s, float q) {
  const float mean = s * (1.0f / 512.0f);
  float var = q * (1.0f / 512.0f) - mean * mean;
  var = fmaxf(var, 0.0f);
  const float rstd = __builtin_amdgcn_rsqf(var + 1e-5f);
  if (ctx.g == 0) {
    *(LAS float*)(ctx.L + L_MEAN + (slot * 16 + ctx.tok) * 4) = mean;
    *(LAS float*)(ctx.L + L_RSTD + (slot * 16 + ctx.tok) * 4) = rstd;
  }
}

DEV v2u fold_T(Ctx& ctx, f4 acc, int slot, int csoff) {
  const float mean = *(LAS float*)(ctx.L + L_MEAN + (slot * 16 + ctx.tok) * 4);
  const float rstd = *(LAS float*)(ctx.L + L_RSTD + (slot * 16 + ctx.tok) * 4);
  const f4 cs = *(LAS f4*)(ctx.L + csoff + ctx.g * 16);
  f4 v;
#pragma unroll
  for (int r = 0; r < 4; ++r) v[r] = (acc[r] - mean * cs[r]) * rstd;
  return v2u{pk2(v[0], v[1]), pk2(v[2], v[3])};
}
DEV v2u fold_F(Ctx& ctx, f4 acc, int slot, int csoff) {
  const f4 mean = *(LAS f4*)(ctx.L + L_MEAN + (slot * 16 + ctx.g * 4) * 4);
  const f4 rstd = *(LAS f4*)(ctx.L + L_RSTD + (slot * 16 + ctx.g * 4) * 4);
  const float cs = *(LAS float*)(ctx.L + csoff + ctx.tok * 4);
  f4 v;
#pragma unroll
  for (int r = 0; r < 4; ++r) v[r] = (acc[r] - mean[r] * cs) * rstd[r];
  return v2u{pk2(v[0], v[1]), pk2(v[2], v[3])};
}
DEV v4u feature_map(v2u xb, v2u wphi) {
  f4 p = mfma16(wphi, xb, ZERO4);
  float mx = fmaxf(fmaxf(p[0], p[1]), fmaxf(p[2], p[3]));
  float mn = fminf(fminf(p[0], p[1]), fminf(p[2], p[3]));
  mx = xg_max(mx);
  mn = -xg_max(-mn);
  f4 ep, en;
#pragma unroll
  for (int r = 0; r < 4; ++r) {
    ep[r] = __builtin_amdgcn_exp2f((p[r] - mx) * LOG2E);
    en[r] = __builtin_amdgcn_exp2f((mn - p[r]) * LOG2E);
  }
  const float sp = xg_sum((ep[0] + ep[1]) + (ep[2] + ep[3]));
  const float sn = xg_sum((en[0] + en[1]) + (en[2] + en[3]));
  const float rp = __builtin_amdgcn_rcpf(sp), rn = __builtin_amdgcn_rcpf(sn);
  return v4u{pk2(ep[0] * rp, ep[1] * rp), pk2(ep[2] * rp, ep[3] * rp), pk2(en[0] * rn, en[1] * rn), pk2(en[2] * rn, en[3] * rn)};
}

DEV void dma_tile(Ctx& ctx, int slot, int soff) {
  const unsigned ldsbase = (unsigned)(size_t)(ctx.L) + L_XT + slot * 16384 + ctx.w * 4096;
  const unsigned lv = ctx.lane * 16;
  unsigned keep;
  asm volatile("s_mov_b32 %0, m0\n\ts_mov_b32 m0, %4\n\ts_nop 2\n\t"
               "buffer_load_dwordx4 %1, %2, %3 offen sc1 lds\n\t"
               "buffer_load_dwordx4 %1, %2, %3 offen offset:1024 sc1 lds\n\t"
               "buffer_load_dwordx4 %1, %2, %3 offen offset:2048 sc1 lds\n\t"
               "buffer_load_dwordx4 %1, %2, %3 offen offset:3072 sc1 lds\n\t"
               "s_mov_b32 m0, %0"
               : "=&s"(keep) : "v"(lv), "s"(ctx.rs), "s"(soff), "s"(ldsbase) : "memory");
}
template <int L>
DEV void gather_issue(Ctx& ctx) {
  constexpr bool HALO = (L & 1) != 0;
  const int c = ctx.c, w = ctx.w;
  const int cprev = c > 0 ? c - 1 : 0;
  wait_flags(ctx, WS_FX + ((L - 1) * 32 + c) * 128, WS_FX + ((L - 1) * 32 + cprev) * 128, HALO);
  STAMP(L * 8 + 0);
  const int sb = WS_STAT + (L - 1) * STAT_L;
  const int sv = ctx.g * 1024 + ctx.tok * 8;
  v2u s0, s1, h0, h1;
  const int so = sb + (c * 8 * 64 + 16 * w) * 8, ho = HALO ? sb + (cprev * 8 * 64 + (c > 0 ? 48 : 0)) * 8 : so;
  asm volatile("s_nop 4\n\tbuffer_load_dwordx2 %0, %4, %5, %6 offen sc1\n\t"
               "buffer_load_dwordx2 %1, %4, %5, %6 offen offset:512 sc1\n\t"
               "buffer_load_dwordx2 %2, %4, %5, %7 offen sc1\n\t"
               "buffer_load_dwordx2 %3, %4, %5, %7 offen offset:512 sc1"
               : "=&v"(s0), "=&v"(s1), "=&v"(h0), "=&v"(h1) : "v"(sv), "s"(ctx.rs), "s"(so), "s"(ho) : "memory");
  const int xb = WS_X + (L - 1) * ACT_L + w * 4096;
  if (HALO) dma_tile(ctx, 0, xb + (c > 0 ? 4 * c - 1 : 0) * 16384);
#pragma unroll
  for (int t = 0; t < 4; ++t) dma_tile(ctx, 1 + t, xb + (4 * c + t) * 16384);
  asm volatile("s_waitcnt vmcnt(%4)" : "+v"(s0), "+v"(s1), "+v"(h0), "+v"(h1) : "n"(HALO ? 20 : 16) : "memory");
  put_stats(ctx, 1 + w, xg_sum(asf(s0.x) + asf(s1.x)), xg_sum(asf(s0.y) + asf(s1.y)));
  if (HALO && w == 0) put_stats(ctx, 0, xg_sum(asf(h0.x) + asf(h1.x)), xg_sum(asf(h0.y) + asf(h1.y)));
}

DEV void publish_o(Ctx& ctx, int L, const v2u (&o)[4]) {
  const int ks = 2 * ctx.m + (ctx.w >> 1);
  const int vo = (ctx.g & 1) * 16384 + ((ctx.g >> 1) * 16 + ctx.tok) * 16;
#pragma unroll
  for (int u = 0; u < 2; ++u) {
    auto r0 = __builtin_amdgcn_permlane16_swap(o[2 * u].x, o[2 * u + 1].x, false, false);
    auto r1 = __builtin_amdgcn_permlane16_swap(o[2 * u].y, o[2 * u + 1].y, false, false);
    st128(ctx, v4u{r0[0], r1[0], r0[1], r1[1]}, vo, WS_O + L * ACT_L + ((4 * ctx.c + 2 * u) * 16 + ks) * 1024 + (ctx.w & 1) * 512);
  }
  set_flag(ctx, WS_FO + (L * 32 + ctx.c) * 128);
}

template <int PQ, int PK, int PV>
DEV void gemm_tile(Ctx& ctx, int slot, f4& aq, f4& ak, f4& av) {
  lds_t* tb = ctx.L + opq(L_XT + slot * 16384 + ctx.lane * 16);
  v4u a[4];
  a[0] = lds_ld128(tb, 0); a[1] = lds_ld128(tb, 1024); a[2] = lds_ld128(tb, 2048);
  sfor<0, 16>([&](auto ks) {
    if constexpr (ks + 3 < 16) a[(ks + 3) & 3] = lds_ld128(tb, (ks + 3) * 1024);
    if constexpr (PQ >= 0) wmfma<(PQ >= 0 ? PQ : 0) + ks, true, ks == 0>(aq, a[ks & 3]);
    wmfma<PK + ks, true, ks == 0>(ak, a[ks & 3]);
    wmfma<PV + ks, false, ks == 0>(av, a[ks & 3]);
  });
}

template <int L>
DEV void layer_A(Ctx& ctx, v2u wphi, const GAS unsigned char* wo, __amdgpu_buffer_rsrc_t rsb) {
  fresh(ctx);
  const int c = ctx.c, m = ctx.m, w = ctx.w, lane = ctx.lane, g = ctx.g, tok = ctx.tok;
  const int csb = L_CS + (w * 6 + 0) * 64;
  if (L > 0) {
    gather_issue<L>(ctx);
    wo_chunk(ctx, wo, 0);
    WAIT_VM(16);
    BAR();
    STAMP(L * 8 + 1);
  } else {
    wo_chunk(ctx, wo, 0);
  }
  v2u ident;
  {
    const int e = tok - 4 * g;
    ident = v2u{e == 0 ? 0x3f80u : e == 1 ? 0x3f800000u : 0u, e == 2 ? 0x3f80u : e == 3 ? 0x3f800000u : 0u};
  }
  f4 kvp = ZERO4, kvm = ZERO4, zz = ZERO4;
  const unsigned lo1 = tok < 8 ? 0x3f803f80u : 0u, hi1 = tok < 8 ? 0u : 0x3f803f80u;
#pragma unroll
  for (int u = 0; u < 2; ++u) {
    v2u php[2], phm[2], vb[2];
#pragma unroll
    for (int tt = 0; tt < 2; ++tt) {
      const int slot = 1 + 2 * u + tt;
      f4 ak, av;
      gemm_tile<-1, P_AK, P_AV>(ctx, slot, ak, ak, av);
      if (L > 0 && slot < 4) { if (slot == 1) WAIT_VM(12); else if (slot == 2) WAIT_VM(8); else WAIT_VM(4); BAR(); }
      if (L == 0 && slot == 1) {
        BAR();
        WAIT_VM(36);
        raw_store<P_BV, 0, 16>(ctx);
        asm volatile("s_waitcnt lgkmcnt(0)" ::: "memory");
        raw_load<P_BV>(ctx, rsb, 2048);
      }
      FENCE2(ak, av);
      const v2u kb = fold_T(ctx, ak, slot, csb + 64);
      vb[tt] = fold_F(ctx, av, slot, csb + 128);
      const v4u ph = feature_map(kb, wphi);
      const f4 tp = mfma16(v2u{ph.x, ph.y}, ident, ZERO4);
      const f4 tm = mfma16(v2u{ph.z, ph.w}, ident, ZERO4);
      php[tt] = v2u{pk2(tp[0], tp[1]), pk2(tp[2], tp[3])};
      phm[tt] = v2u{pk2(tm[0], tm[1]), pk2(tm[2], tm[3])};
    }
    const v4u ap = {php[0].x, php[0].y, php[1].x, php[1].y}, am = {phm[0].x, phm[0].y, phm[1].x, phm[1].y};
    const v4u bv = {vb[0].x, vb[0].y, vb[1].x, vb[1].y};
    kvp = mfma32(ap, bv, kvp); kvm = mfma32(am, bv, kvm);
    zz = mfma32(v4u{lo1, lo1, lo1, lo1}, ap, zz);
    zz = mfma32(v4u{hi1, hi1, hi1, hi1}, am, zz);
  }
  const unsigned T = ctx.T;
  constexpr int SL = 32 * 384;
  {
    const int blk = WS_KV1 + (L >> 1) * KV1_L + ((m * 64 + w * 16) * 32 + c) * 384;
    const int vo = g * 2 * SL + tok * 16;
    st128(ctx, v4u{asu(kvp[0]), T, asu(kvp[1]), T}, vo, blk);
    st128(ctx, v4u{asu(kvp[2]), T, asu(kvp[3]), T}, vo, blk + SL);
    st128(ctx, v4u{asu(kvm[0]), T, asu(kvm[1]), T}, vo, blk + 8 * SL);
    st128(ctx, v4u{asu(kvm[2]), T, asu(kvm[3]), T}, vo, blk + 9 * SL);
    if ((g & 1) == 0) st64(ctx, v2u{asu(zz[0]), T}, ((g >> 1) * 8 + (tok >> 1)) * SL + 256 + (tok & 1) * 8, blk);
  }
  STAMP(L * 8 + 2);
  v2u qb[4];
  {
    v4u wq[16];
    if (L == 0) BAR();
    lds_t* qt = ctx.L + opq(L_RQ + tr_base(lane, w));
#pragma unroll
    for (int ks = 0; ks < 16; ++ks) wq[ks] = tr_frag(qt, ks * 4096);
    if (L == 0) {
      f4 cs;
      v4u ones = ONES4;
      asm volatile("s_nop 1" : "+v"(ones));
      sfor<0, 16>([&](auto ks) { vamfma<ks == 0>(cs, wq[ks], ones); });
      FENCE1(cs);
      if (tok == 0) *(LAS f4*)(ctx.L + csb + g * 16) = cs;
    }
#pragma unroll
    for (int t = 0; t < 4; ++t) {
      lds_t* tb = ctx.L + opq(L_XT + (1 + t) * 16384 + lane * 16);
      f4 aq;
      v4u a[8];
#pragma unroll
      for (int i = 0; i < 6; ++i) a[i] = lds_ld128(tb, i * 1024);
      sfor<0, 16>([&](auto ks) {
        if constexpr (ks + 6 < 16) a[(ks + 6) & 7] = lds_ld128(tb, (ks + 6) * 1024);
        vamfma<ks == 0>(aq, wq[ks], a[ks & 7]);
      });
      if (t < 3) { BAR(); wo_chunk(ctx, wo, 1 + t); }
      FENCE1(aq);
      qb[t] = fold_T(ctx, aq, 1 + t, csb);
    }
  }
  if (w < 2) {
    const int S64 = 2 * c + w;
    const int blk = WS_KV1 + (L >> 1) * KV1_L + ((m * 64 + S64) * 32) * 384;
    float a0, a1, b0, b1;
    unsigned spins = 0;
    for (;;) {
      asm volatile("" ::: "memory");
      v4u dk[8];
#pragma unroll
      for (int i = 0; i < 8; ++i) dk[i] = ld128(ctx, g * 384 + tok * 16, blk + i * 4 * 384);
      const v4u dz = ld128(ctx, (lane & 31) * 384 + 256, blk);
      unsigned bad = (dz.y ^ T) | (dz.w ^ T);
      a0 = a1 = 0.f;
#pragma unroll
      for (int i = 0; i < 8; ++i) {
        bad |= (dk[i].y ^ T) | (dk[i].w ^ T);
        a0 += asf(dk[i].x); a1 += asf(dk[i].z);
      }
      b0 = asf(dz.x); b1 = asf(dz.z);
      if (__all(bad == 0) || ctx.fail) break;
      if (++spins > SPIN_CAP) { ctx.fail = true; break; }
    }
    a0 = xg_sum(a0); a1 = xg_sum(a1); b0 = sum32(b0); b1 = sum32(b1);
    if (g == 0) st128(ctx, v4u{pk2(a0, a1), T, pk2(b0, b1), T}, tok * 16, WS_KV2 + (L >> 1) * KV2_L + (m * 64 + S64) * 256);
  }
  STAMP(L * 8 + 3);
  v4u phq[4];
#pragma unroll
  for (int t = 0; t < 4; ++t) phq[t] = feature_map(qb[t], wphi);
  if (L == 0) { DRAIN(); part_step<P_BQ>(ctx, L_CS + (w * 6 + 3) * 64); }
  v4u akv, az;
  {
    const int b2 = WS_KV2 + (L >> 1) * KV2_L + (m * 64 + w * 16) * 256;
    const int vo = g * 512 + tok * 16;
    unsigned spins = 0;
    for (;;) {
      asm volatile("" ::: "memory");
      const v4u d0 = ld128(ctx, vo, b2);
      const v4u d1 = ld128(ctx, vo + 256, b2);
      const v4u d2 = ld128(ctx, vo + 8 * 256, b2);
      const v4u d3 = ld128(ctx, vo + 9 * 256, b2);
      const unsigned bad = (d0.y ^ T) | (d0.w ^ T) | (d1.y ^ T) | (d1.w ^ T) | (d2.y ^ T) | (d2.w ^ T) | (d3.y ^ T) | (d3.w ^ T);
      akv = v4u{d0.x, d1.x, d2.x, d3.x};
      az = v4u{d0.z, d1.z, d2.z, d3.z};
      if (__all(bad == 0) || ctx.fail) break;
      if (++spins > SPIN_CAP) { ctx.fail = true; break; }
    }
  }
  STAMP(L * 8 + 4);
  v2u o[4];
#pragma unroll
  for (int t = 0; t < 4; ++t) {
    const f4 num = mfma32(akv, phq[t], ZERO4);
    const f4 den = mfma32(az, phq[t], ZERO4);
    const float r = __builtin_amdgcn_rcpf(den[0] + 1e-6f);
    o[t] = v2u{pk2(num[0] * r, num[1] * r), pk2(num[2] * r, num[3] * r)};
  }
  publish_o(ctx, L, o);
}

template <int L>
DEV void layer_B(Ctx& ctx, const GAS unsigned char* wo, const GAS unsigned char* wa) {
  fresh(ctx);
  const int w = ctx.w, g = ctx.g, tok = ctx.tok;
  const int csb = L_CS + (w * 6 + 3) * 64;
  gather_issue<L>(ctx);
  WAIT_VM(16);
  BAR();
  STAMP(L * 8 + 1);
  const bool odd = tok & 1;
  const int ja = 4 * g + (odd ? 1 : 0), jb = ja + 2;
  const bool curA = ja <= tok, curB = jb <= tok;
  const unsigned selA = odd ? 0x01000c0cu : 0x0c0c0100u, selB = odd ? 0x03020c0cu : 0x0c0c0302u, selZ = 0x0c0c0c0cu;
  const unsigned sPA = curA ? selZ : selA, sPB = curB ? selZ : selB, sCA = curA ? selA : selZ, sCB = curB ? selB : selZ;
  v2u kprev, vprev;
  {
    f4 ak, av;
    gemm_tile<-1, P_BK, P_BV>(ctx, 0, ak, ak, av);
    WAIT_VM(12);
    BAR();
    wo_chunk(ctx, wo, 0);
    if (L == 1) img_chunk<3072>(ctx, wa, L_RQ, 0);
    FENCE2(ak, av);
    kprev = fold_T(ctx, ak, 0, csb + 64);
    vprev = fold_F(ctx, av, 0, csb + 128);
  }
  v2u o[4];
#pragma unroll
  for (int t = 0; t < 4; ++t) {
    const int slot = 1 + t;
    f4 aq, ak, av;
    gemm_tile<P_BQ, P_BK, P_BV>(ctx, slot, aq, ak, av);
    if (slot < 4) {
      if (L == 1) { if (slot == 1) WAIT_VM(16); else if (slot == 2) WAIT_VM(20); else WAIT_VM(24); } else WAIT_VM(12);
      BAR();
      wo_chunk(ctx, wo, slot);
      if (L == 1) img_chunk<3072>(ctx, wa, L_RQ, slot);
    }
    FENCE3(aq, ak, av);
    const v2u qb = fold_T(ctx, aq, slot, csb);
    const v2u kb = fold_T(ctx, ak, slot, csb + 64);
    const v2u vb = fold_F(ctx, av, slot, csb + 128);
    const float neg = (t == 0 && ctx.c == 0) ? -1e30f : 0.f;
    const f4 sp = mfma16(kprev, qb, f4{neg, neg, neg, neg});
    const f4 sc = mfma16(kb, qb, ZERO4);
    const float a = curA ? (odd ? sc[1] : sc[0]) : (odd ? sp[1] : sp[0]);
    const float b = curB ? (odd ? sc[3] : sc[2]) : (odd ? sp[3] : sp[2]);
    const float mx = xg_max(fmaxf(a, b));
    const float pa = __builtin_amdgcn_exp2f((a - mx) * (0.25f * LOG2E));
    const float pb = __builtin_amdgcn_exp2f((b - mx) * (0.25f * LOG2E));
    const unsigned pk = pk2(pa, pb);
    const v4u P = {__builtin_amdgcn_perm(pk, pk, sPA), __builtin_amdgcn_perm(pk, pk, sPB),
                   __builtin_amdgcn_perm(pk, pk, sCA), __builtin_amdgcn_perm(pk, pk, sCB)};
    const f4 num = mfma32(v4u{vprev.x, vprev.y, vb.x, vb.y}, P, ZERO4);
    const f4 den = mfma32(ONES4, P, ZERO4);
    const float r = __builtin_amdgcn_rcpf(den[0]);
    o[t] = v2u{pk2(num[0] * r, num[1] * r), pk2(num[2] * r, num[3] * r)};
    kprev = kb; vprev = vb;
  }
  STAMP(L * 8 + 4);
  publish_o(ctx, L, o);
}

template <int L>
DEV void out_proj(Ctx& ctx, f4 (&xacc)[4], GAS unsigned char* out) {
  fresh(ctx);
  const int c = ctx.c, m = ctx.m, w = ctx.w, lane = ctx.lane, g = ctx.g, tok = ctx.tok;
  lds_t* tb[4];
#pragma unroll
  for (int nt = 0; nt < 4; ++nt) tb[nt] = ctx.L + opq(L_XT + tr_base(lane, nt));
  v4u wf[3][4];
#pragma unroll
  for (int i = 0; i < 2; ++i)
#pragma unroll
    for (int nt = 0; nt < 4; ++nt) wf[i][nt] = tr_frag(tb[nt], i * 4096);
  if (L == 0) part_step<P_BK>(ctx, L_CS + (w * 6 + 4) * 64);
  wait_flags(ctx, WS_FO + (L * 32 + c) * 128, 0, false);
  STAMP(L * 8 + 5);
  v4u of[16];
#pragma unroll
  for (int ks = 0; ks < 16; ++ks)
    of[ks] = ld128(ctx, lane * 16 + (ks & 3) * 1024, WS_O + L * ACT_L + ((4 * c + w) * 16 + (ks & ~3)) * 1024);
  asm volatile("s_nop 1" : "+v"(xacc[0]), "+v"(xacc[1]), "+v"(xacc[2]), "+v"(xacc[3]));
  sfor<0, 16>([&](auto ks) {
    if constexpr (ks + 2 < 16) {
#pragma unroll
      for (int nt = 0; nt < 4; ++nt) wf[(ks + 2) % 3][nt] = tr_frag(tb[nt], (ks + 2) * 4096);
    }
#pragma unroll
    for (int nt = 0; nt < 4; ++nt) vbmfma(xacc[nt], wf[ks % 3][nt], of[ks]);
  });
  FENCE4(xacc[0], xacc[1], xacc[2], xacc[3]);
  STAMP(L * 8 + 6);
  const int G = ((g & 1) << 1) | (g >> 1);
  f4 ssum = ZERO4, gram = ZERO4;
#pragma unroll
  for (int u = 0; u < 2; ++u) {
    const f4 a = xacc[2 * u], b = xacc[2 * u + 1];
    auto r0 = __builtin_amdgcn_permlane16_swap(pk2(a[0], a[1]), pk2(b[0], b[1]), false, false);
    auto r1 = __builtin_amdgcn_permlane16_swap(pk2(a[2], a[3]), pk2(b[2], b[3]), false, false);
    const v4u fr = {r0[0], r1[0], r0[1], r1[1]};
    if (L < 3) {
      st128(ctx, fr, (G * 16 + tok) * 16, WS_X + L * ACT_L + ((4 * c + w) * 16 + 2 * m + u) * 1024);
      ssum = mfma32(ONES4, fr, ssum);
      gram = mfma32(fr, fr, gram);
    } else {
      *(GAS v4u*)(out + (size_t)(64 * c + 16 * w) * 1024 + (64 * m + 32 * u) * 2 + (unsigned)(tok * 1024 + G * 16)) = fr;
    }
  }
  if (L < 3) {
    const float q = (tok & 2) ? ((tok & 1) ? gram[3] : gram[2]) : ((tok & 1) ? gram[1] : gram[0]);
    if (g == (tok >> 2)) st64(ctx, v2u{asu(ssum[0]), asu(q)}, tok * 8, WS_STAT + L * STAT_L + ((c * 8 + m) * 64 + 16 * w) * 8);
    set_flag(ctx, WS_FX + (L * 32 + c) * 128);
    if (L == 0) part_step<P_BV>(ctx, L_CS + (w * 6 + 5) * 64);
  }
  STAMP(L * 8 + 7);
}

__global__ void __launch_bounds__(NT, 1) __attribute__((amdgpu_num_vgpr(96)))
fused_kernel(const unsigned char* __restrict__ x_, const unsigned char* __restrict__ wa_, const unsigned char* __restrict__ wphi_,
             const unsigned char* __restrict__ woa_, const unsigned char* __restrict__ wb_, const unsigned char* __restrict__ wob_,
             unsigned char* out_, unsigned char* ws_) {
  extern __shared__ __attribute__((aligned(16))) unsigned char smem[];
  asm volatile("" ::: "v255", "a255");
  const GAS unsigned char* x = (const GAS unsigned char*)x_;
  const GAS unsigned char* wa = (const GAS unsigned char*)wa_;
  const GAS unsigned char* woa = (const GAS unsigned char*)woa_;
  const GAS unsigned char* wob = (const GAS unsigned char*)wob_;
  Ctx ctx;
  ctx.ws = (GAS unsigned char*)ws_;
  ctx.rs = __builtin_amdgcn_make_buffer_rsrc((void*)ws_, 0, WS_END, 0x00020000);
  ctx.L = (lds_t*)smem;
  const int b = blockIdx.x, idx = b >> 3;
  ctx.c = (b & 7) * 4 + (idx >> 3);
  ctx.m = idx & 7;
  ctx.w = __builtin_amdgcn_readfirstlane(threadIdx.x >> 6);
  ctx.fail = false;
  fresh(ctx);
  const int c = ctx.c, m = ctx.m, w = ctx.w, lane = ctx.lane, g = ctx.g, tok = ctx.tok;
  STAMP(40);
  const __amdgpu_buffer_rsrc_t rsa = __builtin_amdgcn_make_buffer_rsrc((void*)wa_, 0, 512 * 3072, 0x00020000);
  const __amdgpu_buffer_rsrc_t rsb = __builtin_amdgcn_make_buffer_rsrc((void*)wb_, 0, 512 * 3072, 0x00020000);
  unsigned epoch, ph0, ph1, ph2, ph3;
  {
    const GAS unsigned char* pp = (const GAS unsigned char*)wphi_ + (4 * m + w) * 512;
    const unsigned pv = (unsigned)(4 * g * 16 + tok) * 2, zero = 0;
    asm volatile("s_nop 4\n\tglobal_load_dword %0, %5, %6 sc1\n\t"
                 "global_load_ushort %1, %7, %8\n\tglobal_load_ushort %2, %7, %8 offset:32\n\t"
                 "global_load_ushort %3, %7, %8 offset:64\n\tglobal_load_ushort %4, %7, %8 offset:96"
                 : "=&v"(epoch), "=&v"(ph0), "=&v"(ph1), "=&v"(ph2), "=&v"(ph3)
                 : "v"(zero), "s"(ctx.ws), "v"(pv), "s"(pp) : "memory");
  }
  {
    const GAS unsigned char* sb = x + (size_t)(64 * c + 16 * w) * 1024;
    const unsigned xv = (unsigned)(tok * 1024 + g * 16);
    const unsigned ldsbase = (unsigned)(size_t)(ctx.L) + L_XT + (1 + w) * 16384;
    unsigned keep;
#define XD(i) "global_load_lds_dwordx4 %1, %2 offset:" #i "\n\ts_add_u32 m0, m0, 0x3c0\n\ts_nop 0\n\t"
    asm volatile("s_mov_b32 %0, m0\n\ts_mov_b32 m0, %3\n\ts_nop 2\n\t"
                 XD(0) XD(64) XD(128) XD(192) XD(256) XD(320) XD(384) XD(448)
                 XD(512) XD(576) XD(640) XD(704) XD(768) XD(832) XD(896) XD(960)
                 "s_mov_b32 m0, %0"
                 : "=&s"(keep) : "v"(xv), "s"(sb), "s"(ldsbase) : "memory", "scc");
#undef XD
  }
  raw_load<P_AK>(ctx, rsa, 1024);
  raw_load<P_AV>(ctx, rsa, 2048);
  asm volatile("s_waitcnt vmcnt(32)" : "+v"(epoch), "+v"(ph0), "+v"(ph1), "+v"(ph2), "+v"(ph3) :: "memory");
  raw_load<P_BV>(ctx, rsa, 0);
  ctx.T = __builtin_amdgcn_readfirstlane(epoch) + 1u;
  v2u wphi = v2u{ph0 | (ph1 << 16), ph2 | (ph3 << 16)};
  const int csa = L_CS + (w * 6) * 64;
  WAIT_VM(40);
  raw_store<P_AK, 0, 8>(ctx);
  f4 xacc[4];
  {
    lds_t* tb = ctx.L + opq(L_XT + (1 + w) * 16384 + lane * 16);
    float s = 0.f, q = 0.f;
    const bf2 one2 = __builtin_bit_cast(bf2, 0x3f803f80u);
#pragma unroll
    for (int ks = 0; ks < 16; ++ks) {
      const v4u xs = lds_ld128(tb, ks * 1024);
#pragma unroll
      for (int j = 0; j < 4; ++j) {
        const unsigned xu = xs[j];
        const bf2 v = __builtin_bit_cast(bf2, xu);
        s = __builtin_amdgcn_fdot2_f32_bf16(v, one2, s, false);
        q = __builtin_amdgcn_fdot2_f32_bf16(v, v, q, false);
      }
    }
    put_stats(ctx, 1 + w, xg_sum(s), xg_sum(q));
    lds_t* xb = ctx.L + opq(L_XT + (1 + w) * 16384 + 2 * m * 1024 + ((g >> 1) * 16 + tok) * 16 + (g & 1) * 8);
#pragma unroll
    for (int nt = 0; nt < 4; ++nt) {
      const v2u v = *(LAS v2u*)(xb + nt * 512);
      xacc[nt] = f4{asf(v.x << 16), asf(v.x & 0xffff0000u), asf(v.y << 16), asf(v.y & 0xffff0000u)};
    }
  }
  BAR();
  part_tr<P_AK, 0, 8>(ctx);
  WAIT_VM(32);
  raw_store<P_AK, 8, 16>(ctx);
  BAR();
  part_tr<P_AK, 8, 16>(ctx);
  raw_load<P_BQ>(ctx, rsb, 0);
  WAIT_VM(40);
  raw_store<P_AV, 0, 8>(ctx);
  BAR();
  part_tr<P_AV, 0, 8>(ctx);
  part_cs<P_AK>(ctx, csa + 64);
  WAIT_VM(32);
  raw_store<P_AV, 8, 16>(ctx);
  BAR();
  part_tr<P_AV, 8, 16>(ctx);
  raw_load<P_BK>(ctx, rsb, 1024);
  part_cs<P_AV>(ctx, csa + 128);
  asm volatile("" : "+v"(xacc[0]), "+v"(xacc[1]), "+v"(xacc[2]), "+v"(xacc[3]), "+v"(wphi));
  STAMP(1);
  GAS unsigned char* out = (GAS unsigned char*)out_;

  layer_A<0>(ctx, wphi, woa, rsb);
  out_proj<0>(ctx, xacc, out);
  layer_B<1>(ctx, wob, wa);
  out_proj<1>(ctx, xacc, out);
  layer_A<2>(ctx, wphi, woa, rsb);
  out_proj<2>(ctx, xacc, out);
  layer_B<3>(ctx, wob, wa);
  out_proj<3>(ctx, xacc, out);
  if (b == 0 && threadIdx.x == 0) __hip_atomic_store((gu32*)(ctx.ws + WS_EPOCH), ctx.T, RLX_AGENT);
  STAMP(41);
}

extern "C" void kernel_launch(void* const* d_in, const int* in_sizes, int n_in, void* d_out, int out_size, void* d_ws,
                              size_t ws_size, hipStream_t stream) {
  (void)in_sizes; (void)n_in; (void)out_size; (void)ws_size;
  (void)hipFuncSetAttribute((const void*)fused_kernel, hipFuncAttributeMaxDynamicSharedMemorySize, LDS_BYTES);
  hipLaunchKernelGGL(fused_kernel, dim3(NWG), dim3(NT), LDS_BYTES, stream,
                     (const unsigned char*)d_in[0], (const unsigned char*)d_in[1], (const unsigned char*)d_in[2],
                     (const unsigned char*)d_in[3], (const unsigned char*)d_in[5], (const unsigned char*)d_in[6],
                     (unsigned char*)d_out, (unsigned char*)d_ws);
}
```

```cpp
#include <hip/hip_runtime.h>
#include <hip/hip_bf16.h>
#include <type_traits>

#define GAS __attribute__((address_space(1)))
#define LAS __attribute__((address_space(3)))
#define DEV __device__ __forceinline__
typedef short s4 __attribute__((ext_vector_type(4)));
typedef float f4 __attribute__((ext_vector_type(4)));
typedef float f2 __attribute__((ext_vector_type(2)));
typedef __bf16 bf2 __attribute__((ext_vector_type(2)));
typedef unsigned v4u __attribute__((ext_vector_type(4)));
typedef unsigned v2u __attribute__((ext_vector_type(2)));
typedef unsigned long long u64;
typedef GAS unsigned gu32;
typedef LAS unsigned char lds_t;
typedef __amdgpu_buffer_rsrc_t rsrc_t;
#define RLX_AGENT __ATOMIC_RELAXED, __HIP_MEMORY_SCOPE_AGENT

namespace {
constexpr int NWG = 256, NT = 256;
constexpr int WS_EPOCH = 0;
constexpr int WS_FX = 4096;
constexpr int WS_FO = WS_FX + 4 * 32 * 128;
constexpr int WS_KV2 = 1 << 16;
constexpr int KV2_L = 8 * 64 * 256;
constexpr int WS_STAMP = 3 << 17;
constexpr int WS_STAT = 1 << 20;
constexpr int STAT_L = 32 * 8 * 64 * 16;
constexpr int WS_X = 2 << 20;
constexpr int ACT_L = 2 << 20;
constexpr int WS_O = WS_X + 3 * ACT_L;
constexpr int WS_KV1 = WS_O + 4 * ACT_L;
constexpr int KV1_L = 8 * 64 * 32 * 384;
constexpr int WS_END = WS_KV1 + 2 * KV1_L;
constexpr int L_XT = 0;
constexpr int L_RQ = 81920;
constexpr int L_REC = 147456 + 256;
constexpr int L_NM = L_REC + 1280;
constexpr int L_RS = L_NM + 320;
constexpr int L_CS = L_RS + 320;
constexpr int L_END = L_CS + 4 * 2 * 3 * 64;
constexpr int LDS_BYTES = 158 * 1024;
static_assert(L_END <= LDS_BYTES, "lds");
constexpr int SPIN_CAP = 1 << 18;
constexpr float LOG2E = 1.4426950408889634f;
#define QPOLL 15
}

DEV float asf(unsigned u) { return __builtin_bit_cast(float, u); }
DEV unsigned asu(float f) { return __builtin_bit_cast(unsigned, f); }
DEV unsigned pk2(float a, float b) { unsigned r; asm("v_cvt_pk_bf16_f32 %0, %1, %2" : "=v"(r) : "v"(a), "v"(b)); return r; }
DEV float xg_sum(float v) {
  auto r = __builtin_amdgcn_permlane16_swap(asu(v), asu(v), false, false);
  v = asf(r[0]) + asf(r[1]);
  auto q = __builtin_amdgcn_permlane32_swap(asu(v), asu(v), false, false);
  return asf(q[0]) + asf(q[1]);
}
DEV float row_sum(float v) {
  v += asf(__builtin_amdgcn_update_dpp(0u, asu(v), 0x128, 0xf, 0xf, false));
  v += asf(__builtin_amdgcn_update_dpp(0u, asu(v), 0x124, 0xf, 0xf, false));
  v += asf(__builtin_amdgcn_update_dpp(0u, asu(v), 0x122, 0xf, 0xf, false));
  v += asf(__builtin_amdgcn_update_dpp(0u, asu(v), 0x121, 0xf, 0xf, false));
  return v;
}
DEV float sum32(float v) {
  v = row_sum(v);
  auto r = __builtin_amdgcn_permlane16_swap(asu(v), asu(v), false, false);
  return asf(r[0]) + asf(r[1]);
}
#define BAR() asm volatile("s_waitcnt lgkmcnt(0)\n\ts_barrier" ::: "memory")
#define RAWBAR() do { asm volatile("" ::: "memory"); __builtin_amdgcn_s_barrier(); asm volatile("" ::: "memory"); } while (0)
#define DRAIN() asm volatile("s_waitcnt vmcnt(0)" ::: "memory")
#define WAIT_VM(n) asm volatile("s_waitcnt vmcnt(%0)" :: "n"(n) : "memory")
#define WAIT_LG(n) __builtin_amdgcn_s_waitcnt(0xC07F | ((n) << 8))
#define WAIT_VMV(n) __builtin_amdgcn_s_waitcnt(0x0F70 | ((n) & 15) | (((n) >> 4) << 14))
#define VNOP() __builtin_amdgcn_s_setprio(0)
#define SB() __builtin_amdgcn_sched_barrier(0)
#define NOP(n) asm volatile("s_nop %0" :: "n"(n))
#define SC1 16
#define ONE2 0x3f803f80u
template <int N> using IC = std::integral_constant<int, N>;

DEV unsigned mk1() { unsigned r; asm volatile("v_mov_b32 %0, 0x3f803f80" : "=v"(r)); return r; }
DEV unsigned mk0() { unsigned r; asm volatile("v_mov_b32 %0, 0" : "=v"(r)); return r; }
DEV int opq(int v) { asm volatile("" : "+v"(v)); return v; }
DEV v4u lds_ld128(lds_t* L, int off) { return *(LAS v4u*)(L + off); }
DEV f4 lds_ldf4(lds_t* L, int off) { return *(LAS f4*)(L + off); }
DEV float lds_ldf(lds_t* L, int off) { return *(LAS float*)(L + off); }
DEV v4u tr_frag(lds_t* L, int off) {
  s4 a = __builtin_amdgcn_ds_read_tr16_b64_v4i16((LAS s4*)(L + off));
  s4 b = __builtin_amdgcn_ds_read_tr16_b64_v4i16((LAS s4*)(L + off + 512));
  v2u ua = __builtin_bit_cast(v2u, a), ub = __builtin_bit_cast(v2u, b);
  return v4u{ua.x, ua.y, ub.x, ub.y};
}
template <int I, int N, class F> DEV void sfor(F&& f) { if constexpr (I < N) { f(IC<I>{}); sfor<I + 1, N>(f); } }

constexpr int P_AK = 0, P_AV = 16, P_BQ = 32, P_BK = 48, P_BV = 64;
#define MF32 "v_mfma_f32_16x16x32_bf16 "
#define MF16 "v_mfma_f32_16x16x16_bf16 "
#define MM_CASE(WC, XC)                                                                                                         \
  if constexpr (WA) {                                                                                                           \
    if constexpr (FIRST) asm volatile(MF32 "%0, " WC "[%c1:%c2], " XC "[%c3:%c4], 0" : "=v"(acc) : "n"(R), "n"(R + 3), "n"(X), "n"(X + 3)); \
    else asm volatile(MF32 "%0, " WC "[%c1:%c2], " XC "[%c3:%c4], %0" : "+v"(acc) : "n"(R), "n"(R + 3), "n"(X), "n"(X + 3));     \
  } else {                                                                                                                      \
    if constexpr (FIRST) asm volatile(MF32 "%0, " XC "[%c3:%c4], " WC "[%c1:%c2], 0" : "=v"(acc) : "n"(R), "n"(R + 3), "n"(X), "n"(X + 3)); \
    else asm volatile(MF32 "%0, " XC "[%c3:%c4], " WC "[%c1:%c2], %0" : "+v"(acc) : "n"(R), "n"(R + 3), "n"(X), "n"(X + 3));     \
  }
template <int F, int RS, bool WA, bool FIRST>
DEV void mm(f4& acc) {
  constexpr int R = 96 + 4 * (F < 40 ? F : F - 40), X = 88 + 4 * (RS & 1);
  if constexpr (F < 40) { if constexpr (RS < 2) { MM_CASE("v", "v") } else { MM_CASE("v", "a") } }
  else { if constexpr (RS < 2) { MM_CASE("a", "v") } else { MM_CASE("a", "a") } }
}
template <int RS, int OFF>
DEV void ring_rd(unsigned addr) {
  constexpr int X = 88 + 4 * (RS & 1);
  if constexpr (RS < 2) asm volatile("ds_read_b128 v[%c1:%c2], %0 offset:%c3" :: "v"(addr), "n"(X), "n"(X + 3), "n"(OFF) : "memory");
  else asm volatile("ds_read_b128 a[%c1:%c2], %0 offset:%c3" :: "v"(addr), "n"(X), "n"(X + 3), "n"(OFF) : "memory");
}
template <int F, bool FIRST>
DEV void wcs(f4& acc, v4u x) {
  constexpr int R = 96 + 4 * (F < 40 ? F : F - 40);
  if constexpr (F < 40) {
    if constexpr (FIRST) asm volatile(MF32 "%0, %1, v[%c2:%c3], 0" : "=&v"(acc) : "v"(x), "n"(R), "n"(R + 3));
    else asm volatile(MF32 "%0, %1, v[%c2:%c3], %0" : "+v"(acc) : "v"(x), "n"(R), "n"(R + 3));
  } else {
    if constexpr (FIRST) asm volatile(MF32 "%0, %1, a[%c2:%c3], 0" : "=&v"(acc) : "v"(x), "n"(R), "n"(R + 3));
    else asm volatile(MF32 "%0, %1, a[%c2:%c3], %0" : "+v"(acc) : "v"(x), "n"(R), "n"(R + 3));
  }
}
template <int RS, bool FIRST>
DEV void mmq(f4& acc, v4u a) {
  constexpr int X = 88 + 4 * (RS & 1);
  if constexpr (RS < 2) {
    if constexpr (FIRST) asm volatile(MF32 "%0, %1, v[%c2:%c3], 0" : "=v"(acc) : "a"(a), "n"(X), "n"(X + 3));
    else asm volatile(MF32 "%0, %1, v[%c2:%c3], %0" : "+v"(acc) : "a"(a), "n"(X), "n"(X + 3));
  } else {
    if constexpr (FIRST) asm volatile(MF32 "%0, %1, a[%c2:%c3], 0" : "=v"(acc) : "a"(a), "n"(X), "n"(X + 3));
    else asm volatile(MF32 "%0, %1, a[%c2:%c3], %0" : "+v"(acc) : "a"(a), "n"(X), "n"(X + 3));
  }
}
template <bool FIRST>
DEV void mf_av(f4& acc, v4u a, v4u b) {
  if constexpr (FIRST) asm volatile(MF32 "%0, %1, %2, 0" : "=&v"(acc) : "a"(a), "v"(b));
  else asm volatile(MF32 "%0, %1, %2, %0" : "+v"(acc) : "a"(a), "v"(b));
}
DEV void mf_va(f4& acc, v4u a, v4u b) { asm volatile(MF32 "%0, %1, %2, %0" : "+v"(acc) : "v"(a), "a"(b)); }
DEV void mf_aa(f4& acc, v4u a, v4u b) { asm volatile(MF32 "%0, %1, %2, %0" : "+v"(acc) : "a"(a), "a"(b)); }
DEV void mf32z(f4& d, v4u a, v4u b) { asm volatile(MF32 "%0, %1, %2, 0" : "=&v"(d) : "v"(a), "v"(b)); VNOP(); }
DEV void mf32c(f4& d, v4u a, v4u b) { asm volatile(MF32 "%0, %1, %2, %0" : "+v"(d) : "v"(a), "v"(b)); VNOP(); }
DEV void mf16z(f4& d, v2u a, v2u b) { asm volatile(MF16 "%0, %1, %2, 0" : "=&v"(d) : "v"(a), "v"(b)); VNOP(); }
DEV void mf16c(f4& d, v2u a, v2u b) { asm volatile(MF16 "%0, %1, %2, %0" : "+v"(d) : "v"(a), "v"(b)); VNOP(); }
DEV void mf16z_a(f4& d, v2u a, v2u b) { asm volatile(MF16 "%0, %1, %2, 0" : "=&v"(d) : "a"(a), "v"(b)); VNOP(); }
DEV void mf16c_a(f4& d, v2u a, v2u b) { asm volatile(MF16 "%0, %1, %2, %0" : "+v"(d) : "a"(a), "v"(b)); VNOP(); }
DEV void mf16z_b(f4& d, v2u a, v2u b) { asm volatile(MF16 "%0, %1, %2, 0" : "=&v"(d) : "v"(a), "a"(b)); VNOP(); }
template <int F, int OFF>
DEV void wtr(unsigned addr) {
  constexpr int R = 96 + 4 * (F < 40 ? F : F - 40);
  if constexpr (F < 40)
    asm volatile("ds_read_b64_tr_b16 v[%c1:%c2], %0 offset:%c5\n\tds_read_b64_tr_b16 v[%c3:%c4], %0 offset:%c6"
                 :: "v"(addr), "n"(R), "n"(R + 1), "n"(R + 2), "n"(R + 3), "n"(OFF), "n"(OFF + 512) : "memory");
  else
    asm volatile("ds_read_b64_tr_b16 a[%c1:%c2], %0 offset:%c5\n\tds_read_b64_tr_b16 a[%c3:%c4], %0 offset:%c6"
                 :: "v"(addr), "n"(R), "n"(R + 1), "n"(R + 2), "n"(R + 3), "n"(OFF), "n"(OFF + 512) : "memory");
}
#define A_FMA(d, a, b, c) asm volatile("v_fma_f32 %0, %1, %2, %3" : "=v"(d) : "v"(a), "v"(b), "v"(c))
#define A_FMAN(d, a, b, c) asm volatile("v_fma_f32 %0, %1, -%2, %3" : "=v"(d) : "v"(a), "v"(b), "v"(c))
#define A_MUL(d, a, b) asm volatile("v_mul_f32 %0, %1, %2" : "=v"(d) : "v"(a), "v"(b))
#define A_MULS(d, k, b) asm volatile("v_mul_f32 %0, %1, %2" : "=v"(d) : "s"(k), "v"(b))
#define A_MAX(d, a, b) asm volatile("v_max_f32 %0, %1, %2" : "=v"(d) : "v"(a), "v"(b))
#define A_SUB(d, a, b) asm volatile("v_sub_f32 %0, %1, %2" : "=v"(d) : "v"(a), "v"(b))
#define A_CVT(d, a, b) asm volatile("v_cvt_pk_bf16_f32 %0, %1, %2" : "=v"(d) : "v"(a), "v"(b))
#define A_EXP(d, a) asm volatile("v_exp_f32 %0, %1" : "=v"(d) : "v"(a))
#define A_RCP(d, x4) asm volatile("v_rcp_f32 %0, %1" : "=&v"(d) : "v"(x4[0]), "v"(x4))
#define A_SEL(d, f, t, m) asm volatile("v_cndmask_b32 %0, %1, %2, %3" : "=v"(d) : "v"(f), "v"(t), "s"(m))
#define A_PERM(d, x, sel) asm volatile("v_perm_b32 %0, %1, %1, %2" : "=v"(d) : "v"(x), "v"(sel))
#define A_SW16(a, b) asm volatile("v_permlane16_swap_b32 %0, %1" : "+v"(a), "+v"(b))
#define A_SW32(a, b) asm volatile("v_permlane32_swap_b32 %0, %1" : "+v"(a), "+v"(b))
#define A_LD64(d, addr, off) asm volatile("ds_read_b64 %0, %1 offset:%2" : "=v"(d) : "v"(addr), "n"(off) : "memory")
#define A_LD128(d, addr, off) asm volatile("ds_read_b128 %0, %1 offset:%2" : "=v"(d) : "v"(addr), "n"(off) : "memory")

#define STAMP(i) do {} while (0)
#define CSTAMP(i) do {} while (0)
#define STAMP_FLUSH() do {} while (0)
#define TS(k) do {} while (0)
#define TS_DECL
#define TS_FLUSH(base, n) do {} while (0)

struct Ctx {
  GAS unsigned char* ws;
  rsrc_t rs;
  lds_t* L;
  unsigned Lb, Lw;
  int c, m, w;
  int lane, tok, g;
  unsigned T;
  int budget;
};
DEV void fresh(Ctx& ctx) {
  int l;
  asm volatile("v_mbcnt_lo_u32_b32 %0, -1, 0\n\tv_mbcnt_hi_u32_b32 %0, -1, %0" : "=v"(l));
  ctx.lane = l; ctx.tok = l & 15; ctx.g = l >> 4;
  asm volatile("" : "+s"(ctx.c), "+s"(ctx.m), "+s"(ctx.w), "+s"(ctx.Lb));
  ctx.Lw = ctx.Lb + 4096 * ctx.w;
}
DEV v4u ld128(Ctx& ctx, int voff, int soff) { return __builtin_amdgcn_raw_buffer_load_b128(ctx.rs, voff, soff, SC1); }
DEV void st128(Ctx& ctx, v4u v, int voff, int soff) { __builtin_amdgcn_raw_buffer_store_b128(v, ctx.rs, voff, soff, SC1); }
DEV void st64(Ctx& ctx, v2u v, int voff, int soff) { __builtin_amdgcn_raw_buffer_store_b64(v, ctx.rs, voff, soff, SC1); }

DEV void wait_flags(Ctx& ctx, int off0, int off1, bool two) {
  if (ctx.w == 0) {
    const int l = ctx.lane & (two ? 15 : 7);
    const int vo = (l < 8 ? off0 : off1 - 128) + l * 16;
    for (;;) {
      asm volatile("" ::: "memory");
      const unsigned v = __builtin_amdgcn_raw_buffer_load_b32(ctx.rs, vo, 0, SC1);
      if (__all(v == ctx.T) || --ctx.budget < 0) break;
    }
    __builtin_amdgcn_fence(__ATOMIC_ACQUIRE, "wavefront");
  }
  BAR();
}
DEV void flag_store(Ctx& ctx, int off) {
  if (ctx.w == 0 && ctx.lane == 0) __hip_atomic_store((gu32*)(ctx.ws + off + ctx.m * 16), ctx.T, RLX_AGENT);
}

DEV int x0_lane(int tok, int g) { return tok * 64 + ((g ^ ((4 - (tok >> 2)) & 3)) << 4); }
DEV int tr_base(int lane, int j) {
  const int g = lane >> 4, q = (lane & 15) >> 2, p = lane & 3;
  const int s = ((q >> 1) & 1) | ((g & 1) << 1);
  return (8 * g + q) * 128 + ((j ^ s) << 5) + p * 8;
}
template <int F>
DEV void raw_ld1(unsigned voff, rsrc_t rs, int soff) {
  constexpr int R = 96 + 4 * (F < 40 ? F : F - 40);
  if constexpr (F < 40) asm volatile("buffer_load_dwordx4 v[%c3:%c4], %0, %1, %2 offen" :: "v"(voff), "s"(rs), "s"(soff), "n"(R), "n"(R + 3) : "memory");
  else asm volatile("buffer_load_dwordx4 a[%c3:%c4], %0, %1, %2 offen" :: "v"(voff), "s"(rs), "s"(soff), "n"(R), "n"(R + 3) : "memory");
}
template <int P, int I0, int I1>
DEV void raw_load(Ctx& ctx, rsrc_t rs, int colbyte) {
  const unsigned vo = (unsigned)((ctx.lane >> 3) * 3072 + (ctx.lane & 7) * 16);
  const int sb = colbyte + ctx.m * 128 + 32 * ctx.w * 3072;
  sfor<I0, I1>([&](auto i) { raw_ld1<P + i>(vo, rs, sb + (128 * (i >> 2) + 8 * (i & 3)) * 3072); });
}
template <int F, int OFF>
DEV void raw_st1(unsigned addr) {
  constexpr int R = 96 + 4 * (F < 40 ? F : F - 40);
  if constexpr (F < 40) asm volatile("ds_write_b128 %0, v[%c1:%c2] offset:%c3" :: "v"(addr), "n"(R), "n"(R + 3), "n"(OFF) : "memory");
  else asm volatile("ds_write_b128 %0, a[%c1:%c2] offset:%c3" :: "v"(addr), "n"(R), "n"(R + 3), "n"(OFF) : "memory");
}
template <int P, int I0, int I1>
DEV void raw_store(Ctx& ctx) {
  const int lane = ctx.lane, ch = lane & 7, rb1 = (lane >> 4) & 1;
  const unsigned base = ctx.Lb + L_RQ + (32 * ctx.w + (lane >> 3)) * 128 + (ch & 1) * 16;
  const unsigned p0 = base + (((ch >> 1) ^ rb1) << 5), p1 = base + (((ch >> 1) ^ (rb1 | 2)) << 5);
  sfor<I0, I1>([&](auto i) { raw_st1<P + i, (i >> 2) * 16384 + (i & 3) * 1024>((i & 1) ? p1 : p0); });
}
template <int P, int K0, int K1>
DEV void part_tr(Ctx& ctx) {
  const unsigned tb = ctx.Lb + L_RQ + tr_base(ctx.lane, ctx.w);
  sfor<K0, K1>([&](auto ks) { wtr<P + ks, ks * 4096>(tb); });
}
template <int P>
DEV void part_cs(Ctx& ctx, int csoff) {
  v4u ones = {mk1(), mk1(), mk1(), mk1()};
  asm volatile("s_waitcnt lgkmcnt(0)\n\ts_nop 1" : "+v"(ones) :: "memory");
  f4 c0, c1;
  sfor<0, 8>([&](auto k) { wcs<P + 2 * k, k == 0>(c0, ones); wcs<P + 2 * k + 1, k == 0>(c1, ones); });
  asm volatile("s_nop 7" : "+v"(c0), "+v"(c1));
  if (ctx.g == 0) *(LAS float*)(ctx.L + csoff + ctx.tok * 4) = c0[0] + c1[0];
}

template <int P>
DEV void part_step(Ctx& ctx, int csoff) {
  raw_store<P, 0, 8>(ctx);
  BAR();
  part_tr<P, 0, 8>(ctx);
  raw_store<P, 8, 16>(ctx);
  BAR();
  part_tr<P, 8, 16>(ctx);
  part_cs<P>(ctx, csoff);
}

template <int OFF>
DEV void pf1(unsigned lv, rsrc_t rs, int so) {
  asm volatile("buffer_load_dwordx4 v[224:227], %0, %1, %2 offen offset:%3" :: "v"(lv), "s"(rs), "s"(so), "n"(OFF) : "memory");
}

struct ImgDma { unsigned v[4]; int sb; };
template <int ROWB>
DEV ImgDma img_prep(Ctx& ctx) {
  const int lane = ctx.lane, cp = lane & 7, rb1 = (lane >> 4) & 1;
  const unsigned vb = (unsigned)((lane >> 3) * ROWB + (cp & 1) * 16);
  const unsigned v0 = vb + (((cp >> 1) ^ rb1) << 5), v1 = vb + (((cp >> 1) ^ (rb1 | 2)) << 5);
  ImgDma d;
  d.v[0] = v0; d.v[1] = v1 + (8 * ROWB - 1024); d.v[2] = v0 + 2 * (8 * ROWB - 1024); d.v[3] = v1 + 3 * (8 * ROWB - 1024);
  d.sb = ctx.m * 128 + 32 * ctx.w * ROWB;
  return d;
}
template <int LOFF> DEV void dma_m0(unsigned lbase) { asm volatile("s_add_u32 m0, %0, %1" :: "s"(lbase), "n"(LOFF) : "scc"); }
template <int SOFF> DEV void dma_so(int& t, int sbase) { asm volatile("s_add_u32 %0, %1, %2" : "=s"(t) : "s"(sbase), "n"(SOFF) : "scc"); }
template <int I> DEV void dma_go(unsigned voff, rsrc_t rs, int so) {
  asm volatile("buffer_load_dwordx4 %0, %1, %2 offen offset:%3 lds" :: "v"(voff), "s"(rs), "s"(so), "n"(I * 1024) : "memory");
}
template <int I> DEV void dma_go_sc1(unsigned voff, rsrc_t rs, int so) {
  asm volatile("buffer_load_dwordx4 %0, %1, %2 offen offset:%3 sc1 lds" :: "v"(voff), "s"(rs), "s"(so), "n"(I * 1024) : "memory");
}
template <int ROWB, int COLB, int LDSOFF, int J>
DEV void img_chunk(Ctx& ctx, const ImgDma& d, rsrc_t rs) {
  int so;
  dma_m0<LDSOFF + J * 16384>(ctx.Lw); dma_so<COLB + 128 * J * ROWB>(so, d.sb);
  sfor<0, 4>([&](auto i) { dma_go<i>(d.v[i], rs, so); });
}
template <int TT>
DEV void x_chunk(Ctx& ctx, unsigned lv, int xs) {
  int so;
  dma_m0<L_XT + (TT + 1) * 16384>(ctx.Lw); dma_so<(TT + 1) * 16384>(so, xs);
  sfor<0, 4>([&](auto i) { dma_go_sc1<i>(lv, ctx.rs, so); });
}

struct ST {
  v4u d[4];
  float s, q, t, bias;
  unsigned bad, sv, wrec, wnm;
  int so;
};
template <bool HALO>
DEV void st_prep(Ctx& ctx, ST& st, int L) {
  const int c = ctx.c, w = ctx.w, r32 = ctx.lane & 31, hf = ctx.lane >> 5;
  const bool halo = HALO && w >= 2;
  const int cc = (halo && c > 0) ? c - 1 : c;
  const int row0 = halo ? (c > 0 ? 48 : 0) : 32 * (w & 1);
  const int r = halo ? (r32 & 15) : r32;
  st.so = WS_STAT + (L - 1) * STAT_L + (cc * 8 * 64 + row0) * 16;
  st.sv = hf * 4096 + r * 16;
  const int lrow = halo ? r : 16 + row0 + r;
  st.wrec = ctx.Lb + L_REC + lrow * 16;
  st.wnm = ctx.Lb + L_NM + lrow * 4;
  st.bias = (halo && c == 0) ? -__builtin_inff() : 0.f;
  asm volatile("" : "+v"(st.sv), "+v"(st.wrec), "+v"(st.wnm), "+v"(st.bias));
}
DEV void st_issue(Ctx& ctx, ST& st) {
  asm volatile("buffer_load_dwordx4 %0, %4, %5, %6 offen sc1\n\t"
               "buffer_load_dwordx4 %1, %4, %5, %6 offen offset:1024 sc1\n\t"
               "buffer_load_dwordx4 %2, %4, %5, %6 offen offset:2048 sc1\n\t"
               "buffer_load_dwordx4 %3, %4, %5, %6 offen offset:3072 sc1"
               : "=&v"(st.d[0]), "=&v"(st.d[1]), "=&v"(st.d[2]), "=&v"(st.d[3]) : "v"(st.sv), "s"(ctx.rs), "s"(st.so) : "memory");
}
DEV void st_retry(Ctx& ctx, ST& st) {
  for (;;) {
    asm volatile("" ::: "memory");
    unsigned bad = 0;
#pragma unroll
    for (int i = 0; i < 4; ++i) { st.d[i] = ld128(ctx, st.sv + i * 1024, st.so); }
#pragma unroll
    for (int i = 0; i < 4; ++i) bad |= (st.d[i].y ^ ctx.T) | (st.d[i].w ^ ctx.T);
    if (__all(bad == 0) || --ctx.budget < 0) break;
  }
}
DEV void st_check(Ctx& ctx, ST& st) {
  asm volatile("" : "+v"(st.d[0]), "+v"(st.d[1]), "+v"(st.d[2]), "+v"(st.d[3]));
  const unsigned T = ctx.T;
  const bool ok = (st.d[0].y == T) & (st.d[0].w == T) & (st.d[1].y == T) & (st.d[1].w == T) &
                  (st.d[2].y == T) & (st.d[2].w == T) & (st.d[3].y == T) & (st.d[3].w == T);
  if (__builtin_expect(!__all(ok), 0)) st_retry(ctx, st);
}
template <int K>
DEV void st_step(ST& st) {
  if constexpr (K == 0) asm volatile("v_add_f32 %0, %2, %3\n\tv_add_f32 %1, %4, %5" : "=&v"(st.s), "=&v"(st.q) : "v"(st.d[0].x), "v"(st.d[1].x), "v"(st.d[0].z), "v"(st.d[1].z));
  if constexpr (K == 1) asm volatile("v_add_f32 %0, %0, %2\n\tv_add_f32 %1, %1, %3" : "+v"(st.s), "+v"(st.q) : "v"(st.d[2].x), "v"(st.d[2].z));
  if constexpr (K == 2) asm volatile("v_add_f32 %0, %0, %2\n\tv_add_f32 %1, %1, %3" : "+v"(st.s), "+v"(st.q) : "v"(st.d[3].x), "v"(st.d[3].z));
  if constexpr (K == 3) A_SW32(st.s, st.q);
  if constexpr (K == 4) { float x = st.s, y = st.q; asm volatile("v_add_f32 %0, %2, %3\n\tv_add_f32 %1, %2, %3" : "=&v"(st.s), "=&v"(st.q) : "v"(x), "v"(y)); }
  if constexpr (K == 5) A_SW32(st.s, st.q);
  if constexpr (K == 6) asm volatile("v_mul_f32 %0, %2, %0\n\tv_mul_f32 %1, %3, %1" : "+v"(st.s), "+v"(st.q) : "s"(-1.0f / 512.0f), "s"(1.0f / 512.0f));
  if constexpr (K == 7) asm volatile("v_fma_f32 %0, -%1, %1, %0\n\tv_max_f32 %0, 0, %0" : "+v"(st.q) : "v"(st.s));
  if constexpr (K == 8) asm volatile("v_add_f32 %0, %1, %0" : "+v"(st.q) : "s"(1e-5f));
  if constexpr (K == 9) asm volatile("v_rsq_f32 %0, %0" : "+v"(st.q));
  if constexpr (K == 10) asm volatile("v_mov_b32 %0, %1" : "=v"(st.t) : "v"(st.q));
  if constexpr (K == 11) asm volatile("ds_write_b128 %0, %1" :: "v"(st.wrec), "v"(f4{st.s, st.q, st.t, st.bias}) : "memory");
  if constexpr (K == 12) asm volatile("ds_write2_b32 %0, %1, %2 offset1:80" :: "v"(st.wnm), "v"(st.s), "v"(st.q) : "memory");
}

template <bool HALO>
DEV void gather_start(Ctx& ctx, ST& st, int L, unsigned lv, int xs) {
  const int c = ctx.c;
  const int cprev = c > 0 ? c - 1 : 0;
  constexpr int T0 = HALO ? -1 : 0;
  const int xs0 = (HALO && c == 0) ? xs + 16384 : xs;
  st_prep<HALO>(ctx, st, L);
  wait_flags(ctx, WS_FX + ((L - 1) * 32 + c) * 128, WS_FX + ((L - 1) * 32 + cprev) * 128, HALO);
  STAMP(L * 8 + 0);
  x_chunk<T0>(ctx, lv, xs0);
  st_issue(ctx, st);
  x_chunk<T0 + 1>(ctx, lv, xs);
  WAIT_VM(8);
  RAWBAR();
  STAMP(L * 8 + 1);
}
template <int H>
DEV void st_gaps(Ctx& ctx, ST& st) {
  if constexpr (H == 4 * 2 + 2) st_check(ctx, st);
  if constexpr (H == 4 * 3 + 2) st_step<0>(st);
  if constexpr (H == 4 * 4 + 2) st_step<1>(st);
  if constexpr (H == 4 * 5) st_step<2>(st);
  if constexpr (H == 4 * 5 + 2) st_step<3>(st);
  if constexpr (H == 4 * 6) st_step<4>(st);
  if constexpr (H == 4 * 6 + 2) st_step<5>(st);
  if constexpr (H == 4 * 7 + 2) st_step<6>(st);
  if constexpr (H == 4 * 8) st_step<7>(st);
  if constexpr (H == 4 * 8 + 2) { st_step<8>(st); }
  if constexpr (H == 4 * 9) st_step<9>(st);
  if constexpr (H == 4 * 9 + 2) { st_step<10>(st); st_step<12>(st); }
  if constexpr (H == 4 * 10) st_step<11>(st);
}

template <int OFF> DEV void ring_prime(unsigned ab) { ring_rd<0, OFF>(ab); ring_rd<1, OFF + 1024>(ab); ring_rd<2, OFF + 2048>(ab); WAIT_LG(2); }
template <int PQ, int PK, int PV, int OFF, bool CINIT, class XL, class SH, class GP>
DEV void gemm_tile(unsigned ab, f4& aq, f4& ak, f4& av, XL&& xl, SH&& sh, GP&& gp) {
  sfor<0, 16>([&](auto ks) {
    constexpr bool F = ks == 0 && !CINIT;
    if constexpr (PQ >= 0) {
      mm<(PQ >= 0 ? PQ : 0) + ks, ks & 3, true, F>(aq);
      if (!sh(IC<4 * ks>{})) VNOP();
      ring_rd<(ks + 3) & 3, OFF + (ks + 3) * 1024>(ab);
      gp(IC<4 * ks>{});
      mm<PK + ks, ks & 3, true, F>(ak);
      if (!sh(IC<4 * ks + 1>{})) VNOP();
      gp(IC<4 * ks + 1>{});
    } else {
      mm<PK + ks, ks & 3, true, F>(ak);
      if (!sh(IC<4 * ks>{})) VNOP();
      ring_rd<(ks + 3) & 3, OFF + (ks + 3) * 1024>(ab);
      gp(IC<4 * ks>{});
    }
    mm<PV + ks, ks & 3, false, F>(av);
    WAIT_LG(2 + decltype(xl(ks))::value);
    gp(IC<4 * ks + 2>{});
    SB();
  });
}
#define GA(k) (4 * (k))
#define GB(k) (4 * (k) + 1)
#define GC(k) (4 * (k) + 2)

struct BE {
  f4 aq, ak, av;
  f2 st, ka, kb2; f4 nm4;
  f2 nst, nka, nkb; f4 nnm4;
  f4 csq, csk; float csv;
  float q[4], k[4], v[4];
  unsigned qb[2], kb[2], vb[2], vb2[2], kprev[2], vprev[2], P[4], pk0, pk, ob[2];
  f4 sp, sc, num, den;
  float a, b, t0, t1, m, m2, ft, fa, fb, pa, pb;
  v2u ones;
  unsigned sPA, sPB, sCA, sCB;
  u64 modd, mA, mB;
  unsigned aT, aF, aKa, aKb;
  int ovo;
};

DEV void layer_B(Ctx& ctx, int L, rsrc_t rwo) {
  fresh(ctx);
  const int c = ctx.c, w = ctx.w, g = ctx.g, tok = ctx.tok;
  const int csb = L_CS + (w * 6 + 3) * 64;
  const unsigned lv = ctx.lane * 16;
  BE e;
  TS_DECL;
  unsigned ab0, ab1;
  ImgDma wd = img_prep<1024>(ctx);
  {
    const bool odd = tok & 1;
    const int ja = 4 * g + (odd ? 1 : 0), jb = ja + 2;
    const bool curA = ja <= tok, curB = jb <= tok;
    const unsigned selA = odd ? 0x01000c0cu : 0x0c0c0100u, selB = odd ? 0x03020c0cu : 0x0c0c0302u, selZ = 0x0c0c0c0cu;
    e.sPA = curA ? selZ : selA; e.sPB = curB ? selZ : selB; e.sCA = curA ? selA : selZ; e.sCB = curB ? selB : selZ;
    e.modd = __ballot(odd); e.mA = __ballot(curA); e.mB = __ballot(curB);
    e.aT = ctx.Lb + L_REC + tok * 16;
    e.aF = ctx.Lb + L_NM + g * 16;
    e.aKa = ctx.Lb + L_REC + (ja - (curA ? 0 : 16)) * 16 + 8;
    e.aKb = ctx.Lb + L_REC + (jb - (curB ? 0 : 16)) * 16 + 8;
    e.csq = lds_ldf4(ctx.L, csb + g * 16); e.csk = lds_ldf4(ctx.L, csb + 64 + g * 16); e.csv = lds_ldf(ctx.L, csb + 128 + tok * 4);
    e.ones = v2u{mk1(), mk0()};
    e.ovo = ((g >> 1) * 16 + tok) * 16 + (g & 1) * 8;
    ab0 = ctx.Lb + L_XT + lv; ab1 = ab0 + 49152;
    asm volatile("" : "+v"(e.sPA), "+v"(e.sPB), "+v"(e.sCA), "+v"(e.sCB), "+v"(e.csq), "+v"(e.csk), "+v"(e.csv), "+a"(e.ones), "+v"(e.ovo),
                      "+v"(e.aT), "+v"(e.aF), "+v"(e.aKa), "+v"(e.aKb), "+v"(ab0), "+v"(ab1), "+v"(wd.v[0]), "+v"(wd.v[1]), "+v"(wd.v[2]), "+v"(wd.v[3]));
  }
  const int xs = WS_X + (L - 1) * ACT_L + c * 65536 + w * 4096 - 16384;
  const int osb = WS_O + L * ACT_L + (4 * c * 16 + 2 * ctx.m + (w >> 1)) * 1024 + (w & 1) * 512;
  ST st;
  gather_start<true>(ctx, st, L, lv, xs);
  ring_prime<0>(ab0);
  f4 xq, xk, xv, yq, yk, yv;
  int so, oso;
  auto stat_rd = [&](auto S, auto h) {
    (void)e;
    if constexpr (h == GC(6)) { A_LD64(e.nst, e.aT, S * 256); A_LD128(e.nnm4, e.aF, S * 64); }
    if constexpr (h == GA(7)) A_LD64(e.nka, e.aKa, S * 256);
    if constexpr (h == GA(8)) A_LD64(e.nkb, e.aKb, S * 256);
  };
  auto xl = [](auto ks) { return IC<(ks == 7 ? 3 : ks == 8 ? 4 : ks == 9 ? 2 : ks == 10 ? 1 : 0)>{}; };
  auto next = [&](f4& q, f4& k, f4& v) { e.aq = q; e.ak = k; e.av = v; e.st = e.nst; e.ka = e.nka; e.kb2 = e.nkb; e.nm4 = e.nnm4; };
  auto epi = [&](auto h) {
    (void)e;
    if constexpr (h == GA(0)) A_FMA(e.q[0], e.st[0], e.csq[0], e.aq[0]);
    if constexpr (h == GB(0)) { A_FMA(e.q[1], e.st[0], e.csq[1], e.aq[1]); A_FMA(e.q[2], e.st[0], e.csq[2], e.aq[2]); }
    if constexpr (h == GC(0)) { A_FMA(e.q[3], e.st[0], e.csq[3], e.aq[3]); A_FMA(e.k[0], e.st[0], e.csk[0], e.ak[0]); }
    if constexpr (h == GA(1)) A_FMA(e.k[1], e.st[0], e.csk[1], e.ak[1]);
    if constexpr (h == GB(1)) { A_FMA(e.k[2], e.st[0], e.csk[2], e.ak[2]); A_FMA(e.k[3], e.st[0], e.csk[3], e.ak[3]); }
    if constexpr (h == GC(1)) { A_CVT(e.qb[0], e.q[0], e.q[1]); A_CVT(e.qb[1], e.q[2], e.q[3]); }
    if constexpr (h == GB(2)) { A_CVT(e.kb[0], e.k[0], e.k[1]); A_CVT(e.kb[1], e.k[2], e.k[3]); }
    if constexpr (h == GC(2)) {
      A_FMA(e.v[0], e.nm4[0], e.csv, e.av[0]); A_FMA(e.v[1], e.nm4[1], e.csv, e.av[1]);
      mf16z(e.sp, v2u{e.kprev[0], e.kprev[1]}, v2u{e.qb[0], e.qb[1]});
      A_FMA(e.v[2], e.nm4[2], e.csv, e.av[2]); A_FMA(e.v[3], e.nm4[3], e.csv, e.av[3]);
      mf16z(e.sc, v2u{e.kb[0], e.kb[1]}, v2u{e.qb[0], e.qb[1]});
      A_CVT(e.vb[0], e.v[0], e.v[1]); A_CVT(e.vb[1], e.v[2], e.v[3]);
    }
    if constexpr (h == GB(3)) { A_CVT(e.vb2[0], e.v[0], e.v[1]); A_CVT(e.vb2[1], e.v[2], e.v[3]); }
    if constexpr (h == GC(3)) A_MULS(e.ft, 0.25f * LOG2E, e.st[1]);
    if constexpr (h == GA(4)) A_MUL(e.fa, e.ka[0], e.ft);
    if constexpr (h == GB(4)) { A_SEL(e.a, e.sc[0], e.sc[1], e.modd); A_SEL(e.t0, e.sp[0], e.sp[1], e.modd); }
    if constexpr (h == GC(4)) { A_SEL(e.b, e.sc[2], e.sc[3], e.modd); A_SEL(e.t1, e.sp[2], e.sp[3], e.modd); }
    if constexpr (h == GB(5)) { A_SEL(e.a, e.t0, e.a, e.mA); A_SEL(e.b, e.t1, e.b, e.mB); }
    if constexpr (h == GC(5)) { A_MUL(e.fb, e.kb2[0], e.ft); A_FMA(e.a, e.a, e.fa, e.ka[1]); }
    if constexpr (h == GB(6)) {
      asm volatile("v_fma_f32 %0, %0, %3, %4\n\tv_max_f32 %1, %2, %0" : "+v"(e.b), "=&v"(e.m) : "v"(e.a), "v"(e.fb), "v"(e.kb2[1]));
    }
    if constexpr (h == GA(7)) A_MAX(e.m2, e.a, e.b);
    if constexpr (h == GC(7)) A_SW16(e.m, e.m2);
    if constexpr (h == GB(8)) { float x = e.m, y = e.m2; A_MAX(e.m, x, y); A_MAX(e.m2, x, y); }
    if constexpr (h == GA(9)) A_SW32(e.m, e.m2);
    if constexpr (h == GB(9)) asm volatile("v_max_f32 %0, %0, %2\n\tv_sub_f32 %1, %1, %0" : "+v"(e.m), "+v"(e.a) : "v"(e.m2));
    if constexpr (h == GC(9)) { A_SUB(e.b, e.b, e.m); }
    if constexpr (h == GB(10)) A_EXP(e.a, e.a);
    if constexpr (h == GC(10)) A_EXP(e.b, e.b);
    if constexpr (h == GB(11)) { A_MUL(e.pa, e.a, e.ka[0]); A_MUL(e.pb, e.b, e.kb2[0]); }
    if constexpr (h == GC(11)) { A_CVT(e.pk, e.pa, e.pb); A_CVT(e.pk0, e.a, e.b); }
    if constexpr (h == GB(12)) { A_PERM(e.P[0], e.pk, e.sPA); A_PERM(e.P[1], e.pk, e.sPB); }
    if constexpr (h == GC(12)) { A_PERM(e.P[2], e.pk, e.sCA); A_PERM(e.P[3], e.pk, e.sCB); }
    if constexpr (h == GA(13)) {
      mf16z_a(e.den, e.ones, v2u{e.pk0, e.pk});
      mf32z(e.num, v4u{e.vprev[0], e.vprev[1], e.vb[0], e.vb[1]}, v4u{e.P[0], e.P[1], e.P[2], e.P[3]});
      e.kprev[0] = e.kb[0]; e.kprev[1] = e.kb[1]; e.vprev[0] = e.vb2[0]; e.vprev[1] = e.vb2[1];
    }
    if constexpr (h == GB(14)) A_RCP(e.m, e.den);
    if constexpr (h == GC(14)) { A_MUL(e.q[0], e.num[0], e.m); A_MUL(e.q[1], e.num[1], e.m); }
    if constexpr (h == GA(15)) A_MUL(e.q[2], e.num[2], e.m);
    if constexpr (h == GB(15)) { A_MUL(e.q[3], e.num[3], e.m); A_CVT(e.ob[0], e.q[0], e.q[1]); }
    if constexpr (h == GC(15)) { A_CVT(e.ob[1], e.q[2], e.q[3]); st64(ctx, v2u{e.ob[0], e.ob[1]}, e.ovo, oso); }
  };
  auto x_sh = [&](auto TT, auto h) {
    if constexpr (h == GB(1)) dma_so<(TT + 1) * 16384>(so, xs);
    if constexpr (h == GA(2)) dma_m0<L_XT + (TT + 1) * 16384>(ctx.Lw);
    return h == GB(1) || h == GA(2);
  };
  auto x_gp = [&](auto h) {
    if constexpr (h == GA(2)) dma_go_sc1<0>(lv, ctx.rs, so);
    if constexpr (h == GA(3)) dma_go_sc1<1>(lv, ctx.rs, so);
    if constexpr (h == GA(5)) dma_go_sc1<2>(lv, ctx.rs, so);
    if constexpr (h == GA(6)) dma_go_sc1<3>(lv, ctx.rs, so);
  };
  auto w_sh = [&](auto J, auto N, auto h) {
    if constexpr (h == GA(7)) WAIT_VMV(N.value);
    if constexpr (h == GB(7)) RAWBAR();
    if constexpr (h == GB(9)) dma_so<128 * J * 1024>(so, wd.sb);
    if constexpr (h == GA(10)) dma_m0<L_XT + J * 16384>(ctx.Lw);
    if constexpr (h == GB(13) && J > 0) dma_so<(J - 1) * 16384>(oso, osb);
    return h == GA(7) || h == GB(7) || h == GB(9) || h == GA(10) || (h == GB(13) && J > 0);
  };
  auto w_gp = [&](auto h) {
    if constexpr (h == GA(10)) dma_go<0>(wd.v[0], rwo, so);
    if constexpr (h == GA(11)) dma_go<1>(wd.v[1], rwo, so);
    if constexpr (h == GA(12)) dma_go<2>(wd.v[2], rwo, so);
    if constexpr (h == GA(14)) dma_go<3>(wd.v[3], rwo, so);
  };
  TS(0);
  gemm_tile<-1, P_BK, P_BV, 0, false>(ab0, xq, xk, xv,
    [](auto ks) { return IC<(ks == 10 ? 2 : ks == 11 ? -1 : (ks == 12 || ks == 13 || ks == 14) ? 2 : 0)>{}; },
    [&](auto h) {
      if constexpr (h == GA(0)) dma_so<2 * 16384>(so, xs);
      if constexpr (h == GA(1)) dma_m0<L_XT + 2 * 16384>(ctx.Lw);
      if constexpr (h == GA(2)) WAIT_VMV(5);
      if constexpr (h == GA(11)) WAIT_VMV(4);
      if constexpr (h == GA(12)) RAWBAR();
      return h == GA(0) || h == GA(1) || h == GA(2) || h == GA(11) || h == GA(12);
    },
    [&](auto h) {
      (void)e;
      if constexpr (h == GA(1)) dma_go_sc1<0>(lv, ctx.rs, so);
      if constexpr (h == GA(4)) dma_go_sc1<1>(lv, ctx.rs, so);
      if constexpr (h == GA(7)) dma_go_sc1<2>(lv, ctx.rs, so);
      if constexpr (h == GC(10)) dma_go_sc1<3>(lv, ctx.rs, so);
      st_gaps<h>(ctx, st);
      if constexpr (h == GA(12)) { A_LD64(e.nst, e.aT, 0); A_LD128(e.nnm4, e.aF, 0); }
    });
  TS(1);
  gemm_tile<P_BQ, P_BK, P_BV, 16384, false>(ab0, yq, yk, yv, xl,
    [&](auto h) { return x_sh(IC<2>{}, h) | w_sh(IC<0>{}, IC<4>{}, h); },
    [&](auto h) {
      (void)e; (void)xk; (void)xv;
      if constexpr (h == GB(1)) { A_FMA(e.k[0], e.nst[0], e.csk[0], xk[0]); A_FMA(e.k[1], e.nst[0], e.csk[1], xk[1]); }
      if constexpr (h == GC(1)) { A_FMA(e.k[2], e.nst[0], e.csk[2], xk[2]); A_FMA(e.k[3], e.nst[0], e.csk[3], xk[3]); }
      if constexpr (h == GB(2)) { A_FMA(e.v[0], e.nnm4[0], e.csv, xv[0]); A_FMA(e.v[1], e.nnm4[1], e.csv, xv[1]); }
      if constexpr (h == GC(2)) { A_FMA(e.v[2], e.nnm4[2], e.csv, xv[2]); A_FMA(e.v[3], e.nnm4[3], e.csv, xv[3]); }
      if constexpr (h == GB(3)) { A_CVT(e.kprev[0], e.k[0], e.k[1]); A_CVT(e.kprev[1], e.k[2], e.k[3]); }
      if constexpr (h == GC(3)) { A_CVT(e.vprev[0], e.v[0], e.v[1]); A_CVT(e.vprev[1], e.v[2], e.v[3]); }
      if constexpr (h == GC(15)) NOP(1);
      x_gp(h); stat_rd(IC<1>{}, h); w_gp(h);
    });
  next(yq, yk, yv);
  TS(2);
  gemm_tile<P_BQ, P_BK, P_BV, 32768, false>(ab0, xq, xk, xv, xl,
    [&](auto h) { return x_sh(IC<3>{}, h) | w_sh(IC<1>{}, IC<8>{}, h); },
    [&](auto h) { x_gp(h); stat_rd(IC<2>{}, h); w_gp(h); epi(h); });
  next(xq, xk, xv);
  TS(3);
  gemm_tile<P_BQ, P_BK, P_BV, 0, false>(ab1, yq, yk, yv, xl,
    [&](auto h) { return w_sh(IC<2>{}, IC<5>{}, h); },
    [&](auto h) { stat_rd(IC<3>{}, h); w_gp(h); epi(h); });
  next(yq, yk, yv);
  TS(4);
  gemm_tile<P_BQ, P_BK, P_BV, 16384, false>(ab1, xq, xk, xv, xl,
    [&](auto h) { return w_sh(IC<3>{}, IC<63>{}, h); },
    [&](auto h) { stat_rd(IC<4>{}, h); w_gp(h); epi(h); });
  next(xq, xk, xv);
  TS(5);
  {
    dma_so<3 * 16384>(oso, osb);
    A_MULS(e.ft, 0.25f * LOG2E, e.st[1]);
    A_FMA(e.q[0], e.st[0], e.csq[0], e.aq[0]); A_FMA(e.q[1], e.st[0], e.csq[1], e.aq[1]);
    A_FMA(e.q[2], e.st[0], e.csq[2], e.aq[2]); A_FMA(e.q[3], e.st[0], e.csq[3], e.aq[3]);
    A_FMA(e.k[0], e.st[0], e.csk[0], e.ak[0]); A_FMA(e.k[1], e.st[0], e.csk[1], e.ak[1]);
    A_CVT(e.qb[0], e.q[0], e.q[1]); A_CVT(e.qb[1], e.q[2], e.q[3]);
    A_FMA(e.k[2], e.st[0], e.csk[2], e.ak[2]); A_FMA(e.k[3], e.st[0], e.csk[3], e.ak[3]);
    mf16z(e.sp, v2u{e.kprev[0], e.kprev[1]}, v2u{e.qb[0], e.qb[1]});
    A_CVT(e.kb[0], e.k[0], e.k[1]); A_CVT(e.kb[1], e.k[2], e.k[3]);
    A_MUL(e.fa, e.ka[0], e.ft); A_MUL(e.fb, e.kb2[0], e.ft);
    mf16z(e.sc, v2u{e.kb[0], e.kb[1]}, v2u{e.qb[0], e.qb[1]});
    A_FMA(e.v[0], e.nm4[0], e.csv, e.av[0]); A_FMA(e.v[1], e.nm4[1], e.csv, e.av[1]);
    A_FMA(e.v[2], e.nm4[2], e.csv, e.av[2]); A_FMA(e.v[3], e.nm4[3], e.csv, e.av[3]);
    A_SEL(e.t0, e.sp[0], e.sp[1], e.modd); A_SEL(e.t1, e.sp[2], e.sp[3], e.modd);
    A_CVT(e.vb[0], e.v[0], e.v[1]); A_CVT(e.vb[1], e.v[2], e.v[3]);
    asm volatile("v_cndmask_b32 %0, %4, %5, %10\n\tv_cndmask_b32 %1, %6, %7, %10\n\t"
                 "v_cndmask_b32 %0, %8, %0, %11\n\tv_cndmask_b32 %1, %9, %1, %12\n\t"
                 "v_fma_f32 %0, %0, %13, %15\n\tv_fma_f32 %1, %1, %14, %16\n\t"
                 "v_max_f32 %2, %0, %1\n\tv_max_f32 %3, %0, %1\n\ts_nop 1\n\t"
                 "v_permlane16_swap_b32 %2, %3\n\tv_max_f32 %2, %2, %3\n\tv_mov_b32 %3, %2\n\ts_nop 1\n\t"
                 "v_permlane32_swap_b32 %2, %3\n\tv_max_f32 %2, %2, %3\n\tv_sub_f32 %0, %0, %2\n\tv_sub_f32 %1, %1, %2\n\t"
                 "v_exp_f32 %0, %0\n\tv_exp_f32 %1, %1"
                 : "=&v"(e.a), "=&v"(e.b), "=&v"(e.m), "=&v"(e.m2)
                 : "v"(e.sc[0]), "v"(e.sc[1]), "v"(e.sc[2]), "v"(e.sc[3]), "v"(e.t0), "v"(e.t1), "s"(e.modd), "s"(e.mA), "s"(e.mB),
                   "v"(e.fa), "v"(e.fb), "v"(e.ka[1]), "v"(e.kb2[1]));
    asm volatile("v_mul_f32 %4, %7, %9\n\tv_mul_f32 %5, %8, %10\n\tv_cvt_pk_bf16_f32 %6, %7, %8\n\tv_cvt_pk_bf16_f32 %4, %4, %5\n\t"
                 "v_perm_b32 %0, %4, %4, %11\n\tv_perm_b32 %1, %4, %4, %12\n\tv_perm_b32 %2, %4, %4, %13\n\tv_perm_b32 %3, %4, %4, %14"
                 : "=&v"(e.P[0]), "=&v"(e.P[1]), "=&v"(e.P[2]), "=&v"(e.P[3]), "=&v"(e.pk), "=&v"(e.pb), "=&v"(e.pk0)
                 : "v"(e.a), "v"(e.b), "v"(e.ka[0]), "v"(e.kb2[0]), "v"(e.sPA), "v"(e.sPB), "v"(e.sCA), "v"(e.sCB));
    mf16z_a(e.den, e.ones, v2u{e.pk0, e.pk});
    NOP(0);
    mf32z(e.num, v4u{e.vprev[0], e.vprev[1], e.vb[0], e.vb[1]}, v4u{e.P[0], e.P[1], e.P[2], e.P[3]});
    NOP(5);
    asm volatile("v_rcp_f32 %2, %7\n\ts_nop 0\n\t"
                 "v_mul_f32 %3, %8, %2\n\tv_mul_f32 %4, %9, %2\n\tv_mul_f32 %5, %10, %2\n\tv_mul_f32 %6, %11, %2\n\t"
                 "v_cvt_pk_bf16_f32 %0, %3, %4\n\tv_cvt_pk_bf16_f32 %1, %5, %6"
                 : "=&v"(e.ob[0]), "=&v"(e.ob[1]), "=&v"(e.m), "=&v"(e.q[0]), "=&v"(e.q[1]), "=&v"(e.q[2]), "=&v"(e.q[3])
                 : "v"(e.den[0]), "v"(e.num[0]), "v"(e.num[1]), "v"(e.num[2]), "v"(e.num[3]), "v"(e.den));
    st64(ctx, v2u{e.ob[0], e.ob[1]}, e.ovo, oso);
  }
  TS(6);
  STAMP(L * 8 + 4);
  TS_FLUSH(L * 8 + 2, 7);
  DRAIN();
  BAR();
  flag_store(ctx, WS_FO + (L * 32 + c) * 128);
}

struct FM {
  f4 p, sp, sn;
  float ep[4], en[4], m, m2, c;
  unsigned eb[2], nb[2];
};
#define FM_MM1(f) asm volatile("v_max3_f32 %0, %2, %3, %4\n\tv_max3_f32 %1, -%2, -%3, -%4" : "=&v"(f.m), "=&v"(f.m2) : "v"(f.p[0]), "v"(f.p[1]), "v"(f.p[2]))
#define FM_MM2(f) asm volatile("v_max_f32 %0, %0, %2\n\tv_max_f32 %1, %1, -%2" : "+v"(f.m), "+v"(f.m2) : "v"(f.p[3]))
#define FM_MX2(f) do { float x_ = f.m, y_ = f.m2; A_MAX(f.m, x_, y_); A_MAX(f.m2, x_, y_); } while (0)
#define FM_MXC(f) asm volatile("v_max_f32 %0, %0, %1\n\tv_mul_f32 %1, %0, -%2\n\tv_mul_f32 %0, %0, -%2" : "+v"(f.m), "+v"(f.m2) : "v"(f.c))

struct AE {
  f4 ak, av;
  f2 st, nst; f4 nm4, rs4, nnm4, nrs4;
  f4 csk; float csv;
  float k[4], v[4];
  unsigned kb[2], vb[2], vbo[2], php[2], phm[2];
  v2u wphi, ident, ones, lo1, hi1;
  f4 tp, tm, kvp, kvm, zz;
  FM f;
  unsigned aT, aF;
};

DEV void layer_A(Ctx& ctx, int lp, v2u wphi, rsrc_t rwo, rsrc_t rsa, rsrc_t rsb) {
  fresh(ctx);
  const int L = 2 * lp;
  const int c = ctx.c, m = ctx.m, w = ctx.w, lane = ctx.lane, g = ctx.g, tok = ctx.tok;
  const int csb = L_CS + (w * 6 + 0) * 64;
  const unsigned lv = lane * 16;
  const unsigned T = ctx.T;
  AE e;
  TS_DECL;
  unsigned ab0, ab1;
  ImgDma wd = img_prep<1024>(ctx), qd = img_prep<3072>(ctx);
  {
    const int d = tok - 4 * g;
    e.ident = v2u{d == 0 ? 0x3f80u : d == 1 ? 0x3f800000u : 0u, d == 2 ? 0x3f80u : d == 3 ? 0x3f800000u : 0u};
    const unsigned lo = tok < 8 ? ONE2 : 0u, hi = tok < 8 ? 0u : ONE2;
    e.lo1 = v2u{lo, lo}; e.hi1 = v2u{hi, hi}; e.ones = v2u{mk1(), mk1()}; e.wphi = wphi;
    e.aT = ctx.Lb + L_REC + tok * 16;
    e.aF = ctx.Lb + L_NM + g * 16;
    e.csk = lds_ldf4(ctx.L, csb + 64 + g * 16); e.csv = lds_ldf(ctx.L, csb + 128 + tok * 4);
    ab0 = ctx.Lb + L_XT + (lp == 0 ? x0_lane(tok, g) : lv); ab1 = ab0 + 49152;
    asm volatile("" : "+a"(e.ident), "+a"(e.lo1), "+a"(e.hi1), "+a"(e.ones), "+a"(e.wphi), "+v"(e.csk), "+v"(e.csv), "+v"(e.aT), "+v"(e.aF),
                      "+v"(ab0), "+v"(ab1), "+v"(qd.v[0]), "+v"(qd.v[1]), "+v"(qd.v[2]), "+v"(qd.v[3]));
  }
  const int xs = WS_X + (L - 1) * ACT_L + c * 65536 + w * 4096 - 16384;
  f4 xk, xv, yk, yv;
  int so;
  auto stat_rd = [&](auto S, auto h) {
    (void)e;
    if constexpr (h == GA(12)) A_LD64(e.nst, e.aT, S * 256);
    if constexpr (h == GC(12)) { A_LD128(e.nnm4, e.aF, S * 64); A_LD128(e.nrs4, e.aF, S * 64 + 320); }
  };
  auto xl = [](auto ks) { return IC<(ks == 12 ? 1 : (ks == 13 || ks == 14) ? 3 : 0)>{}; };
  auto next = [&](f4& k, f4& v) { e.ak = k; e.av = v; e.st = e.nst; e.nm4 = e.nnm4; e.rs4 = e.nrs4; };
  auto epi = [&](auto h) {
    (void)e;
    if constexpr (h == GC(0)) { A_FMA(e.k[0], e.st[0], e.csk[0], e.ak[0]); A_FMA(e.k[1], e.st[0], e.csk[1], e.ak[1]); }
    if constexpr (h == GA(1)) { A_FMA(e.k[2], e.st[0], e.csk[2], e.ak[2]); A_FMA(e.k[3], e.st[0], e.csk[3], e.ak[3]); }
    if constexpr (h == GC(1)) { A_CVT(e.kb[0], e.k[0], e.k[1]); A_CVT(e.kb[1], e.k[2], e.k[3]); A_FMA(e.v[0], e.nm4[0], e.csv, e.av[0]); }
    if constexpr (h == GA(2)) {
      A_FMA(e.v[1], e.nm4[1], e.csv, e.av[1]);
      mf16z_a(e.f.p, e.wphi, v2u{e.kb[0], e.kb[1]});
      A_FMA(e.v[2], e.nm4[2], e.csv, e.av[2]); A_FMA(e.v[3], e.nm4[3], e.csv, e.av[3]);
    }
    if constexpr (h == GC(2)) { A_MUL(e.v[0], e.v[0], e.rs4[0]); A_MUL(e.v[1], e.v[1], e.rs4[1]); A_MUL(e.v[2], e.v[2], e.rs4[2]); }
    if constexpr (h == GA(3)) { A_MUL(e.v[3], e.v[3], e.rs4[3]); A_MULS(e.f.c, LOG2E, e.st[1]); }
    if constexpr (h == GC(3)) { FM_MM1(e.f); A_CVT(e.vb[0], e.v[0], e.v[1]); }
    if constexpr (h == GA(4)) FM_MM2(e.f);
    if constexpr (h == GC(4)) { A_CVT(e.vb[1], e.v[2], e.v[3]); A_SW16(e.f.m, e.f.m2); }
    if constexpr (h == GA(5)) FM_MX2(e.f);
    if constexpr (h == GC(5)) A_SW32(e.f.m, e.f.m2);
    if constexpr (h == GA(6)) FM_MXC(e.f);
    if constexpr (h == GC(6)) A_SW16(e.f.m, e.f.m2);
    if constexpr (h == GA(7)) { A_FMA(e.f.ep[0], e.f.p[0], e.f.c, e.f.m); A_FMA(e.f.ep[1], e.f.p[1], e.f.c, e.f.m); }
    if constexpr (h == GC(7)) { A_FMA(e.f.ep[2], e.f.p[2], e.f.c, e.f.m); A_FMA(e.f.ep[3], e.f.p[3], e.f.c, e.f.m); A_FMAN(e.f.en[0], e.f.p[0], e.f.c, e.f.m2); }
    if constexpr (h == GA(8)) { A_FMAN(e.f.en[1], e.f.p[1], e.f.c, e.f.m2); A_FMAN(e.f.en[2], e.f.p[2], e.f.c, e.f.m2); }
    if constexpr (h == GC(8)) { A_FMAN(e.f.en[3], e.f.p[3], e.f.c, e.f.m2); A_EXP(e.f.ep[0], e.f.ep[0]); A_EXP(e.f.ep[1], e.f.ep[1]); }
    if constexpr (h == GA(9)) A_EXP(e.f.ep[2], e.f.ep[2]);
    if constexpr (h == GC(9)) { A_EXP(e.f.ep[3], e.f.ep[3]); A_EXP(e.f.en[0], e.f.en[0]); }
    if constexpr (h == GA(10)) A_EXP(e.f.en[1], e.f.en[1]);
    if constexpr (h == GC(10)) { A_EXP(e.f.en[2], e.f.en[2]); A_EXP(e.f.en[3], e.f.en[3]); }
    if constexpr (h == GA(11)) { A_CVT(e.f.eb[0], e.f.ep[0], e.f.ep[1]); A_CVT(e.f.eb[1], e.f.ep[2], e.f.ep[3]); }
    if constexpr (h == GC(11)) {
      A_CVT(e.f.nb[0], e.f.en[0], e.f.en[1]); A_CVT(e.f.nb[1], e.f.en[2], e.f.en[3]);
      mf16z_a(e.f.sp, e.ones, v2u{e.f.eb[0], e.f.eb[1]});
    }
    if constexpr (h == GA(12)) mf16z_a(e.f.sn, e.ones, v2u{e.f.nb[0], e.f.nb[1]});
    if constexpr (h == GA(13)) A_RCP(e.f.m, e.f.sp);
    if constexpr (h == GC(13)) { A_MUL(e.f.ep[0], e.f.ep[0], e.f.m); A_RCP(e.f.m2, e.f.sn); A_MUL(e.f.ep[1], e.f.ep[1], e.f.m); }
    if constexpr (h == GA(14)) { A_MUL(e.f.ep[2], e.f.ep[2], e.f.m); A_MUL(e.f.ep[3], e.f.ep[3], e.f.m); }
    if constexpr (h == GC(14)) { A_MUL(e.f.en[0], e.f.en[0], e.f.m2); A_MUL(e.f.en[1], e.f.en[1], e.f.m2); A_CVT(e.f.eb[0], e.f.ep[0], e.f.ep[1]); }
    if constexpr (h == GA(15)) { A_CVT(e.f.eb[1], e.f.ep[2], e.f.ep[3]); A_MUL(e.f.en[2], e.f.en[2], e.f.m2); }
    if constexpr (h == GC(15)) {
      A_MUL(e.f.en[3], e.f.en[3], e.f.m2); A_CVT(e.f.nb[0], e.f.en[0], e.f.en[1]);
      mf16z_b(e.tp, v2u{e.f.eb[0], e.f.eb[1]}, e.ident);
      A_CVT(e.f.nb[1], e.f.en[2], e.f.en[3]);
      e.vbo[0] = e.vb[0]; e.vbo[1] = e.vb[1];
    }
  };
  auto epi2 = [&](auto FT, auto h) {
    (void)e;
    if constexpr (h == GA(0)) mf16z_b(e.tm, v2u{e.f.nb[0], e.f.nb[1]}, e.ident);
    if constexpr (h == GC(5)) { A_CVT(e.php[0], e.tp[0], e.tp[1]); }
    if constexpr (h == GC(6)) { A_CVT(e.php[1], e.tp[2], e.tp[3]); }
    if constexpr (h == GA(9)) {
      if constexpr (FT) mf16z(e.kvp, v2u{e.php[0], e.php[1]}, v2u{e.vbo[0], e.vbo[1]}); else mf16c(e.kvp, v2u{e.php[0], e.php[1]}, v2u{e.vbo[0], e.vbo[1]});
      A_CVT(e.phm[0], e.tm[0], e.tm[1]); A_CVT(e.phm[1], e.tm[2], e.tm[3]);
      if constexpr (FT) mf16z_a(e.zz, e.lo1, v2u{e.php[0], e.php[1]}); else mf16c_a(e.zz, e.lo1, v2u{e.php[0], e.php[1]});
    }
    if constexpr (h == GA(10)) {
      if constexpr (FT) mf16z(e.kvm, v2u{e.phm[0], e.phm[1]}, v2u{e.vbo[0], e.vbo[1]}); else mf16c(e.kvm, v2u{e.phm[0], e.phm[1]}, v2u{e.vbo[0], e.vbo[1]});
    }
    if constexpr (h == GA(13)) mf16c_a(e.zz, e.hi1, v2u{e.phm[0], e.phm[1]});
  };
  auto c_sh = [&](auto K0, auto SOFF, auto LOFF, int sb, auto h) {
    if constexpr (h == GA(K0 - 1)) dma_so<SOFF>(so, sb);
    if constexpr (h == GA(K0)) dma_m0<LOFF>(ctx.Lw);
    return h == GA(K0 - 1) || h == GA(K0);
  };
  auto c_gp = [&](auto K0, const ImgDma& d, rsrc_t rs, auto h) {
    if constexpr (h == GC(K0)) dma_go<0>(d.v[0], rs, so);
    if constexpr (h == GC(K0 + 2)) dma_go<1>(d.v[1], rs, so);
    if constexpr (h == GC(K0 + 4)) dma_go<2>(d.v[2], rs, so);
    if constexpr (h == GC(K0 + 6)) dma_go<3>(d.v[3], rs, so);
  };
  constexpr int QS = 128 * 3072;
  constexpr int SL = 32 * 384;
  auto kv_pass = [&](auto FIRST) {
    ST st;
    unsigned rvo = 0; int rsb0 = 0, rso = 0;
    if constexpr (FIRST) {
      rvo = (unsigned)((lane >> 3) * 3072 + (lane & 7) * 16);
      rsb0 = m * 128 + 32 * w * 3072;
      asm volatile("" : "+v"(rvo));
      RAWBAR();
    } else {
      gather_start<false>(ctx, st, L, lv, xs);
    }
    auto r_sh = [&](auto I) { dma_so<1024 + (128 * (I >> 2) + 8 * (I & 3)) * 3072>(rso, rsb0); };
    auto r_gp = [&](auto I) { raw_ld1<P_BK + I>(rvo, rsb, rso); };
    const int pfr = ((blockIdx.x >> 3) * 16 + 4 * w) * 3072;
    ring_prime<16384>(ab0);
    TS(0);
    gemm_tile<-1, P_AK, P_AV, 16384, false>(ab0, xk, xk, xv,
      [&](auto ks) { return IC<(FIRST ? 0 : ks == 10 ? 2 : ks == 11 ? -1 : 0) + decltype(xl(ks))::value>{}; },
      [&](auto h) {
        if constexpr (FIRST) {
          if constexpr (h == GA(0)) dma_so<2 * 3072>(rso, pfr);
          if constexpr (h == GA(6)) dma_so<3 * 3072>(rso, pfr);
          return (h == GA(0) || h == GA(6)) | c_sh(IC<9>{}, IC<0>{}, IC<L_RQ>{}, qd.sb, h);
        } else {
          if constexpr (h == GA(0)) dma_so<3 * 16384>(so, xs);
          if constexpr (h == GA(1)) dma_m0<L_XT + 3 * 16384>(ctx.Lw);
          if constexpr (h == GA(2)) WAIT_VMV(5);
          if constexpr (h == GA(11)) WAIT_VMV(4);
          if constexpr (h == GA(12)) RAWBAR();
          if constexpr (h == GA(13)) dma_so<4 * 16384>(so, xs);
          if constexpr (h == GA(14)) dma_m0<L_XT + 4 * 16384>(ctx.Lw);
          return h == GA(0) || h == GA(1) || h == GA(2) || (h >= GA(11) && h <= GA(14));
        }
      },
      [&](auto h) {
        if constexpr (FIRST) {
          if constexpr ((h & 7) == 2 && h / 4 < 12) pf1<((h / 8) % 3) * 1024>(lv, rsb, rso);
          c_gp(IC<9>{}, qd, rsa, h);
        } else {
          if constexpr (h == GA(1)) dma_go_sc1<0>(lv, ctx.rs, so);
          if constexpr (h == GA(4)) dma_go_sc1<1>(lv, ctx.rs, so);
          if constexpr (h == GA(7)) dma_go_sc1<2>(lv, ctx.rs, so);
          if constexpr (h == GC(10)) dma_go_sc1<3>(lv, ctx.rs, so);
          if constexpr (h == GC(14)) dma_go_sc1<0>(lv, ctx.rs, so);
          st_gaps<h>(ctx, st);
        }
        if constexpr (h == GC(15)) NOP(1);
        stat_rd(IC<1>{}, h);
      });
    next(xk, xv);
    TS(1);
    gemm_tile<-1, P_AK, P_AV, 32768, false>(ab0, yk, yk, yv, xl,
      [&](auto h) {
        if constexpr (FIRST) {
          if constexpr (h == GA(2)) { dma_so<0>(rso, pfr / 3); return true; }
          else return c_sh(IC<1>{}, IC<QS>{}, IC<L_RQ + 16384>{}, qd.sb, h) | c_sh(IC<9>{}, IC<2 * QS>{}, IC<L_RQ + 32768>{}, qd.sb, h);
        } else {
          if constexpr (h == GA(11)) WAIT_VMV(6);
          if constexpr (h == GA(12)) RAWBAR();
          return c_sh(IC<7>{}, IC<0>{}, IC<L_RQ>{}, qd.sb, h) | (h == GA(11) || h == GA(12));
        }
      },
      [&](auto h) {
        if constexpr (FIRST) {
          c_gp(IC<1>{}, qd, rsa, h); c_gp(IC<9>{}, qd, rsa, h);
          if constexpr (h == GC(2) || h == GC(6) || h == GC(10) || h == GC(14)) pf1<((h / 4 - 2) / 4) * 1024>(lv, rwo, rso);
        } else {
          if constexpr (h == GA(0)) dma_go_sc1<1>(lv, ctx.rs, so);
          if constexpr (h == GC(3)) dma_go_sc1<2>(lv, ctx.rs, so);
          if constexpr (h == GC(5)) dma_go_sc1<3>(lv, ctx.rs, so);
          c_gp(IC<7>{}, qd, rsa, h);
        }
        stat_rd(IC<2>{}, h); epi(h);
      });
    next(yk, yv);
    TS(2);
    gemm_tile<-1, P_AK, P_AV, 0, false>(ab1, xk, xk, xv, xl,
      [&](auto h) {
        if constexpr (FIRST) {
          if constexpr (h / 4 >= 8) { r_sh(IC<h / 4 - 8>{}); return true; }
          else return c_sh(IC<1>{}, IC<3 * QS>{}, IC<L_RQ + 49152>{}, qd.sb, h);
        } else {
          if constexpr (h == GA(11)) WAIT_VMV(8);
          if constexpr (h == GA(12)) RAWBAR();
          return c_sh(IC<1>{}, IC<QS>{}, IC<L_RQ + 16384>{}, qd.sb, h) | c_sh(IC<9>{}, IC<2 * QS>{}, IC<L_RQ + 32768>{}, qd.sb, h) | (h == GA(11) || h == GA(12));
        }
      },
      [&](auto h) {
        if constexpr (FIRST) { c_gp(IC<1>{}, qd, rsa, h); if constexpr ((h & 3) == 2 && h / 4 >= 8) r_gp(IC<h / 4 - 8>{}); }
        else { c_gp(IC<1>{}, qd, rsa, h); c_gp(IC<9>{}, qd, rsa, h); }
        stat_rd(IC<3>{}, h); epi2(IC<1>{}, h); epi(h);
      });
    next(xk, xv);
    TS(3);
    gemm_tile<-1, P_AK, P_AV, 16384, false>(ab1, yk, yk, yv, xl,
      [&](auto h) {
        if constexpr (FIRST) {
          if constexpr (h / 4 < 8) { r_sh(IC<8 + h / 4>{}); return true; }
          else return c_sh(IC<9>{}, IC<0>{}, IC<L_XT>{}, wd.sb, h);
        } else {
          return c_sh(IC<1>{}, IC<3 * QS>{}, IC<L_RQ + 49152>{}, qd.sb, h) | c_sh(IC<9>{}, IC<0>{}, IC<L_XT>{}, wd.sb, h);
        }
      },
      [&](auto h) {
        if constexpr (FIRST) { if constexpr ((h & 3) == 2 && h / 4 < 8) r_gp(IC<8 + h / 4>{}); }
        else c_gp(IC<1>{}, qd, rsa, h);
        c_gp(IC<9>{}, wd, rwo, h); stat_rd(IC<4>{}, h); epi2(IC<0>{}, h); epi(h);
      });
    next(yk, yv);
  TS(4);
  {
    FM& f = e.f;
    A_MULS(f.c, LOG2E, e.st[1]);
    NOP(0);
    mf16z_b(e.tm, v2u{f.nb[0], f.nb[1]}, e.ident);
    A_FMA(e.k[0], e.st[0], e.csk[0], e.ak[0]); A_FMA(e.k[1], e.st[0], e.csk[1], e.ak[1]);
    A_FMA(e.k[2], e.st[0], e.csk[2], e.ak[2]); A_FMA(e.k[3], e.st[0], e.csk[3], e.ak[3]);
    A_CVT(e.kb[0], e.k[0], e.k[1]); A_CVT(e.php[0], e.tp[0], e.tp[1]); A_CVT(e.kb[1], e.k[2], e.k[3]); A_CVT(e.php[1], e.tp[2], e.tp[3]);
    A_CVT(e.phm[0], e.tm[0], e.tm[1]);
    mf16z_a(f.p, e.wphi, v2u{e.kb[0], e.kb[1]});
    A_CVT(e.phm[1], e.tm[2], e.tm[3]); A_FMA(e.v[0], e.nm4[0], e.csv, e.av[0]);
    mf16c(e.kvp, v2u{e.php[0], e.php[1]}, v2u{e.vbo[0], e.vbo[1]});
    A_FMA(e.v[1], e.nm4[1], e.csv, e.av[1]); A_FMA(e.v[2], e.nm4[2], e.csv, e.av[2]);
    mf16c(e.kvm, v2u{e.phm[0], e.phm[1]}, v2u{e.vbo[0], e.vbo[1]});
    A_FMA(e.v[3], e.nm4[3], e.csv, e.av[3]); A_MUL(e.v[0], e.v[0], e.rs4[0]);
    FM_MM1(f);
    mf16c_a(e.zz, e.lo1, v2u{e.php[0], e.php[1]});
    FM_MM2(f);
    A_MUL(e.v[1], e.v[1], e.rs4[1]); A_MUL(e.v[2], e.v[2], e.rs4[2]);
    A_SW16(f.m, f.m2);
    mf16c_a(e.zz, e.hi1, v2u{e.phm[0], e.phm[1]});
    FM_MX2(f);
    A_MUL(e.v[3], e.v[3], e.rs4[3]); A_CVT(e.vb[0], e.v[0], e.v[1]);
    A_SW32(f.m, f.m2);
    FM_MXC(f);
    A_CVT(e.vb[1], e.v[2], e.v[3]);
    NOP(0);
    A_SW16(f.m, f.m2);
#pragma unroll
    for (int r = 0; r < 4; ++r) A_FMA(f.ep[r], f.p[r], f.c, f.m);
#pragma unroll
    for (int r = 0; r < 4; ++r) A_FMAN(f.en[r], f.p[r], f.c, f.m2);
#pragma unroll
    for (int r = 0; r < 4; ++r) A_EXP(f.ep[r], f.ep[r]);
    A_EXP(f.en[0], f.en[0]);
    A_CVT(f.eb[0], f.ep[0], f.ep[1]); A_CVT(f.eb[1], f.ep[2], f.ep[3]);
    A_EXP(f.en[1], f.en[1]); A_EXP(f.en[2], f.en[2]);
    mf16z_a(f.sp, e.ones, v2u{f.eb[0], f.eb[1]});
    A_EXP(f.en[3], f.en[3]);
    A_CVT(f.nb[0], f.en[0], f.en[1]); A_CVT(f.nb[1], f.en[2], f.en[3]);
    NOP(1);
    mf16z_a(f.sn, e.ones, v2u{f.nb[0], f.nb[1]});
    NOP(1);
    A_RCP(f.m, f.sp);
    NOP(0);
#pragma unroll
    for (int r = 0; r < 4; ++r) A_MUL(f.ep[r], f.ep[r], f.m);
    A_RCP(f.m2, f.sn);
    A_CVT(f.eb[0], f.ep[0], f.ep[1]); A_CVT(f.eb[1], f.ep[2], f.ep[3]);
    A_MUL(f.en[0], f.en[0], f.m2); A_MUL(f.en[1], f.en[1], f.m2);
    mf16z_b(e.tp, v2u{f.eb[0], f.eb[1]}, e.ident);
    A_MUL(f.en[2], f.en[2], f.m2); A_MUL(f.en[3], f.en[3], f.m2);
    A_CVT(f.nb[0], f.en[0], f.en[1]); A_CVT(f.nb[1], f.en[2], f.en[3]);
    NOP(1);
    mf16z_b(e.tm, v2u{f.nb[0], f.nb[1]}, e.ident);
    NOP(0);
    A_CVT(e.php[0], e.tp[0], e.tp[1]); A_CVT(e.php[1], e.tp[2], e.tp[3]);
    NOP(1);
    mf16c(e.kvp, v2u{e.php[0], e.php[1]}, v2u{e.vb[0], e.vb[1]});
    mf16c_a(e.zz, e.lo1, v2u{e.php[0], e.php[1]});
    NOP(0);
    A_CVT(e.phm[0], e.tm[0], e.tm[1]); A_CVT(e.phm[1], e.tm[2], e.tm[3]);
    NOP(1);
    mf16c(e.kvm, v2u{e.phm[0], e.phm[1]}, v2u{e.vb[0], e.vb[1]});
    mf16c_a(e.zz, e.hi1, v2u{e.phm[0], e.phm[1]});
    asm volatile("s_nop 0" : "+v"(e.kvp));
  }
  TS(5);
  {
    const int blk = WS_KV1 + lp * KV1_L + ((m * 64 + w * 16) * 32 + c) * 384;
    const int vo = g * 2 * SL + tok * 16;
    st128(ctx, v4u{asu(e.kvp[0]), T, asu(e.kvp[1]), T}, vo, blk);
    st128(ctx, v4u{asu(e.kvp[2]), T, asu(e.kvp[3]), T}, vo, blk + SL);
    asm volatile("s_nop 3" : "+v"(e.kvm), "+v"(e.zz));
    st128(ctx, v4u{asu(e.kvm[0]), T, asu(e.kvm[1]), T}, vo, blk + 8 * SL);
    st128(ctx, v4u{asu(e.kvm[2]), T, asu(e.kvm[3]), T}, vo, blk + 9 * SL);
    if ((g & 1) == 0) st64(ctx, v2u{asu(e.zz[0]), T}, ((g >> 1) * 8 + (tok >> 1)) * SL + 256 + (tok & 1) * 8, blk);
  }
  };
  if (lp == 0) kv_pass(std::true_type{}); else kv_pass(std::false_type{});
  STAMP(L * 8 + 2);
  TS_FLUSH(L * 8 + 2, 6);
  f4 aq[4];
  v4u dk[8], dz;
  const int rblk = WS_KV1 + lp * KV1_L + ((m * 64 + 2 * c + (w & 1)) * 32) * 384;
  const int kvo = g * 384 + tok * 16, zvo = (lane & 31) * 384 + 256;
  {
    WAIT_VM(8);
    RAWBAR();
    v4u wq[16];
    lds_t* qt = ctx.L + opq(L_RQ + tr_base(lane, w));
#pragma unroll
    for (int ks = 0; ks < 16; ++ks) wq[ks] = tr_frag(qt, ks * 4096);
    if (lp == 0) {
      f4 c0, c1;
      v4u ones = {mk1(), mk1(), mk1(), mk1()};
      asm volatile("s_nop 1" : "+v"(ones));
      sfor<0, 8>([&](auto k) { mf_av<k == 0>(c0, wq[2 * k], ones); mf_av<k == 0>(c1, wq[2 * k + 1], ones); });
      asm volatile("s_nop 7" : "+v"(c0), "+v"(c1));
      if (tok == 0) *(LAS f4*)(ctx.L + csb + g * 16) = c0 + c1;
    }
    v4u a1[4];
    auto rd = [&](auto k) {
      (void)a1;
      if constexpr (k < 16) { ring_rd<k & 3, 16384 + k * 1024>(ab0); } else if constexpr (k < 32) { ring_rd<k & 3, (k - 16) * 1024>(ab1); }
    };
    auto rd1 = [&](auto k) {
      (void)a1; (void)ab0; (void)ab1;
      if constexpr (k < 16) { A_LD128(a1[k & 3], ab0, 32768 + k * 1024); } else if constexpr (k < 32) { A_LD128(a1[k & 3], ab1, 16384 + (k - 16) * 1024); }
    };
#pragma unroll
    for (int ks = 0; ks < 16; ++ks) asm volatile("" : "+a"(wq[ks]));
    sfor<0, 3>([&](auto k) { rd(k); rd1(k); });
    WAIT_LG(4);
    sfor<0, 32>([&](auto k) {
      constexpr int ks = k & 15, u = k >> 4;
      (void)dk; (void)dz; (void)kvo; (void)zvo; (void)rblk;
      mmq<k & 3, ks == 0>(aq[2 * u], wq[ks]);
      if constexpr (k == 17) RAWBAR();
      else if constexpr (k == 16 || k == 24) dma_so<128 * 1024 * (1 + (ks >> 3))>(so, wd.sb);
      else if constexpr (k == 18 || k == 25) dma_m0<L_XT + 16384 * (1 + (ks >> 3))>(ctx.Lw);
      else VNOP();
      rd(IC<k + 3>{});
      mf_av<ks == 0>(aq[2 * u + 1], wq[ks], a1[k & 3]);
      WAIT_LG(k + 3 < 32 ? 3 : k == 29 ? 2 : 0);
      rd1(IC<k + 3>{});
      if constexpr (k == 18 || k == 19 || k == 21 || k == 23) dma_go<(k == 18 ? 0 : (k - 17) / 2)>(wd.v[k == 18 ? 0 : (k - 17) / 2], rwo, so);
      if constexpr (k >= 25 && (k & 1) == 1) dma_go<(k - 25) / 2>(wd.v[(k - 25) / 2], rwo, so);
      if constexpr (k == QPOLL) {
        if (w < 2) {
          asm volatile("buffer_load_dwordx4 %0, %9, %11, %12 offen sc1\n\t"
                       "buffer_load_dwordx4 %1, %9, %11, %12 offen offset:1536 sc1\n\t"
                       "buffer_load_dwordx4 %2, %9, %11, %12 offen offset:3072 sc1\n\t"
                       "buffer_load_dwordx4 %3, %9, %11, %13 offen sc1\n\t"
                       "buffer_load_dwordx4 %4, %9, %11, %13 offen offset:1536 sc1\n\t"
                       "buffer_load_dwordx4 %5, %9, %11, %13 offen offset:3072 sc1\n\t"
                       "buffer_load_dwordx4 %6, %9, %11, %14 offen sc1\n\t"
                       "buffer_load_dwordx4 %7, %9, %11, %14 offen offset:1536 sc1\n\t"
                       "buffer_load_dwordx4 %8, %10, %11, %12 offen sc1"
                       : "=&v"(dk[0]), "=&v"(dk[1]), "=&v"(dk[2]), "=&v"(dk[3]), "=&v"(dk[4]), "=&v"(dk[5]), "=&v"(dk[6]), "=&v"(dk[7]), "=&v"(dz)
                       : "v"(kvo), "v"(zvo), "s"(ctx.rs), "s"(rblk), "s"(rblk + 3 * 1536), "s"(rblk + 6 * 1536) : "memory");
        }
      }
      SB();
    });
  }
  if (w < 2) {
    asm volatile("s_waitcnt vmcnt(8)" : "+v"(dk[0]), "+v"(dk[1]), "+v"(dk[2]), "+v"(dk[3]), "+v"(dk[4]), "+v"(dk[5]), "+v"(dk[6]), "+v"(dk[7]), "+v"(dz) :: "memory");
    float a0, a1, b0, b1;
    for (;;) {
      unsigned bad = (dz.y ^ T) | (dz.w ^ T);
      a0 = a1 = 0.f;
#pragma unroll
      for (int i = 0; i < 8; ++i) {
        bad |= (dk[i].y ^ T) | (dk[i].w ^ T);
        a0 += asf(dk[i].x); a1 += asf(dk[i].z);
      }
      b0 = asf(dz.x); b1 = asf(dz.z);
      if (__all(bad == 0) || --ctx.budget < 0) break;
      asm volatile("" ::: "memory");
#pragma unroll
      for (int i = 0; i < 8; ++i) dk[i] = ld128(ctx, kvo, rblk + i * 1536);
      dz = ld128(ctx, zvo, rblk);
    }
    a0 = xg_sum(a0); a1 = xg_sum(a1); b0 = sum32(b0); b1 = sum32(b1);
    if (g == 0) st128(ctx, v4u{pk2(a0, a1), T, pk2(b0, b1), T}, tok * 16, WS_KV2 + lp * KV2_L + (m * 64 + 2 * c + w) * 256);
  }
  if (lp == 0) raw_load<P_BV, 0, 16>(ctx, rsb, 2048);
  STAMP(L * 8 + 3);
  v4u phq[4];
  {
    asm volatile("s_nop 7" : "+v"(aq[0]), "+v"(aq[1]), "+v"(aq[2]), "+v"(aq[3]));
    const f4 csq = lds_ldf4(ctx.L, csb + g * 16);
    FM f[4];
    unsigned qb[4][2];
#pragma unroll
    for (int t = 0; t < 4; ++t) {
      const f2 st = *(LAS f2*)(ctx.L + L_REC + ((1 + t) * 16 + tok) * 16);
      float q[4];
#pragma unroll
      for (int r = 0; r < 4; ++r) A_FMA(q[r], st[0], csq[r], aq[t][r]);
      A_MULS(f[t].c, LOG2E, st[1]);
      A_CVT(qb[t][0], q[0], q[1]); A_CVT(qb[t][1], q[2], q[3]);
      if (t > 0) mf16z_a(f[t - 1].p, e.wphi, v2u{qb[t - 1][0], qb[t - 1][1]});
    }
    FM_MM1(f[0]);
    mf16z_a(f[3].p, e.wphi, v2u{qb[3][0], qb[3][1]});
    FM_MM1(f[1]); FM_MM2(f[0]); FM_MM2(f[1]); FM_MM1(f[2]);
    A_SW16(f[0].m, f[0].m2); FM_MM2(f[2]); FM_MM1(f[3]); A_SW16(f[1].m, f[1].m2); FM_MM2(f[3]);
    FM_MX2(f[0]); A_SW16(f[2].m, f[2].m2); FM_MX2(f[1]); A_SW16(f[3].m, f[3].m2);
    A_SW32(f[0].m, f[0].m2); FM_MX2(f[2]); A_SW32(f[1].m, f[1].m2); FM_MX2(f[3]);
    FM_MXC(f[0]); A_SW32(f[2].m, f[2].m2); FM_MXC(f[1]); A_SW32(f[3].m, f[3].m2);
    A_SW16(f[0].m, f[0].m2); FM_MXC(f[2]); A_SW16(f[1].m, f[1].m2); FM_MXC(f[3]);
#pragma unroll
    for (int t = 0; t < 4; ++t) {
#pragma unroll
      for (int r = 0; r < 4; ++r) A_FMA(f[t].ep[r], f[t].p[r], f[t].c, f[t].m);
      if (t < 2) A_SW16(f[t + 2].m, f[t + 2].m2);
#pragma unroll
      for (int r = 0; r < 4; ++r) A_FMAN(f[t].en[r], f[t].p[r], f[t].c, f[t].m2);
      if (t > 0) mf16z_a(f[t - 1].sn, e.ones, v2u{f[t - 1].nb[0], f[t - 1].nb[1]});
#pragma unroll
      for (int r = 0; r < 4; ++r) A_EXP(f[t].ep[r], f[t].ep[r]);
      A_EXP(f[t].en[0], f[t].en[0]);
      A_CVT(f[t].eb[0], f[t].ep[0], f[t].ep[1]); A_CVT(f[t].eb[1], f[t].ep[2], f[t].ep[3]);
      A_EXP(f[t].en[1], f[t].en[1]); A_EXP(f[t].en[2], f[t].en[2]);
      mf16z_a(f[t].sp, e.ones, v2u{f[t].eb[0], f[t].eb[1]});
      A_EXP(f[t].en[3], f[t].en[3]);
      A_CVT(f[t].nb[0], f[t].en[0], f[t].en[1]); A_CVT(f[t].nb[1], f[t].en[2], f[t].en[3]);
    }
#pragma unroll
    for (int t = 0; t < 4; ++t) {
      A_RCP(f[t].m, f[t].sp); A_RCP(f[t].m2, f[t].sn);
      if (t == 0) mf16z_a(f[3].sn, e.ones, v2u{f[3].nb[0], f[3].nb[1]});
#pragma unroll
      for (int r = 0; r < 4; ++r) A_MUL(f[t].ep[r], f[t].ep[r], f[t].m);
#pragma unroll
      for (int r = 0; r < 4; ++r) A_MUL(f[t].en[r], f[t].en[r], f[t].m2);
      unsigned x0, x1, x2, x3;
      A_CVT(x0, f[t].ep[0], f[t].ep[1]); A_CVT(x1, f[t].ep[2], f[t].ep[3]); A_CVT(x2, f[t].en[0], f[t].en[1]); A_CVT(x3, f[t].en[2], f[t].en[3]);
      phq[t] = v4u{x0, x1, x2, x3};
    }
  }
  RAWBAR();
  img_chunk<1024, 0, L_XT, 3>(ctx, wd, rwo);
  if (lp == 0) part_step<P_BK>(ctx, L_CS + (w * 6 + 4) * 64);
  v4u akv, az;
  {
    const int b2 = WS_KV2 + lp * KV2_L + (m * 64 + w * 16) * 256;
    const int vo = g * 512 + tok * 16;
    for (;;) {
      asm volatile("" ::: "memory");
      const v4u d0 = ld128(ctx, vo, b2);
      const v4u d1 = ld128(ctx, vo + 256, b2);
      const v4u d2 = ld128(ctx, vo + 8 * 256, b2);
      const v4u d3 = ld128(ctx, vo + 9 * 256, b2);
      const unsigned bad = (d0.y ^ T) | (d0.w ^ T) | (d1.y ^ T) | (d1.w ^ T) | (d2.y ^ T) | (d2.w ^ T) | (d3.y ^ T) | (d3.w ^ T);
      akv = v4u{d0.x, d1.x, d2.x, d3.x};
      az = v4u{d0.z, d1.z, d2.z, d3.z};
      if (__all(bad == 0) || --ctx.budget < 0) break;
    }
  }
  STAMP(L * 8 + 4);
  {
    const int ovo = ((g >> 1) * 16 + tok) * 16 + (g & 1) * 8;
    const int osb = WS_O + L * ACT_L + (4 * c * 16 + 2 * m + (w >> 1)) * 1024 + (w & 1) * 512;
    f4 num[4], den[4];
    asm volatile("s_nop 1" : "+v"(akv), "+v"(az));
#pragma unroll
    for (int t = 0; t < 4; ++t) { mf32z(den[t], az, phq[t]); mf32z(num[t], akv, phq[t]); }
    NOP(1);
#pragma unroll
    for (int t = 0; t < 4; ++t) {
      float r, q0, q1, q2, q3; unsigned o0, o1;
      asm volatile("v_add_f32 %2, %12, %7\n\tv_rcp_f32 %2, %2\n\ts_nop 0\n\t"
                   "v_mul_f32 %3, %8, %2\n\tv_mul_f32 %4, %9, %2\n\tv_mul_f32 %5, %10, %2\n\tv_mul_f32 %6, %11, %2\n\t"
                   "v_cvt_pk_bf16_f32 %0, %3, %4\n\tv_cvt_pk_bf16_f32 %1, %5, %6"
                   : "=&v"(o0), "=&v"(o1), "=&v"(r), "=&v"(q0), "=&v"(q1), "=&v"(q2), "=&v"(q3)
                   : "v"(den[t][0]), "v"(num[t][0]), "v"(num[t][1]), "v"(num[t][2]), "v"(num[t][3]), "s"(1e-6f), "v"(den[t]));
      st64(ctx, v2u{o0, o1}, ovo, osb + t * 16384);
    }
  }
  DRAIN();
  BAR();
  flag_store(ctx, WS_FO + (L * 32 + c) * 128);
}

template <bool ODD>
DEV void out_proj(Ctx& ctx, int lp, f4 (&xacc)[4], rsrc_t out, rsrc_t rsb) {
  fresh(ctx);
  const int L = 2 * lp + (ODD ? 1 : 0);
  const bool last = ODD && lp == 1, first = !ODD && lp == 0;
  const int c = ctx.c, m = ctx.m, w = ctx.w, lane = ctx.lane, g = ctx.g, tok = ctx.tok;
  if (first) {
    raw_load<P_BQ, 0, 4>(ctx, rsb, 0);
    raw_store<P_BV, 0, 8>(ctx);
    raw_load<P_BQ, 4, 8>(ctx, rsb, 0);
    BAR();
    part_tr<P_BV, 0, 8>(ctx);
    raw_load<P_BQ, 8, 12>(ctx, rsb, 0);
    raw_store<P_BV, 8, 16>(ctx);
    raw_load<P_BQ, 12, 16>(ctx, rsb, 0);
    BAR();
    part_tr<P_BV, 8, 16>(ctx);
    part_cs<P_BV>(ctx, L_CS + (w * 6 + 5) * 64);
  }
  lds_t* tb[4];
#pragma unroll
  for (int nt = 0; nt < 4; ++nt) tb[nt] = ctx.L + opq(L_XT + tr_base(lane, nt));
  v4u wf[4][4];
#pragma unroll
  for (int i = 0; i < 4; ++i)
#pragma unroll
    for (int nt = 0; nt < 4; ++nt) wf[i][nt] = tr_frag(tb[nt], i * 4096);
  const int G = ((g & 1) << 1) | (g >> 1);
  int xvo = (G * 16 + tok) * 16;
  u64 m1 = __ballot(tok & 1), m2 = __ballot(tok & 2);
  const int osb = WS_O + L * ACT_L + (4 * c + w) * 16384;
  const int xsb = WS_X + L * ACT_L + ((4 * c + w) * 16 + 2 * m) * 1024;
  const int ssb = WS_STAT + L * STAT_L + ((c * 8 + m) * 64 + 16 * w) * 16;
  asm volatile("" : "+v"(xvo), "+v"(xacc[0]), "+v"(xacc[1]), "+v"(xacc[2]), "+v"(xacc[3]));
#pragma unroll
  for (int nt = 0; nt < 4; ++nt) asm volatile("" : "+v"(wf[0][nt]), "+v"(wf[1][nt]), "+v"(wf[2][nt]), "+a"(wf[3][nt]));
  wait_flags(ctx, WS_FO + (L * 32 + c) * 128, 0, false);
  STAMP(L * 8 + 5);
  v4u of[16];
  constexpr int PRE = 8;
#pragma unroll
  for (int ks = 0; ks < PRE; ++ks) of[ks] = ld128(ctx, lane * 16 + (ks & 3) * 1024, osb + (ks & ~3) * 1024);
  SB();
  sfor<0, 16>([&](auto ks) {
    if constexpr (ks + PRE < 16) { of[ks + PRE] = ld128(ctx, lane * 16 + ((ks + PRE) & 3) * 1024, osb + ((ks + PRE) & ~3) * 1024); SB(); }
#pragma unroll
    for (int nt = 0; nt < 4; ++nt) {
      if constexpr ((ks & 3) < 3) mf_va(xacc[nt], wf[ks & 3][nt], of[ks]); else mf_aa(xacc[nt], wf[ks & 3][nt], of[ks]);
      if constexpr (ks + 4 < 16) wf[ks & 3][nt] = tr_frag(tb[nt], (ks + 4) * 4096);
      SB();
    }
  });
  NOP(4);
  STAMP(L * 8 + 6);
  v4u fr[2];
#pragma unroll
  for (int u = 0; u < 2; ++u) {
    unsigned a0, a1, b0, b1;
    const f4 a = xacc[2 * u], b = xacc[2 * u + 1];
    asm volatile("v_cvt_pk_bf16_f32 %0, %4, %5\n\tv_cvt_pk_bf16_f32 %1, %6, %7\n\tv_cvt_pk_bf16_f32 %2, %8, %9\n\tv_cvt_pk_bf16_f32 %3, %10, %11\n\t"
                 "s_nop 0\n\tv_permlane16_swap_b32 %0, %2\n\tv_permlane16_swap_b32 %1, %3"
                 : "=&v"(a0), "=&v"(a1), "=&v"(b0), "=&v"(b1)
                 : "v"(a[0]), "v"(a[1]), "v"(a[2]), "v"(a[3]), "v"(b[0]), "v"(b[1]), "v"(b[2]), "v"(b[3]));
    fr[u] = v4u{a0, a1, b0, b1};
    if (!last) st128(ctx, fr[u], xvo + u * 1024, xsb);
  }
  if (last) {
#pragma unroll
    for (int u = 0; u < 2; ++u)
      __builtin_amdgcn_raw_buffer_store_b128(fr[u], out, tok * 1024 + G * 16 + u * 64, (64 * c + 16 * w) * 1024 + 128 * m, 0);
  } else {
    f4 ssum, gram;
    v4u ones = {mk1(), mk1(), mk1(), mk1()};
    asm volatile("s_nop 1" : "+v"(ones));
    mf32z(gram, fr[0], fr[0]); mf32z(ssum, ones, fr[0]);
    mf32c(gram, fr[1], fr[1]); mf32c(ssum, ones, fr[1]);
    asm volatile("s_nop 7" : "+v"(gram), "+v"(ssum));
    float q, t;
    asm volatile("v_cndmask_b32 %0, %2, %3, %6\n\tv_cndmask_b32 %1, %4, %5, %6\n\tv_cndmask_b32 %0, %0, %1, %7"
                 : "=&v"(q), "=&v"(t) : "v"(gram[0]), "v"(gram[1]), "v"(gram[2]), "v"(gram[3]), "s"(m1), "s"(m2));
    if (g == (tok >> 2)) st128(ctx, v4u{asu(ssum[0]), ctx.T, asu(q), ctx.T}, tok * 16, ssb);
    WAIT_VM(1);
    BAR();
    flag_store(ctx, WS_FX + (L * 32 + c) * 128);
    if (first) part_step<P_BQ>(ctx, L_CS + (w * 6 + 3) * 64);
  }
  asm volatile("" : "+a"(xacc[0]), "+a"(xacc[1]), "+a"(xacc[2]), "+a"(xacc[3]));
  STAMP(L * 8 + 7);
}

__global__ void __launch_bounds__(NT, 1) __attribute__((amdgpu_num_vgpr(88)))
fused_kernel(const unsigned char* __restrict__ x_, const unsigned char* __restrict__ wa_, const unsigned char* __restrict__ wphi_,
             const unsigned char* __restrict__ woa_, const unsigned char* __restrict__ wb_, const unsigned char* __restrict__ wob_,
             unsigned char* out_, unsigned char* ws_) {
  extern __shared__ __attribute__((aligned(16))) unsigned char smem[];
  asm volatile("" ::: "v255", "a255");
  const GAS unsigned char* x = (const GAS unsigned char*)x_;
  Ctx ctx;
  ctx.ws = (GAS unsigned char*)ws_;
  ctx.rs = __builtin_amdgcn_make_buffer_rsrc((void*)ws_, 0, WS_END, 0x00020000);
  ctx.L = (lds_t*)smem;
  ctx.Lb = (unsigned)(size_t)ctx.L;
  const int b = blockIdx.x, idx = b >> 3;
  ctx.c = (b & 7) * 4 + (idx >> 3);
  ctx.m = idx & 7;
  ctx.w = __builtin_amdgcn_readfirstlane(threadIdx.x >> 6);
  ctx.budget = SPIN_CAP;
  fresh(ctx);
  const int c = ctx.c, m = ctx.m, w = ctx.w, lane = ctx.lane, g = ctx.g, tok = ctx.tok;
  STAMP(40); CSTAMP(0);
  const rsrc_t rsa = __builtin_amdgcn_make_buffer_rsrc((void*)wa_, 0, 512 * 3072, 0x00020000);
  const rsrc_t rsb = __builtin_amdgcn_make_buffer_rsrc((void*)wb_, 0, 512 * 3072, 0x00020000);
  const rsrc_t rwa = __builtin_amdgcn_make_buffer_rsrc((void*)woa_, 0, 512 * 1024, 0x00020000);
  const rsrc_t rwb = __builtin_amdgcn_make_buffer_rsrc((void*)wob_, 0, 512 * 1024, 0x00020000);
  unsigned epoch, ph0, ph1, ph2, ph3;
  {
    const GAS unsigned char* pp = (const GAS unsigned char*)wphi_ + (4 * m + w) * 512;
    const unsigned pv = (unsigned)(4 * g * 16 + tok) * 2, zero = 0;
    asm volatile("s_nop 4\n\tglobal_load_dword %0, %5, %6 sc1\n\t"
                 "global_load_ushort %1, %7, %8\n\tglobal_load_ushort %2, %7, %8 offset:32\n\t"
                 "global_load_ushort %3, %7, %8 offset:64\n\tglobal_load_ushort %4, %7, %8 offset:96"
                 : "=&v"(epoch), "=&v"(ph0), "=&v"(ph1), "=&v"(ph2), "=&v"(ph3)
                 : "v"(zero), "s"(ctx.ws), "v"(pv), "s"(pp) : "memory");
  }
  const unsigned lv = lane * 16;
  const int pfr = (16 * idx + 4 * w) * 3072;
  sfor<0, 4>([&](auto i) { pf1<2048>(lv, rsa, pfr + i * 3072); });
  {
    const ImgDma kd = img_prep<3072>(ctx);
    sfor<0, 4>([&](auto j) { img_chunk<3072, 1024, L_RQ, j>(ctx, kd, rsa); });
  }
  {
    const GAS unsigned char* sb = x + (size_t)(64 * c + 16 * w) * 1024;
    const unsigned xv = (unsigned)((lane >> 2) * 1024 + (((lane & 3) ^ ((4 - (lane >> 4)) & 3)) << 4));
    const unsigned ldsbase = ctx.Lb + L_XT + (1 + w) * 16384;
#define XD(i) "global_load_lds_dwordx4 %0, %1 offset:" #i "\n\ts_add_u32 m0, m0, 0x3c0\n\ts_nop 0\n\t"
    asm volatile("s_mov_b32 m0, %2\n\ts_nop 0\n\t"
                 XD(0) XD(64) XD(128) XD(192) XD(256) XD(320) XD(384) XD(448)
                 XD(512) XD(576) XD(640) XD(704) XD(768) XD(832) XD(896) XD(960)
                 :: "v"(xv), "s"(sb), "s"(ldsbase) : "memory", "scc");
#undef XD
  }
  STAMP(42);
  asm volatile("s_waitcnt vmcnt(16)" : "+v"(epoch), "+v"(ph0), "+v"(ph1), "+v"(ph2), "+v"(ph3) :: "memory");
  RAWBAR();
  STAMP(43);
  const unsigned rvo = (unsigned)((lane >> 3) * 3072 + (lane & 7) * 16);
  const int rvb = 2048 + m * 128 + 32 * w * 3072;
  const unsigned trb = ctx.Lb + L_RQ + tr_base(lane, w);
  sfor<0, 8>([&](auto i) {
    raw_ld1<P_AV + i>(rvo, rsa, rvb + (128 * (i >> 2) + 8 * (i & 3)) * 3072);
    wtr<P_AK + 2 * i, 2 * i * 4096>(trb); wtr<P_AK + 2 * i + 1, (2 * i + 1) * 4096>(trb);
  });
  ctx.T = __builtin_amdgcn_readfirstlane(epoch) + 1u;
  v2u wphi = v2u{ph0 | (ph1 << 16), ph2 | (ph3 << 16)};
  const int csa = L_CS + (w * 6) * 64;
  unsigned wp0, wp1;
  {
    const int ch = lane & 7, rb1 = (lane >> 4) & 1;
    const unsigned base = ctx.Lb + L_RQ + (32 * w + (lane >> 3)) * 128 + (ch & 1) * 16;
    wp0 = base + (((ch >> 1) ^ rb1) << 5); wp1 = base + (((ch >> 1) ^ (rb1 | 2)) << 5);
  }
  f4 xacc[4];
  WAIT_VM(8);
  STAMP(44);
  {
    lds_t* tb = ctx.L + opq(L_XT + (1 + w) * 16384 + x0_lane(tok, g));
    v4u xs[16];
#pragma unroll
    for (int ks = 0; ks < 16; ++ks) xs[ks] = lds_ld128(tb, ks * 1024);
    v2u xr[4];
#pragma unroll
    for (int nt = 0; nt < 4; ++nt)
      xr[nt] = *(LAS v2u*)(ctx.L + L_XT + (1 + w) * 16384 + (2 * m + (nt >> 1)) * 1024 + x0_lane(tok, 2 * (nt & 1) + (g >> 1)) + (g & 1) * 8);
    v4u ones = {mk1(), mk1(), mk1(), mk1()};
    BAR();
#pragma unroll
    for (int ks = 0; ks < 16; ++ks) asm volatile("" : "+a"(xs[ks]));
    f4 gram = {0.f, 0.f, 0.f, 0.f}, ssum = {0.f, 0.f, 0.f, 0.f};
    asm volatile("s_nop 1" : "+v"(gram), "+v"(ssum), "+v"(ones));
    sfor<0, 16>([&](auto ks) {
      mf_aa(gram, xs[ks], xs[ks]); VNOP();
      if constexpr (ks < 8) raw_ld1<P_AV + 8 + ks>(rvo, rsa, rvb + (128 * ((8 + ks) >> 2) + 8 * (ks & 3)) * 3072);
      if constexpr (ks == 8) WAIT_VM(8);
      if constexpr (ks >= 8) raw_st1<P_AV + ks - 8, ((ks - 8) >> 2) * 16384 + (ks & 3) * 1024>((ks & 1) ? wp1 : wp0);
      mf_va(ssum, ones, xs[ks]); VNOP();
    });
#pragma unroll
    for (int nt = 0; nt < 4; ++nt)
      xacc[nt] = f4{asf(xr[nt].x << 16), asf(xr[nt].x & 0xffff0000u), asf(xr[nt].y << 16), asf(xr[nt].y & 0xffff0000u)};
    asm volatile("" : "+a"(xacc[0]), "+a"(xacc[1]), "+a"(xacc[2]), "+a"(xacc[3]));
    BAR();
    STAMP(45);
    f4 c0, c1;
    sfor<0, 8>([&](auto k) {
      wtr<P_AV + k, k * 4096>(trb);
      wcs<P_AK + 2 * k, k == 0>(c0, ones);
      if constexpr (k < 4) pf1<0>(lv, rsa, pfr + k * 3072);
      wcs<P_AK + 2 * k + 1, k == 0>(c1, ones);
    });
    asm volatile("" : "+v"(gram), "+v"(ssum));
    const float q = (tok & 2) ? ((tok & 1) ? gram[3] : gram[2]) : ((tok & 1) ? gram[1] : gram[0]);
    const float mean = ssum[0] * (1.0f / 512.0f);
    const float var = fmaxf(q * (1.0f / 512.0f) - mean * mean, 0.0f);
    const float rstd = __builtin_amdgcn_rsqf(var + 1e-5f);
    WAIT_VM(4);
    sfor<8, 16>([&](auto i) { raw_st1<P_AV + i, (i >> 2) * 16384 + (i & 3) * 1024>((i & 1) ? wp1 : wp0); });
    if (g == (tok >> 2)) {
      *(LAS f4*)(ctx.L + L_REC + ((1 + w) * 16 + tok) * 16) = f4{-mean, rstd, rstd, 0.f};
      *(LAS float*)(ctx.L + L_NM + ((1 + w) * 16 + tok) * 4) = -mean;
      *(LAS float*)(ctx.L + L_RS + ((1 + w) * 16 + tok) * 4) = rstd;
    }
    asm volatile("" : "+v"(c0), "+v"(c1));
    if (g == 0) *(LAS float*)(ctx.L + csa + 64 + tok * 4) = c0[0] + c1[0];
    BAR();
    STAMP(46);
    sfor<8, 16>([&](auto k) { wtr<P_AV + k, k * 4096>(trb); });
    sfor<0, 8>([&](auto k) {
      if constexpr (k == 4) asm volatile("s_waitcnt lgkmcnt(0)" ::: "memory");
      wcs<P_AV + 2 * k, k == 0>(c0, ones);
      if constexpr (k < 6) pf1<(k % 3) * 1024>(lv, rsb, pfr + (k / 3) * 3072);
      wcs<P_AV + 2 * k + 1, k == 0>(c1, ones);
    });
    asm volatile("s_nop 7" : "+v"(c0), "+v"(c1));
    if (g == 0) *(LAS float*)(ctx.L + csa + 128 + tok * 4) = c0[0] + c1[0];
  }
  asm volatile("" : "+a"(xacc[0]), "+a"(xacc[1]), "+a"(xacc[2]), "+a"(xacc[3]), "+a"(wphi));
  STAMP(1);
  const rsrc_t out = __builtin_amdgcn_make_buffer_rsrc((void*)out_, 0, 2048 * 1024, 0x00020000);

#pragma nounroll
  for (int lp = 0; lp < 2; ++lp) {
    asm volatile("" : "+a"(xacc[0]), "+a"(xacc[1]), "+a"(xacc[2]), "+a"(xacc[3]), "+a"(wphi));
    layer_A(ctx, lp, wphi, rwa, rsa, rsb);
    out_proj<false>(ctx, lp, xacc, out, rsb);
    layer_B(ctx, 2 * lp + 1, rwb);
    out_proj<true>(ctx, lp, xacc, out, rsb);
  }
  if (b == 0 && threadIdx.x == 0) __hip_atomic_store((gu32*)(ctx.ws + WS_EPOCH), ctx.T, RLX_AGENT);
  STAMP(41); CSTAMP(1);
  STAMP_FLUSH();
}

extern "C" void kernel_launch(void* const* d_in, const int* in_sizes, int n_in, void* d_out, int out_size, void* d_ws,
                              size_t ws_size, hipStream_t stream) {
  (void)in_sizes; (void)n_in; (void)out_size; (void)ws_size;
  (void)hipFuncSetAttribute((const void*)fused_kernel, hipFuncAttributeMaxDynamicSharedMemorySize, LDS_BYTES);
  hipLaunchKernelGGL(fused_kernel, dim3(NWG), dim3(NT), LDS_BYTES, stream,
                     (const unsigned char*)d_in[0], (const unsigned char*)d_in[1], (const unsigned char*)d_in[2],
                     (const unsigned char*)d_in[3], (const unsigned char*)d_in[5], (const unsigned char*)d_in[6],
                     (unsigned char*)d_out, (unsigned char*)d_ws);
}
```

```cpp
#include <hip/hip_runtime.h>
#include <hip/hip_bf16.h>
#include <type_traits>

#define GAS __attribute__((address_space(1)))
#define LAS __attribute__((address_space(3)))
#define DEV __device__ __forceinline__
typedef short s4 __attribute__((ext_vector_type(4)));
typedef float f4 __attribute__((ext_vector_type(4)));
typedef float f2 __attribute__((ext_vector_type(2)));
typedef __bf16 bf2 __attribute__((ext_vector_type(2)));
typedef unsigned v4u __attribute__((ext_vector_type(4)));
typedef unsigned v2u __attribute__((ext_vector_type(2)));
typedef unsigned long long u64;
typedef GAS unsigned gu32;
typedef LAS unsigned char lds_t;
typedef __amdgpu_buffer_rsrc_t rsrc_t;
#define RLX_AGENT __ATOMIC_RELAXED, __HIP_MEMORY_SCOPE_AGENT

namespace {
constexpr int NWG = 256, NT = 256;
constexpr int WS_EPOCH = 0;
constexpr int WS_FX = 4096;
constexpr int WS_FO = WS_FX + 4 * 32 * 128;
constexpr int WS_KV2 = 1 << 16;
constexpr int KV2_L = 8 * 64 * 256;
constexpr int WS_STAMP = 3 << 17;
constexpr int WS_STAT = 1 << 20;
constexpr int STAT_L = 32 * 8 * 64 * 16;
constexpr int WS_X = 2 << 20;
constexpr int ACT_L = 2 << 20;
constexpr int WS_O = WS_X + 3 * ACT_L;
constexpr int WS_KV1 = WS_O + 4 * ACT_L;
constexpr int KV1_L = 8 * 64 * 32 * 256;
constexpr int WS_KVZ = WS_KV1 + 2 * KV1_L;
constexpr int KVZ_L = 32 * 32 * 256;
constexpr int WS_END = WS_KVZ + 2 * KVZ_L;
constexpr int L_XT = 0;
constexpr int L_RQ = 81920;
constexpr int L_REC = 147456 + 256;
constexpr int L_NM = L_REC + 1280;
constexpr int L_RS = L_NM + 320;
constexpr int L_CS = L_RS + 320;
constexpr int L_END = L_CS + 4 * 2 * 3 * 64;
constexpr int LDS_BYTES = 158 * 1024;
static_assert(L_END <= LDS_BYTES, "lds");
constexpr int SPIN_CAP = 1 << 18;
constexpr float LOG2E = 1.4426950408889634f;
#define QPOLL 6
#define S2POLL 0
}

DEV float asf(unsigned u) { return __builtin_bit_cast(float, u); }
DEV unsigned asu(float f) { return __builtin_bit_cast(unsigned, f); }
DEV unsigned pk2(float a, float b) { unsigned r; asm("v_cvt_pk_bf16_f32 %0, %1, %2" : "=v"(r) : "v"(a), "v"(b)); return r; }
DEV float xg_sum(float v) {
  auto r = __builtin_amdgcn_permlane16_swap(asu(v), asu(v), false, false);
  v = asf(r[0]) + asf(r[1]);
  auto q = __builtin_amdgcn_permlane32_swap(asu(v), asu(v), false, false);
  return asf(q[0]) + asf(q[1]);
}
DEV float row_sum(float v) {
  v += asf(__builtin_amdgcn_update_dpp(0u, asu(v), 0x128, 0xf, 0xf, false));
  v += asf(__builtin_amdgcn_update_dpp(0u, asu(v), 0x124, 0xf, 0xf, false));
  v += asf(__builtin_amdgcn_update_dpp(0u, asu(v), 0x122, 0xf, 0xf, false));
  v += asf(__builtin_amdgcn_update_dpp(0u, asu(v), 0x121, 0xf, 0xf, false));
  return v;
}
DEV float sum32(float v) {
  v = row_sum(v);
  auto r = __builtin_amdgcn_permlane16_swap(asu(v), asu(v), false, false);
  return asf(r[0]) + asf(r[1]);
}
#define BAR() asm volatile("s_waitcnt lgkmcnt(0)\n\ts_barrier" ::: "memory")
#define RAWBAR() do { asm volatile("" ::: "memory"); __builtin_amdgcn_s_barrier(); asm volatile("" ::: "memory"); } while (0)
#define DRAIN() asm volatile("s_waitcnt vmcnt(0)" ::: "memory")
#define WAIT_VM(n) asm volatile("s_waitcnt vmcnt(%0)" :: "n"(n) : "memory")
#define WAIT_LG(n) __builtin_amdgcn_s_waitcnt(0xC07F | ((n) << 8))
#define WAIT_VMV(n) __builtin_amdgcn_s_waitcnt(0x0F70 | ((n) & 15) | (((n) >> 4) << 14))
#define VNOP() __builtin_amdgcn_s_setprio(0)
#define SB() __builtin_amdgcn_sched_barrier(0)
#define NOP(n) asm volatile("s_nop %0" :: "n"(n))
#define SC1 16
#define ONE2 0x3f803f80u
template <int N> using IC = std::integral_constant<int, N>;

DEV unsigned mk1() { unsigned r; asm volatile("v_mov_b32 %0, 0x3f803f80" : "=v"(r)); return r; }
DEV unsigned mk0() { unsigned r; asm volatile("v_mov_b32 %0, 0" : "=v"(r)); return r; }
DEV int opq(int v) { asm volatile("" : "+v"(v)); return v; }
DEV v4u lds_ld128(lds_t* L, int off) { return *(LAS v4u*)(L + off); }
DEV f4 lds_ldf4(lds_t* L, int off) { return *(LAS f4*)(L + off); }
DEV float lds_ldf(lds_t* L, int off) { return *(LAS float*)(L + off); }
DEV v4u tr_frag(lds_t* L, int off) {
  s4 a = __builtin_amdgcn_ds_read_tr16_b64_v4i16((LAS s4*)(L + off));
  s4 b = __builtin_amdgcn_ds_read_tr16_b64_v4i16((LAS s4*)(L + off + 512));
  v2u ua = __builtin_bit_cast(v2u, a), ub = __builtin_bit_cast(v2u, b);
  return v4u{ua.x, ua.y, ub.x, ub.y};
}
template <int I, int N, class F> DEV void sfor(F&& f) { if constexpr (I < N) { f(IC<I>{}); sfor<I + 1, N>(f); } }

constexpr int P_AK = 0, P_AV = 16, P_BQ = 32, P_BK = 48, P_BV = 64;
#define MF32 "v_mfma_f32_16x16x32_bf16 "
#define MF16 "v_mfma_f32_16x16x16_bf16 "
#define MM_CASE(WC, XC)                                                                                                         \
  if constexpr (WA) {                                                                                                           \
    if constexpr (FIRST) asm volatile(MF32 "%0, " WC "[%c1:%c2], " XC "[%c3:%c4], 0" : "=v"(acc) : "n"(R), "n"(R + 3), "n"(X), "n"(X + 3)); \
    else asm volatile(MF32 "%0, " WC "[%c1:%c2], " XC "[%c3:%c4], %0" : "+v"(acc) : "n"(R), "n"(R + 3), "n"(X), "n"(X + 3));     \
  } else {                                                                                                                      \
    if constexpr (FIRST) asm volatile(MF32 "%0, " XC "[%c3:%c4], " WC "[%c1:%c2], 0" : "=v"(acc) : "n"(R), "n"(R + 3), "n"(X), "n"(X + 3)); \
    else asm volatile(MF32 "%0, " XC "[%c3:%c4], " WC "[%c1:%c2], %0" : "+v"(acc) : "n"(R), "n"(R + 3), "n"(X), "n"(X + 3));     \
  }
template <int F, int RS, bool WA, bool FIRST>
DEV void mm(f4& acc) {
  constexpr int R = 96 + 4 * (F < 40 ? F : F - 40), X = 88 + 4 * (RS & 1);
  if constexpr (F < 40) { if constexpr (RS < 2) { MM_CASE("v", "v") } else { MM_CASE("v", "a") } }
  else { if constexpr (RS < 2) { MM_CASE("a", "v") } else { MM_CASE("a", "a") } }
}
template <int RS, int OFF>
DEV void ring_rd(unsigned addr) {
  constexpr int X = 88 + 4 * (RS & 1);
  if constexpr (RS < 2) asm volatile("ds_read_b128 v[%c1:%c2], %0 offset:%c3" :: "v"(addr), "n"(X), "n"(X + 3), "n"(OFF) : "memory");
  else asm volatile("ds_read_b128 a[%c1:%c2], %0 offset:%c3" :: "v"(addr), "n"(X), "n"(X + 3), "n"(OFF) : "memory");
}
template <int F, bool FIRST>
DEV void wcs(f4& acc, v4u x) {
  constexpr int R = 96 + 4 * (F < 40 ? F : F - 40);
  if constexpr (F < 40) {
    if constexpr (FIRST) asm volatile(MF32 "%0, %1, v[%c2:%c3], 0" : "=&v"(acc) : "v"(x), "n"(R), "n"(R + 3));
    else asm volatile(MF32 "%0, %1, v[%c2:%c3], %0" : "+v"(acc) : "v"(x), "n"(R), "n"(R + 3));
  } else {
    if constexpr (FIRST) asm volatile(MF32 "%0, %1, a[%c2:%c3], 0" : "=&v"(acc) : "v"(x), "n"(R), "n"(R + 3));
    else asm volatile(MF32 "%0, %1, a[%c2:%c3], %0" : "+v"(acc) : "v"(x), "n"(R), "n"(R + 3));
  }
}
template <int RS, bool FIRST>
DEV void mmq(f4& acc, v4u a) {
  constexpr int X = 88 + 4 * (RS & 1);
  if constexpr (RS < 2) {
    if constexpr (FIRST) asm volatile(MF32 "%0, %1, v[%c2:%c3], 0" : "=v"(acc) : "a"(a), "n"(X), "n"(X + 3));
    else asm volatile(MF32 "%0, %1, v[%c2:%c3], %0" : "+v"(acc) : "a"(a), "n"(X), "n"(X + 3));
  } else {
    if constexpr (FIRST) asm volatile(MF32 "%0, %1, a[%c2:%c3], 0" : "=v"(acc) : "a"(a), "n"(X), "n"(X + 3));
    else asm volatile(MF32 "%0, %1, a[%c2:%c3], %0" : "+v"(acc) : "a"(a), "n"(X), "n"(X + 3));
  }
}
template <bool FIRST>
DEV void mf_av(f4& acc, v4u a, v4u b) {
  if constexpr (FIRST) asm volatile(MF32 "%0, %1, %2, 0" : "=&v"(acc) : "a"(a), "v"(b));
  else asm volatile(MF32 "%0, %1, %2, %0" : "+v"(acc) : "a"(a), "v"(b));
}
DEV void mf_va(f4& acc, v4u a, v4u b) { asm volatile(MF32 "%0, %1, %2, %0" : "+v"(acc) : "v"(a), "a"(b)); }
DEV void mf_aa(f4& acc, v4u a, v4u b) { asm volatile(MF32 "%0, %1, %2, %0" : "+v"(acc) : "a"(a), "a"(b)); }
DEV void mf32z(f4& d, v4u a, v4u b) { asm volatile(MF32 "%0, %1, %2, 0" : "=&v"(d) : "v"(a), "v"(b)); VNOP(); }
DEV void mf32c(f4& d, v4u a, v4u b) { asm volatile(MF32 "%0, %1, %2, %0" : "+v"(d) : "v"(a), "v"(b)); VNOP(); }
DEV void mf16z(f4& d, v2u a, v2u b) { asm volatile(MF16 "%0, %1, %2, 0" : "=&v"(d) : "v"(a), "v"(b)); VNOP(); }
DEV void mf16c(f4& d, v2u a, v2u b) { asm volatile(MF16 "%0, %1, %2, %0" : "+v"(d) : "v"(a), "v"(b)); VNOP(); }
DEV void mf16z_a(f4& d, v2u a, v2u b) { asm volatile(MF16 "%0, %1, %2, 0" : "=&v"(d) : "a"(a), "v"(b)); VNOP(); }
DEV void mf16c_a(f4& d, v2u a, v2u b) { asm volatile(MF16 "%0, %1, %2, %0" : "+v"(d) : "a"(a), "v"(b)); VNOP(); }
DEV void mf16z_b(f4& d, v2u a, v2u b) { asm volatile(MF16 "%0, %1, %2, 0" : "=&v"(d) : "v"(a), "a"(b)); VNOP(); }
template <int F, int OFF>
DEV void wtr(unsigned addr) {
  constexpr int R = 96 + 4 * (F < 40 ? F : F - 40);
  if constexpr (F < 40)
    asm volatile("ds_read_b64_tr_b16 v[%c1:%c2], %0 offset:%c5\n\tds_read_b64_tr_b16 v[%c3:%c4], %0 offset:%c6"
                 :: "v"(addr), "n"(R), "n"(R + 1), "n"(R + 2), "n"(R + 3), "n"(OFF), "n"(OFF + 512) : "memory");
  else
    asm volatile("ds_read_b64_tr_b16 a[%c1:%c2], %0 offset:%c5\n\tds_read_b64_tr_b16 a[%c3:%c4], %0 offset:%c6"
                 :: "v"(addr), "n"(R), "n"(R + 1), "n"(R + 2), "n"(R + 3), "n"(OFF), "n"(OFF + 512) : "memory");
}
#define A_FMA(d, a, b, c) asm volatile("v_fma_f32 %0, %1, %2, %3" : "=v"(d) : "v"(a), "v"(b), "v"(c))
#define A_FMAN(d, a, b, c) asm volatile("v_fma_f32 %0, %1, -%2, %3" : "=v"(d) : "v"(a), "v"(b), "v"(c))
#define A_MUL(d, a, b) asm volatile("v_mul_f32 %0, %1, %2" : "=v"(d) : "v"(a), "v"(b))
#define A_MULS(d, k, b) asm volatile("v_mul_f32 %0, %1, %2" : "=v"(d) : "s"(k), "v"(b))
#define A_MAX(d, a, b) asm volatile("v_max_f32 %0, %1, %2" : "=v"(d) : "v"(a), "v"(b))
#define A_SUB(d, a, b) asm volatile("v_sub_f32 %0, %1, %2" : "=v"(d) : "v"(a), "v"(b))
#define A_CVT(d, a, b) asm volatile("v_cvt_pk_bf16_f32 %0, %1, %2" : "=v"(d) : "v"(a), "v"(b))
#define A_EXP(d, a) asm volatile("v_exp_f32 %0, %1" : "=v"(d) : "v"(a))
#define A_RCP(d, x4) asm volatile("v_rcp_f32 %0, %1" : "=&v"(d) : "v"(x4[0]), "v"(x4))
#define A_SEL(d, f, t, m) asm volatile("v_cndmask_b32 %0, %1, %2, %3" : "=v"(d) : "v"(f), "v"(t), "s"(m))
#define A_PERM(d, x, sel) asm volatile("v_perm_b32 %0, %1, %1, %2" : "=v"(d) : "v"(x), "v"(sel))
#define A_SW16(a, b) asm volatile("v_permlane16_swap_b32 %0, %1" : "+v"(a), "+v"(b))
#define A_SW32(a, b) asm volatile("v_permlane32_swap_b32 %0, %1" : "+v"(a), "+v"(b))
#define A_LD64(d, addr, off) asm volatile("ds_read_b64 %0, %1 offset:%2" : "=v"(d) : "v"(addr), "n"(off) : "memory")
#define A_LD128(d, addr, off) asm volatile("ds_read_b128 %0, %1 offset:%2" : "=v"(d) : "v"(addr), "n"(off) : "memory")

#define STAMP(i) do {} while (0)
#define CSTAMP(i) do {} while (0)
#define STAMP_FLUSH() do {} while (0)
#define TS(k) do {} while (0)
#define TS_DECL
#define TS_FLUSH(base, n) do {} while (0)

struct Ctx {
  GAS unsigned char* ws;
  rsrc_t rs;
  lds_t* L;
  unsigned Lb, Lw;
  int c, m, w;
  int lane, tok, g;
  unsigned T;
  int budget;
};
DEV void fresh(Ctx& ctx) {
  int l;
  asm volatile("v_mbcnt_lo_u32_b32 %0, -1, 0\n\tv_mbcnt_hi_u32_b32 %0, -1, %0" : "=v"(l));
  ctx.lane = l; ctx.tok = l & 15; ctx.g = l >> 4;
  asm volatile("" : "+s"(ctx.c), "+s"(ctx.m), "+s"(ctx.w), "+s"(ctx.Lb));
  ctx.Lw = ctx.Lb + 4096 * ctx.w;
}
DEV v4u ld128(Ctx& ctx, int voff, int soff) { return __builtin_amdgcn_raw_buffer_load_b128(ctx.rs, voff, soff, SC1); }
DEV void st128(Ctx& ctx, v4u v, int voff, int soff) { __builtin_amdgcn_raw_buffer_store_b128(v, ctx.rs, voff, soff, SC1); }
DEV void st64(Ctx& ctx, v2u v, int voff, int soff) { __builtin_amdgcn_raw_buffer_store_b64(v, ctx.rs, voff, soff, SC1); }

DEV void wait_flags(Ctx& ctx, int off0, int off1, bool two) {
  if (ctx.w == 0) {
    const int l = ctx.lane & (two ? 15 : 7);
    const int vo = (l < 8 ? off0 : off1 - 128) + l * 16;
    for (;;) {
      asm volatile("" ::: "memory");
      const unsigned v = __builtin_amdgcn_raw_buffer_load_b32(ctx.rs, vo, 0, SC1);
      if (__all(v == ctx.T) || --ctx.budget < 0) break;
    }
    __builtin_amdgcn_fence(__ATOMIC_ACQUIRE, "wavefront");
  }
  BAR();
}
DEV void flag_store(Ctx& ctx, int off) {
  if (ctx.w == 0) {
    unsigned tv, zv;
    asm volatile("v_mov_b32 %0, %2\n\tv_mov_b32 %1, 0" : "=v"(tv), "=v"(zv) : "s"(ctx.T));
    __builtin_amdgcn_raw_buffer_store_b32(tv, ctx.rs, zv, off + ctx.m * 16, SC1);
  }
}

DEV int x0_lane(int tok, int g) { return tok * 64 + ((g ^ ((4 - (tok >> 2)) & 3)) << 4); }
DEV int tr_base(int lane, int j) {
  const int g = lane >> 4, q = (lane & 15) >> 2, p = lane & 3;
  const int s = ((q >> 1) & 1) | ((g & 1) << 1);
  return (8 * g + q) * 128 + ((j ^ s) << 5) + p * 8;
}
template <int F>
DEV void raw_ld1(unsigned voff, rsrc_t rs, int soff) {
  constexpr int R = 96 + 4 * (F < 40 ? F : F - 40);
  if constexpr (F < 40) asm volatile("buffer_load_dwordx4 v[%c3:%c4], %0, %1, %2 offen" :: "v"(voff), "s"(rs), "s"(soff), "n"(R), "n"(R + 3) : "memory");
  else asm volatile("buffer_load_dwordx4 a[%c3:%c4], %0, %1, %2 offen" :: "v"(voff), "s"(rs), "s"(soff), "n"(R), "n"(R + 3) : "memory");
}
template <int P, int I0, int I1>
DEV void raw_load(Ctx& ctx, rsrc_t rs, int colbyte) {
  const unsigned vo = (unsigned)((ctx.lane >> 3) * 3072 + (ctx.lane & 7) * 16);
  const int sb = colbyte + ctx.m * 128 + 32 * ctx.w * 3072;
  sfor<I0, I1>([&](auto i) { raw_ld1<P + i>(vo, rs, sb + (128 * (i >> 2) + 8 * (i & 3)) * 3072); });
}
template <int F, int OFF>
DEV void raw_st1(unsigned addr) {
  constexpr int R = 96 + 4 * (F < 40 ? F : F - 40);
  if constexpr (F < 40) asm volatile("ds_write_b128 %0, v[%c1:%c2] offset:%c3" :: "v"(addr), "n"(R), "n"(R + 3), "n"(OFF) : "memory");
  else asm volatile("ds_write_b128 %0, a[%c1:%c2] offset:%c3" :: "v"(addr), "n"(R), "n"(R + 3), "n"(OFF) : "memory");
}
template <int P, int I0, int I1>
DEV void raw_store(Ctx& ctx) {
  const int lane = ctx.lane, ch = lane & 7, rb1 = (lane >> 4) & 1;
  const unsigned base = ctx.Lb + L_RQ + (32 * ctx.w + (lane >> 3)) * 128 + (ch & 1) * 16;
  const unsigned p0 = base + (((ch >> 1) ^ rb1) << 5), p1 = base + (((ch >> 1) ^ (rb1 | 2)) << 5);
  sfor<I0, I1>([&](auto i) { raw_st1<P + i, (i >> 2) * 16384 + (i & 3) * 1024>((i & 1) ? p1 : p0); });
}
template <int P, int K0, int K1>
DEV void part_tr(Ctx& ctx) {
  const unsigned tb = ctx.Lb + L_RQ + tr_base(ctx.lane, ctx.w);
  sfor<K0, K1>([&](auto ks) { wtr<P + ks, ks * 4096>(tb); });
}
template <int P>
DEV void part_cs(Ctx& ctx, int csoff) {
  v4u ones = {mk1(), mk1(), mk1(), mk1()};
  asm volatile("s_waitcnt lgkmcnt(0)\n\ts_nop 1" : "+v"(ones) :: "memory");
  f4 c0, c1;
  sfor<0, 8>([&](auto k) { wcs<P + 2 * k, k == 0>(c0, ones); wcs<P + 2 * k + 1, k == 0>(c1, ones); });
  asm volatile("s_nop 7" : "+v"(c0), "+v"(c1));
  if (ctx.g == 0) *(LAS float*)(ctx.L + csoff + ctx.tok * 4) = c0[0] + c1[0];
}

template <int P>
DEV void part_step(Ctx& ctx, int csoff) {
  raw_store<P, 0, 8>(ctx);
  BAR();
  part_tr<P, 0, 8>(ctx);
  raw_store<P, 8, 16>(ctx);
  BAR();
  part_tr<P, 8, 16>(ctx);
  part_cs<P>(ctx, csoff);
}

template <int P, int PN>
DEV void part_step_ld(Ctx& ctx, int csoff, rsrc_t rs, int colbyte) {
  raw_load<PN, 0, 4>(ctx, rs, colbyte);
  raw_store<P, 0, 8>(ctx);
  raw_load<PN, 4, 8>(ctx, rs, colbyte);
  BAR();
  part_tr<P, 0, 8>(ctx);
  raw_load<PN, 8, 12>(ctx, rs, colbyte);
  raw_store<P, 8, 16>(ctx);
  raw_load<PN, 12, 16>(ctx, rs, colbyte);
  BAR();
  part_tr<P, 8, 16>(ctx);
  part_cs<P>(ctx, csoff);
}
template <int OFF>
DEV void pf1(unsigned lv, rsrc_t rs, int so) {
  asm volatile("buffer_load_dwordx4 v[224:227], %0, %1, %2 offen offset:%3" :: "v"(lv), "s"(rs), "s"(so), "n"(OFF) : "memory");
}

struct ImgDma { unsigned v[4]; int sb; };
template <int ROWB>
DEV ImgDma img_prep(Ctx& ctx) {
  const int lane = ctx.lane, cp = lane & 7, rb1 = (lane >> 4) & 1;
  const unsigned vb = (unsigned)((lane >> 3) * ROWB + (cp & 1) * 16);
  const unsigned v0 = vb + (((cp >> 1) ^ rb1) << 5), v1 = vb + (((cp >> 1) ^ (rb1 | 2)) << 5);
  ImgDma d;
  d.v[0] = v0; d.v[1] = v1 + (8 * ROWB - 1024); d.v[2] = v0 + 2 * (8 * ROWB - 1024); d.v[3] = v1 + 3 * (8 * ROWB - 1024);
  d.sb = ctx.m * 128 + 32 * ctx.w * ROWB;
  return d;
}
template <int LOFF> DEV void dma_m0(unsigned lbase) { asm volatile("s_add_u32 m0, %0, %1" :: "s"(lbase), "n"(LOFF) : "scc"); }
template <int SOFF> DEV void dma_so(int& t, int sbase) { asm volatile("s_add_u32 %0, %1, %2" : "=s"(t) : "s"(sbase), "n"(SOFF) : "scc"); }
template <int I> DEV void dma_go(unsigned voff, rsrc_t rs, int so) {
  asm volatile("buffer_load_dwordx4 %0, %1, %2 offen offset:%3 lds" :: "v"(voff), "s"(rs), "s"(so), "n"(I * 1024) : "memory");
}
template <int I> DEV void dma_go_sc1(unsigned voff, rsrc_t rs, int so) {
  asm volatile("buffer_load_dwordx4 %0, %1, %2 offen offset:%3 sc1 lds" :: "v"(voff), "s"(rs), "s"(so), "n"(I * 1024) : "memory");
}
template <int ROWB, int COLB, int LDSOFF, int J>
DEV void img_chunk(Ctx& ctx, const ImgDma& d, rsrc_t rs) {
  int so;
  dma_m0<LDSOFF + J * 16384>(ctx.Lw); dma_so<COLB + 128 * J * ROWB>(so, d.sb);
  sfor<0, 4>([&](auto i) { dma_go<i>(d.v[i], rs, so); });
}
template <int TT>
DEV void x_chunk(Ctx& ctx, unsigned lv, int xs) {
  int so;
  dma_m0<L_XT + (TT + 1) * 16384>(ctx.Lw); dma_so<(TT + 1) * 16384>(so, xs);
  sfor<0, 4>([&](auto i) { dma_go_sc1<i>(lv, ctx.rs, so); });
}

struct ST {
  v4u d[4];
  float s, q, t, bias;
  unsigned bad, sv, wrec, wnm;
  int so;
};
template <bool HALO>
DEV void st_prep(Ctx& ctx, ST& st, int L) {
  const int c = ctx.c, w = ctx.w, r32 = ctx.lane & 31, hf = ctx.lane >> 5;
  const bool halo = HALO && w >= 2;
  const int cc = (halo && c > 0) ? c - 1 : c;
  const int row0 = halo ? (c > 0 ? 48 : 0) : 32 * (w & 1);
  const int r = halo ? (r32 & 15) : r32;
  st.so = WS_STAT + (L - 1) * STAT_L + (cc * 8 * 64 + row0) * 16;
  st.sv = hf * 4096 + r * 16;
  const int lrow = halo ? r : 16 + row0 + r;
  st.wrec = ctx.Lb + L_REC + lrow * 16;
  st.wnm = ctx.Lb + L_NM + lrow * 4;
  st.bias = (halo && c == 0) ? -__builtin_inff() : 0.f;
  asm volatile("" : "+v"(st.sv), "+v"(st.wrec), "+v"(st.wnm), "+v"(st.bias));
}
DEV void st_issue(Ctx& ctx, ST& st) {
  asm volatile("buffer_load_dwordx4 %0, %4, %5, %6 offen sc1\n\t"
               "buffer_load_dwordx4 %1, %4, %5, %6 offen offset:1024 sc1\n\t"
               "buffer_load_dwordx4 %2, %4, %5, %6 offen offset:2048 sc1\n\t"
               "buffer_load_dwordx4 %3, %4, %5, %6 offen offset:3072 sc1"
               : "=&v"(st.d[0]), "=&v"(st.d[1]), "=&v"(st.d[2]), "=&v"(st.d[3]) : "v"(st.sv), "s"(ctx.rs), "s"(st.so) : "memory");
}
DEV void st_retry(Ctx& ctx, ST& st) {
  for (;;) {
    asm volatile("" ::: "memory");
    unsigned bad = 0;
#pragma unroll
    for (int i = 0; i < 4; ++i) { st.d[i] = ld128(ctx, st.sv + i * 1024, st.so); }
#pragma unroll
    for (int i = 0; i < 4; ++i) bad |= (st.d[i].y ^ ctx.T) | (st.d[i].w ^ ctx.T);
    if (__all(bad == 0) || --ctx.budget < 0) break;
  }
}
DEV void st_check(Ctx& ctx, ST& st) {
  asm volatile("" : "+v"(st.d[0]), "+v"(st.d[1]), "+v"(st.d[2]), "+v"(st.d[3]));
  const unsigned T = ctx.T;
  const bool ok = (st.d[0].y == T) & (st.d[0].w == T) & (st.d[1].y == T) & (st.d[1].w == T) &
                  (st.d[2].y == T) & (st.d[2].w == T) & (st.d[3].y == T) & (st.d[3].w == T);
  if (__builtin_expect(!__all(ok), 0)) st_retry(ctx, st);
}
template <int K>
DEV void st_step(ST& st) {
  if constexpr (K == 0) asm volatile("v_add_f32 %0, %2, %3\n\tv_add_f32 %1, %4, %5" : "=&v"(st.s), "=&v"(st.q) : "v"(st.d[0].x), "v"(st.d[1].x), "v"(st.d[0].z), "v"(st.d[1].z));
  if constexpr (K == 1) asm volatile("v_add_f32 %0, %0, %2\n\tv_add_f32 %1, %1, %3" : "+v"(st.s), "+v"(st.q) : "v"(st.d[2].x), "v"(st.d[2].z));
  if constexpr (K == 2) asm volatile("v_add_f32 %0, %0, %2\n\tv_add_f32 %1, %1, %3" : "+v"(st.s), "+v"(st.q) : "v"(st.d[3].x), "v"(st.d[3].z));
  if constexpr (K == 3) A_SW32(st.s, st.q);
  if constexpr (K == 4) { float x = st.s, y = st.q; asm volatile("v_add_f32 %0, %2, %3\n\tv_add_f32 %1, %2, %3" : "=&v"(st.s), "=&v"(st.q) : "v"(x), "v"(y)); }
  if constexpr (K == 5) A_SW32(st.s, st.q);
  if constexpr (K == 6) asm volatile("v_mul_f32 %0, %2, %0\n\tv_mul_f32 %1, %3, %1" : "+v"(st.s), "+v"(st.q) : "s"(-1.0f / 512.0f), "s"(1.0f / 512.0f));
  if constexpr (K == 7) asm volatile("v_fma_f32 %0, -%1, %1, %0\n\tv_max_f32 %0, 0, %0" : "+v"(st.q) : "v"(st.s));
  if constexpr (K == 8) asm volatile("v_add_f32 %0, %1, %0" : "+v"(st.q) : "s"(1e-5f));
  if constexpr (K == 9) asm volatile("v_rsq_f32 %0, %0" : "+v"(st.q));
  if constexpr (K == 10) asm volatile("v_mov_b32 %0, %1" : "=v"(st.t) : "v"(st.q));
  if constexpr (K == 11) asm volatile("ds_write_b128 %0, %1" :: "v"(st.wrec), "v"(f4{st.s, st.q, st.t, st.bias}) : "memory");
  if constexpr (K == 12) asm volatile("ds_write2_b32 %0, %1, %2 offset1:80" :: "v"(st.wnm), "v"(st.s), "v"(st.q) : "memory");
}

template <bool HALO>
DEV void gather_start(Ctx& ctx, ST& st, int L, unsigned lv, int xs) {
  const int c = ctx.c;
  const int cprev = c > 0 ? c - 1 : 0;
  constexpr int T0 = HALO ? -1 : 0;
  const int xs0 = (HALO && c == 0) ? xs + 16384 : xs;
  st_prep<HALO>(ctx, st, L);
  wait_flags(ctx, WS_FX + ((L - 1) * 32 + c) * 128, WS_FX + ((L - 1) * 32 + cprev) * 128, HALO);
  STAMP(L * 8 + 0);
  x_chunk<T0>(ctx, lv, xs0);
  st_issue(ctx, st);
  x_chunk<T0 + 1>(ctx, lv, xs);
  WAIT_VM(8);
  RAWBAR();
  STAMP(L * 8 + 1);
}
template <int H>
DEV void st_gaps(Ctx& ctx, ST& st) {
  if constexpr (H == 4 * 2 + 2) st_check(ctx, st);
  if constexpr (H == 4 * 3 + 2) st_step<0>(st);
  if constexpr (H == 4 * 4 + 2) st_step<1>(st);
  if constexpr (H == 4 * 5) st_step<2>(st);
  if constexpr (H == 4 * 5 + 2) st_step<3>(st);
  if constexpr (H == 4 * 6) st_step<4>(st);
  if constexpr (H == 4 * 6 + 2) st_step<5>(st);
  if constexpr (H == 4 * 7 + 2) st_step<6>(st);
  if constexpr (H == 4 * 8) st_step<7>(st);
  if constexpr (H == 4 * 8 + 2) { st_step<8>(st); }
  if constexpr (H == 4 * 9) st_step<9>(st);
  if constexpr (H == 4 * 9 + 2) { st_step<10>(st); st_step<12>(st); }
  if constexpr (H == 4 * 10) st_step<11>(st);
}

template <int OFF> DEV void ring_prime(unsigned ab) { ring_rd<0, OFF>(ab); ring_rd<1, OFF + 1024>(ab); ring_rd<2, OFF + 2048>(ab); WAIT_LG(2); }
template <int PQ, int PK, int PV, int OFF, bool CINIT, class XL, class SH, class GP>
DEV void gemm_tile(unsigned ab, f4& aq, f4& ak, f4& av, XL&& xl, SH&& sh, GP&& gp) {
  sfor<0, 16>([&](auto ks) {
    constexpr bool F = ks == 0 && !CINIT;
    if constexpr (PQ >= 0) {
      mm<(PQ >= 0 ? PQ : 0) + ks, ks & 3, true, F>(aq);
      if (!sh(IC<4 * ks>{})) VNOP();
      ring_rd<(ks + 3) & 3, OFF + (ks + 3) * 1024>(ab);
      gp(IC<4 * ks>{});
      mm<PK + ks, ks & 3, true, F>(ak);
      if (!sh(IC<4 * ks + 1>{})) VNOP();
      gp(IC<4 * ks + 1>{});
    } else {
      mm<PK + ks, ks & 3, true, F>(ak);
      if (!sh(IC<4 * ks>{})) VNOP();
      ring_rd<(ks + 3) & 3, OFF + (ks + 3) * 1024>(ab);
      gp(IC<4 * ks>{});
    }
    mm<PV + ks, ks & 3, false, F>(av);
    WAIT_LG(2 + decltype(xl(ks))::value);
    gp(IC<4 * ks + 2>{});
    SB();
  });
}
#define GA(k) (4 * (k))
#define GB(k) (4 * (k) + 1)
#define GC(k) (4 * (k) + 2)

struct BE {
  f4 aq, ak, av;
  f2 st, ka, kb2; f4 nm4;
  f2 nst, nka, nkb; f4 nnm4;
  f4 csq, csk; float csv;
  float q[4], k[4], v[4];
  unsigned qb[2], kb[2], vb[2], vb2[2], kprev[2], vprev[2], P[4], pk0, pk, ob[2];
  f4 sp, sc, num, den;
  float a, b, t0, t1, m, m2, ft, fa, fb, pa, pb;
  v2u ones;
  unsigned sPA, sPB, sCA, sCB;
  u64 modd, mA, mB;
  unsigned aT, aF, aKa, aKb;
  int ovo;
};

DEV void layer_B(Ctx& ctx, int L, rsrc_t rwo) {
  fresh(ctx);
  const int c = ctx.c, w = ctx.w, g = ctx.g, tok = ctx.tok;
  const int csb = L_CS + (w * 6 + 3) * 64;
  const unsigned lv = ctx.lane * 16;
  BE e;
  TS_DECL;
  unsigned ab0, ab1;
  ImgDma wd = img_prep<1024>(ctx);
  {
    const bool odd = tok & 1;
    const int ja = 4 * g + (odd ? 1 : 0), jb = ja + 2;
    const bool curA = ja <= tok, curB = jb <= tok;
    const unsigned selA = odd ? 0x01000c0cu : 0x0c0c0100u, selB = odd ? 0x03020c0cu : 0x0c0c0302u, selZ = 0x0c0c0c0cu;
    e.sPA = curA ? selZ : selA; e.sPB = curB ? selZ : selB; e.sCA = curA ? selA : selZ; e.sCB = curB ? selB : selZ;
    e.modd = __ballot(odd); e.mA = __ballot(curA); e.mB = __ballot(curB);
    e.aT = ctx.Lb + L_REC + tok * 16;
    e.aF = ctx.Lb + L_NM + g * 16;
    e.aKa = ctx.Lb + L_REC + (ja - (curA ? 0 : 16)) * 16 + 8;
    e.aKb = ctx.Lb + L_REC + (jb - (curB ? 0 : 16)) * 16 + 8;
    e.csq = lds_ldf4(ctx.L, csb + g * 16); e.csk = lds_ldf4(ctx.L, csb + 64 + g * 16); e.csv = lds_ldf(ctx.L, csb + 128 + tok * 4);
    e.ones = v2u{mk1(), mk0()};
    e.ovo = ((g >> 1) * 16 + tok) * 16 + (g & 1) * 8;
    ab0 = ctx.Lb + L_XT + lv; ab1 = ab0 + 49152;
    asm volatile("" : "+v"(e.sPA), "+v"(e.sPB), "+v"(e.sCA), "+v"(e.sCB), "+v"(e.csq), "+v"(e.csk), "+v"(e.csv), "+a"(e.ones), "+v"(e.ovo),
                      "+v"(e.aT), "+v"(e.aF), "+v"(e.aKa), "+v"(e.aKb), "+v"(ab0), "+v"(ab1), "+v"(wd.v[0]), "+v"(wd.v[1]), "+v"(wd.v[2]), "+v"(wd.v[3]));
  }
  const int xs = WS_X + (L - 1) * ACT_L + c * 65536 + w * 4096 - 16384;
  const int osb = WS_O + L * ACT_L + (4 * c * 16 + 2 * ctx.m + (w >> 1)) * 1024 + (w & 1) * 512;
  ST st;
  gather_start<true>(ctx, st, L, lv, xs);
  ring_prime<0>(ab0);
  f4 xq, xk, xv, yq, yk, yv;
  int so, oso;
  auto stat_rd = [&](auto S, auto h) {
    (void)e;
    if constexpr (h == GC(6)) { A_LD64(e.nst, e.aT, S * 256); A_LD128(e.nnm4, e.aF, S * 64); }
    if constexpr (h == GA(7)) A_LD64(e.nka, e.aKa, S * 256);
    if constexpr (h == GA(8)) A_LD64(e.nkb, e.aKb, S * 256);
  };
  auto xl = [](auto ks) { return IC<(ks == 7 ? 3 : ks == 8 ? 4 : ks == 9 ? 2 : ks == 10 ? 1 : 0)>{}; };
  auto next = [&](f4& q, f4& k, f4& v) { e.aq = q; e.ak = k; e.av = v; e.st = e.nst; e.ka = e.nka; e.kb2 = e.nkb; e.nm4 = e.nnm4; };
  auto epi = [&](auto h) {
    (void)e;
    if constexpr (h == GA(0)) A_FMA(e.q[0], e.st[0], e.csq[0], e.aq[0]);
    if constexpr (h == GB(0)) { A_FMA(e.q[1], e.st[0], e.csq[1], e.aq[1]); A_FMA(e.q[2], e.st[0], e.csq[2], e.aq[2]); }
    if constexpr (h == GC(0)) { A_FMA(e.q[3], e.st[0], e.csq[3], e.aq[3]); A_FMA(e.k[0], e.st[0], e.csk[0], e.ak[0]); }
    if constexpr (h == GA(1)) A_FMA(e.k[1], e.st[0], e.csk[1], e.ak[1]);
    if constexpr (h == GB(1)) { A_FMA(e.k[2], e.st[0], e.csk[2], e.ak[2]); A_FMA(e.k[3], e.st[0], e.csk[3], e.ak[3]); }
    if constexpr (h == GC(1)) { A_CVT(e.qb[0], e.q[0], e.q[1]); A_CVT(e.qb[1], e.q[2], e.q[3]); }
    if constexpr (h == GB(2)) { A_CVT(e.kb[0], e.k[0], e.k[1]); A_CVT(e.kb[1], e.k[2], e.k[3]); }
    if constexpr (h == GC(2)) {
      A_FMA(e.v[0], e.nm4[0], e.csv, e.av[0]); A_FMA(e.v[1], e.nm4[1], e.csv, e.av[1]);
      mf16z(e.sp, v2u{e.kprev[0], e.kprev[1]}, v2u{e.qb[0], e.qb[1]});
      A_FMA(e.v[2], e.nm4[2], e.csv, e.av[2]); A_FMA(e.v[3], e.nm4[3], e.csv, e.av[3]);
      mf16z(e.sc, v2u{e.kb[0], e.kb[1]}, v2u{e.qb[0], e.qb[1]});
      A_CVT(e.vb[0], e.v[0], e.v[1]); A_CVT(e.vb[1], e.v[2], e.v[3]);
    }
    if constexpr (h == GB(3)) { A_CVT(e.vb2[0], e.v[0], e.v[1]); A_CVT(e.vb2[1], e.v[2], e.v[3]); }
    if constexpr (h == GC(3)) A_MULS(e.ft, 0.25f * LOG2E, e.st[1]);
    if constexpr (h == GA(4)) A_MUL(e.fa, e.ka[0], e.ft);
    if constexpr (h == GB(4)) { A_SEL(e.a, e.sc[0], e.sc[1], e.modd); A_SEL(e.t0, e.sp[0], e.sp[1], e.modd); }
    if constexpr (h == GC(4)) { A_SEL(e.b, e.sc[2], e.sc[3], e.modd); A_SEL(e.t1, e.sp[2], e.sp[3], e.modd); }
    if constexpr (h == GB(5)) { A_SEL(e.a, e.t0, e.a, e.mA); A_SEL(e.b, e.t1, e.b, e.mB); }
    if constexpr (h == GC(5)) { A_MUL(e.fb, e.kb2[0], e.ft); A_FMA(e.a, e.a, e.fa, e.ka[1]); }
    if constexpr (h == GB(6)) {
      asm volatile("v_fma_f32 %0, %0, %3, %4\n\tv_max_f32 %1, %2, %0" : "+v"(e.b), "=&v"(e.m) : "v"(e.a), "v"(e.fb), "v"(e.kb2[1]));
    }
    if constexpr (h == GA(7)) A_MAX(e.m2, e.a, e.b);
    if constexpr (h == GC(7)) A_SW16(e.m, e.m2);
    if constexpr (h == GB(8)) { float x = e.m, y = e.m2; A_MAX(e.m, x, y); A_MAX(e.m2, x, y); }
    if constexpr (h == GA(9)) A_SW32(e.m, e.m2);
    if constexpr (h == GB(9)) asm volatile("v_max_f32 %0, %0, %2\n\tv_sub_f32 %1, %1, %0" : "+v"(e.m), "+v"(e.a) : "v"(e.m2));
    if constexpr (h == GC(9)) { A_SUB(e.b, e.b, e.m); }
    if constexpr (h == GB(10)) A_EXP(e.a, e.a);
    if constexpr (h == GC(10)) A_EXP(e.b, e.b);
    if constexpr (h == GB(11)) { A_MUL(e.pa, e.a, e.ka[0]); A_MUL(e.pb, e.b, e.kb2[0]); }
    if constexpr (h == GC(11)) { A_CVT(e.pk, e.pa, e.pb); A_CVT(e.pk0, e.a, e.b); }
    if constexpr (h == GB(12)) { A_PERM(e.P[0], e.pk, e.sPA); A_PERM(e.P[1], e.pk, e.sPB); }
    if constexpr (h == GC(12)) { A_PERM(e.P[2], e.pk, e.sCA); A_PERM(e.P[3], e.pk, e.sCB); }
    if constexpr (h == GA(13)) {
      mf16z_a(e.den, e.ones, v2u{e.pk0, e.pk});
      mf32z(e.num, v4u{e.vprev[0], e.vprev[1], e.vb[0], e.vb[1]}, v4u{e.P[0], e.P[1], e.P[2], e.P[3]});
      e.kprev[0] = e.kb[0]; e.kprev[1] = e.kb[1]; e.vprev[0] = e.vb2[0]; e.vprev[1] = e.vb2[1];
    }
    if constexpr (h == GB(14)) A_RCP(e.m, e.den);
    if constexpr (h == GC(14)) { A_MUL(e.q[0], e.num[0], e.m); A_MUL(e.q[1], e.num[1], e.m); }
    if constexpr (h == GA(15)) A_MUL(e.q[2], e.num[2], e.m);
    if constexpr (h == GB(15)) { A_MUL(e.q[3], e.num[3], e.m); A_CVT(e.ob[0], e.q[0], e.q[1]); }
    if constexpr (h == GC(15)) { A_CVT(e.ob[1], e.q[2], e.q[3]); st64(ctx, v2u{e.ob[0], e.ob[1]}, e.ovo, oso); }
  };
  auto x_sh = [&](auto TT, auto h) {
    if constexpr (h == GB(1)) dma_so<(TT + 1) * 16384>(so, xs);
    if constexpr (h == GA(2)) dma_m0<L_XT + (TT + 1) * 16384>(ctx.Lw);
    return h == GB(1) || h == GA(2);
  };
  auto x_gp = [&](auto h) {
    if constexpr (h == GA(2)) dma_go_sc1<0>(lv, ctx.rs, so);
    if constexpr (h == GA(3)) dma_go_sc1<1>(lv, ctx.rs, so);
    if constexpr (h == GA(5)) dma_go_sc1<2>(lv, ctx.rs, so);
    if constexpr (h == GA(6)) dma_go_sc1<3>(lv, ctx.rs, so);
  };
  auto w_sh = [&](auto J, auto N, auto h) {
    if constexpr (h == GA(7)) WAIT_VMV(N.value);
    if constexpr (h == GB(7)) RAWBAR();
    if constexpr (h == GB(9)) dma_so<128 * J * 1024>(so, wd.sb);
    if constexpr (h == GA(10)) dma_m0<L_XT + J * 16384>(ctx.Lw);
    if constexpr (h == GB(13) && J > 0) dma_so<(J - 1) * 16384>(oso, osb);
    return h == GA(7) || h == GB(7) || h == GB(9) || h == GA(10) || (h == GB(13) && J > 0);
  };
  auto w_gp = [&](auto h) {
    if constexpr (h == GA(10)) dma_go<0>(wd.v[0], rwo, so);
    if constexpr (h == GA(11)) dma_go<1>(wd.v[1], rwo, so);
    if constexpr (h == GA(12)) dma_go<2>(wd.v[2], rwo, so);
    if constexpr (h == GA(14)) dma_go<3>(wd.v[3], rwo, so);
  };
  TS(0);
  gemm_tile<-1, P_BK, P_BV, 0, false>(ab0, xq, xk, xv,
    [](auto ks) { return IC<(ks == 10 ? 2 : ks == 11 ? -1 : (ks == 12 || ks == 13 || ks == 14) ? 2 : 0)>{}; },
    [&](auto h) {
      if constexpr (h == GA(0)) dma_so<2 * 16384>(so, xs);
      if constexpr (h == GA(1)) dma_m0<L_XT + 2 * 16384>(ctx.Lw);
      if constexpr (h == GA(2)) WAIT_VMV(5);
      if constexpr (h == GA(11)) WAIT_VMV(4);
      if constexpr (h == GA(12)) RAWBAR();
      return h == GA(0) || h == GA(1) || h == GA(2) || h == GA(11) || h == GA(12);
    },
    [&](auto h) {
      (void)e;
      if constexpr (h == GA(1)) dma_go_sc1<0>(lv, ctx.rs, so);
      if constexpr (h == GA(4)) dma_go_sc1<1>(lv, ctx.rs, so);
      if constexpr (h == GA(7)) dma_go_sc1<2>(lv, ctx.rs, so);
      if constexpr (h == GC(10)) dma_go_sc1<3>(lv, ctx.rs, so);
      st_gaps<h>(ctx, st);
      if constexpr (h == GA(12)) { A_LD64(e.nst, e.aT, 0); A_LD128(e.nnm4, e.aF, 0); }
    });
  TS(1);
  gemm_tile<P_BQ, P_BK, P_BV, 16384, false>(ab0, yq, yk, yv, xl,
    [&](auto h) { return x_sh(IC<2>{}, h) | w_sh(IC<0>{}, IC<4>{}, h); },
    [&](auto h) {
      (void)e; (void)xk; (void)xv;
      if constexpr (h == GB(1)) { A_FMA(e.k[0], e.nst[0], e.csk[0], xk[0]); A_FMA(e.k[1], e.nst[0], e.csk[1], xk[1]); }
      if constexpr (h == GC(1)) { A_FMA(e.k[2], e.nst[0], e.csk[2], xk[2]); A_FMA(e.k[3], e.nst[0], e.csk[3], xk[3]); }
      if constexpr (h == GB(2)) { A_FMA(e.v[0], e.nnm4[0], e.csv, xv[0]); A_FMA(e.v[1], e.nnm4[1], e.csv, xv[1]); }
      if constexpr (h == GC(2)) { A_FMA(e.v[2], e.nnm4[2], e.csv, xv[2]); A_FMA(e.v[3], e.nnm4[3], e.csv, xv[3]); }
      if constexpr (h == GB(3)) { A_CVT(e.kprev[0], e.k[0], e.k[1]); A_CVT(e.kprev[1], e.k[2], e.k[3]); }
      if constexpr (h == GC(3)) { A_CVT(e.vprev[0], e.v[0], e.v[1]); A_CVT(e.vprev[1], e.v[2], e.v[3]); }
      if constexpr (h == GC(15)) NOP(1);
      x_gp(h); stat_rd(IC<1>{}, h); w_gp(h);
    });
  next(yq, yk, yv);
  TS(2);
  gemm_tile<P_BQ, P_BK, P_BV, 32768, false>(ab0, xq, xk, xv, xl,
    [&](auto h) { return x_sh(IC<3>{}, h) | w_sh(IC<1>{}, IC<8>{}, h); },
    [&](auto h) { x_gp(h); stat_rd(IC<2>{}, h); w_gp(h); epi(h); });
  next(xq, xk, xv);
  TS(3);
  gemm_tile<P_BQ, P_BK, P_BV, 0, false>(ab1, yq, yk, yv, xl,
    [&](auto h) { return w_sh(IC<2>{}, IC<5>{}, h); },
    [&](auto h) { stat_rd(IC<3>{}, h); w_gp(h); epi(h); });
  next(yq, yk, yv);
  TS(4);
  gemm_tile<P_BQ, P_BK, P_BV, 16384, false>(ab1, xq, xk, xv, xl,
    [&](auto h) { return w_sh(IC<3>{}, IC<63>{}, h); },
    [&](auto h) { stat_rd(IC<4>{}, h); w_gp(h); epi(h); });
  next(xq, xk, xv);
  TS(5);
  {
    dma_so<3 * 16384>(oso, osb);
    A_MULS(e.ft, 0.25f * LOG2E, e.st[1]);
    A_FMA(e.q[0], e.st[0], e.csq[0], e.aq[0]); A_FMA(e.q[1], e.st[0], e.csq[1], e.aq[1]);
    A_FMA(e.q[2], e.st[0], e.csq[2], e.aq[2]); A_FMA(e.q[3], e.st[0], e.csq[3], e.aq[3]);
    A_FMA(e.k[0], e.st[0], e.csk[0], e.ak[0]); A_FMA(e.k[1], e.st[0], e.csk[1], e.ak[1]);
    A_CVT(e.qb[0], e.q[0], e.q[1]); A_CVT(e.qb[1], e.q[2], e.q[3]);
    A_FMA(e.k[2], e.st[0], e.csk[2], e.ak[2]); A_FMA(e.k[3], e.st[0], e.csk[3], e.ak[3]);
    mf16z(e.sp, v2u{e.kprev[0], e.kprev[1]}, v2u{e.qb[0], e.qb[1]});
    A_CVT(e.kb[0], e.k[0], e.k[1]); A_CVT(e.kb[1], e.k[2], e.k[3]);
    A_MUL(e.fa, e.ka[0], e.ft); A_MUL(e.fb, e.kb2[0], e.ft);
    mf16z(e.sc, v2u{e.kb[0], e.kb[1]}, v2u{e.qb[0], e.qb[1]});
    A_FMA(e.v[0], e.nm4[0], e.csv, e.av[0]); A_FMA(e.v[1], e.nm4[1], e.csv, e.av[1]);
    A_FMA(e.v[2], e.nm4[2], e.csv, e.av[2]); A_FMA(e.v[3], e.nm4[3], e.csv, e.av[3]);
    A_SEL(e.t0, e.sp[0], e.sp[1], e.modd); A_SEL(e.t1, e.sp[2], e.sp[3], e.modd);
    A_CVT(e.vb[0], e.v[0], e.v[1]); A_CVT(e.vb[1], e.v[2], e.v[3]);
    asm volatile("v_cndmask_b32 %0, %4, %5, %10\n\tv_cndmask_b32 %1, %6, %7, %10\n\t"
                 "v_cndmask_b32 %0, %8, %0, %11\n\tv_cndmask_b32 %1, %9, %1, %12\n\t"
                 "v_fma_f32 %0, %0, %13, %15\n\tv_fma_f32 %1, %1, %14, %16\n\t"
                 "v_max_f32 %2, %0, %1\n\tv_max_f32 %3, %0, %1\n\ts_nop 1\n\t"
                 "v_permlane16_swap_b32 %2, %3\n\tv_max_f32 %2, %2, %3\n\tv_mov_b32 %3, %2\n\ts_nop 1\n\t"
                 "v_permlane32_swap_b32 %2, %3\n\tv_max_f32 %2, %2, %3\n\tv_sub_f32 %0, %0, %2\n\tv_sub_f32 %1, %1, %2\n\t"
                 "v_exp_f32 %0, %0\n\tv_exp_f32 %1, %1"
                 : "=&v"(e.a), "=&v"(e.b), "=&v"(e.m), "=&v"(e.m2)
                 : "v"(e.sc[0]), "v"(e.sc[1]), "v"(e.sc[2]), "v"(e.sc[3]), "v"(e.t0), "v"(e.t1), "s"(e.modd), "s"(e.mA), "s"(e.mB),
                   "v"(e.fa), "v"(e.fb), "v"(e.ka[1]), "v"(e.kb2[1]));
    asm volatile("v_mul_f32 %4, %7, %9\n\tv_mul_f32 %5, %8, %10\n\tv_cvt_pk_bf16_f32 %6, %7, %8\n\tv_cvt_pk_bf16_f32 %4, %4, %5\n\t"
                 "v_perm_b32 %0, %4, %4, %11\n\tv_perm_b32 %1, %4, %4, %12\n\tv_perm_b32 %2, %4, %4, %13\n\tv_perm_b32 %3, %4, %4, %14"
                 : "=&v"(e.P[0]), "=&v"(e.P[1]), "=&v"(e.P[2]), "=&v"(e.P[3]), "=&v"(e.pk), "=&v"(e.pb), "=&v"(e.pk0)
                 : "v"(e.a), "v"(e.b), "v"(e.ka[0]), "v"(e.kb2[0]), "v"(e.sPA), "v"(e.sPB), "v"(e.sCA), "v"(e.sCB));
    mf16z_a(e.den, e.ones, v2u{e.pk0, e.pk});
    NOP(0);
    mf32z(e.num, v4u{e.vprev[0], e.vprev[1], e.vb[0], e.vb[1]}, v4u{e.P[0], e.P[1], e.P[2], e.P[3]});
    NOP(5);
    asm volatile("v_rcp_f32 %2, %7\n\ts_nop 0\n\t"
                 "v_mul_f32 %3, %8, %2\n\tv_mul_f32 %4, %9, %2\n\tv_mul_f32 %5, %10, %2\n\tv_mul_f32 %6, %11, %2\n\t"
                 "v_cvt_pk_bf16_f32 %0, %3, %4\n\tv_cvt_pk_bf16_f32 %1, %5, %6"
                 : "=&v"(e.ob[0]), "=&v"(e.ob[1]), "=&v"(e.m), "=&v"(e.q[0]), "=&v"(e.q[1]), "=&v"(e.q[2]), "=&v"(e.q[3])
                 : "v"(e.den[0]), "v"(e.num[0]), "v"(e.num[1]), "v"(e.num[2]), "v"(e.num[3]), "v"(e.den));
    st64(ctx, v2u{e.ob[0], e.ob[1]}, e.ovo, oso);
  }
  TS(6);
  STAMP(L * 8 + 4);
  TS_FLUSH(L * 8 + 2, 7);
  DRAIN();
  BAR();
  flag_store(ctx, WS_FO + (L * 32 + c) * 128);
}

struct FM {
  f4 p, sp, sn;
  float ep[4], en[4], m, m2, c;
  unsigned eb[2], nb[2];
};
#define FM_MM1(f) asm volatile("v_max3_f32 %0, %2, %3, %4\n\tv_max3_f32 %1, -%2, -%3, -%4" : "=&v"(f.m), "=&v"(f.m2) : "v"(f.p[0]), "v"(f.p[1]), "v"(f.p[2]))
#define FM_MM2(f) asm volatile("v_max_f32 %0, %0, %2\n\tv_max_f32 %1, %1, -%2" : "+v"(f.m), "+v"(f.m2) : "v"(f.p[3]))
#define FM_MX2(f) do { float x_ = f.m, y_ = f.m2; A_MAX(f.m, x_, y_); A_MAX(f.m2, x_, y_); } while (0)
#define FM_MXC(f) asm volatile("v_max_f32 %0, %0, %1\n\tv_mul_f32 %1, %0, -%2\n\tv_mul_f32 %0, %0, -%2" : "+v"(f.m), "+v"(f.m2) : "v"(f.c))

struct AE {
  f4 ak, av;
  f2 st, nst; f4 nm4, rs4, nnm4, nrs4;
  f4 csk; float csv;
  float k[4], v[4];
  unsigned kb[2], vb[2], vbo[2], php[2], phm[2];
  v2u wphi, ident, ones, lo1, hi1;
  f4 tp, tm, kvp, kvm, zz;
  FM f;
  unsigned aT, aF;
};

DEV void layer_A(Ctx& ctx, int lp, v2u wphi, rsrc_t rwo, rsrc_t rsa, rsrc_t rsb) {
  fresh(ctx);
  const int L = 2 * lp;
  const int c = ctx.c, m = ctx.m, w = ctx.w, lane = ctx.lane, g = ctx.g, tok = ctx.tok;
  const int csb = L_CS + (w * 6 + 0) * 64;
  const unsigned lv = lane * 16;
  const unsigned T = ctx.T;
  AE e;
  TS_DECL;
  unsigned ab0, ab1;
  ImgDma wd = img_prep<1024>(ctx), qd = img_prep<3072>(ctx);
  {
    const int d = tok - 4 * g;
    e.ident = v2u{d == 0 ? 0x3f80u : d == 1 ? 0x3f800000u : 0u, d == 2 ? 0x3f80u : d == 3 ? 0x3f800000u : 0u};
    const unsigned lo = tok < 8 ? ONE2 : 0u, hi = tok < 8 ? 0u : ONE2;
    e.lo1 = v2u{lo, lo}; e.hi1 = v2u{hi, hi}; e.ones = v2u{mk1(), mk1()}; e.wphi = wphi;
    e.aT = ctx.Lb + L_REC + tok * 16;
    e.aF = ctx.Lb + L_NM + g * 16;
    e.csk = lds_ldf4(ctx.L, csb + 64 + g * 16); e.csv = lds_ldf(ctx.L, csb + 128 + tok * 4);
    ab0 = ctx.Lb + L_XT + (lp == 0 ? x0_lane(tok, g) : lv); ab1 = ab0 + 49152;
    asm volatile("" : "+a"(e.ident), "+a"(e.lo1), "+a"(e.hi1), "+a"(e.ones), "+a"(e.wphi), "+v"(e.csk), "+v"(e.csv), "+v"(e.aT), "+v"(e.aF),
                      "+v"(ab0), "+v"(ab1), "+v"(qd.v[0]), "+v"(qd.v[1]), "+v"(qd.v[2]), "+v"(qd.v[3]));
  }
  const int xs = WS_X + (L - 1) * ACT_L + c * 65536 + w * 4096 - 16384;
  f4 xk, xv, yk, yv;
  int so;
  auto stat_rd = [&](auto S, auto h) {
    (void)e;
    if constexpr (h == GA(12)) A_LD64(e.nst, e.aT, S * 256);
    if constexpr (h == GC(12)) { A_LD128(e.nnm4, e.aF, S * 64); A_LD128(e.nrs4, e.aF, S * 64 + 320); }
  };
  auto xl = [](auto ks) { return IC<(ks == 12 ? 1 : (ks == 13 || ks == 14) ? 3 : 0)>{}; };
  auto next = [&](f4& k, f4& v) { e.ak = k; e.av = v; e.st = e.nst; e.nm4 = e.nnm4; e.rs4 = e.nrs4; };
  auto epi = [&](auto h) {
    (void)e;
    if constexpr (h == GC(0)) { A_FMA(e.k[0], e.st[0], e.csk[0], e.ak[0]); A_FMA(e.k[1], e.st[0], e.csk[1], e.ak[1]); }
    if constexpr (h == GA(1)) { A_FMA(e.k[2], e.st[0], e.csk[2], e.ak[2]); A_FMA(e.k[3], e.st[0], e.csk[3], e.ak[3]); }
    if constexpr (h == GC(1)) { A_CVT(e.kb[0], e.k[0], e.k[1]); A_CVT(e.kb[1], e.k[2], e.k[3]); A_FMA(e.v[0], e.nm4[0], e.csv, e.av[0]); }
    if constexpr (h == GA(2)) {
      A_FMA(e.v[1], e.nm4[1], e.csv, e.av[1]);
      mf16z_a(e.f.p, e.wphi, v2u{e.kb[0], e.kb[1]});
      A_FMA(e.v[2], e.nm4[2], e.csv, e.av[2]); A_FMA(e.v[3], e.nm4[3], e.csv, e.av[3]);
    }
    if constexpr (h == GC(2)) { A_MUL(e.v[0], e.v[0], e.rs4[0]); A_MUL(e.v[1], e.v[1], e.rs4[1]); A_MUL(e.v[2], e.v[2], e.rs4[2]); }
    if constexpr (h == GA(3)) { A_MUL(e.v[3], e.v[3], e.rs4[3]); A_MULS(e.f.c, LOG2E, e.st[1]); }
    if constexpr (h == GC(3)) { FM_MM1(e.f); A_CVT(e.vb[0], e.v[0], e.v[1]); }
    if constexpr (h == GA(4)) FM_MM2(e.f);
    if constexpr (h == GC(4)) { A_CVT(e.vb[1], e.v[2], e.v[3]); A_SW16(e.f.m, e.f.m2); }
    if constexpr (h == GA(5)) FM_MX2(e.f);
    if constexpr (h == GC(5)) A_SW32(e.f.m, e.f.m2);
    if constexpr (h == GA(6)) FM_MXC(e.f);
    if constexpr (h == GC(6)) A_SW16(e.f.m, e.f.m2);
    if constexpr (h == GA(7)) { A_FMA(e.f.ep[0], e.f.p[0], e.f.c, e.f.m); A_FMA(e.f.ep[1], e.f.p[1], e.f.c, e.f.m); }
    if constexpr (h == GC(7)) { A_FMA(e.f.ep[2], e.f.p[2], e.f.c, e.f.m); A_FMA(e.f.ep[3], e.f.p[3], e.f.c, e.f.m); A_FMAN(e.f.en[0], e.f.p[0], e.f.c, e.f.m2); }
    if constexpr (h == GA(8)) { A_FMAN(e.f.en[1], e.f.p[1], e.f.c, e.f.m2); A_FMAN(e.f.en[2], e.f.p[2], e.f.c, e.f.m2); }
    if constexpr (h == GC(8)) { A_FMAN(e.f.en[3], e.f.p[3], e.f.c, e.f.m2); A_EXP(e.f.ep[0], e.f.ep[0]); A_EXP(e.f.ep[1], e.f.ep[1]); }
    if constexpr (h == GA(9)) A_EXP(e.f.ep[2], e.f.ep[2]);
    if constexpr (h == GC(9)) { A_EXP(e.f.ep[3], e.f.ep[3]); A_EXP(e.f.en[0], e.f.en[0]); }
    if constexpr (h == GA(10)) A_EXP(e.f.en[1], e.f.en[1]);
    if constexpr (h == GC(10)) { A_EXP(e.f.en[2], e.f.en[2]); A_EXP(e.f.en[3], e.f.en[3]); }
    if constexpr (h == GA(11)) { A_CVT(e.f.eb[0], e.f.ep[0], e.f.ep[1]); A_CVT(e.f.eb[1], e.f.ep[2], e.f.ep[3]); }
    if constexpr (h == GC(11)) {
      A_CVT(e.f.nb[0], e.f.en[0], e.f.en[1]); A_CVT(e.f.nb[1], e.f.en[2], e.f.en[3]);
      mf16z_a(e.f.sp, e.ones, v2u{e.f.eb[0], e.f.eb[1]});
    }
    if constexpr (h == GA(12)) mf16z_a(e.f.sn, e.ones, v2u{e.f.nb[0], e.f.nb[1]});
    if constexpr (h == GA(13)) A_RCP(e.f.m, e.f.sp);
    if constexpr (h == GC(13)) { A_MUL(e.f.ep[0], e.f.ep[0], e.f.m); A_RCP(e.f.m2, e.f.sn); A_MUL(e.f.ep[1], e.f.ep[1], e.f.m); }
    if constexpr (h == GA(14)) { A_MUL(e.f.ep[2], e.f.ep[2], e.f.m); A_MUL(e.f.ep[3], e.f.ep[3], e.f.m); }
    if constexpr (h == GC(14)) { A_MUL(e.f.en[0], e.f.en[0], e.f.m2); A_MUL(e.f.en[1], e.f.en[1], e.f.m2); A_CVT(e.f.eb[0], e.f.ep[0], e.f.ep[1]); }
    if constexpr (h == GA(15)) { A_CVT(e.f.eb[1], e.f.ep[2], e.f.ep[3]); A_MUL(e.f.en[2], e.f.en[2], e.f.m2); }
    if constexpr (h == GC(15)) {
      A_MUL(e.f.en[3], e.f.en[3], e.f.m2); A_CVT(e.f.nb[0], e.f.en[0], e.f.en[1]);
      mf16z_b(e.tp, v2u{e.f.eb[0], e.f.eb[1]}, e.ident);
      A_CVT(e.f.nb[1], e.f.en[2], e.f.en[3]);
      e.vbo[0] = e.vb[0]; e.vbo[1] = e.vb[1];
    }
  };
  auto epi2 = [&](auto FT, auto h) {
    (void)e;
    if constexpr (h == GA(0)) mf16z_b(e.tm, v2u{e.f.nb[0], e.f.nb[1]}, e.ident);
    if constexpr (h == GC(5)) { A_CVT(e.php[0], e.tp[0], e.tp[1]); }
    if constexpr (h == GC(6)) { A_CVT(e.php[1], e.tp[2], e.tp[3]); }
    if constexpr (h == GA(9)) {
      if constexpr (FT) mf16z(e.kvp, v2u{e.php[0], e.php[1]}, v2u{e.vbo[0], e.vbo[1]}); else mf16c(e.kvp, v2u{e.php[0], e.php[1]}, v2u{e.vbo[0], e.vbo[1]});
      A_CVT(e.phm[0], e.tm[0], e.tm[1]); A_CVT(e.phm[1], e.tm[2], e.tm[3]);
      if constexpr (FT) mf16z_a(e.zz, e.lo1, v2u{e.php[0], e.php[1]}); else mf16c_a(e.zz, e.lo1, v2u{e.php[0], e.php[1]});
    }
    if constexpr (h == GA(10)) {
      if constexpr (FT) mf16z(e.kvm, v2u{e.phm[0], e.phm[1]}, v2u{e.vbo[0], e.vbo[1]}); else mf16c(e.kvm, v2u{e.phm[0], e.phm[1]}, v2u{e.vbo[0], e.vbo[1]});
    }
    if constexpr (h == GA(13)) mf16c_a(e.zz, e.hi1, v2u{e.phm[0], e.phm[1]});
  };
  auto c_sh = [&](auto K0, auto SOFF, auto LOFF, int sb, auto h) {
    if constexpr (h == GA(K0 - 1)) dma_so<SOFF>(so, sb);
    if constexpr (h == GA(K0)) dma_m0<LOFF>(ctx.Lw);
    return h == GA(K0 - 1) || h == GA(K0);
  };
  auto c_gp = [&](auto K0, const ImgDma& d, rsrc_t rs, auto h) {
    if constexpr (h == GC(K0)) dma_go<0>(d.v[0], rs, so);
    if constexpr (h == GC(K0 + 2)) dma_go<1>(d.v[1], rs, so);
    if constexpr (h == GC(K0 + 4)) dma_go<2>(d.v[2], rs, so);
    if constexpr (h == GC(K0 + 6)) dma_go<3>(d.v[3], rs, so);
  };
  constexpr int QS = 128 * 3072;
  constexpr int SL = 32 * 256;
  const int kblk = WS_KV1 + lp * KV1_L + ((m * 64 + w * 16) * 32 + c) * 256;
  const int zblk = WS_KVZ + lp * KVZ_L + ((m * 4 + w) * 32 + c) * 256;
  int kvo1 = g * 2 * SL + tok * 16, zvo1 = ((g >> 1) * 16 + tok) * 8;
  asm volatile("" : "+v"(kvo1), "+v"(zvo1));
  auto kv_pass = [&](auto FIRST) {
    ST st;
    unsigned rvo = 0; int rsb0 = 0, rso = 0;
    if constexpr (FIRST) {
      rvo = (unsigned)((lane >> 3) * 3072 + (lane & 7) * 16);
      rsb0 = m * 128 + 32 * w * 3072;
      asm volatile("" : "+v"(rvo));
      RAWBAR();
    } else {
      gather_start<false>(ctx, st, L, lv, xs);
    }
    auto r_sh = [&](auto I) { dma_so<1024 + (128 * (I >> 2) + 8 * (I & 3)) * 3072>(rso, rsb0); };
    auto r_gp = [&](auto I) { raw_ld1<P_BK + I>(rvo, rsb, rso); };
    const int pfr = ((blockIdx.x >> 3) * 16 + 4 * w) * 3072;
    ring_prime<16384>(ab0);
    TS(0);
    gemm_tile<-1, P_AK, P_AV, 16384, false>(ab0, xk, xk, xv,
      [&](auto ks) { return IC<(FIRST ? 0 : ks == 10 ? 2 : ks == 11 ? -1 : 0) + decltype(xl(ks))::value>{}; },
      [&](auto h) {
        if constexpr (FIRST) {
          if constexpr (h == GA(0)) dma_so<2 * 3072>(rso, pfr);
          if constexpr (h == GA(6)) dma_so<3 * 3072>(rso, pfr);
          return (h == GA(0) || h == GA(6)) | c_sh(IC<9>{}, IC<0>{}, IC<L_RQ>{}, qd.sb, h);
        } else {
          if constexpr (h == GA(0)) dma_so<3 * 16384>(so, xs);
          if constexpr (h == GA(1)) dma_m0<L_XT + 3 * 16384>(ctx.Lw);
          if constexpr (h == GA(2)) WAIT_VMV(5);
          if constexpr (h == GA(11)) WAIT_VMV(4);
          if constexpr (h == GA(12)) RAWBAR();
          if constexpr (h == GA(13)) dma_so<4 * 16384>(so, xs);
          if constexpr (h == GA(14)) dma_m0<L_XT + 4 * 16384>(ctx.Lw);
          return h == GA(0) || h == GA(1) || h == GA(2) || (h >= GA(11) && h <= GA(14));
        }
      },
      [&](auto h) {
        if constexpr (FIRST) {
          if constexpr ((h & 7) == 2 && h / 4 < 12) pf1<((h / 8) % 3) * 1024>(lv, rsb, rso);
          c_gp(IC<9>{}, qd, rsa, h);
        } else {
          if constexpr (h == GA(1)) dma_go_sc1<0>(lv, ctx.rs, so);
          if constexpr (h == GA(4)) dma_go_sc1<1>(lv, ctx.rs, so);
          if constexpr (h == GA(7)) dma_go_sc1<2>(lv, ctx.rs, so);
          if constexpr (h == GC(10)) dma_go_sc1<3>(lv, ctx.rs, so);
          if constexpr (h == GC(14)) dma_go_sc1<0>(lv, ctx.rs, so);
          st_gaps<h>(ctx, st);
        }
        if constexpr (h == GC(15)) NOP(1);
        stat_rd(IC<1>{}, h);
      });
    next(xk, xv);
    TS(1);
    gemm_tile<-1, P_AK, P_AV, 32768, false>(ab0, yk, yk, yv, xl,
      [&](auto h) {
        if constexpr (FIRST) {
          if constexpr (h == GA(2)) { dma_so<0>(rso, pfr / 3); return true; }
          else return c_sh(IC<1>{}, IC<QS>{}, IC<L_RQ + 16384>{}, qd.sb, h) | c_sh(IC<9>{}, IC<2 * QS>{}, IC<L_RQ + 32768>{}, qd.sb, h);
        } else {
          if constexpr (h == GA(11)) WAIT_VMV(6);
          if constexpr (h == GA(12)) RAWBAR();
          return c_sh(IC<7>{}, IC<0>{}, IC<L_RQ>{}, qd.sb, h) | (h == GA(11) || h == GA(12));
        }
      },
      [&](auto h) {
        if constexpr (FIRST) {
          c_gp(IC<1>{}, qd, rsa, h); c_gp(IC<9>{}, qd, rsa, h);
          if constexpr (h == GC(2) || h == GC(6) || h == GC(10) || h == GC(14)) pf1<((h / 4 - 2) / 4) * 1024>(lv, rwo, rso);
        } else {
          if constexpr (h == GA(0)) dma_go_sc1<1>(lv, ctx.rs, so);
          if constexpr (h == GC(3)) dma_go_sc1<2>(lv, ctx.rs, so);
          if constexpr (h == GC(5)) dma_go_sc1<3>(lv, ctx.rs, so);
          c_gp(IC<7>{}, qd, rsa, h);
        }
        stat_rd(IC<2>{}, h); epi(h);
      });
    next(yk, yv);
    TS(2);
    gemm_tile<-1, P_AK, P_AV, 0, false>(ab1, xk, xk, xv, xl,
      [&](auto h) {
        if constexpr (FIRST) {
          if constexpr (h / 4 >= 8) { r_sh(IC<h / 4 - 8>{}); return true; }
          else return c_sh(IC<1>{}, IC<3 * QS>{}, IC<L_RQ + 49152>{}, qd.sb, h);
        } else {
          if constexpr (h == GA(11)) WAIT_VMV(8);
          if constexpr (h == GA(12)) RAWBAR();
          return c_sh(IC<1>{}, IC<QS>{}, IC<L_RQ + 16384>{}, qd.sb, h) | c_sh(IC<9>{}, IC<2 * QS>{}, IC<L_RQ + 32768>{}, qd.sb, h) | (h == GA(11) || h == GA(12));
        }
      },
      [&](auto h) {
        if constexpr (FIRST) { c_gp(IC<1>{}, qd, rsa, h); if constexpr ((h & 3) == 2 && h / 4 >= 8) r_gp(IC<h / 4 - 8>{}); }
        else { c_gp(IC<1>{}, qd, rsa, h); c_gp(IC<9>{}, qd, rsa, h); }
        stat_rd(IC<3>{}, h); epi2(IC<1>{}, h); epi(h);
      });
    next(xk, xv);
    TS(3);
    gemm_tile<-1, P_AK, P_AV, 16384, false>(ab1, yk, yk, yv, xl,
      [&](auto h) {
        if constexpr (FIRST) {
          if constexpr (h / 4 < 8) { r_sh(IC<8 + h / 4>{}); return true; }
          else return c_sh(IC<9>{}, IC<0>{}, IC<L_XT>{}, wd.sb, h);
        } else {
          return c_sh(IC<1>{}, IC<3 * QS>{}, IC<L_RQ + 49152>{}, qd.sb, h) | c_sh(IC<9>{}, IC<0>{}, IC<L_XT>{}, wd.sb, h);
        }
      },
      [&](auto h) {
        if constexpr (FIRST) { if constexpr ((h & 3) == 2 && h / 4 < 8) r_gp(IC<8 + h / 4>{}); }
        else c_gp(IC<1>{}, qd, rsa, h);
        c_gp(IC<9>{}, wd, rwo, h); stat_rd(IC<4>{}, h); epi2(IC<0>{}, h); epi(h);
      });
    next(yk, yv);
  TS(4);
  {
    FM& f = e.f;
    A_MULS(f.c, LOG2E, e.st[1]);
    NOP(0);
    mf16z_b(e.tm, v2u{f.nb[0], f.nb[1]}, e.ident);
    A_FMA(e.k[0], e.st[0], e.csk[0], e.ak[0]); A_FMA(e.k[1], e.st[0], e.csk[1], e.ak[1]);
    A_FMA(e.k[2], e.st[0], e.csk[2], e.ak[2]); A_FMA(e.k[3], e.st[0], e.csk[3], e.ak[3]);
    A_CVT(e.kb[0], e.k[0], e.k[1]); A_CVT(e.php[0], e.tp[0], e.tp[1]); A_CVT(e.kb[1], e.k[2], e.k[3]); A_CVT(e.php[1], e.tp[2], e.tp[3]);
    A_CVT(e.phm[0], e.tm[0], e.tm[1]);
    mf16z_a(f.p, e.wphi, v2u{e.kb[0], e.kb[1]});
    A_CVT(e.phm[1], e.tm[2], e.tm[3]); A_FMA(e.v[0], e.nm4[0], e.csv, e.av[0]);
    mf16c(e.kvp, v2u{e.php[0], e.php[1]}, v2u{e.vbo[0], e.vbo[1]});
    A_FMA(e.v[1], e.nm4[1], e.csv, e.av[1]); A_FMA(e.v[2], e.nm4[2], e.csv, e.av[2]);
    mf16c(e.kvm, v2u{e.phm[0], e.phm[1]}, v2u{e.vbo[0], e.vbo[1]});
    A_FMA(e.v[3], e.nm4[3], e.csv, e.av[3]); A_MUL(e.v[0], e.v[0], e.rs4[0]);
    FM_MM1(f);
    mf16c_a(e.zz, e.lo1, v2u{e.php[0], e.php[1]});
    FM_MM2(f);
    A_MUL(e.v[1], e.v[1], e.rs4[1]); A_MUL(e.v[2], e.v[2], e.rs4[2]);
    A_SW16(f.m, f.m2);
    mf16c_a(e.zz, e.hi1, v2u{e.phm[0], e.phm[1]});
    FM_MX2(f);
    A_MUL(e.v[3], e.v[3], e.rs4[3]); A_CVT(e.vb[0], e.v[0], e.v[1]);
    A_SW32(f.m, f.m2);
    FM_MXC(f);
    A_CVT(e.vb[1], e.v[2], e.v[3]);
    NOP(0);
    A_SW16(f.m, f.m2);
#pragma unroll
    for (int r = 0; r < 4; ++r) A_FMA(f.ep[r], f.p[r], f.c, f.m);
#pragma unroll
    for (int r = 0; r < 4; ++r) A_FMAN(f.en[r], f.p[r], f.c, f.m2);
#pragma unroll
    for (int r = 0; r < 4; ++r) A_EXP(f.ep[r], f.ep[r]);
    A_EXP(f.en[0], f.en[0]);
    A_CVT(f.eb[0], f.ep[0], f.ep[1]); A_CVT(f.eb[1], f.ep[2], f.ep[3]);
    A_EXP(f.en[1], f.en[1]); A_EXP(f.en[2], f.en[2]);
    mf16z_a(f.sp, e.ones, v2u{f.eb[0], f.eb[1]});
    A_EXP(f.en[3], f.en[3]);
    A_CVT(f.nb[0], f.en[0], f.en[1]); A_CVT(f.nb[1], f.en[2], f.en[3]);
    NOP(1);
    mf16z_a(f.sn, e.ones, v2u{f.nb[0], f.nb[1]});
    NOP(1);
    A_RCP(f.m, f.sp);
    NOP(0);
#pragma unroll
    for (int r = 0; r < 4; ++r) A_MUL(f.ep[r], f.ep[r], f.m);
    A_RCP(f.m2, f.sn);
    A_CVT(f.eb[0], f.ep[0], f.ep[1]); A_CVT(f.eb[1], f.ep[2], f.ep[3]);
    A_MUL(f.en[0], f.en[0], f.m2); A_MUL(f.en[1], f.en[1], f.m2);
    mf16z_b(e.tp, v2u{f.eb[0], f.eb[1]}, e.ident);
    A_MUL(f.en[2], f.en[2], f.m2); A_MUL(f.en[3], f.en[3], f.m2);
    A_CVT(f.nb[0], f.en[0], f.en[1]); A_CVT(f.nb[1], f.en[2], f.en[3]);
    NOP(1);
    mf16z_b(e.tm, v2u{f.nb[0], f.nb[1]}, e.ident);
    NOP(0);
    A_CVT(e.php[0], e.tp[0], e.tp[1]); A_CVT(e.php[1], e.tp[2], e.tp[3]);
    NOP(1);
    mf16c(e.kvp, v2u{e.php[0], e.php[1]}, v2u{e.vb[0], e.vb[1]});
    mf16c_a(e.zz, e.lo1, v2u{e.php[0], e.php[1]});
    NOP(0);
    A_CVT(e.phm[0], e.tm[0], e.tm[1]); A_CVT(e.phm[1], e.tm[2], e.tm[3]);
    NOP(1);
    mf16c(e.kvm, v2u{e.phm[0], e.phm[1]}, v2u{e.vb[0], e.vb[1]});
    mf16c_a(e.zz, e.hi1, v2u{e.phm[0], e.phm[1]});
    asm volatile("s_nop 0" : "+v"(e.kvp));
  }
  TS(5);
  {
    const int blk = kblk, vo = kvo1;
    st128(ctx, v4u{asu(e.kvp[0]), T, asu(e.kvp[1]), T}, vo, blk);
    st128(ctx, v4u{asu(e.kvp[2]), T, asu(e.kvp[3]), T}, vo, blk + SL);
    asm volatile("s_nop 3" : "+v"(e.kvm), "+v"(e.zz));
    st128(ctx, v4u{asu(e.kvm[0]), T, asu(e.kvm[1]), T}, vo, blk + 8 * SL);
    st128(ctx, v4u{asu(e.kvm[2]), T, asu(e.kvm[3]), T}, vo, blk + 9 * SL);
    st64(ctx, v2u{asu(e.zz[0]), T}, zvo1, zblk);
  }
  };
  if (lp == 0) kv_pass(std::true_type{}); else kv_pass(std::false_type{});
  STAMP(L * 8 + 2);
  TS_FLUSH(L * 8 + 2, 6);
  f4 aq[4];
  v4u dk[4], dz;
  const int S64 = 2 * c + (w >> 1);
  const int rblk = WS_KV1 + lp * KV1_L + ((m * 64 + S64) * 32) * 256 + (w & 1) * 128;
  const int rzb = WS_KVZ + lp * KVZ_L + ((m * 4 + (S64 >> 4)) * 32) * 256 + (S64 & 15) * 16;
  int kvo = (lane >> 3) * 256 + (lane & 7) * 16, zvo = (lane & 31) * 256;
  {
    lds_t* qt = ctx.L + opq(L_RQ + tr_base(lane, w));
    asm volatile("" : "+v"(kvo), "+v"(zvo));
    TS(0);
    WAIT_VM(8);
    RAWBAR();
    TS(1);
    v4u wq[16];
#pragma unroll
    for (int ks = 0; ks < 16; ++ks) wq[ks] = tr_frag(qt, ks * 4096);
    if (lp == 0) {
      f4 c0, c1;
      v4u ones = {mk1(), mk1(), mk1(), mk1()};
      asm volatile("s_nop 1" : "+v"(ones));
      sfor<0, 8>([&](auto k) { mf_av<k == 0>(c0, wq[2 * k], ones); mf_av<k == 0>(c1, wq[2 * k + 1], ones); });
      asm volatile("s_nop 7" : "+v"(c0), "+v"(c1));
      if (tok == 0) *(LAS f4*)(ctx.L + csb + g * 16) = c0 + c1;
    }
    v4u a1[4];
    auto rd = [&](auto k) {
      (void)a1;
      if constexpr (k < 16) { ring_rd<k & 3, 16384 + k * 1024>(ab0); } else if constexpr (k < 32) { ring_rd<k & 3, (k - 16) * 1024>(ab1); }
    };
    auto rd1 = [&](auto k) {
      (void)a1; (void)ab0; (void)ab1;
      if constexpr (k < 16) { A_LD128(a1[k & 3], ab0, 32768 + k * 1024); } else if constexpr (k < 32) { A_LD128(a1[k & 3], ab1, 16384 + (k - 16) * 1024); }
    };
    sfor<0, 3>([&](auto k) { rd(k); rd1(k); });
#pragma unroll
    for (int ks = 0; ks < 16; ++ks) asm volatile("" : "+a"(wq[ks]));
    WAIT_LG(4);
    TS(2);
    sfor<0, 32>([&](auto k) {
      constexpr int ks = k & 15, u = k >> 4;
      (void)dk; (void)dz; (void)kvo; (void)zvo; (void)rblk; (void)rzb;
      mmq<k & 3, ks == 0>(aq[2 * u], wq[ks]);
      VNOP();
      rd(IC<k + 3>{});
      if constexpr (k >= QPOLL && k < QPOLL + 4) {
        constexpr int i = k - QPOLL;
        asm volatile("buffer_load_dwordx4 %0, %1, %2, %3 offen offset:%4 sc1" : "=v"(dk[i]) : "v"(kvo), "s"(ctx.rs), "s"(i < 2 ? rblk : rblk + 4096), "n"((i & 1) * 2048) : "memory");
      }
      if constexpr (k == QPOLL + 4) asm volatile("buffer_load_dwordx4 %0, %1, %2, %3 offen sc1" : "=v"(dz) : "v"(zvo), "s"(ctx.rs), "s"(rzb) : "memory");
      mf_av<ks == 0>(aq[2 * u + 1], wq[ks], a1[k & 3]);
      WAIT_LG(k + 3 < 32 ? 3 : k == 29 ? 2 : 0);
      rd1(IC<k + 3>{});
      SB();
    });
  }
  TS(6);
  TS_FLUSH(34 + lp * 8, 7);
  STAMP(48 + lp * 4);
  v4u phq[4], d2[4];
  const int b2 = WS_KV2 + lp * KV2_L + (m * 64 + w * 16) * 256;
  int vo2 = g * 512 + tok * 16;
  {
    const f4 csq = lds_ldf4(ctx.L, csb + g * 16);
    f2 st[4];
#pragma unroll
    for (int t = 0; t < 4; ++t) st[t] = *(LAS f2*)(ctx.L + L_REC + ((1 + t) * 16 + tok) * 16);
    int svo = (lane & 7) * 16 + ((lane >> 4) & 1) * 8;
    const u64 mhi = __ballot(lane >= 32);
    const int sblk = WS_KV2 + lp * KV2_L + (m * 64 + S64) * 256 + (w & 1) * 128;
    asm volatile("s_nop 3" : "+v"(aq[0]), "+v"(aq[1]), "+v"(aq[2]), "+v"(aq[3]), "+v"(svo), "+v"(vo2));
    FM f[4];
    unsigned qb[4][2];
#pragma unroll
    for (int t = 0; t < 4; ++t) {
      float q[4];
#pragma unroll
      for (int r = 0; r < 4; ++r) A_FMA(q[r], st[t][0], csq[r], aq[t][r]);
      A_MULS(f[t].c, LOG2E, st[t][1]);
      VNOP();
      A_CVT(qb[t][0], q[0], q[1]); A_CVT(qb[t][1], q[2], q[3]);
      if (t > 0) mf16z_a(f[t - 1].p, e.wphi, v2u{qb[t - 1][0], qb[t - 1][1]});
    }
    FM_MM1(f[0]);
    mf16z_a(f[3].p, e.wphi, v2u{qb[3][0], qb[3][1]});
    FM_MM1(f[1]); FM_MM2(f[0]); FM_MM2(f[1]); FM_MM1(f[2]);
    A_SW16(f[0].m, f[0].m2); FM_MM2(f[2]); FM_MM1(f[3]); A_SW16(f[1].m, f[1].m2); FM_MM2(f[3]);
    FM_MX2(f[0]); A_SW16(f[2].m, f[2].m2); FM_MX2(f[1]); A_SW16(f[3].m, f[3].m2);
    A_SW32(f[0].m, f[0].m2); FM_MX2(f[2]); A_SW32(f[1].m, f[1].m2); FM_MX2(f[3]);
    FM_MXC(f[0]); A_SW32(f[2].m, f[2].m2); FM_MXC(f[1]); A_SW32(f[3].m, f[3].m2);
    A_SW16(f[0].m, f[0].m2); FM_MXC(f[2]); A_SW16(f[1].m, f[1].m2); FM_MXC(f[3]);
    {
      asm volatile("s_waitcnt vmcnt(0)" : "+v"(dk[0]), "+v"(dk[1]), "+v"(dk[2]), "+v"(dk[3]), "+v"(dz) :: "memory");
      STAMP(49 + lp * 4);
      for (;;) {
        unsigned bad;
        asm volatile("v_sad_u32 %0, %1, %11, 0 clamp\n\tv_sad_u32 %0, %2, %11, %0 clamp\n\tv_sad_u32 %0, %3, %11, %0 clamp\n\tv_sad_u32 %0, %4, %11, %0 clamp\n\t"
                     "v_sad_u32 %0, %5, %11, %0 clamp\n\tv_sad_u32 %0, %6, %11, %0 clamp\n\tv_sad_u32 %0, %7, %11, %0 clamp\n\tv_sad_u32 %0, %8, %11, %0 clamp\n\t"
                     "v_sad_u32 %0, %9, %11, %0 clamp\n\tv_sad_u32 %0, %10, %11, %0 clamp"
                     : "=&v"(bad) : "v"(dk[0].y), "v"(dk[0].w), "v"(dk[1].y), "v"(dk[1].w), "v"(dk[2].y), "v"(dk[2].w), "v"(dk[3].y), "v"(dk[3].w),
                       "v"(dz.y), "v"(dz.w), "s"(T));
        if (__builtin_expect(__all(bad == 0), 1) || --ctx.budget < 0) break;
        asm volatile("" ::: "memory");
#pragma unroll
        for (int i = 0; i < 4; ++i) dk[i] = ld128(ctx, kvo, rblk + i * 2048);
        dz = ld128(ctx, zvo, rzb);
      }
      float K, K1, Z; unsigned pk, tg;
      asm volatile("v_add_f32 %0, %5, %6\n\tv_add_f32 %1, %9, %10\n\tv_add_f32 %2, %7, %8\n\tv_add_f32 %3, %11, %12\n\t"
                   "v_add_f32 %0, %0, %2\n\tv_add_f32 %1, %1, %3\n\tv_cndmask_b32 %2, %13, %14, %15\n\tv_mov_b32 %4, %16\n\t"
                   "v_permlane32_swap_b32 %0, %1\n\tv_add_f32 %0, %0, %1\n\ts_nop 1\n\t"
                   "v_permlane16_swap_b32 %0, %2\n\tv_add_f32 %0, %0, %2\n\ts_nop 1\n\t"
                   "v_add_f32_dpp %0, %0, %0 row_ror:8 row_mask:0xf bank_mask:0xf\n\ts_nop 1\n\t"
                   "v_add_f32_dpp %0, %0, %0 row_ror:4 row_mask:0xa bank_mask:0xf\n\ts_nop 1\n\t"
                   "v_add_f32_dpp %0, %0, %0 row_ror:2 row_mask:0xa bank_mask:0xf\n\ts_nop 1\n\t"
                   "v_add_f32_dpp %0, %0, %0 row_ror:1 row_mask:0xa bank_mask:0xf\n\t"
                   "v_mov_b32 %1, %0\n\ts_nop 1\n\tv_permlane32_swap_b32 %0, %1\n\tv_cvt_pk_bf16_f32 %3, %0, %1"
                   : "=&v"(K), "=&v"(K1), "=&v"(Z), "=&v"(pk), "=&v"(tg)
                   : "v"(dk[0].x), "v"(dk[1].x), "v"(dk[2].x), "v"(dk[3].x), "v"(dk[0].z), "v"(dk[1].z), "v"(dk[2].z), "v"(dk[3].z),
                     "v"(dz.x), "v"(dz.z), "s"(mhi), "s"(T));
      st64(ctx, v2u{pk, tg}, svo, sblk);
      STAMP(L * 8 + 3);
    }
    RAWBAR();
    sfor<0, 4>([&](auto t) {
      (void)f; (void)e;
      auto piece = [&](auto p) {
        if constexpr ((p & 3) == 0) { dma_so<128 * 1024 * (1 + (p >> 2))>(so, wd.sb); dma_m0<L_XT + 16384 * (1 + (p >> 2))>(ctx.Lw); VNOP(); }
        dma_go<p & 3>(wd.v[p & 3], rwo, so);
      };
      piece(IC<3 * t>{});
#pragma unroll
      for (int r = 0; r < 4; ++r) A_FMA(f[t].ep[r], f[t].p[r], f[t].c, f[t].m);
      if constexpr (t < 2) A_SW16(f[t + 2].m, f[t + 2].m2);
#pragma unroll
      for (int r = 0; r < 4; ++r) A_FMAN(f[t].en[r], f[t].p[r], f[t].c, f[t].m2);
      if constexpr (t > 0) mf16z_a(f[t - 1].sn, e.ones, v2u{f[t - 1].nb[0], f[t - 1].nb[1]}); else VNOP();
      piece(IC<3 * t + 1>{});
#pragma unroll
      for (int r = 0; r < 4; ++r) A_EXP(f[t].ep[r], f[t].ep[r]);
      A_EXP(f[t].en[0], f[t].en[0]);
      piece(IC<3 * t + 2>{});
      A_CVT(f[t].eb[0], f[t].ep[0], f[t].ep[1]); A_CVT(f[t].eb[1], f[t].ep[2], f[t].ep[3]);
      A_EXP(f[t].en[1], f[t].en[1]); A_EXP(f[t].en[2], f[t].en[2]);
      mf16z_a(f[t].sp, e.ones, v2u{f[t].eb[0], f[t].eb[1]});
      A_EXP(f[t].en[3], f[t].en[3]);
      A_CVT(f[t].nb[0], f[t].en[0], f[t].en[1]); A_CVT(f[t].nb[1], f[t].en[2], f[t].en[3]);
    });
    if (lp == 0) { Ctx c2 = ctx; fresh(c2); part_step_ld<P_BK, P_BV>(c2, L_CS + (w * 6 + 4) * 64, rsb, 2048); }
#pragma unroll
    for (int t = 0; t < 4; ++t) {
      A_RCP(f[t].m, f[t].sp); A_RCP(f[t].m2, f[t].sn);
      if (t == 0) mf16z_a(f[3].sn, e.ones, v2u{f[3].nb[0], f[3].nb[1]});
      if (t == S2POLL) {
        asm volatile("buffer_load_dwordx4 %0, %4, %5, %6 offen sc1\n\tbuffer_load_dwordx4 %1, %4, %5, %6 offen offset:256 sc1\n\t"
                     "buffer_load_dwordx4 %2, %4, %5, %6 offen offset:2048 sc1\n\tbuffer_load_dwordx4 %3, %4, %5, %6 offen offset:2304 sc1"
                     : "=&v"(d2[0]), "=&v"(d2[1]), "=&v"(d2[2]), "=&v"(d2[3]) : "v"(vo2), "s"(ctx.rs), "s"(b2) : "memory");
      }
#pragma unroll
      for (int r = 0; r < 4; ++r) A_MUL(f[t].ep[r], f[t].ep[r], f[t].m);
#pragma unroll
      for (int r = 0; r < 4; ++r) A_MUL(f[t].en[r], f[t].en[r], f[t].m2);
      unsigned x0, x1, x2, x3;
      A_CVT(x0, f[t].ep[0], f[t].ep[1]); A_CVT(x1, f[t].ep[2], f[t].ep[3]); A_CVT(x2, f[t].en[0], f[t].en[1]); A_CVT(x3, f[t].en[2], f[t].en[3]);
      phq[t] = v4u{x0, x1, x2, x3};
    }
  }
  STAMP(50 + lp * 4);
  {
    int ovo = ((g >> 1) * 16 + tok) * 16 + (g & 1) * 8;
    const int osb = WS_O + L * ACT_L + (4 * c * 16 + 2 * m + (w >> 1)) * 1024 + (w & 1) * 512;
    asm volatile("s_waitcnt vmcnt(0)" : "+v"(d2[0]), "+v"(d2[1]), "+v"(d2[2]), "+v"(d2[3]), "+v"(ovo) :: "memory");
    for (;;) {
      unsigned bad;
      asm volatile("v_sad_u32 %0, %1, %9, 0 clamp\n\tv_sad_u32 %0, %2, %9, %0 clamp\n\tv_sad_u32 %0, %3, %9, %0 clamp\n\tv_sad_u32 %0, %4, %9, %0 clamp\n\t"
                   "v_sad_u32 %0, %5, %9, %0 clamp\n\tv_sad_u32 %0, %6, %9, %0 clamp\n\tv_sad_u32 %0, %7, %9, %0 clamp\n\tv_sad_u32 %0, %8, %9, %0 clamp"
                   : "=&v"(bad) : "v"(d2[0].y), "v"(d2[0].w), "v"(d2[1].y), "v"(d2[1].w), "v"(d2[2].y), "v"(d2[2].w), "v"(d2[3].y), "v"(d2[3].w), "s"(T));
      if (__builtin_expect(__all(bad == 0), 1) || --ctx.budget < 0) break;
      asm volatile("" ::: "memory");
      d2[0] = ld128(ctx, vo2, b2); d2[1] = ld128(ctx, vo2 + 256, b2); d2[2] = ld128(ctx, vo2 + 2048, b2); d2[3] = ld128(ctx, vo2 + 2304, b2);
    }
    STAMP(L * 8 + 4);
    v4u akv = {d2[0].x, d2[1].x, d2[2].x, d2[3].x}, az = {d2[0].z, d2[1].z, d2[2].z, d2[3].z};
    f4 num[4], den[4];
    asm volatile("s_nop 1" : "+v"(akv), "+v"(az));
#pragma unroll
    for (int t = 0; t < 4; ++t) { mf32z(den[t], az, phq[t]); mf32z(num[t], akv, phq[t]); }
#pragma unroll
    for (int t = 0; t < 4; ++t) {
      float r, q0, q1, q2, q3; unsigned o0, o1;
      asm volatile("v_add_f32 %2, %12, %7\n\tv_rcp_f32 %2, %2\n\ts_nop 0\n\t"
                   "v_mul_f32 %3, %8, %2\n\tv_mul_f32 %4, %9, %2\n\tv_mul_f32 %5, %10, %2\n\tv_mul_f32 %6, %11, %2\n\t"
                   "v_cvt_pk_bf16_f32 %0, %3, %4\n\tv_cvt_pk_bf16_f32 %1, %5, %6"
                   : "=&v"(o0), "=&v"(o1), "=&v"(r), "=&v"(q0), "=&v"(q1), "=&v"(q2), "=&v"(q3)
                   : "v"(den[t][0]), "v"(num[t][0]), "v"(num[t][1]), "v"(num[t][2]), "v"(num[t][3]), "s"(1e-6f), "v"(den[t]));
      st64(ctx, v2u{o0, o1}, ovo, osb + t * 16384);
    }
  }
  DRAIN();
  BAR();
  flag_store(ctx, WS_FO + (L * 32 + c) * 128);
}

template <bool ODD>
DEV void out_proj(Ctx& ctx, int lp, f4 (&xacc)[4], rsrc_t out, rsrc_t rsb) {
  fresh(ctx);
  const int L = 2 * lp + (ODD ? 1 : 0);
  const bool last = ODD && lp == 1, first = !ODD && lp == 0;
  const int c = ctx.c, m = ctx.m, w = ctx.w, lane = ctx.lane, g = ctx.g, tok = ctx.tok;
  if (first) part_step_ld<P_BV, P_BQ>(ctx, L_CS + (w * 6 + 5) * 64, rsb, 0);
  lds_t* tb[4];
#pragma unroll
  for (int nt = 0; nt < 4; ++nt) tb[nt] = ctx.L + opq(L_XT + tr_base(lane, nt));
  v4u wf[4][4];
#pragma unroll
  for (int i = 0; i < 4; ++i)
#pragma unroll
    for (int nt = 0; nt < 4; ++nt) wf[i][nt] = tr_frag(tb[nt], i * 4096);
  const int G = ((g & 1) << 1) | (g >> 1);
  int xvo = (G * 16 + tok) * 16;
  u64 m1 = __ballot(tok & 1), m2 = __ballot(tok & 2);
  const int osb = WS_O + L * ACT_L + (4 * c + w) * 16384;
  const int xsb = WS_X + L * ACT_L + ((4 * c + w) * 16 + 2 * m) * 1024;
  const int ssb = WS_STAT + L * STAT_L + ((c * 8 + m) * 64 + 16 * w) * 16;
  asm volatile("" : "+v"(xvo), "+v"(xacc[0]), "+v"(xacc[1]), "+v"(xacc[2]), "+v"(xacc[3]));
#pragma unroll
  for (int nt = 0; nt < 4; ++nt) asm volatile("" : "+v"(wf[0][nt]), "+v"(wf[1][nt]), "+v"(wf[2][nt]), "+a"(wf[3][nt]));
  wait_flags(ctx, WS_FO + (L * 32 + c) * 128, 0, false);
  STAMP(L * 8 + 5);
  v4u of[16];
  constexpr int PRE = 8;
#pragma unroll
  for (int ks = 0; ks < PRE; ++ks) of[ks] = ld128(ctx, lane * 16 + (ks & 3) * 1024, osb + (ks & ~3) * 1024);
  SB();
  sfor<0, 16>([&](auto ks) {
    if constexpr (ks + PRE < 16) { of[ks + PRE] = ld128(ctx, lane * 16 + ((ks + PRE) & 3) * 1024, osb + ((ks + PRE) & ~3) * 1024); SB(); }
#pragma unroll
    for (int nt = 0; nt < 4; ++nt) {
      if constexpr ((ks & 3) < 3) mf_va(xacc[nt], wf[ks & 3][nt], of[ks]); else mf_aa(xacc[nt], wf[ks & 3][nt], of[ks]);
      if constexpr (ks + 4 < 16) wf[ks & 3][nt] = tr_frag(tb[nt], (ks + 4) * 4096);
      SB();
    }
  });
  NOP(4);
  STAMP(L * 8 + 6);
  v4u fr[2];
#pragma unroll
  for (int u = 0; u < 2; ++u) {
    unsigned a0, a1, b0, b1;
    const f4 a = xacc[2 * u], b = xacc[2 * u + 1];
    asm volatile("v_cvt_pk_bf16_f32 %0, %4, %5\n\tv_cvt_pk_bf16_f32 %1, %6, %7\n\tv_cvt_pk_bf16_f32 %2, %8, %9\n\tv_cvt_pk_bf16_f32 %3, %10, %11\n\t"
                 "s_nop 0\n\tv_permlane16_swap_b32 %0, %2\n\tv_permlane16_swap_b32 %1, %3"
                 : "=&v"(a0), "=&v"(a1), "=&v"(b0), "=&v"(b1)
                 : "v"(a[0]), "v"(a[1]), "v"(a[2]), "v"(a[3]), "v"(b[0]), "v"(b[1]), "v"(b[2]), "v"(b[3]));
    fr[u] = v4u{a0, a1, b0, b1};
    if (!last) st128(ctx, fr[u], xvo + u * 1024, xsb);
  }
  if (last) {
#pragma unroll
    for (int u = 0; u < 2; ++u)
      __builtin_amdgcn_raw_buffer_store_b128(fr[u], out, tok * 1024 + G * 16 + u * 64, (64 * c + 16 * w) * 1024 + 128 * m, 0);
  } else {
    f4 ssum, gram;
    v4u ones = {mk1(), mk1(), mk1(), mk1()};
    asm volatile("s_nop 1" : "+v"(ones));
    mf32z(gram, fr[0], fr[0]); mf32z(ssum, ones, fr[0]);
    mf32c(gram, fr[1], fr[1]); mf32c(ssum, ones, fr[1]);
    asm volatile("s_nop 7" : "+v"(gram), "+v"(ssum));
    float q, t;
    asm volatile("v_cndmask_b32 %0, %2, %3, %6\n\tv_cndmask_b32 %1, %4, %5, %6\n\tv_cndmask_b32 %0, %0, %1, %7"
                 : "=&v"(q), "=&v"(t) : "v"(gram[0]), "v"(gram[1]), "v"(gram[2]), "v"(gram[3]), "s"(m1), "s"(m2));
    if (g == (tok >> 2)) st128(ctx, v4u{asu(ssum[0]), ctx.T, asu(q), ctx.T}, tok * 16, ssb);
    WAIT_VM(1);
    BAR();
    flag_store(ctx, WS_FX + (L * 32 + c) * 128);
    if (first) part_step<P_BQ>(ctx, L_CS + (w * 6 + 3) * 64);
  }
  asm volatile("" : "+a"(xacc[0]), "+a"(xacc[1]), "+a"(xacc[2]), "+a"(xacc[3]));
  STAMP(L * 8 + 7);
}

__global__ void __launch_bounds__(NT, 1) __attribute__((amdgpu_num_vgpr(88)))
fused_kernel(const unsigned char* __restrict__ x_, const unsigned char* __restrict__ wa_, const unsigned char* __restrict__ wphi_,
             const unsigned char* __restrict__ woa_, const unsigned char* __restrict__ wb_, const unsigned char* __restrict__ wob_,
             unsigned char* out_, unsigned char* ws_) {
  extern __shared__ __attribute__((aligned(16))) unsigned char smem[];
  asm volatile("" ::: "v255", "a255");
  const GAS unsigned char* x = (const GAS unsigned char*)x_;
  Ctx ctx;
  ctx.ws = (GAS unsigned char*)ws_;
  ctx.rs = __builtin_amdgcn_make_buffer_rsrc((void*)ws_, 0, WS_END, 0x00020000);
  ctx.L = (lds_t*)smem;
  ctx.Lb = (unsigned)(size_t)ctx.L;
  const int b = blockIdx.x, idx = b >> 3;
  ctx.c = (b & 7) * 4 + (idx >> 3);
  ctx.m = idx & 7;
  ctx.w = __builtin_amdgcn_readfirstlane(threadIdx.x >> 6);
  ctx.budget = SPIN_CAP;
  fresh(ctx);
  const int c = ctx.c, m = ctx.m, w = ctx.w, lane = ctx.lane, g = ctx.g, tok = ctx.tok;
  STAMP(40); CSTAMP(0);
  const rsrc_t rsa = __builtin_amdgcn_make_buffer_rsrc((void*)wa_, 0, 512 * 3072, 0x00020000);
  const rsrc_t rsb = __builtin_amdgcn_make_buffer_rsrc((void*)wb_, 0, 512 * 3072, 0x00020000);
  const rsrc_t rwa = __builtin_amdgcn_make_buffer_rsrc((void*)woa_, 0, 512 * 1024, 0x00020000);
  const rsrc_t rwb = __builtin_amdgcn_make_buffer_rsrc((void*)wob_, 0, 512 * 1024, 0x00020000);
  {
    u64 pc; unsigned len;
    asm volatile("s_getpc_b64 %0\n.Lfk_here:\n\ts_mov_b32 %1, .Lfk_end-.Lfk_here" : "=s"(pc), "=s"(len));
    const rsrc_t rc = __builtin_amdgcn_make_buffer_rsrc((void*)pc, 0, len, 0x00020000);
    asm volatile("s_nop 4" ::: "memory");
    pf1<0>(lane * 16, rc, (idx * 4 + w) * 1024);
  }
  unsigned epoch, ph0, ph1, ph2, ph3;
  {
    const GAS unsigned char* pp = (const GAS unsigned char*)wphi_ + (4 * m + w) * 512;
    const unsigned pv = (unsigned)(4 * g * 16 + tok) * 2, zero = 0;
    asm volatile("s_nop 4\n\tglobal_load_dword %0, %5, %6 sc1\n\t"
                 "global_load_ushort %1, %7, %8\n\tglobal_load_ushort %2, %7, %8 offset:32\n\t"
                 "global_load_ushort %3, %7, %8 offset:64\n\tglobal_load_ushort %4, %7, %8 offset:96"
                 : "=&v"(epoch), "=&v"(ph0), "=&v"(ph1), "=&v"(ph2), "=&v"(ph3)
                 : "v"(zero), "s"(ctx.ws), "v"(pv), "s"(pp) : "memory");
  }
  const unsigned lv = lane * 16;
  const int pfr = (16 * idx + 4 * w) * 3072;
  sfor<0, 4>([&](auto i) { pf1<2048>(lv, rsa, pfr + i * 3072); });
  {
    const ImgDma kd = img_prep<3072>(ctx);
    sfor<0, 4>([&](auto j) { img_chunk<3072, 1024, L_RQ, j>(ctx, kd, rsa); });
  }
  {
    const GAS unsigned char* sb = x + (size_t)(64 * c + 16 * w) * 1024;
    const unsigned xv = (unsigned)((lane >> 2) * 1024 + (((lane & 3) ^ ((4 - (lane >> 4)) & 3)) << 4));
    const unsigned ldsbase = ctx.Lb + L_XT + (1 + w) * 16384;
#define XD(i) "global_load_lds_dwordx4 %0, %1 offset:" #i "\n\ts_add_u32 m0, m0, 0x3c0\n\ts_nop 0\n\t"
    asm volatile("s_mov_b32 m0, %2\n\ts_nop 0\n\t"
                 XD(0) XD(64) XD(128) XD(192) XD(256) XD(320) XD(384) XD(448)
                 XD(512) XD(576) XD(640) XD(704) XD(768) XD(832) XD(896) XD(960)
                 :: "v"(xv), "s"(sb), "s"(ldsbase) : "memory", "scc");
#undef XD
  }
  STAMP(42);
  asm volatile("s_waitcnt vmcnt(16)" : "+v"(epoch), "+v"(ph0), "+v"(ph1), "+v"(ph2), "+v"(ph3) :: "memory");
  RAWBAR();
  STAMP(43);
  const unsigned rvo = (unsigned)((lane >> 3) * 3072 + (lane & 7) * 16);
  const int rvb = 2048 + m * 128 + 32 * w * 3072;
  const unsigned trb = ctx.Lb + L_RQ + tr_base(lane, w);
  sfor<0, 8>([&](auto i) {
    raw_ld1<P_AV + i>(rvo, rsa, rvb + (128 * (i >> 2) + 8 * (i & 3)) * 3072);
    wtr<P_AK + 2 * i, 2 * i * 4096>(trb); wtr<P_AK + 2 * i + 1, (2 * i + 1) * 4096>(trb);
  });
  ctx.T = __builtin_amdgcn_readfirstlane(epoch) + 1u;
  v2u wphi = v2u{ph0 | (ph1 << 16), ph2 | (ph3 << 16)};
  const int csa = L_CS + (w * 6) * 64;
  unsigned wp0, wp1;
  {
    const int ch = lane & 7, rb1 = (lane >> 4) & 1;
    const unsigned base = ctx.Lb + L_RQ + (32 * w + (lane >> 3)) * 128 + (ch & 1) * 16;
    wp0 = base + (((ch >> 1) ^ rb1) << 5); wp1 = base + (((ch >> 1) ^ (rb1 | 2)) << 5);
  }
  f4 xacc[4];
  WAIT_VM(8);
  STAMP(44);
  {
    lds_t* tb = ctx.L + opq(L_XT + (1 + w) * 16384 + x0_lane(tok, g));
    v4u xs[16];
#pragma unroll
    for (int ks = 0; ks < 16; ++ks) xs[ks] = lds_ld128(tb, ks * 1024);
    v2u xr[4];
#pragma unroll
    for (int nt = 0; nt < 4; ++nt)
      xr[nt] = *(LAS v2u*)(ctx.L + L_XT + (1 + w) * 16384 + (2 * m + (nt >> 1)) * 1024 + x0_lane(tok, 2 * (nt & 1) + (g >> 1)) + (g & 1) * 8);
    v4u ones = {mk1(), mk1(), mk1(), mk1()};
    BAR();
#pragma unroll
    for (int ks = 0; ks < 16; ++ks) asm volatile("" : "+a"(xs[ks]));
    f4 gram = {0.f, 0.f, 0.f, 0.f}, ssum = {0.f, 0.f, 0.f, 0.f};
    asm volatile("s_nop 1" : "+v"(gram), "+v"(ssum), "+v"(ones));
    sfor<0, 16>([&](auto ks) {
      mf_aa(gram, xs[ks], xs[ks]); VNOP();
      if constexpr (ks < 8) raw_ld1<P_AV + 8 + ks>(rvo, rsa, rvb + (128 * ((8 + ks) >> 2) + 8 * (ks & 3)) * 3072);
      if constexpr (ks == 8) WAIT_VM(8);
      if constexpr (ks >= 8) raw_st1<P_AV + ks - 8, ((ks - 8) >> 2) * 16384 + (ks & 3) * 1024>((ks & 1) ? wp1 : wp0);
      mf_va(ssum, ones, xs[ks]); VNOP();
    });
#pragma unroll
    for (int nt = 0; nt < 4; ++nt)
      xacc[nt] = f4{asf(xr[nt].x << 16), asf(xr[nt].x & 0xffff0000u), asf(xr[nt].y << 16), asf(xr[nt].y & 0xffff0000u)};
    asm volatile("" : "+a"(xacc[0]), "+a"(xacc[1]), "+a"(xacc[2]), "+a"(xacc[3]));
    BAR();
    STAMP(45);
    f4 c0, c1;
    sfor<0, 8>([&](auto k) {
      wtr<P_AV + k, k * 4096>(trb);
      wcs<P_AK + 2 * k, k == 0>(c0, ones);
      if constexpr (k < 4) pf1<0>(lv, rsa, pfr + k * 3072);
      wcs<P_AK + 2 * k + 1, k == 0>(c1, ones);
    });
    asm volatile("" : "+v"(gram), "+v"(ssum));
    const float q = (tok & 2) ? ((tok & 1) ? gram[3] : gram[2]) : ((tok & 1) ? gram[1] : gram[0]);
    const float mean = ssum[0] * (1.0f / 512.0f);
    const float var = fmaxf(q * (1.0f / 512.0f) - mean * mean, 0.0f);
    const float rstd = __builtin_amdgcn_rsqf(var + 1e-5f);
    WAIT_VM(4);
    sfor<8, 16>([&](auto i) { raw_st1<P_AV + i, (i >> 2) * 16384 + (i & 3) * 1024>((i & 1) ? wp1 : wp0); });
    if (g == (tok >> 2)) {
      *(LAS f4*)(ctx.L + L_REC + ((1 + w) * 16 + tok) * 16) = f4{-mean, rstd, rstd, 0.f};
      *(LAS float*)(ctx.L + L_NM + ((1 + w) * 16 + tok) * 4) = -mean;
      *(LAS float*)(ctx.L + L_RS + ((1 + w) * 16 + tok) * 4) = rstd;
    }
    asm volatile("" : "+v"(c0), "+v"(c1));
    if (g == 0) *(LAS float*)(ctx.L + csa + 64 + tok * 4) = c0[0] + c1[0];
    BAR();
    STAMP(46);
    sfor<8, 16>([&](auto k) { wtr<P_AV + k, k * 4096>(trb); });
    sfor<0, 8>([&](auto k) {
      if constexpr (k == 4) asm volatile("s_waitcnt lgkmcnt(0)" ::: "memory");
      wcs<P_AV + 2 * k, k == 0>(c0, ones);
      if constexpr (k < 6) pf1<(k % 3) * 1024>(lv, rsb, pfr + (k / 3) * 3072);
      wcs<P_AV + 2 * k + 1, k == 0>(c1, ones);
    });
    asm volatile("s_nop 7" : "+v"(c0), "+v"(c1));
    if (g == 0) *(LAS float*)(ctx.L + csa + 128 + tok * 4) = c0[0] + c1[0];
  }
  asm volatile("" : "+a"(xacc[0]), "+a"(xacc[1]), "+a"(xacc[2]), "+a"(xacc[3]), "+a"(wphi));
  STAMP(1);
  const rsrc_t out = __builtin_amdgcn_make_buffer_rsrc((void*)out_, 0, 2048 * 1024, 0x00020000);

#pragma nounroll
  for (int lp = 0; lp < 2; ++lp) {
    asm volatile("" : "+a"(xacc[0]), "+a"(xacc[1]), "+a"(xacc[2]), "+a"(xacc[3]), "+a"(wphi));
    layer_A(ctx, lp, wphi, rwa, rsa, rsb);
    out_proj<false>(ctx, lp, xacc, out, rsb);
    layer_B(ctx, 2 * lp + 1, rwb);
    out_proj<true>(ctx, lp, xacc, out, rsb);
  }
  asm volatile(".Lfk_end:");
  if (b == 0) { ctx.m = 0; flag_store(ctx, WS_EPOCH); }
  STAMP(41); CSTAMP(1);
  STAMP_FLUSH();
}

extern "C" void kernel_launch(void* const* d_in, const int* in_sizes, int n_in, void* d_out, int out_size, void* d_ws,
                              size_t ws_size, hipStream_t stream) {
  (void)in_sizes; (void)n_in; (void)out_size; (void)ws_size;
  (void)hipFuncSetAttribute((const void*)fused_kernel, hipFuncAttributeMaxDynamicSharedMemorySize, LDS_BYTES);
  hipLaunchKernelGGL(fused_kernel, dim3(NWG), dim3(NT), LDS_BYTES, stream,
                     (const unsigned char*)d_in[0], (const unsigned char*)d_in[1], (const unsigned char*)d_in[2],
                     (const unsigned char*)d_in[3], (const unsigned char*)d_in[5], (const unsigned char*)d_in[6],
                     (unsigned char*)d_out, (unsigned char*)d_ws);
}
```

```cpp
#include <hip/hip_runtime.h>
#include <hip/hip_bf16.h>
#include <type_traits>

#define GAS __attribute__((address_space(1)))
#define LAS __attribute__((address_space(3)))
#define DEV __device__ __forceinline__
typedef short s4 __attribute__((ext_vector_type(4)));
typedef float f4 __attribute__((ext_vector_type(4)));
typedef float f2 __attribute__((ext_vector_type(2)));
typedef __bf16 bf2 __attribute__((ext_vector_type(2)));
typedef unsigned v4u __attribute__((ext_vector_type(4)));
typedef unsigned v2u __attribute__((ext_vector_type(2)));
typedef unsigned long long u64;
typedef GAS unsigned gu32;
typedef LAS unsigned char lds_t;
typedef __amdgpu_buffer_rsrc_t rsrc_t;
#define RLX_AGENT __ATOMIC_RELAXED, __HIP_MEMORY_SCOPE_AGENT

namespace {
constexpr int NWG = 256, NT = 256;
constexpr int WS_EPOCH = 0;
constexpr int WS_FX = 4096;
constexpr int WS_FO = WS_FX + 2 * 32 * 128;
constexpr int WS_KV2 = 1 << 16;
constexpr int WS_KVZ = 1 << 18;
constexpr int WS_STAT = 2 << 18;
constexpr int STAT_L = 32 * 8 * 64 * 16;
constexpr int WS_STAMP = 4 << 18;
constexpr int WS_X = 2 << 20;
constexpr int ACT_L = 2 << 20;
constexpr int WS_O = WS_X + 2 * ACT_L;
constexpr int WS_KV1 = WS_O + ACT_L;
constexpr int WS_END = WS_KV1 + 8 * 64 * 32 * 256;
constexpr int L_XT = 0;
constexpr int L_RQ = 81920;
constexpr int L_REC = 147456 + 256;
constexpr int L_NM = L_REC + 1280;
constexpr int L_RS = L_NM + 320;
constexpr int L_CS = L_RS + 320;
constexpr int L_END = L_CS + 4 * 2 * 3 * 64;
constexpr int LDS_BYTES = 158 * 1024;
static_assert(L_END <= LDS_BYTES, "lds");
constexpr int SPIN_CAP = 1 << 18;
constexpr float LOG2E = 1.4426950408889634f;
#define QPOLL 6
#define S2POLL 0
}

DEV float asf(unsigned u) { return __builtin_bit_cast(float, u); }
DEV unsigned asu(float f) { return __builtin_bit_cast(unsigned, f); }
DEV unsigned pk2(float a, float b) { unsigned r; asm("v_cvt_pk_bf16_f32 %0, %1, %2" : "=v"(r) : "v"(a), "v"(b)); return r; }
DEV float xg_sum(float v) {
  auto r = __builtin_amdgcn_permlane16_swap(asu(v), asu(v), false, false);
  v = asf(r[0]) + asf(r[1]);
  auto q = __builtin_amdgcn_permlane32_swap(asu(v), asu(v), false, false);
  return asf(q[0]) + asf(q[1]);
}
DEV float row_sum(float v) {
  v += asf(__builtin_amdgcn_update_dpp(0u, asu(v), 0x128, 0xf, 0xf, false));
  v += asf(__builtin_amdgcn_update_dpp(0u, asu(v), 0x124, 0xf, 0xf, false));
  v += asf(__builtin_amdgcn_update_dpp(0u, asu(v), 0x122, 0xf, 0xf, false));
  v += asf(__builtin_amdgcn_update_dpp(0u, asu(v), 0x121, 0xf, 0xf, false));
  return v;
}
DEV float sum32(float v) {
  v = row_sum(v);
  auto r = __builtin_amdgcn_permlane16_swap(asu(v), asu(v), false, false);
  return asf(r[0]) + asf(r[1]);
}
#define BAR() asm volatile("s_waitcnt lgkmcnt(0)\n\ts_barrier" ::: "memory")
#define RAWBAR() do { asm volatile("" ::: "memory"); __builtin_amdgcn_s_barrier(); asm volatile("" ::: "memory"); } while (0)
#define DRAIN() asm volatile("s_waitcnt vmcnt(0)" ::: "memory")
#define WAIT_VM(n) asm volatile("s_waitcnt vmcnt(%0)" :: "n"(n) : "memory")
#define WAIT_LG(n) __builtin_amdgcn_s_waitcnt(0xC07F | ((n) << 8))
#define WAIT_VMV(n) __builtin_amdgcn_s_waitcnt(0x0F70 | ((n) & 15) | (((n) >> 4) << 14))
#define VNOP() __builtin_amdgcn_s_setprio(0)
#define SB() __builtin_amdgcn_sched_barrier(0)
#define NOP(n) asm volatile("s_nop %0" :: "n"(n))
#define SC1 16
#define ONE2 0x3f803f80u
template <int N> using IC = std::integral_constant<int, N>;

DEV unsigned mk1() { unsigned r; asm volatile("v_mov_b32 %0, 0x3f803f80" : "=v"(r)); return r; }
DEV unsigned mk0() { unsigned r; asm volatile("v_mov_b32 %0, 0" : "=v"(r)); return r; }
DEV int opq(int v) { asm volatile("" : "+v"(v)); return v; }
DEV v4u lds_ld128(lds_t* L, int off) { return *(LAS v4u*)(L + off); }
DEV f4 lds_ldf4(lds_t* L, int off) { return *(LAS f4*)(L + off); }
DEV float lds_ldf(lds_t* L, int off) { return *(LAS float*)(L + off); }
DEV v4u tr_frag(lds_t* L, int off) {
  s4 a = __builtin_amdgcn_ds_read_tr16_b64_v4i16((LAS s4*)(L + off));
  s4 b = __builtin_amdgcn_ds_read_tr16_b64_v4i16((LAS s4*)(L + off + 512));
  v2u ua = __builtin_bit_cast(v2u, a), ub = __builtin_bit_cast(v2u, b);
  return v4u{ua.x, ua.y, ub.x, ub.y};
}
template <int I, int N, class F> DEV void sfor(F&& f) { if constexpr (I < N) { f(IC<I>{}); sfor<I + 1, N>(f); } }

constexpr int P_AK = 0, P_AV = 16, P_BQ = 32, P_BK = 48, P_BV = 64;
#define MF32 "v_mfma_f32_16x16x32_bf16 "
#define MF16 "v_mfma_f32_16x16x16_bf16 "
#define MM_CASE(WC, XC)                                                                                                         \
  if constexpr (WA) {                                                                                                           \
    if constexpr (FIRST) asm volatile(MF32 "%0, " WC "[%c1:%c2], " XC "[%c3:%c4], 0" : "=v"(acc) : "n"(R), "n"(R + 3), "n"(X), "n"(X + 3)); \
    else asm volatile(MF32 "%0, " WC "[%c1:%c2], " XC "[%c3:%c4], %0" : "+v"(acc) : "n"(R), "n"(R + 3), "n"(X), "n"(X + 3));     \
  } else {                                                                                                                      \
    if constexpr (FIRST) asm volatile(MF32 "%0, " XC "[%c3:%c4], " WC "[%c1:%c2], 0" : "=v"(acc) : "n"(R), "n"(R + 3), "n"(X), "n"(X + 3)); \
    else asm volatile(MF32 "%0, " XC "[%c3:%c4], " WC "[%c1:%c2], %0" : "+v"(acc) : "n"(R), "n"(R + 3), "n"(X), "n"(X + 3));     \
  }
template <int F, int RS, bool WA, bool FIRST>
DEV void mm(f4& acc) {
  constexpr int R = 96 + 4 * (F < 40 ? F : F - 40), X = 88 + 4 * (RS & 1);
  if constexpr (F < 40) { if constexpr (RS < 2) { MM_CASE("v", "v") } else { MM_CASE("v", "a") } }
  else { if constexpr (RS < 2) { MM_CASE("a", "v") } else { MM_CASE("a", "a") } }
}
template <int RS, int OFF>
DEV void ring_rd(unsigned addr) {
  constexpr int X = 88 + 4 * (RS & 1);
  if constexpr (RS < 2) asm volatile("ds_read_b128 v[%c1:%c2], %0 offset:%c3" :: "v"(addr), "n"(X), "n"(X + 3), "n"(OFF) : "memory");
  else asm volatile("ds_read_b128 a[%c1:%c2], %0 offset:%c3" :: "v"(addr), "n"(X), "n"(X + 3), "n"(OFF) : "memory");
}
template <int F, bool FIRST>
DEV void wcs(f4& acc, v4u x) {
  constexpr int R = 96 + 4 * (F < 40 ? F : F - 40);
  if constexpr (F < 40) {
    if constexpr (FIRST) asm volatile(MF32 "%0, %1, v[%c2:%c3], 0" : "=&v"(acc) : "v"(x), "n"(R), "n"(R + 3));
    else asm volatile(MF32 "%0, %1, v[%c2:%c3], %0" : "+v"(acc) : "v"(x), "n"(R), "n"(R + 3));
  } else {
    if constexpr (FIRST) asm volatile(MF32 "%0, %1, a[%c2:%c3], 0" : "=&v"(acc) : "v"(x), "n"(R), "n"(R + 3));
    else asm volatile(MF32 "%0, %1, a[%c2:%c3], %0" : "+v"(acc) : "v"(x), "n"(R), "n"(R + 3));
  }
}
template <int RS, bool FIRST>
DEV void mmq(f4& acc, v4u a) {
  constexpr int X = 88 + 4 * (RS & 1);
  if constexpr (RS < 2) {
    if constexpr (FIRST) asm volatile(MF32 "%0, %1, v[%c2:%c3], 0" : "=v"(acc) : "a"(a), "n"(X), "n"(X + 3));
    else asm volatile(MF32 "%0, %1, v[%c2:%c3], %0" : "+v"(acc) : "a"(a), "n"(X), "n"(X + 3));
  } else {
    if constexpr (FIRST) asm volatile(MF32 "%0, %1, a[%c2:%c3], 0" : "=v"(acc) : "a"(a), "n"(X), "n"(X + 3));
    else asm volatile(MF32 "%0, %1, a[%c2:%c3], %0" : "+v"(acc) : "a"(a), "n"(X), "n"(X + 3));
  }
}
template <bool FIRST>
DEV void mf_av(f4& acc, v4u a, v4u b) {
  if constexpr (FIRST) asm volatile(MF32 "%0, %1, %2, 0" : "=&v"(acc) : "a"(a), "v"(b));
  else asm volatile(MF32 "%0, %1, %2, %0" : "+v"(acc) : "a"(a), "v"(b));
}
DEV void mf_va(f4& acc, v4u a, v4u b) { asm volatile(MF32 "%0, %1, %2, %0" : "+v"(acc) : "v"(a), "a"(b)); }
DEV void mf_aa(f4& acc, v4u a, v4u b) { asm volatile(MF32 "%0, %1, %2, %0" : "+v"(acc) : "a"(a), "a"(b)); }
DEV void mf32z(f4& d, v4u a, v4u b) { asm volatile(MF32 "%0, %1, %2, 0" : "=&v"(d) : "v"(a), "v"(b)); VNOP(); }
DEV void mf32c(f4& d, v4u a, v4u b) { asm volatile(MF32 "%0, %1, %2, %0" : "+v"(d) : "v"(a), "v"(b)); VNOP(); }
DEV void mf16z(f4& d, v2u a, v2u b) { asm volatile(MF16 "%0, %1, %2, 0" : "=&v"(d) : "v"(a), "v"(b)); VNOP(); }
DEV void mf16c(f4& d, v2u a, v2u b) { asm volatile(MF16 "%0, %1, %2, %0" : "+v"(d) : "v"(a), "v"(b)); VNOP(); }
DEV void mf16z_a(f4& d, v2u a, v2u b) { asm volatile(MF16 "%0, %1, %2, 0" : "=&v"(d) : "a"(a), "v"(b)); VNOP(); }
DEV void mf16c_a(f4& d, v2u a, v2u b) { asm volatile(MF16 "%0, %1, %2, %0" : "+v"(d) : "a"(a), "v"(b)); VNOP(); }
DEV void mf16z_b(f4& d, v2u a, v2u b) { asm volatile(MF16 "%0, %1, %2, 0" : "=&v"(d) : "v"(a), "a"(b)); VNOP(); }
template <int F, int OFF>
DEV void wtr(unsigned addr) {
  constexpr int R = 96 + 4 * (F < 40 ? F : F - 40);
  if constexpr (F < 40)
    asm volatile("ds_read_b64_tr_b16 v[%c1:%c2], %0 offset:%c5\n\tds_read_b64_tr_b16 v[%c3:%c4], %0 offset:%c6"
                 :: "v"(addr), "n"(R), "n"(R + 1), "n"(R + 2), "n"(R + 3), "n"(OFF), "n"(OFF + 512) : "memory");
  else
    asm volatile("ds_read_b64_tr_b16 a[%c1:%c2], %0 offset:%c5\n\tds_read_b64_tr_b16 a[%c3:%c4], %0 offset:%c6"
                 :: "v"(addr), "n"(R), "n"(R + 1), "n"(R + 2), "n"(R + 3), "n"(OFF), "n"(OFF + 512) : "memory");
}
#define A_FMA(d, a, b, c) asm volatile("v_fma_f32 %0, %1, %2, %3" : "=v"(d) : "v"(a), "v"(b), "v"(c))
#define A_FMAN(d, a, b, c) asm volatile("v_fma_f32 %0, %1, -%2, %3" : "=v"(d) : "v"(a), "v"(b), "v"(c))
#define A_MUL(d, a, b) asm volatile("v_mul_f32 %0, %1, %2" : "=v"(d) : "v"(a), "v"(b))
#define A_MULS(d, k, b) asm volatile("v_mul_f32 %0, %1, %2" : "=v"(d) : "s"(k), "v"(b))
#define A_MAX(d, a, b) asm volatile("v_max_f32 %0, %1, %2" : "=v"(d) : "v"(a), "v"(b))
#define A_SUB(d, a, b) asm volatile("v_sub_f32 %0, %1, %2" : "=v"(d) : "v"(a), "v"(b))
#define A_CVT(d, a, b) asm volatile("v_cvt_pk_bf16_f32 %0, %1, %2" : "=v"(d) : "v"(a), "v"(b))
#define A_EXP(d, a) asm volatile("v_exp_f32 %0, %1" : "=v"(d) : "v"(a))
#define A_RCP(d, x4) asm volatile("v_rcp_f32 %0, %1" : "=&v"(d) : "v"(x4[0]), "v"(x4))
#define A_SEL(d, f, t, m) asm volatile("v_cndmask_b32 %0, %1, %2, %3" : "=v"(d) : "v"(f), "v"(t), "s"(m))
#define A_PERM(d, x, sel) asm volatile("v_perm_b32 %0, %1, %1, %2" : "=v"(d) : "v"(x), "v"(sel))
#define A_SW16(a, b) asm volatile("v_permlane16_swap_b32 %0, %1" : "+v"(a), "+v"(b))
#define A_SW32(a, b) asm volatile("v_permlane32_swap_b32 %0, %1" : "+v"(a), "+v"(b))
#define A_LD64(d, addr, off) asm volatile("ds_read_b64 %0, %1 offset:%2" : "=v"(d) : "v"(addr), "n"(off) : "memory")
#define A_LD128(d, addr, off) asm volatile("ds_read_b128 %0, %1 offset:%2" : "=v"(d) : "v"(addr), "n"(off) : "memory")

#define STAMP(i) do {} while (0)
#define CSTAMP(i) do {} while (0)
#define STAMP_FLUSH() do {} while (0)
#define TS(k) do {} while (0)
#define TS_DECL
#define TS_FLUSH(base, n) do {} while (0)

struct Ctx {
  GAS unsigned char* ws;
  rsrc_t rs;
  lds_t* L;
  unsigned Lb, Lw;
  int c, m, w;
  int lane, tok, g;
  unsigned T;
  int budget;
};
DEV void fresh(Ctx& ctx) {
  int l;
  asm volatile("v_mbcnt_lo_u32_b32 %0, -1, 0\n\tv_mbcnt_hi_u32_b32 %0, -1, %0" : "=v"(l));
  ctx.lane = l; ctx.tok = l & 15; ctx.g = l >> 4;
  asm volatile("" : "+s"(ctx.c), "+s"(ctx.m), "+s"(ctx.w), "+s"(ctx.Lb));
  ctx.Lw = ctx.Lb + 4096 * ctx.w;
}
DEV v4u ld128(Ctx& ctx, int voff, int soff) { return __builtin_amdgcn_raw_buffer_load_b128(ctx.rs, voff, soff, SC1); }
DEV void st128(Ctx& ctx, v4u v, int voff, int soff) { __builtin_amdgcn_raw_buffer_store_b128(v, ctx.rs, voff, soff, SC1); }
DEV void st64(Ctx& ctx, v2u v, int voff, int soff) { __builtin_amdgcn_raw_buffer_store_b64(v, ctx.rs, voff, soff, SC1); }

DEV void wait_flags(Ctx& ctx, int off0, int off1, bool two) {
  if (ctx.w == 0) {
    const int l = ctx.lane & (two ? 15 : 7);
    const int vo = (l < 8 ? off0 : off1 - 128) + l * 16;
    for (;;) {
      asm volatile("" ::: "memory");
      const unsigned v = __builtin_amdgcn_raw_buffer_load_b32(ctx.rs, vo, 0, SC1);
      if (__all(v == ctx.T) || --ctx.budget < 0) break;
    }
    __builtin_amdgcn_fence(__ATOMIC_ACQUIRE, "wavefront");
  }
  BAR();
}
DEV void flag_store(Ctx& ctx, int off) {
  if (ctx.w == 0) {
    unsigned tv, zv;
    asm volatile("v_mov_b32 %0, %2\n\tv_mov_b32 %1, 0" : "=v"(tv), "=v"(zv) : "s"(ctx.T));
    __builtin_amdgcn_raw_buffer_store_b32(tv, ctx.rs, zv, off + ctx.m * 16, SC1);
  }
}

DEV int x0_lane(int tok, int g) { return tok * 64 + ((g ^ ((4 - (tok >> 2)) & 3)) << 4); }
DEV int tr_base(int lane, int j) {
  const int g = lane >> 4, q = (lane & 15) >> 2, p = lane & 3;
  const int s = ((q >> 1) & 1) | ((g & 1) << 1);
  return (8 * g + q) * 128 + ((j ^ s) << 5) + p * 8;
}
template <int F>
DEV void raw_ld1(unsigned voff, rsrc_t rs, int soff) {
  constexpr int R = 96 + 4 * (F < 40 ? F : F - 40);
  if constexpr (F < 40) asm volatile("buffer_load_dwordx4 v[%c3:%c4], %0, %1, %2 offen" :: "v"(voff), "s"(rs), "s"(soff), "n"(R), "n"(R + 3) : "memory");
  else asm volatile("buffer_load_dwordx4 a[%c3:%c4], %0, %1, %2 offen" :: "v"(voff), "s"(rs), "s"(soff), "n"(R), "n"(R + 3) : "memory");
}
template <int P, int I0, int I1>
DEV void raw_load(Ctx& ctx, rsrc_t rs, int colbyte) {
  const unsigned vo = (unsigned)((ctx.lane >> 3) * 3072 + (ctx.lane & 7) * 16);
  const int sb = colbyte + ctx.m * 128 + 32 * ctx.w * 3072;
  sfor<I0, I1>([&](auto i) { raw_ld1<P + i>(vo, rs, sb + (128 * (i >> 2) + 8 * (i & 3)) * 3072); });
}
template <int F, int OFF>
DEV void raw_st1(unsigned addr) {
  constexpr int R = 96 + 4 * (F < 40 ? F : F - 40);
  if constexpr (F < 40) asm volatile("ds_write_b128 %0, v[%c1:%c2] offset:%c3" :: "v"(addr), "n"(R), "n"(R + 3), "n"(OFF) : "memory");
  else asm volatile("ds_write_b128 %0, a[%c1:%c2] offset:%c3" :: "v"(addr), "n"(R), "n"(R + 3), "n"(OFF) : "memory");
}
template <int P, int I0, int I1>
DEV void raw_store(Ctx& ctx) {
  const int lane = ctx.lane, ch = lane & 7, rb1 = (lane >> 4) & 1;
  const unsigned base = ctx.Lb + L_RQ + (32 * ctx.w + (lane >> 3)) * 128 + (ch & 1) * 16;
  const unsigned p0 = base + (((ch >> 1) ^ rb1) << 5), p1 = base + (((ch >> 1) ^ (rb1 | 2)) << 5);
  sfor<I0, I1>([&](auto i) { raw_st1<P + i, (i >> 2) * 16384 + (i & 3) * 1024>((i & 1) ? p1 : p0); });
}
template <int P, int K0, int K1>
DEV void part_tr(Ctx& ctx) {
  const unsigned tb = ctx.Lb + L_RQ + tr_base(ctx.lane, ctx.w);
  sfor<K0, K1>([&](auto ks) { wtr<P + ks, ks * 4096>(tb); });
}
template <int P>
DEV void part_cs(Ctx& ctx, int csoff) {
  v4u ones = {mk1(), mk1(), mk1(), mk1()};
  asm volatile("s_waitcnt lgkmcnt(0)\n\ts_nop 1" : "+v"(ones) :: "memory");
  f4 c0, c1;
  sfor<0, 8>([&](auto k) { wcs<P + 2 * k, k == 0>(c0, ones); wcs<P + 2 * k + 1, k == 0>(c1, ones); });
  asm volatile("s_nop 7" : "+v"(c0), "+v"(c1));
  if (ctx.g == 0) *(LAS float*)(ctx.L + csoff + ctx.tok * 4) = c0[0] + c1[0];
}

template <int P>
DEV void part_step(Ctx& ctx, int csoff) {
  raw_store<P, 0, 8>(ctx);
  BAR();
  part_tr<P, 0, 8>(ctx);
  raw_store<P, 8, 16>(ctx);
  BAR();
  part_tr<P, 8, 16>(ctx);
  part_cs<P>(ctx, csoff);
}

template <int P, int PN>
DEV void part_step_ld(Ctx& ctx, int csoff, rsrc_t rs, int colbyte) {
  raw_load<PN, 0, 4>(ctx, rs, colbyte);
  raw_store<P, 0, 8>(ctx);
  raw_load<PN, 4, 8>(ctx, rs, colbyte);
  BAR();
  part_tr<P, 0, 8>(ctx);
  raw_load<PN, 8, 12>(ctx, rs, colbyte);
  raw_store<P, 8, 16>(ctx);
  raw_load<PN, 12, 16>(ctx, rs, colbyte);
  BAR();
  part_tr<P, 8, 16>(ctx);
  part_cs<P>(ctx, csoff);
}
template <int OFF>
DEV void pf1(unsigned lv, rsrc_t rs, int so) {
  asm volatile("buffer_load_dwordx4 v[224:227], %0, %1, %2 offen offset:%3" :: "v"(lv), "s"(rs), "s"(so), "n"(OFF) : "memory");
}

struct ImgDma { unsigned v[4]; int sb; };
template <int ROWB>
DEV ImgDma img_prep(Ctx& ctx) {
  const int lane = ctx.lane, cp = lane & 7, rb1 = (lane >> 4) & 1;
  const unsigned vb = (unsigned)((lane >> 3) * ROWB + (cp & 1) * 16);
  const unsigned v0 = vb + (((cp >> 1) ^ rb1) << 5), v1 = vb + (((cp >> 1) ^ (rb1 | 2)) << 5);
  ImgDma d;
  d.v[0] = v0; d.v[1] = v1 + (8 * ROWB - 1024); d.v[2] = v0 + 2 * (8 * ROWB - 1024); d.v[3] = v1 + 3 * (8 * ROWB - 1024);
  d.sb = ctx.m * 128 + 32 * ctx.w * ROWB;
  return d;
}
template <int LOFF> DEV void dma_m0(unsigned lbase) { asm volatile("s_add_u32 m0, %0, %1" :: "s"(lbase), "n"(LOFF) : "scc"); }
template <int SOFF> DEV void dma_so(int& t, int sbase) { asm volatile("s_add_u32 %0, %1, %2" : "=s"(t) : "s"(sbase), "n"(SOFF) : "scc"); }
template <int I> DEV void dma_go(unsigned voff, rsrc_t rs, int so) {
  asm volatile("buffer_load_dwordx4 %0, %1, %2 offen offset:%3 lds" :: "v"(voff), "s"(rs), "s"(so), "n"(I * 1024) : "memory");
}
template <int I> DEV void dma_go_sc1(unsigned voff, rsrc_t rs, int so) {
  asm volatile("buffer_load_dwordx4 %0, %1, %2 offen offset:%3 sc1 lds" :: "v"(voff), "s"(rs), "s"(so), "n"(I * 1024) : "memory");
}
template <int ROWB, int COLB, int LDSOFF, int J>
DEV void img_chunk(Ctx& ctx, const ImgDma& d, rsrc_t rs) {
  int so;
  dma_m0<LDSOFF + J * 16384>(ctx.Lw); dma_so<COLB + 128 * J * ROWB>(so, d.sb);
  sfor<0, 4>([&](auto i) { dma_go<i>(d.v[i], rs, so); });
}
template <int TT>
DEV void x_chunk(Ctx& ctx, unsigned lv, int xs) {
  int so;
  dma_m0<L_XT + (TT + 1) * 16384>(ctx.Lw); dma_so<(TT + 1) * 16384>(so, xs);
  sfor<0, 4>([&](auto i) { dma_go_sc1<i>(lv, ctx.rs, so); });
}

struct ST {
  v4u d[4];
  float s, q, t, bias;
  unsigned bad, sv, wrec, wnm;
  int so;
};
template <bool HALO>
DEV void st_prep(Ctx& ctx, ST& st, int L) {
  (void)L;
  const int c = ctx.c, w = ctx.w, r32 = ctx.lane & 31, hf = ctx.lane >> 5;
  const bool halo = HALO && w >= 2;
  const int cc = (halo && c > 0) ? c - 1 : c;
  const int row0 = halo ? (c > 0 ? 48 : 0) : 32 * (w & 1);
  const int r = halo ? (r32 & 15) : r32;
  st.so = WS_STAT + (HALO ? 0 : STAT_L) + (cc * 8 * 64 + row0) * 16;
  st.sv = hf * 4096 + r * 16;
  const int lrow = halo ? r : 16 + row0 + r;
  st.wrec = ctx.Lb + L_REC + lrow * 16;
  st.wnm = ctx.Lb + L_NM + lrow * 4;
  st.bias = (halo && c == 0) ? -__builtin_inff() : 0.f;
  asm volatile("" : "+v"(st.sv), "+v"(st.wrec), "+v"(st.wnm), "+v"(st.bias));
}
DEV void st_issue(Ctx& ctx, ST& st) {
  asm volatile("buffer_load_dwordx4 %0, %4, %5, %6 offen sc1\n\t"
               "buffer_load_dwordx4 %1, %4, %5, %6 offen offset:1024 sc1\n\t"
               "buffer_load_dwordx4 %2, %4, %5, %6 offen offset:2048 sc1\n\t"
               "buffer_load_dwordx4 %3, %4, %5, %6 offen offset:3072 sc1"
               : "=&v"(st.d[0]), "=&v"(st.d[1]), "=&v"(st.d[2]), "=&v"(st.d[3]) : "v"(st.sv), "s"(ctx.rs), "s"(st.so) : "memory");
}
DEV void st_retry(Ctx& ctx, ST& st) {
  for (;;) {
    asm volatile("" ::: "memory");
    unsigned bad = 0;
#pragma unroll
    for (int i = 0; i < 4; ++i) { st.d[i] = ld128(ctx, st.sv + i * 1024, st.so); }
#pragma unroll
    for (int i = 0; i < 4; ++i) bad |= (st.d[i].y ^ ctx.T) | (st.d[i].w ^ ctx.T);
    if (__all(bad == 0) || --ctx.budget < 0) break;
  }
}
DEV void st_check(Ctx& ctx, ST& st) {
  asm volatile("" : "+v"(st.d[0]), "+v"(st.d[1]), "+v"(st.d[2]), "+v"(st.d[3]));
  const unsigned T = ctx.T;
  const bool ok = (st.d[0].y == T) & (st.d[0].w == T) & (st.d[1].y == T) & (st.d[1].w == T) &
                  (st.d[2].y == T) & (st.d[2].w == T) & (st.d[3].y == T) & (st.d[3].w == T);
  if (__builtin_expect(!__all(ok), 0)) st_retry(ctx, st);
}
template <int K>
DEV void st_step(ST& st) {
  if constexpr (K == 0) asm volatile("v_add_f32 %0, %2, %3\n\tv_add_f32 %1, %4, %5" : "=&v"(st.s), "=&v"(st.q) : "v"(st.d[0].x), "v"(st.d[1].x), "v"(st.d[0].z), "v"(st.d[1].z));
  if constexpr (K == 1) asm volatile("v_add_f32 %0, %0, %2\n\tv_add_f32 %1, %1, %3" : "+v"(st.s), "+v"(st.q) : "v"(st.d[2].x), "v"(st.d[2].z));
  if constexpr (K == 2) asm volatile("v_add_f32 %0, %0, %2\n\tv_add_f32 %1, %1, %3" : "+v"(st.s), "+v"(st.q) : "v"(st.d[3].x), "v"(st.d[3].z));
  if constexpr (K == 3) A_SW32(st.s, st.q);
  if constexpr (K == 4) { float x = st.s, y = st.q; asm volatile("v_add_f32 %0, %2, %3\n\tv_add_f32 %1, %2, %3" : "=&v"(st.s), "=&v"(st.q) : "v"(x), "v"(y)); }
  if constexpr (K == 5) A_SW32(st.s, st.q);
  if constexpr (K == 6) asm volatile("v_mul_f32 %0, %2, %0\n\tv_mul_f32 %1, %3, %1" : "+v"(st.s), "+v"(st.q) : "s"(-1.0f / 512.0f), "s"(1.0f / 512.0f));
  if constexpr (K == 7) asm volatile("v_fma_f32 %0, -%1, %1, %0\n\tv_max_f32 %0, 0, %0" : "+v"(st.q) : "v"(st.s));
  if constexpr (K == 8) asm volatile("v_add_f32 %0, %1, %0" : "+v"(st.q) : "s"(1e-5f));
  if constexpr (K == 9) asm volatile("v_rsq_f32 %0, %0" : "+v"(st.q));
  if constexpr (K == 10) asm volatile("v_mov_b32 %0, %1" : "=v"(st.t) : "v"(st.q));
  if constexpr (K == 11) asm volatile("ds_write_b128 %0, %1" :: "v"(st.wrec), "v"(f4{st.s, st.q, st.t, st.bias}) : "memory");
  if constexpr (K == 12) asm volatile("ds_write2_b32 %0, %1, %2 offset1:80" :: "v"(st.wnm), "v"(st.s), "v"(st.q) : "memory");
}

template <bool HALO>
DEV void gather_start(Ctx& ctx, ST& st, int L, unsigned lv, int xs) {
  const int c = ctx.c;
  const int cprev = c > 0 ? c - 1 : 0;
  constexpr int T0 = HALO ? -1 : 0;
  const int xs0 = (HALO && c == 0) ? xs + 16384 : xs;
  st_prep<HALO>(ctx, st, L);
  wait_flags(ctx, WS_FX + ((HALO ? 0 : 32) + c) * 128, WS_FX + ((HALO ? 0 : 32) + cprev) * 128, HALO);
  STAMP(L * 8 + 0);
  x_chunk<T0>(ctx, lv, xs0);
  st_issue(ctx, st);
  x_chunk<T0 + 1>(ctx, lv, xs);
  WAIT_VM(8);
  RAWBAR();
  STAMP(L * 8 + 1);
}
template <int H>
DEV void st_gaps(Ctx& ctx, ST& st) {
  if constexpr (H == 4 * 2 + 2) st_check(ctx, st);
  if constexpr (H == 4 * 3 + 2) st_step<0>(st);
  if constexpr (H == 4 * 4 + 2) st_step<1>(st);
  if constexpr (H == 4 * 5) st_step<2>(st);
  if constexpr (H == 4 * 5 + 2) st_step<3>(st);
  if constexpr (H == 4 * 6) st_step<4>(st);
  if constexpr (H == 4 * 6 + 2) st_step<5>(st);
  if constexpr (H == 4 * 7 + 2) st_step<6>(st);
  if constexpr (H == 4 * 8) st_step<7>(st);
  if constexpr (H == 4 * 8 + 2) { st_step<8>(st); }
  if constexpr (H == 4 * 9) st_step<9>(st);
  if constexpr (H == 4 * 9 + 2) { st_step<10>(st); st_step<12>(st); }
  if constexpr (H == 4 * 10) st_step<11>(st);
}

template <int OFF> DEV void ring_prime(unsigned ab) { ring_rd<0, OFF>(ab); ring_rd<1, OFF + 1024>(ab); ring_rd<2, OFF + 2048>(ab); WAIT_LG(2); }
template <int PQ, int PK, int PV, int OFF, bool CINIT, class XL, class SH, class GP>
DEV void gemm_tile(unsigned ab, f4& aq, f4& ak, f4& av, XL&& xl, SH&& sh, GP&& gp) {
  sfor<0, 16>([&](auto ks) {
    constexpr bool F = ks == 0 && !CINIT;
    if constexpr (PQ >= 0) {
      mm<(PQ >= 0 ? PQ : 0) + ks, ks & 3, true, F>(aq);
      if (!sh(IC<4 * ks>{})) VNOP();
      ring_rd<(ks + 3) & 3, OFF + (ks + 3) * 1024>(ab);
      gp(IC<4 * ks>{});
      mm<PK + ks, ks & 3, true, F>(ak);
      if (!sh(IC<4 * ks + 1>{})) VNOP();
      gp(IC<4 * ks + 1>{});
    } else {
      mm<PK + ks, ks & 3, true, F>(ak);
      if (!sh(IC<4 * ks>{})) VNOP();
      ring_rd<(ks + 3) & 3, OFF + (ks + 3) * 1024>(ab);
      gp(IC<4 * ks>{});
    }
    mm<PV + ks, ks & 3, false, F>(av);
    WAIT_LG(2 + decltype(xl(ks))::value);
    gp(IC<4 * ks + 2>{});
    SB();
  });
}
#define GA(k) (4 * (k))
#define GB(k) (4 * (k) + 1)
#define GC(k) (4 * (k) + 2)

struct BE {
  f4 aq, ak, av;
  f2 st, ka, kb2; f4 nm4;
  f2 nst, nka, nkb; f4 nnm4;
  f4 csq, csk; float csv;
  float q[4], k[4], v[4];
  unsigned qb[2], kb[2], vb[2], vb2[2], kprev[2], vprev[2], P[4], pk0, pk, ob[2];
  f4 sp, sc, num, den;
  float a, b, t0, t1, m, m2, ft, fa, fb, pa, pb;
  v2u ones;
  unsigned sPA, sPB, sCA, sCB;
  u64 modd, mA, mB;
  unsigned aT, aF, aKa, aKb;
  int ovo;
};

DEV void layer_B(Ctx& ctx, int L, rsrc_t rwo) {
  fresh(ctx);
  const int c = ctx.c, w = ctx.w, g = ctx.g, tok = ctx.tok;
  const int csb = L_CS + (w * 6 + 3) * 64;
  const unsigned lv = ctx.lane * 16;
  BE e;
  TS_DECL;
  unsigned ab0, ab1;
  ImgDma wd = img_prep<1024>(ctx);
  {
    const bool odd = tok & 1;
    const int ja = 4 * g + (odd ? 1 : 0), jb = ja + 2;
    const bool curA = ja <= tok, curB = jb <= tok;
    const unsigned selA = odd ? 0x01000c0cu : 0x0c0c0100u, selB = odd ? 0x03020c0cu : 0x0c0c0302u, selZ = 0x0c0c0c0cu;
    e.sPA = curA ? selZ : selA; e.sPB = curB ? selZ : selB; e.sCA = curA ? selA : selZ; e.sCB = curB ? selB : selZ;
    e.modd = __ballot(odd); e.mA = __ballot(curA); e.mB = __ballot(curB);
    e.aT = ctx.Lb + L_REC + tok * 16;
    e.aF = ctx.Lb + L_NM + g * 16;
    e.aKa = ctx.Lb + L_REC + (ja - (curA ? 0 : 16)) * 16 + 8;
    e.aKb = ctx.Lb + L_REC + (jb - (curB ? 0 : 16)) * 16 + 8;
    e.csq = lds_ldf4(ctx.L, csb + g * 16); e.csk = lds_ldf4(ctx.L, csb + 64 + g * 16); e.csv = lds_ldf(ctx.L, csb + 128 + tok * 4);
    e.ones = v2u{mk1(), mk0()};
    e.ovo = ((g >> 1) * 16 + tok) * 16 + (g & 1) * 8;
    ab0 = ctx.Lb + L_XT + lv; ab1 = ab0 + 49152;
    asm volatile("" : "+v"(e.sPA), "+v"(e.sPB), "+v"(e.sCA), "+v"(e.sCB), "+v"(e.csq), "+v"(e.csk), "+v"(e.csv), "+a"(e.ones), "+v"(e.ovo),
                      "+v"(e.aT), "+v"(e.aF), "+v"(e.aKa), "+v"(e.aKb), "+v"(ab0), "+v"(ab1), "+v"(wd.v[0]), "+v"(wd.v[1]), "+v"(wd.v[2]), "+v"(wd.v[3]));
  }
  const int xs = WS_X + c * 65536 + w * 4096 - 16384;
  const int osb = WS_O + (4 * c * 16 + 2 * ctx.m + (w >> 1)) * 1024 + (w & 1) * 512;
  ST st;
  gather_start<true>(ctx, st, L, lv, xs);
  ring_prime<0>(ab0);
  f4 xq, xk, xv, yq, yk, yv;
  int so, oso;
  auto stat_rd = [&](auto S, auto h) {
    (void)e;
    if constexpr (h == GC(6)) { A_LD64(e.nst, e.aT, S * 256); A_LD128(e.nnm4, e.aF, S * 64); }
    if constexpr (h == GA(7)) A_LD64(e.nka, e.aKa, S * 256);
    if constexpr (h == GA(8)) A_LD64(e.nkb, e.aKb, S * 256);
  };
  auto xl = [](auto ks) { return IC<(ks == 7 ? 3 : ks == 8 ? 4 : ks == 9 ? 2 : ks == 10 ? 1 : 0)>{}; };
  auto next = [&](f4& q, f4& k, f4& v) { e.aq = q; e.ak = k; e.av = v; e.st = e.nst; e.ka = e.nka; e.kb2 = e.nkb; e.nm4 = e.nnm4; };
  auto epi = [&](auto h) {
    (void)e;
    if constexpr (h == GA(0)) A_FMA(e.q[0], e.st[0], e.csq[0], e.aq[0]);
    if constexpr (h == GB(0)) { A_FMA(e.q[1], e.st[0], e.csq[1], e.aq[1]); A_FMA(e.q[2], e.st[0], e.csq[2], e.aq[2]); }
    if constexpr (h == GC(0)) { A_FMA(e.q[3], e.st[0], e.csq[3], e.aq[3]); A_FMA(e.k[0], e.st[0], e.csk[0], e.ak[0]); }
    if constexpr (h == GA(1)) A_FMA(e.k[1], e.st[0], e.csk[1], e.ak[1]);
    if constexpr (h == GB(1)) { A_FMA(e.k[2], e.st[0], e.csk[2], e.ak[2]); A_FMA(e.k[3], e.st[0], e.csk[3], e.ak[3]); }
    if constexpr (h == GC(1)) { A_CVT(e.qb[0], e.q[0], e.q[1]); A_CVT(e.qb[1], e.q[2], e.q[3]); }
    if constexpr (h == GB(2)) { A_CVT(e.kb[0], e.k[0], e.k[1]); A_CVT(e.kb[1], e.k[2], e.k[3]); }
    if constexpr (h == GC(2)) {
      A_FMA(e.v[0], e.nm4[0], e.csv, e.av[0]); A_FMA(e.v[1], e.nm4[1], e.csv, e.av[1]);
      mf16z(e.sp, v2u{e.kprev[0], e.kprev[1]}, v2u{e.qb[0], e.qb[1]});
      A_FMA(e.v[2], e.nm4[2], e.csv, e.av[2]); A_FMA(e.v[3], e.nm4[3], e.csv, e.av[3]);
      mf16z(e.sc, v2u{e.kb[0], e.kb[1]}, v2u{e.qb[0], e.qb[1]});
      A_CVT(e.vb[0], e.v[0], e.v[1]); A_CVT(e.vb[1], e.v[2], e.v[3]);
    }
    if constexpr (h == GB(3)) { A_CVT(e.vb2[0], e.v[0], e.v[1]); A_CVT(e.vb2[1], e.v[2], e.v[3]); }
    if constexpr (h == GC(3)) A_MULS(e.ft, 0.25f * LOG2E, e.st[1]);
    if constexpr (h == GA(4)) A_MUL(e.fa, e.ka[0], e.ft);
    if constexpr (h == GB(4)) { A_SEL(e.a, e.sc[0], e.sc[1], e.modd); A_SEL(e.t0, e.sp[0], e.sp[1], e.modd); }
    if constexpr (h == GC(4)) { A_SEL(e.b, e.sc[2], e.sc[3], e.modd); A_SEL(e.t1, e.sp[2], e.sp[3], e.modd); }
    if constexpr (h == GB(5)) { A_SEL(e.a, e.t0, e.a, e.mA); A_SEL(e.b, e.t1, e.b, e.mB); }
    if constexpr (h == GC(5)) { A_MUL(e.fb, e.kb2[0], e.ft); A_FMA(e.a, e.a, e.fa, e.ka[1]); }
    if constexpr (h == GB(6)) {
      asm volatile("v_fma_f32 %0, %0, %3, %4\n\tv_max_f32 %1, %2, %0" : "+v"(e.b), "=&v"(e.m) : "v"(e.a), "v"(e.fb), "v"(e.kb2[1]));
    }
    if constexpr (h == GA(7)) A_MAX(e.m2, e.a, e.b);
    if constexpr (h == GC(7)) A_SW16(e.m, e.m2);
    if constexpr (h == GB(8)) { float x = e.m, y = e.m2; A_MAX(e.m, x, y); A_MAX(e.m2, x, y); }
    if constexpr (h == GA(9)) A_SW32(e.m, e.m2);
    if constexpr (h == GB(9)) asm volatile("v_max_f32 %0, %0, %2\n\tv_sub_f32 %1, %1, %0" : "+v"(e.m), "+v"(e.a) : "v"(e.m2));
    if constexpr (h == GC(9)) { A_SUB(e.b, e.b, e.m); }
    if constexpr (h == GB(10)) A_EXP(e.a, e.a);
    if constexpr (h == GC(10)) A_EXP(e.b, e.b);
    if constexpr (h == GB(11)) { A_MUL(e.pa, e.a, e.ka[0]); A_MUL(e.pb, e.b, e.kb2[0]); }
    if constexpr (h == GC(11)) { A_CVT(e.pk, e.pa, e.pb); A_CVT(e.pk0, e.a, e.b); }
    if constexpr (h == GB(12)) { A_PERM(e.P[0], e.pk, e.sPA); A_PERM(e.P[1], e.pk, e.sPB); }
    if constexpr (h == GC(12)) { A_PERM(e.P[2], e.pk, e.sCA); A_PERM(e.P[3], e.pk, e.sCB); }
    if constexpr (h == GA(13)) {
      mf16z_a(e.den, e.ones, v2u{e.pk0, e.pk});
      mf32z(e.num, v4u{e.vprev[0], e.vprev[1], e.vb[0], e.vb[1]}, v4u{e.P[0], e.P[1], e.P[2], e.P[3]});
      e.kprev[0] = e.kb[0]; e.kprev[1] = e.kb[1]; e.vprev[0] = e.vb2[0]; e.vprev[1] = e.vb2[1];
    }
    if constexpr (h == GB(14)) A_RCP(e.m, e.den);
    if constexpr (h == GC(14)) { A_MUL(e.q[0], e.num[0], e.m); A_MUL(e.q[1], e.num[1], e.m); }
    if constexpr (h == GA(15)) A_MUL(e.q[2], e.num[2], e.m);
    if constexpr (h == GB(15)) { A_MUL(e.q[3], e.num[3], e.m); A_CVT(e.ob[0], e.q[0], e.q[1]); }
    if constexpr (h == GC(15)) { A_CVT(e.ob[1], e.q[2], e.q[3]); st64(ctx, v2u{e.ob[0], e.ob[1]}, e.ovo, oso); }
  };
  auto x_sh = [&](auto TT, auto h) {
    if constexpr (h == GB(1)) dma_so<(TT + 1) * 16384>(so, xs);
    if constexpr (h == GA(2)) dma_m0<L_XT + (TT + 1) * 16384>(ctx.Lw);
    return h == GB(1) || h == GA(2);
  };
  auto x_gp = [&](auto h) {
    if constexpr (h == GA(2)) dma_go_sc1<0>(lv, ctx.rs, so);
    if constexpr (h == GA(3)) dma_go_sc1<1>(lv, ctx.rs, so);
    if constexpr (h == GA(5)) dma_go_sc1<2>(lv, ctx.rs, so);
    if constexpr (h == GA(6)) dma_go_sc1<3>(lv, ctx.rs, so);
  };
  auto w_sh = [&](auto J, auto N, auto h) {
    if constexpr (h == GA(7)) WAIT_VMV(N.value);
    if constexpr (h == GB(7)) RAWBAR();
    if constexpr (h == GB(9)) dma_so<128 * J * 1024>(so, wd.sb);
    if constexpr (h == GA(10)) dma_m0<L_XT + J * 16384>(ctx.Lw);
    if constexpr (h == GB(13) && J > 0) dma_so<(J - 1) * 16384>(oso, osb);
    return h == GA(7) || h == GB(7) || h == GB(9) || h == GA(10) || (h == GB(13) && J > 0);
  };
  auto w_gp = [&](auto h) {
    if constexpr (h == GA(10)) dma_go<0>(wd.v[0], rwo, so);
    if constexpr (h == GA(11)) dma_go<1>(wd.v[1], rwo, so);
    if constexpr (h == GA(12)) dma_go<2>(wd.v[2], rwo, so);
    if constexpr (h == GA(14)) dma_go<3>(wd.v[3], rwo, so);
  };
  TS(0);
  gemm_tile<-1, P_BK, P_BV, 0, false>(ab0, xq, xk, xv,
    [](auto ks) { return IC<(ks == 10 ? 2 : ks == 11 ? -1 : (ks == 12 || ks == 13 || ks == 14) ? 2 : 0)>{}; },
    [&](auto h) {
      if constexpr (h == GA(0)) dma_so<2 * 16384>(so, xs);
      if constexpr (h == GA(1)) dma_m0<L_XT + 2 * 16384>(ctx.Lw);
      if constexpr (h == GA(2)) WAIT_VMV(5);
      if constexpr (h == GA(11)) WAIT_VMV(4);
      if constexpr (h == GA(12)) RAWBAR();
      return h == GA(0) || h == GA(1) || h == GA(2) || h == GA(11) || h == GA(12);
    },
    [&](auto h) {
      (void)e;
      if constexpr (h == GA(1)) dma_go_sc1<0>(lv, ctx.rs, so);
      if constexpr (h == GA(4)) dma_go_sc1<1>(lv, ctx.rs, so);
      if constexpr (h == GA(7)) dma_go_sc1<2>(lv, ctx.rs, so);
      if constexpr (h == GC(10)) dma_go_sc1<3>(lv, ctx.rs, so);
      st_gaps<h>(ctx, st);
      if constexpr (h == GA(12)) { A_LD64(e.nst, e.aT, 0); A_LD128(e.nnm4, e.aF, 0); }
    });
  TS(1);
  gemm_tile<P_BQ, P_BK, P_BV, 16384, false>(ab0, yq, yk, yv, xl,
    [&](auto h) { return x_sh(IC<2>{}, h) | w_sh(IC<0>{}, IC<4>{}, h); },
    [&](auto h) {
      (void)e; (void)xk; (void)xv;
      if constexpr (h == GB(1)) { A_FMA(e.k[0], e.nst[0], e.csk[0], xk[0]); A_FMA(e.k[1], e.nst[0], e.csk[1], xk[1]); }
      if constexpr (h == GC(1)) { A_FMA(e.k[2], e.nst[0], e.csk[2], xk[2]); A_FMA(e.k[3], e.nst[0], e.csk[3], xk[3]); }
      if constexpr (h == GB(2)) { A_FMA(e.v[0], e.nnm4[0], e.csv, xv[0]); A_FMA(e.v[1], e.nnm4[1], e.csv, xv[1]); }
      if constexpr (h == GC(2)) { A_FMA(e.v[2], e.nnm4[2], e.csv, xv[2]); A_FMA(e.v[3], e.nnm4[3], e.csv, xv[3]); }
      if constexpr (h == GB(3)) { A_CVT(e.kprev[0], e.k[0], e.k[1]); A_CVT(e.kprev[1], e.k[2], e.k[3]); }
      if constexpr (h == GC(3)) { A_CVT(e.vprev[0], e.v[0], e.v[1]); A_CVT(e.vprev[1], e.v[2], e.v[3]); }
      if constexpr (h == GC(15)) NOP(1);
      x_gp(h); stat_rd(IC<1>{}, h); w_gp(h);
    });
  next(yq, yk, yv);
  TS(2);
  gemm_tile<P_BQ, P_BK, P_BV, 32768, false>(ab0, xq, xk, xv, xl,
    [&](auto h) { return x_sh(IC<3>{}, h) | w_sh(IC<1>{}, IC<8>{}, h); },
    [&](auto h) { x_gp(h); stat_rd(IC<2>{}, h); w_gp(h); epi(h); });
  next(xq, xk, xv);
  TS(3);
  gemm_tile<P_BQ, P_BK, P_BV, 0, false>(ab1, yq, yk, yv, xl,
    [&](auto h) { return w_sh(IC<2>{}, IC<5>{}, h); },
    [&](auto h) { stat_rd(IC<3>{}, h); w_gp(h); epi(h); });
  next(yq, yk, yv);
  TS(4);
  gemm_tile<P_BQ, P_BK, P_BV, 16384, false>(ab1, xq, xk, xv, xl,
    [&](auto h) { return w_sh(IC<3>{}, IC<63>{}, h); },
    [&](auto h) { stat_rd(IC<4>{}, h); w_gp(h); epi(h); });
  next(xq, xk, xv);
  TS(5);
  {
    dma_so<3 * 16384>(oso, osb);
    A_MULS(e.ft, 0.25f * LOG2E, e.st[1]);
    A_FMA(e.q[0], e.st[0], e.csq[0], e.aq[0]); A_FMA(e.q[1], e.st[0], e.csq[1], e.aq[1]);
    A_FMA(e.q[2], e.st[0], e.csq[2], e.aq[2]); A_FMA(e.q[3], e.st[0], e.csq[3], e.aq[3]);
    A_FMA(e.k[0], e.st[0], e.csk[0], e.ak[0]); A_FMA(e.k[1], e.st[0], e.csk[1], e.ak[1]);
    A_CVT(e.qb[0], e.q[0], e.q[1]); A_CVT(e.qb[1], e.q[2], e.q[3]);
    A_FMA(e.k[2], e.st[0], e.csk[2], e.ak[2]); A_FMA(e.k[3], e.st[0], e.csk[3], e.ak[3]);
    mf16z(e.sp, v2u{e.kprev[0], e.kprev[1]}, v2u{e.qb[0], e.qb[1]});
    A_CVT(e.kb[0], e.k[0], e.k[1]); A_CVT(e.kb[1], e.k[2], e.k[3]);
    A_MUL(e.fa, e.ka[0], e.ft); A_MUL(e.fb, e.kb2[0], e.ft);
    mf16z(e.sc, v2u{e.kb[0], e.kb[1]}, v2u{e.qb[0], e.qb[1]});
    A_FMA(e.v[0], e.nm4[0], e.csv, e.av[0]); A_FMA(e.v[1], e.nm4[1], e.csv, e.av[1]);
    A_FMA(e.v[2], e.nm4[2], e.csv, e.av[2]); A_FMA(e.v[3], e.nm4[3], e.csv, e.av[3]);
    A_SEL(e.t0, e.sp[0], e.sp[1], e.modd); A_SEL(e.t1, e.sp[2], e.sp[3], e.modd);
    A_CVT(e.vb[0], e.v[0], e.v[1]); A_CVT(e.vb[1], e.v[2], e.v[3]);
    asm volatile("v_cndmask_b32 %0, %4, %5, %10\n\tv_cndmask_b32 %1, %6, %7, %10\n\t"
                 "v_cndmask_b32 %0, %8, %0, %11\n\tv_cndmask_b32 %1, %9, %1, %12\n\t"
                 "v_fma_f32 %0, %0, %13, %15\n\tv_fma_f32 %1, %1, %14, %16\n\t"
                 "v_max_f32 %2, %0, %1\n\tv_max_f32 %3, %0, %1\n\ts_nop 1\n\t"
                 "v_permlane16_swap_b32 %2, %3\n\tv_max_f32 %2, %2, %3\n\tv_mov_b32 %3, %2\n\ts_nop 1\n\t"
                 "v_permlane32_swap_b32 %2, %3\n\tv_max_f32 %2, %2, %3\n\tv_sub_f32 %0, %0, %2\n\tv_sub_f32 %1, %1, %2\n\t"
                 "v_exp_f32 %0, %0\n\tv_exp_f32 %1, %1"
                 : "=&v"(e.a), "=&v"(e.b), "=&v"(e.m), "=&v"(e.m2)
                 : "v"(e.sc[0]), "v"(e.sc[1]), "v"(e.sc[2]), "v"(e.sc[3]), "v"(e.t0), "v"(e.t1), "s"(e.modd), "s"(e.mA), "s"(e.mB),
                   "v"(e.fa), "v"(e.fb), "v"(e.ka[1]), "v"(e.kb2[1]));
    asm volatile("v_mul_f32 %4, %7, %9\n\tv_mul_f32 %5, %8, %10\n\tv_cvt_pk_bf16_f32 %6, %7, %8\n\tv_cvt_pk_bf16_f32 %4, %4, %5\n\t"
                 "v_perm_b32 %0, %4, %4, %11\n\tv_perm_b32 %1, %4, %4, %12\n\tv_perm_b32 %2, %4, %4, %13\n\tv_perm_b32 %3, %4, %4, %14"
                 : "=&v"(e.P[0]), "=&v"(e.P[1]), "=&v"(e.P[2]), "=&v"(e.P[3]), "=&v"(e.pk), "=&v"(e.pb), "=&v"(e.pk0)
                 : "v"(e.a), "v"(e.b), "v"(e.ka[0]), "v"(e.kb2[0]), "v"(e.sPA), "v"(e.sPB), "v"(e.sCA), "v"(e.sCB));
    mf16z_a(e.den, e.ones, v2u{e.pk0, e.pk});
    NOP(0);
    mf32z(e.num, v4u{e.vprev[0], e.vprev[1], e.vb[0], e.vb[1]}, v4u{e.P[0], e.P[1], e.P[2], e.P[3]});
    NOP(5);
    asm volatile("v_rcp_f32 %2, %7\n\ts_nop 0\n\t"
                 "v_mul_f32 %3, %8, %2\n\tv_mul_f32 %4, %9, %2\n\tv_mul_f32 %5, %10, %2\n\tv_mul_f32 %6, %11, %2\n\t"
                 "v_cvt_pk_bf16_f32 %0, %3, %4\n\tv_cvt_pk_bf16_f32 %1, %5, %6"
                 : "=&v"(e.ob[0]), "=&v"(e.ob[1]), "=&v"(e.m), "=&v"(e.q[0]), "=&v"(e.q[1]), "=&v"(e.q[2]), "=&v"(e.q[3])
                 : "v"(e.den[0]), "v"(e.num[0]), "v"(e.num[1]), "v"(e.num[2]), "v"(e.num[3]), "v"(e.den));
    st64(ctx, v2u{e.ob[0], e.ob[1]}, e.ovo, oso);
  }
  TS(6);
  STAMP(L * 8 + 4);
  TS_FLUSH(L * 8 + 2, 7);
  DRAIN();
  BAR();
  flag_store(ctx, WS_FO + (32 + c) * 128);
}

struct FM {
  f4 p, sp, sn;
  float ep[4], en[4], m, m2, c;
  unsigned eb[2], nb[2];
};
#define FM_MM1(f) asm volatile("v_max3_f32 %0, %2, %3, %4\n\tv_max3_f32 %1, -%2, -%3, -%4" : "=&v"(f.m), "=&v"(f.m2) : "v"(f.p[0]), "v"(f.p[1]), "v"(f.p[2]))
#define FM_MM2(f) asm volatile("v_max_f32 %0, %0, %2\n\tv_max_f32 %1, %1, -%2" : "+v"(f.m), "+v"(f.m2) : "v"(f.p[3]))
#define FM_MX2(f) do { float x_ = f.m, y_ = f.m2; A_MAX(f.m, x_, y_); A_MAX(f.m2, x_, y_); } while (0)
#define FM_MXC(f) asm volatile("v_max_f32 %0, %0, %1\n\tv_mul_f32 %1, %0, -%2\n\tv_mul_f32 %0, %0, -%2" : "+v"(f.m), "+v"(f.m2) : "v"(f.c))

struct AE {
  f4 ak, av;
  f2 st, nst; f4 nm4, rs4, nnm4, nrs4;
  f4 csk; float csv;
  float k[4], v[4];
  unsigned kb[2], vb[2], vbo[2], php[2], phm[2];
  v2u wphi, ident, ones, lo1, hi1;
  f4 tp, tm, kvp, kvm, zz;
  FM f;
  unsigned aT, aF;
};

DEV void layer_A(Ctx& ctx, int lp, v2u wphi, rsrc_t rwo, rsrc_t rsa, rsrc_t rsb) {
  fresh(ctx);
  const int L = 2 * lp;
  const int c = ctx.c, m = ctx.m, w = ctx.w, lane = ctx.lane, g = ctx.g, tok = ctx.tok;
  const int csb = L_CS + (w * 6 + 0) * 64;
  const unsigned lv = lane * 16;
  const unsigned T = ctx.T;
  AE e;
  TS_DECL;
  unsigned ab0, ab1;
  ImgDma wd = img_prep<1024>(ctx), qd = img_prep<3072>(ctx);
  {
    const int d = tok - 4 * g;
    e.ident = v2u{d == 0 ? 0x3f80u : d == 1 ? 0x3f800000u : 0u, d == 2 ? 0x3f80u : d == 3 ? 0x3f800000u : 0u};
    const unsigned lo = tok < 8 ? ONE2 : 0u, hi = tok < 8 ? 0u : ONE2;
    e.lo1 = v2u{lo, lo}; e.hi1 = v2u{hi, hi}; e.ones = v2u{mk1(), mk1()}; e.wphi = wphi;
    e.aT = ctx.Lb + L_REC + tok * 16;
    e.aF = ctx.Lb + L_NM + g * 16;
    e.csk = lds_ldf4(ctx.L, csb + 64 + g * 16); e.csv = lds_ldf(ctx.L, csb + 128 + tok * 4);
    ab0 = ctx.Lb + L_XT + (lp == 0 ? x0_lane(tok, g) : lv); ab1 = ab0 + 49152;
    asm volatile("" : "+a"(e.ident), "+a"(e.lo1), "+a"(e.hi1), "+a"(e.ones), "+a"(e.wphi), "+v"(e.csk), "+v"(e.csv), "+v"(e.aT), "+v"(e.aF),
                      "+v"(ab0), "+v"(ab1), "+v"(qd.v[0]), "+v"(qd.v[1]), "+v"(qd.v[2]), "+v"(qd.v[3]));
  }
  const int xs = WS_X + ACT_L + c * 65536 + w * 4096 - 16384;
  f4 xk, xv, yk, yv;
  int so;
  auto stat_rd = [&](auto S, auto h) {
    (void)e;
    if constexpr (h == GA(12)) A_LD64(e.nst, e.aT, S * 256);
    if constexpr (h == GC(12)) { A_LD128(e.nnm4, e.aF, S * 64); A_LD128(e.nrs4, e.aF, S * 64 + 320); }
  };
  auto xl = [](auto ks) { return IC<(ks == 12 ? 1 : (ks == 13 || ks == 14) ? 3 : 0)>{}; };
  auto next = [&](f4& k, f4& v) { e.ak = k; e.av = v; e.st = e.nst; e.nm4 = e.nnm4; e.rs4 = e.nrs4; };
  auto epi = [&](auto h) {
    (void)e;
    if constexpr (h == GC(0)) { A_FMA(e.k[0], e.st[0], e.csk[0], e.ak[0]); A_FMA(e.k[1], e.st[0], e.csk[1], e.ak[1]); }
    if constexpr (h == GA(1)) { A_FMA(e.k[2], e.st[0], e.csk[2], e.ak[2]); A_FMA(e.k[3], e.st[0], e.csk[3], e.ak[3]); }
    if constexpr (h == GC(1)) { A_CVT(e.kb[0], e.k[0], e.k[1]); A_CVT(e.kb[1], e.k[2], e.k[3]); A_FMA(e.v[0], e.nm4[0], e.csv, e.av[0]); }
    if constexpr (h == GA(2)) {
      A_FMA(e.v[1], e.nm4[1], e.csv, e.av[1]);
      mf16z_a(e.f.p, e.wphi, v2u{e.kb[0], e.kb[1]});
      A_FMA(e.v[2], e.nm4[2], e.csv, e.av[2]); A_FMA(e.v[3], e.nm4[3], e.csv, e.av[3]);
    }
    if constexpr (h == GC(2)) { A_MUL(e.v[0], e.v[0], e.rs4[0]); A_MUL(e.v[1], e.v[1], e.rs4[1]); A_MUL(e.v[2], e.v[2], e.rs4[2]); }
    if constexpr (h == GA(3)) { A_MUL(e.v[3], e.v[3], e.rs4[3]); A_MULS(e.f.c, LOG2E, e.st[1]); }
    if constexpr (h == GC(3)) { FM_MM1(e.f); A_CVT(e.vb[0], e.v[0], e.v[1]); }
    if constexpr (h == GA(4)) FM_MM2(e.f);
    if constexpr (h == GC(4)) { A_CVT(e.vb[1], e.v[2], e.v[3]); A_SW16(e.f.m, e.f.m2); }
    if constexpr (h == GA(5)) FM_MX2(e.f);
    if constexpr (h == GC(5)) A_SW32(e.f.m, e.f.m2);
    if constexpr (h == GA(6)) FM_MXC(e.f);
    if constexpr (h == GC(6)) A_SW16(e.f.m, e.f.m2);
    if constexpr (h == GA(7)) { A_FMA(e.f.ep[0], e.f.p[0], e.f.c, e.f.m); A_FMA(e.f.ep[1], e.f.p[1], e.f.c, e.f.m); }
    if constexpr (h == GC(7)) { A_FMA(e.f.ep[2], e.f.p[2], e.f.c, e.f.m); A_FMA(e.f.ep[3], e.f.p[3], e.f.c, e.f.m); A_FMAN(e.f.en[0], e.f.p[0], e.f.c, e.f.m2); }
    if constexpr (h == GA(8)) { A_FMAN(e.f.en[1], e.f.p[1], e.f.c, e.f.m2); A_FMAN(e.f.en[2], e.f.p[2], e.f.c, e.f.m2); }
    if constexpr (h == GC(8)) { A_FMAN(e.f.en[3], e.f.p[3], e.f.c, e.f.m2); A_EXP(e.f.ep[0], e.f.ep[0]); A_EXP(e.f.ep[1], e.f.ep[1]); }
    if constexpr (h == GA(9)) A_EXP(e.f.ep[2], e.f.ep[2]);
    if constexpr (h == GC(9)) { A_EXP(e.f.ep[3], e.f.ep[3]); A_EXP(e.f.en[0], e.f.en[0]); }
    if constexpr (h == GA(10)) A_EXP(e.f.en[1], e.f.en[1]);
    if constexpr (h == GC(10)) { A_EXP(e.f.en[2], e.f.en[2]); A_EXP(e.f.en[3], e.f.en[3]); }
    if constexpr (h == GA(11)) { A_CVT(e.f.eb[0], e.f.ep[0], e.f.ep[1]); A_CVT(e.f.eb[1], e.f.ep[2], e.f.ep[3]); }
    if constexpr (h == GC(11)) {
      A_CVT(e.f.nb[0], e.f.en[0], e.f.en[1]); A_CVT(e.f.nb[1], e.f.en[2], e.f.en[3]);
      mf16z_a(e.f.sp, e.ones, v2u{e.f.eb[0], e.f.eb[1]});
    }
    if constexpr (h == GA(12)) mf16z_a(e.f.sn, e.ones, v2u{e.f.nb[0], e.f.nb[1]});
    if constexpr (h == GA(13)) A_RCP(e.f.m, e.f.sp);
    if constexpr (h == GC(13)) { A_MUL(e.f.ep[0], e.f.ep[0], e.f.m); A_RCP(e.f.m2, e.f.sn); A_MUL(e.f.ep[1], e.f.ep[1], e.f.m); }
    if constexpr (h == GA(14)) { A_MUL(e.f.ep[2], e.f.ep[2], e.f.m); A_MUL(e.f.ep[3], e.f.ep[3], e.f.m); }
    if constexpr (h == GC(14)) { A_MUL(e.f.en[0], e.f.en[0], e.f.m2); A_MUL(e.f.en[1], e.f.en[1], e.f.m2); A_CVT(e.f.eb[0], e.f.ep[0], e.f.ep[1]); }
    if constexpr (h == GA(15)) { A_CVT(e.f.eb[1], e.f.ep[2], e.f.ep[3]); A_MUL(e.f.en[2], e.f.en[2], e.f.m2); }
    if constexpr (h == GC(15)) {
      A_MUL(e.f.en[3], e.f.en[3], e.f.m2); A_CVT(e.f.nb[0], e.f.en[0], e.f.en[1]);
      mf16z_b(e.tp, v2u{e.f.eb[0], e.f.eb[1]}, e.ident);
      A_CVT(e.f.nb[1], e.f.en[2], e.f.en[3]);
      e.vbo[0] = e.vb[0]; e.vbo[1] = e.vb[1];
    }
  };
  auto epi2 = [&](auto FT, auto h) {
    (void)e;
    if constexpr (h == GA(0)) mf16z_b(e.tm, v2u{e.f.nb[0], e.f.nb[1]}, e.ident);
    if constexpr (h == GC(5)) { A_CVT(e.php[0], e.tp[0], e.tp[1]); }
    if constexpr (h == GC(6)) { A_CVT(e.php[1], e.tp[2], e.tp[3]); }
    if constexpr (h == GA(9)) {
      if constexpr (FT) mf16z(e.kvp, v2u{e.php[0], e.php[1]}, v2u{e.vbo[0], e.vbo[1]}); else mf16c(e.kvp, v2u{e.php[0], e.php[1]}, v2u{e.vbo[0], e.vbo[1]});
      A_CVT(e.phm[0], e.tm[0], e.tm[1]); A_CVT(e.phm[1], e.tm[2], e.tm[3]);
      if constexpr (FT) mf16z_a(e.zz, e.lo1, v2u{e.php[0], e.php[1]}); else mf16c_a(e.zz, e.lo1, v2u{e.php[0], e.php[1]});
    }
    if constexpr (h == GA(10)) {
      if constexpr (FT) mf16z(e.kvm, v2u{e.phm[0], e.phm[1]}, v2u{e.vbo[0], e.vbo[1]}); else mf16c(e.kvm, v2u{e.phm[0], e.phm[1]}, v2u{e.vbo[0], e.vbo[1]});
    }
    if constexpr (h == GA(13)) mf16c_a(e.zz, e.hi1, v2u{e.phm[0], e.phm[1]});
  };
  auto c_sh = [&](auto K0, auto SOFF, auto LOFF, int sb, auto h) {
    if constexpr (h == GA(K0 - 1)) dma_so<SOFF>(so, sb);
    if constexpr (h == GA(K0)) dma_m0<LOFF>(ctx.Lw);
    return h == GA(K0 - 1) || h == GA(K0);
  };
  auto c_gp = [&](auto K0, const ImgDma& d, rsrc_t rs, auto h) {
    if constexpr (h == GC(K0)) dma_go<0>(d.v[0], rs, so);
    if constexpr (h == GC(K0 + 2)) dma_go<1>(d.v[1], rs, so);
    if constexpr (h == GC(K0 + 4)) dma_go<2>(d.v[2], rs, so);
    if constexpr (h == GC(K0 + 6)) dma_go<3>(d.v[3], rs, so);
  };
  constexpr int QS = 128 * 3072;
  constexpr int SL = 32 * 256;
  const int kblk = WS_KV1 + ((m * 64 + w * 16) * 32 + c) * 256;
  const int zblk = WS_KVZ + ((m * 4 + w) * 32 + c) * 256;
  int kvo1 = g * 2 * SL + tok * 16, zvo1 = ((g >> 1) * 16 + tok) * 8;
  asm volatile("" : "+v"(kvo1), "+v"(zvo1));
  auto kv_pass = [&](auto FIRST) {
    ST st;
    unsigned rvo = 0; int rsb0 = 0, rso = 0;
    if constexpr (FIRST) {
      rvo = (unsigned)((lane >> 3) * 3072 + (lane & 7) * 16);
      rsb0 = m * 128 + 32 * w * 3072;
      asm volatile("" : "+v"(rvo));
      RAWBAR();
    } else {
      gather_start<false>(ctx, st, L, lv, xs);
    }
    auto r_sh = [&](auto I) { dma_so<1024 + (128 * (I >> 2) + 8 * (I & 3)) * 3072>(rso, rsb0); };
    auto r_gp = [&](auto I) { raw_ld1<P_BK + I>(rvo, rsb, rso); };
    const int pfr = ((blockIdx.x >> 3) * 16 + 4 * w) * 3072;
    ring_prime<16384>(ab0);
    TS(0);
    gemm_tile<-1, P_AK, P_AV, 16384, false>(ab0, xk, xk, xv,
      [&](auto ks) { return IC<(FIRST ? 0 : ks == 10 ? 2 : ks == 11 ? -1 : 0) + decltype(xl(ks))::value>{}; },
      [&](auto h) {
        if constexpr (FIRST) {
          if constexpr (h == GA(0)) dma_so<2 * 3072>(rso, pfr);
          if constexpr (h == GA(6)) dma_so<3 * 3072>(rso, pfr);
          return (h == GA(0) || h == GA(6)) | c_sh(IC<9>{}, IC<0>{}, IC<L_RQ>{}, qd.sb, h);
        } else {
          if constexpr (h == GA(0)) dma_so<3 * 16384>(so, xs);
          if constexpr (h == GA(1)) dma_m0<L_XT + 3 * 16384>(ctx.Lw);
          if constexpr (h == GA(2)) WAIT_VMV(5);
          if constexpr (h == GA(11)) WAIT_VMV(4);
          if constexpr (h == GA(12)) RAWBAR();
          if constexpr (h == GA(13)) dma_so<4 * 16384>(so, xs);
          if constexpr (h == GA(14)) dma_m0<L_XT + 4 * 16384>(ctx.Lw);
          return h == GA(0) || h == GA(1) || h == GA(2) || (h >= GA(11) && h <= GA(14));
        }
      },
      [&](auto h) {
        if constexpr (FIRST) {
          if constexpr ((h & 7) == 2 && h / 4 < 12) pf1<((h / 8) % 3) * 1024>(lv, rsb, rso);
          c_gp(IC<9>{}, qd, rsa, h);
        } else {
          if constexpr (h == GA(1)) dma_go_sc1<0>(lv, ctx.rs, so);
          if constexpr (h == GA(4)) dma_go_sc1<1>(lv, ctx.rs, so);
          if constexpr (h == GA(7)) dma_go_sc1<2>(lv, ctx.rs, so);
          if constexpr (h == GC(10)) dma_go_sc1<3>(lv, ctx.rs, so);
          if constexpr (h == GC(14)) dma_go_sc1<0>(lv, ctx.rs, so);
          st_gaps<h>(ctx, st);
        }
        if constexpr (h == GC(15)) NOP(1);
        stat_rd(IC<1>{}, h);
      });
    next(xk, xv);
    TS(1);
    gemm_tile<-1, P_AK, P_AV, 32768, false>(ab0, yk, yk, yv, xl,
      [&](auto h) {
        if constexpr (FIRST) {
          if constexpr (h == GA(2)) { dma_so<0>(rso, pfr / 3); return true; }
          else return c_sh(IC<1>{}, IC<QS>{}, IC<L_RQ + 16384>{}, qd.sb, h) | c_sh(IC<9>{}, IC<2 * QS>{}, IC<L_RQ + 32768>{}, qd.sb, h);
        } else {
          if constexpr (h == GA(11)) WAIT_VMV(6);
          if constexpr (h == GA(12)) RAWBAR();
          return c_sh(IC<7>{}, IC<0>{}, IC<L_RQ>{}, qd.sb, h) | (h == GA(11) || h == GA(12));
        }
      },
      [&](auto h) {
        if constexpr (FIRST) {
          c_gp(IC<1>{}, qd, rsa, h); c_gp(IC<9>{}, qd, rsa, h);
          if constexpr (h == GC(2) || h == GC(6) || h == GC(10) || h == GC(14)) pf1<((h / 4 - 2) / 4) * 1024>(lv, rwo, rso);
        } else {
          if constexpr (h == GA(0)) dma_go_sc1<1>(lv, ctx.rs, so);
          if constexpr (h == GC(3)) dma_go_sc1<2>(lv, ctx.rs, so);
          if constexpr (h == GC(5)) dma_go_sc1<3>(lv, ctx.rs, so);
          c_gp(IC<7>{}, qd, rsa, h);
        }
        stat_rd(IC<2>{}, h); epi(h);
      });
    next(yk, yv);
    TS(2);
    gemm_tile<-1, P_AK, P_AV, 0, false>(ab1, xk, xk, xv, xl,
      [&](auto h) {
        if constexpr (FIRST) {
          if constexpr (h / 4 >= 8) { r_sh(IC<h / 4 - 8>{}); return true; }
          else return c_sh(IC<1>{}, IC<3 * QS>{}, IC<L_RQ + 49152>{}, qd.sb, h);
        } else {
          if constexpr (h == GA(11)) WAIT_VMV(8);
          if constexpr (h == GA(12)) RAWBAR();
          return c_sh(IC<1>{}, IC<QS>{}, IC<L_RQ + 16384>{}, qd.sb, h) | c_sh(IC<9>{}, IC<2 * QS>{}, IC<L_RQ + 32768>{}, qd.sb, h) | (h == GA(11) || h == GA(12));
        }
      },
      [&](auto h) {
        if constexpr (FIRST) { c_gp(IC<1>{}, qd, rsa, h); if constexpr ((h & 3) == 2 && h / 4 >= 8) r_gp(IC<h / 4 - 8>{}); }
        else { c_gp(IC<1>{}, qd, rsa, h); c_gp(IC<9>{}, qd, rsa, h); }
        stat_rd(IC<3>{}, h); epi2(IC<1>{}, h); epi(h);
      });
    next(xk, xv);
    TS(3);
    gemm_tile<-1, P_AK, P_AV, 16384, false>(ab1, yk, yk, yv, xl,
      [&](auto h) {
        if constexpr (FIRST) {
          if constexpr (h / 4 < 8) { r_sh(IC<8 + h / 4>{}); return true; }
          else return c_sh(IC<9>{}, IC<0>{}, IC<L_XT>{}, wd.sb, h);
        } else {
          return c_sh(IC<1>{}, IC<3 * QS>{}, IC<L_RQ + 49152>{}, qd.sb, h) | c_sh(IC<9>{}, IC<0>{}, IC<L_XT>{}, wd.sb, h);
        }
      },
      [&](auto h) {
        if constexpr (FIRST) { if constexpr ((h & 3) == 2 && h / 4 < 8) r_gp(IC<8 + h / 4>{}); }
        else c_gp(IC<1>{}, qd, rsa, h);
        c_gp(IC<9>{}, wd, rwo, h); stat_rd(IC<4>{}, h); epi2(IC<0>{}, h); epi(h);
      });
    next(yk, yv);
  TS(4);
  {
    FM& f = e.f;
    A_MULS(f.c, LOG2E, e.st[1]);
    NOP(0);
    mf16z_b(e.tm, v2u{f.nb[0], f.nb[1]}, e.ident);
    A_FMA(e.k[0], e.st[0], e.csk[0], e.ak[0]); A_FMA(e.k[1], e.st[0], e.csk[1], e.ak[1]);
    A_FMA(e.k[2], e.st[0], e.csk[2], e.ak[2]); A_FMA(e.k[3], e.st[0], e.csk[3], e.ak[3]);
    A_CVT(e.kb[0], e.k[0], e.k[1]); A_CVT(e.php[0], e.tp[0], e.tp[1]); A_CVT(e.kb[1], e.k[2], e.k[3]); A_CVT(e.php[1], e.tp[2], e.tp[3]);
    A_CVT(e.phm[0], e.tm[0], e.tm[1]);
    mf16z_a(f.p, e.wphi, v2u{e.kb[0], e.kb[1]});
    A_CVT(e.phm[1], e.tm[2], e.tm[3]); A_FMA(e.v[0], e.nm4[0], e.csv, e.av[0]);
    mf16c(e.kvp, v2u{e.php[0], e.php[1]}, v2u{e.vbo[0], e.vbo[1]});
    A_FMA(e.v[1], e.nm4[1], e.csv, e.av[1]); A_FMA(e.v[2], e.nm4[2], e.csv, e.av[2]);
    mf16c(e.kvm, v2u{e.phm[0], e.phm[1]}, v2u{e.vbo[0], e.vbo[1]});
    A_FMA(e.v[3], e.nm4[3], e.csv, e.av[3]); A_MUL(e.v[0], e.v[0], e.rs4[0]);
    FM_MM1(f);
    mf16c_a(e.zz, e.lo1, v2u{e.php[0], e.php[1]});
    FM_MM2(f);
    A_MUL(e.v[1], e.v[1], e.rs4[1]); A_MUL(e.v[2], e.v[2], e.rs4[2]);
    A_SW16(f.m, f.m2);
    mf16c_a(e.zz, e.hi1, v2u{e.phm[0], e.phm[1]});
    FM_MX2(f);
    A_MUL(e.v[3], e.v[3], e.rs4[3]); A_CVT(e.vb[0], e.v[0], e.v[1]);
    A_SW32(f.m, f.m2);
    FM_MXC(f);
    A_CVT(e.vb[1], e.v[2], e.v[3]);
    NOP(0);
    A_SW16(f.m, f.m2);
#pragma unroll
    for (int r = 0; r < 4; ++r) A_FMA(f.ep[r], f.p[r], f.c, f.m);
#pragma unroll
    for (int r = 0; r < 4; ++r) A_FMAN(f.en[r], f.p[r], f.c, f.m2);
#pragma unroll
    for (int r = 0; r < 4; ++r) A_EXP(f.ep[r], f.ep[r]);
    A_EXP(f.en[0], f.en[0]);
    A_CVT(f.eb[0], f.ep[0], f.ep[1]); A_CVT(f.eb[1], f.ep[2], f.ep[3]);
    A_EXP(f.en[1], f.en[1]); A_EXP(f.en[2], f.en[2]);
    mf16z_a(f.sp, e.ones, v2u{f.eb[0], f.eb[1]});
    A_EXP(f.en[3], f.en[3]);
    A_CVT(f.nb[0], f.en[0], f.en[1]); A_CVT(f.nb[1], f.en[2], f.en[3]);
    NOP(1);
    mf16z_a(f.sn, e.ones, v2u{f.nb[0], f.nb[1]});
    NOP(1);
    A_RCP(f.m, f.sp);
    NOP(0);
#pragma unroll
    for (int r = 0; r < 4; ++r) A_MUL(f.ep[r], f.ep[r], f.m);
    A_RCP(f.m2, f.sn);
    A_CVT(f.eb[0], f.ep[0], f.ep[1]); A_CVT(f.eb[1], f.ep[2], f.ep[3]);
    A_MUL(f.en[0], f.en[0], f.m2); A_MUL(f.en[1], f.en[1], f.m2);
    mf16z_b(e.tp, v2u{f.eb[0], f.eb[1]}, e.ident);
    A_MUL(f.en[2], f.en[2], f.m2); A_MUL(f.en[3], f.en[3], f.m2);
    A_CVT(f.nb[0], f.en[0], f.en[1]); A_CVT(f.nb[1], f.en[2], f.en[3]);
    NOP(1);
    mf16z_b(e.tm, v2u{f.nb[0], f.nb[1]}, e.ident);
    NOP(0);
    A_CVT(e.php[0], e.tp[0], e.tp[1]); A_CVT(e.php[1], e.tp[2], e.tp[3]);
    NOP(1);
    mf16c(e.kvp, v2u{e.php[0], e.php[1]}, v2u{e.vb[0], e.vb[1]});
    mf16c_a(e.zz, e.lo1, v2u{e.php[0], e.php[1]});
    NOP(0);
    A_CVT(e.phm[0], e.tm[0], e.tm[1]); A_CVT(e.phm[1], e.tm[2], e.tm[3]);
    NOP(1);
    mf16c(e.kvm, v2u{e.phm[0], e.phm[1]}, v2u{e.vb[0], e.vb[1]});
    mf16c_a(e.zz, e.hi1, v2u{e.phm[0], e.phm[1]});
    asm volatile("s_nop 0" : "+v"(e.kvp));
  }
  TS(5);
  {
    const int blk = kblk, vo = kvo1;
    st128(ctx, v4u{asu(e.kvp[0]), T, asu(e.kvp[1]), T}, vo, blk);
    st128(ctx, v4u{asu(e.kvp[2]), T, asu(e.kvp[3]), T}, vo, blk + SL);
    asm volatile("s_nop 3" : "+v"(e.kvm), "+v"(e.zz));
    st128(ctx, v4u{asu(e.kvm[0]), T, asu(e.kvm[1]), T}, vo, blk + 8 * SL);
    st128(ctx, v4u{asu(e.kvm[2]), T, asu(e.kvm[3]), T}, vo, blk + 9 * SL);
    st64(ctx, v2u{asu(e.zz[0]), T}, zvo1, zblk);
  }
  };
  if (lp == 0) kv_pass(std::true_type{}); else kv_pass(std::false_type{});
  STAMP(L * 8 + 2);
  TS_FLUSH(L * 8 + 2, 6);
  f4 aq[4];
  v4u dk[4], dz;
  const int S64 = 2 * c + (w >> 1);
  const int rblk = WS_KV1 + ((m * 64 + S64) * 32) * 256 + (w & 1) * 128;
  const int rzb = WS_KVZ + ((m * 4 + (S64 >> 4)) * 32) * 256 + (S64 & 15) * 16;
  int kvo = (lane >> 3) * 256 + (lane & 7) * 16, zvo = (lane & 31) * 256;
  {
    lds_t* qt = ctx.L + opq(L_RQ + tr_base(lane, w));
    asm volatile("" : "+v"(kvo), "+v"(zvo));
    TS(0);
    WAIT_VM(8);
    RAWBAR();
    TS(1);
    v4u wq[16];
#pragma unroll
    for (int ks = 0; ks < 16; ++ks) wq[ks] = tr_frag(qt, ks * 4096);
    if (lp == 0) {
      f4 c0, c1;
      v4u ones = {mk1(), mk1(), mk1(), mk1()};
      asm volatile("s_nop 1" : "+v"(ones));
      sfor<0, 8>([&](auto k) { mf_av<k == 0>(c0, wq[2 * k], ones); mf_av<k == 0>(c1, wq[2 * k + 1], ones); });
      asm volatile("s_nop 7" : "+v"(c0), "+v"(c1));
      if (tok == 0) *(LAS f4*)(ctx.L + csb + g * 16) = c0 + c1;
    }
    v4u a1[4];
    auto rd = [&](auto k) {
      (void)a1;
      if constexpr (k < 16) { ring_rd<k & 3, 16384 + k * 1024>(ab0); } else if constexpr (k < 32) { ring_rd<k & 3, (k - 16) * 1024>(ab1); }
    };
    auto rd1 = [&](auto k) {
      (void)a1; (void)ab0; (void)ab1;
      if constexpr (k < 16) { A_LD128(a1[k & 3], ab0, 32768 + k * 1024); } else if constexpr (k < 32) { A_LD128(a1[k & 3], ab1, 16384 + (k - 16) * 1024); }
    };
    sfor<0, 3>([&](auto k) { rd(k); rd1(k); });
#pragma unroll
    for (int ks = 0; ks < 16; ++ks) asm volatile("" : "+a"(wq[ks]));
    WAIT_LG(4);
    TS(2);
    sfor<0, 32>([&](auto k) {
      constexpr int ks = k & 15, u = k >> 4;
      (void)dk; (void)dz; (void)kvo; (void)zvo; (void)rblk; (void)rzb;
      mmq<k & 3, ks == 0>(aq[2 * u], wq[ks]);
      VNOP();
      rd(IC<k + 3>{});
      if constexpr (k >= QPOLL && k < QPOLL + 4) {
        constexpr int i = k - QPOLL;
        asm volatile("buffer_load_dwordx4 %0, %1, %2, %3 offen offset:%4 sc1" : "=v"(dk[i]) : "v"(kvo), "s"(ctx.rs), "s"(i < 2 ? rblk : rblk + 4096), "n"((i & 1) * 2048) : "memory");
      }
      if constexpr (k == QPOLL + 4) asm volatile("buffer_load_dwordx4 %0, %1, %2, %3 offen sc1" : "=v"(dz) : "v"(zvo), "s"(ctx.rs), "s"(rzb) : "memory");
      mf_av<ks == 0>(aq[2 * u + 1], wq[ks], a1[k & 3]);
      WAIT_LG(k + 3 < 32 ? 3 : k == 29 ? 2 : 0);
      rd1(IC<k + 3>{});
      SB();
    });
  }
  TS(6);
  TS_FLUSH(34 + lp * 8, 7);
  STAMP(48 + lp * 4);
  v4u phq[4], d2[4];
  const int b2 = WS_KV2 + (m * 64 + w * 16) * 256;
  int vo2 = g * 512 + tok * 16;
  {
    const f4 csq = lds_ldf4(ctx.L, csb + g * 16);
    f2 st[4];
#pragma unroll
    for (int t = 0; t < 4; ++t) st[t] = *(LAS f2*)(ctx.L + L_REC + ((1 + t) * 16 + tok) * 16);
    int svo = (lane & 7) * 16 + ((lane >> 4) & 1) * 8;
    const u64 mhi = __ballot(lane >= 32);
    const int sblk = WS_KV2 + (m * 64 + S64) * 256 + (w & 1) * 128;
    asm volatile("s_nop 3" : "+v"(aq[0]), "+v"(aq[1]), "+v"(aq[2]), "+v"(aq[3]), "+v"(svo), "+v"(vo2));
    FM f[4];
    unsigned qb[4][2];
#pragma unroll
    for (int t = 0; t < 4; ++t) {
      float q[4];
#pragma unroll
      for (int r = 0; r < 4; ++r) A_FMA(q[r], st[t][0], csq[r], aq[t][r]);
      A_MULS(f[t].c, LOG2E, st[t][1]);
      VNOP();
      A_CVT(qb[t][0], q[0], q[1]); A_CVT(qb[t][1], q[2], q[3]);
      if (t > 0) mf16z_a(f[t - 1].p, e.wphi, v2u{qb[t - 1][0], qb[t - 1][1]});
    }
    FM_MM1(f[0]);
    mf16z_a(f[3].p, e.wphi, v2u{qb[3][0], qb[3][1]});
    FM_MM1(f[1]); FM_MM2(f[0]); FM_MM2(f[1]); FM_MM1(f[2]);
    A_SW16(f[0].m, f[0].m2); FM_MM2(f[2]); FM_MM1(f[3]); A_SW16(f[1].m, f[1].m2); FM_MM2(f[3]);
    FM_MX2(f[0]); A_SW16(f[2].m, f[2].m2); FM_MX2(f[1]); A_SW16(f[3].m, f[3].m2);
    A_SW32(f[0].m, f[0].m2); FM_MX2(f[2]); A_SW32(f[1].m, f[1].m2); FM_MX2(f[3]);
    FM_MXC(f[0]); A_SW32(f[2].m, f[2].m2); FM_MXC(f[1]); A_SW32(f[3].m, f[3].m2);
    A_SW16(f[0].m, f[0].m2); FM_MXC(f[2]); A_SW16(f[1].m, f[1].m2); FM_MXC(f[3]);
    {
      asm volatile("s_waitcnt vmcnt(0)" : "+v"(dk[0]), "+v"(dk[1]), "+v"(dk[2]), "+v"(dk[3]), "+v"(dz) :: "memory");
      STAMP(49 + lp * 4);
      for (;;) {
        unsigned bad;
        asm volatile("v_sad_u32 %0, %1, %11, 0 clamp\n\tv_sad_u32 %0, %2, %11, %0 clamp\n\tv_sad_u32 %0, %3, %11, %0 clamp\n\tv_sad_u32 %0, %4, %11, %0 clamp\n\t"
                     "v_sad_u32 %0, %5, %11, %0 clamp\n\tv_sad_u32 %0, %6, %11, %0 clamp\n\tv_sad_u32 %0, %7, %11, %0 clamp\n\tv_sad_u32 %0, %8, %11, %0 clamp\n\t"
                     "v_sad_u32 %0, %9, %11, %0 clamp\n\tv_sad_u32 %0, %10, %11, %0 clamp"
                     : "=&v"(bad) : "v"(dk[0].y), "v"(dk[0].w), "v"(dk[1].y), "v"(dk[1].w), "v"(dk[2].y), "v"(dk[2].w), "v"(dk[3].y), "v"(dk[3].w),
                       "v"(dz.y), "v"(dz.w), "s"(T));
        if (__builtin_expect(__all(bad == 0), 1) || --ctx.budget < 0) break;
        asm volatile("" ::: "memory");
#pragma unroll
        for (int i = 0; i < 4; ++i) dk[i] = ld128(ctx, kvo, rblk + i * 2048);
        dz = ld128(ctx, zvo, rzb);
      }
      float K, K1, Z; unsigned pk, tg;
      asm volatile("v_add_f32 %0, %5, %6\n\tv_add_f32 %1, %9, %10\n\tv_add_f32 %2, %7, %8\n\tv_add_f32 %3, %11, %12\n\t"
                   "v_add_f32 %0, %0, %2\n\tv_add_f32 %1, %1, %3\n\tv_cndmask_b32 %2, %13, %14, %15\n\tv_mov_b32 %4, %16\n\t"
                   "v_permlane32_swap_b32 %0, %1\n\tv_add_f32 %0, %0, %1\n\ts_nop 1\n\t"
                   "v_permlane16_swap_b32 %0, %2\n\tv_add_f32 %0, %0, %2\n\ts_nop 1\n\t"
                   "v_add_f32_dpp %0, %0, %0 row_ror:8 row_mask:0xf bank_mask:0xf\n\ts_nop 1\n\t"
                   "v_add_f32_dpp %0, %0, %0 row_ror:4 row_mask:0xa bank_mask:0xf\n\ts_nop 1\n\t"
                   "v_add_f32_dpp %0, %0, %0 row_ror:2 row_mask:0xa bank_mask:0xf\n\ts_nop 1\n\t"
                   "v_add_f32_dpp %0, %0, %0 row_ror:1 row_mask:0xa bank_mask:0xf\n\t"
                   "v_mov_b32 %1, %0\n\ts_nop 1\n\tv_permlane32_swap_b32 %0, %1\n\tv_cvt_pk_bf16_f32 %3, %0, %1"
                   : "=&v"(K), "=&v"(K1), "=&v"(Z), "=&v"(pk), "=&v"(tg)
                   : "v"(dk[0].x), "v"(dk[1].x), "v"(dk[2].x), "v"(dk[3].x), "v"(dk[0].z), "v"(dk[1].z), "v"(dk[2].z), "v"(dk[3].z),
                     "v"(dz.x), "v"(dz.z), "s"(mhi), "s"(T));
      st64(ctx, v2u{pk, tg}, svo, sblk);
      STAMP(L * 8 + 3);
    }
    RAWBAR();
    sfor<0, 4>([&](auto t) {
      (void)f; (void)e;
      auto piece = [&](auto p) {
        if constexpr ((p & 3) == 0) { dma_so<128 * 1024 * (1 + (p >> 2))>(so, wd.sb); dma_m0<L_XT + 16384 * (1 + (p >> 2))>(ctx.Lw); VNOP(); }
        dma_go<p & 3>(wd.v[p & 3], rwo, so);
      };
      piece(IC<3 * t>{});
#pragma unroll
      for (int r = 0; r < 4; ++r) A_FMA(f[t].ep[r], f[t].p[r], f[t].c, f[t].m);
      if constexpr (t < 2) A_SW16(f[t + 2].m, f[t + 2].m2);
#pragma unroll
      for (int r = 0; r < 4; ++r) A_FMAN(f[t].en[r], f[t].p[r], f[t].c, f[t].m2);
      if constexpr (t > 0) mf16z_a(f[t - 1].sn, e.ones, v2u{f[t - 1].nb[0], f[t - 1].nb[1]}); else VNOP();
      piece(IC<3 * t + 1>{});
#pragma unroll
      for (int r = 0; r < 4; ++r) A_EXP(f[t].ep[r], f[t].ep[r]);
      A_EXP(f[t].en[0], f[t].en[0]);
      piece(IC<3 * t + 2>{});
      A_CVT(f[t].eb[0], f[t].ep[0], f[t].ep[1]); A_CVT(f[t].eb[1], f[t].ep[2], f[t].ep[3]);
      A_EXP(f[t].en[1], f[t].en[1]); A_EXP(f[t].en[2], f[t].en[2]);
      mf16z_a(f[t].sp, e.ones, v2u{f[t].eb[0], f[t].eb[1]});
      A_EXP(f[t].en[3], f[t].en[3]);
      A_CVT(f[t].nb[0], f[t].en[0], f[t].en[1]); A_CVT(f[t].nb[1], f[t].en[2], f[t].en[3]);
    });
    if (lp == 0) { Ctx c2 = ctx; fresh(c2); part_step_ld<P_BK, P_BV>(c2, L_CS + (w * 6 + 4) * 64, rsb, 2048); }
#pragma unroll
    for (int t = 0; t < 4; ++t) {
      A_RCP(f[t].m, f[t].sp); A_RCP(f[t].m2, f[t].sn);
      if (t == 0) mf16z_a(f[3].sn, e.ones, v2u{f[3].nb[0], f[3].nb[1]});
      if (t == S2POLL) {
        asm volatile("buffer_load_dwordx4 %0, %4, %5, %6 offen sc1\n\tbuffer_load_dwordx4 %1, %4, %5, %6 offen offset:256 sc1\n\t"
                     "buffer_load_dwordx4 %2, %4, %5, %6 offen offset:2048 sc1\n\tbuffer_load_dwordx4 %3, %4, %5, %6 offen offset:2304 sc1"
                     : "=&v"(d2[0]), "=&v"(d2[1]), "=&v"(d2[2]), "=&v"(d2[3]) : "v"(vo2), "s"(ctx.rs), "s"(b2) : "memory");
      }
#pragma unroll
      for (int r = 0; r < 4; ++r) A_MUL(f[t].ep[r], f[t].ep[r], f[t].m);
#pragma unroll
      for (int r = 0; r < 4; ++r) A_MUL(f[t].en[r], f[t].en[r], f[t].m2);
      unsigned x0, x1, x2, x3;
      A_CVT(x0, f[t].ep[0], f[t].ep[1]); A_CVT(x1, f[t].ep[2], f[t].ep[3]); A_CVT(x2, f[t].en[0], f[t].en[1]); A_CVT(x3, f[t].en[2], f[t].en[3]);
      phq[t] = v4u{x0, x1, x2, x3};
    }
  }
  STAMP(50 + lp * 4);
  {
    int ovo = ((g >> 1) * 16 + tok) * 16 + (g & 1) * 8;
    const int osb = WS_O + (4 * c * 16 + 2 * m + (w >> 1)) * 1024 + (w & 1) * 512;
    asm volatile("s_waitcnt vmcnt(0)" : "+v"(d2[0]), "+v"(d2[1]), "+v"(d2[2]), "+v"(d2[3]), "+v"(ovo) :: "memory");
    for (;;) {
      unsigned bad;
      asm volatile("v_sad_u32 %0, %1, %9, 0 clamp\n\tv_sad_u32 %0, %2, %9, %0 clamp\n\tv_sad_u32 %0, %3, %9, %0 clamp\n\tv_sad_u32 %0, %4, %9, %0 clamp\n\t"
                   "v_sad_u32 %0, %5, %9, %0 clamp\n\tv_sad_u32 %0, %6, %9, %0 clamp\n\tv_sad_u32 %0, %7, %9, %0 clamp\n\tv_sad_u32 %0, %8, %9, %0 clamp"
                   : "=&v"(bad) : "v"(d2[0].y), "v"(d2[0].w), "v"(d2[1].y), "v"(d2[1].w), "v"(d2[2].y), "v"(d2[2].w), "v"(d2[3].y), "v"(d2[3].w), "s"(T));
      if (__builtin_expect(__all(bad == 0), 1) || --ctx.budget < 0) break;
      asm volatile("" ::: "memory");
      d2[0] = ld128(ctx, vo2, b2); d2[1] = ld128(ctx, vo2 + 256, b2); d2[2] = ld128(ctx, vo2 + 2048, b2); d2[3] = ld128(ctx, vo2 + 2304, b2);
    }
    STAMP(L * 8 + 4);
    v4u akv = {d2[0].x, d2[1].x, d2[2].x, d2[3].x}, az = {d2[0].z, d2[1].z, d2[2].z, d2[3].z};
    f4 num[4], den[4];
    asm volatile("s_nop 1" : "+v"(akv), "+v"(az));
#pragma unroll
    for (int t = 0; t < 4; ++t) { mf32z(den[t], az, phq[t]); mf32z(num[t], akv, phq[t]); }
#pragma unroll
    for (int t = 0; t < 4; ++t) {
      float r, q0, q1, q2, q3; unsigned o0, o1;
      asm volatile("v_add_f32 %2, %12, %7\n\tv_rcp_f32 %2, %2\n\ts_nop 0\n\t"
                   "v_mul_f32 %3, %8, %2\n\tv_mul_f32 %4, %9, %2\n\tv_mul_f32 %5, %10, %2\n\tv_mul_f32 %6, %11, %2\n\t"
                   "v_cvt_pk_bf16_f32 %0, %3, %4\n\tv_cvt_pk_bf16_f32 %1, %5, %6"
                   : "=&v"(o0), "=&v"(o1), "=&v"(r), "=&v"(q0), "=&v"(q1), "=&v"(q2), "=&v"(q3)
                   : "v"(den[t][0]), "v"(num[t][0]), "v"(num[t][1]), "v"(num[t][2]), "v"(num[t][3]), "s"(1e-6f), "v"(den[t]));
      st64(ctx, v2u{o0, o1}, ovo, osb + t * 16384);
    }
  }
  DRAIN();
  BAR();
  flag_store(ctx, WS_FO + c * 128);
}

template <bool ODD>
DEV void out_proj(Ctx& ctx, int lp, f4 (&xacc)[4], rsrc_t out, rsrc_t rsb) {
  fresh(ctx);
  const int L = 2 * lp + (ODD ? 1 : 0);
  const bool last = ODD && lp == 1, first = !ODD && lp == 0;
  const int c = ctx.c, m = ctx.m, w = ctx.w, lane = ctx.lane, g = ctx.g, tok = ctx.tok;
  if (first) part_step_ld<P_BV, P_BQ>(ctx, L_CS + (w * 6 + 5) * 64, rsb, 0);
  lds_t* tb[4];
#pragma unroll
  for (int nt = 0; nt < 4; ++nt) tb[nt] = ctx.L + opq(L_XT + tr_base(lane, nt));
  v4u wf[4][4];
#pragma unroll
  for (int i = 0; i < 4; ++i)
#pragma unroll
    for (int nt = 0; nt < 4; ++nt) wf[i][nt] = tr_frag(tb[nt], i * 4096);
  const int G = ((g & 1) << 1) | (g >> 1);
  int xvo = (G * 16 + tok) * 16;
  u64 m1 = __ballot(tok & 1), m2 = __ballot(tok & 2);
  const int osb = WS_O + (4 * c + w) * 16384;
  const int xsb = WS_X + (ODD ? ACT_L : 0) + ((4 * c + w) * 16 + 2 * m) * 1024;
  const int ssb = WS_STAT + (ODD ? STAT_L : 0) + ((c * 8 + m) * 64 + 16 * w) * 16;
  asm volatile("" : "+v"(xvo), "+v"(xacc[0]), "+v"(xacc[1]), "+v"(xacc[2]), "+v"(xacc[3]));
#pragma unroll
  for (int nt = 0; nt < 4; ++nt) asm volatile("" : "+v"(wf[0][nt]), "+v"(wf[1][nt]), "+v"(wf[2][nt]), "+a"(wf[3][nt]));
  wait_flags(ctx, WS_FO + ((ODD ? 32 : 0) + c) * 128, 0, false);
  if (ODD) ctx.T += 1u;
  STAMP(L * 8 + 5);
  v4u of[16];
  constexpr int PRE = 8;
#pragma unroll
  for (int ks = 0; ks < PRE; ++ks) of[ks] = ld128(ctx, lane * 16 + (ks & 3) * 1024, osb + (ks & ~3) * 1024);
  SB();
  sfor<0, 16>([&](auto ks) {
    if constexpr (ks + PRE < 16) { of[ks + PRE] = ld128(ctx, lane * 16 + ((ks + PRE) & 3) * 1024, osb + ((ks + PRE) & ~3) * 1024); SB(); }
#pragma unroll
    for (int nt = 0; nt < 4; ++nt) {
      if constexpr ((ks & 3) < 3) mf_va(xacc[nt], wf[ks & 3][nt], of[ks]); else mf_aa(xacc[nt], wf[ks & 3][nt], of[ks]);
      if constexpr (ks + 4 < 16) wf[ks & 3][nt] = tr_frag(tb[nt], (ks + 4) * 4096);
      SB();
    }
  });
  NOP(4);
  STAMP(L * 8 + 6);
  v4u fr[2];
#pragma unroll
  for (int u = 0; u < 2; ++u) {
    unsigned a0, a1, b0, b1;
    const f4 a = xacc[2 * u], b = xacc[2 * u + 1];
    asm volatile("v_cvt_pk_bf16_f32 %0, %4, %5\n\tv_cvt_pk_bf16_f32 %1, %6, %7\n\tv_cvt_pk_bf16_f32 %2, %8, %9\n\tv_cvt_pk_bf16_f32 %3, %10, %11\n\t"
                 "s_nop 0\n\tv_permlane16_swap_b32 %0, %2\n\tv_permlane16_swap_b32 %1, %3"
                 : "=&v"(a0), "=&v"(a1), "=&v"(b0), "=&v"(b1)
                 : "v"(a[0]), "v"(a[1]), "v"(a[2]), "v"(a[3]), "v"(b[0]), "v"(b[1]), "v"(b[2]), "v"(b[3]));
    fr[u] = v4u{a0, a1, b0, b1};
    if (!last) st128(ctx, fr[u], xvo + u * 1024, xsb);
  }
  if (last) {
#pragma unroll
    for (int u = 0; u < 2; ++u)
      __builtin_amdgcn_raw_buffer_store_b128(fr[u], out, tok * 1024 + G * 16 + u * 64, (64 * c + 16 * w) * 1024 + 128 * m, 0);
  } else {
    f4 ssum, gram;
    v4u ones = {mk1(), mk1(), mk1(), mk1()};
    asm volatile("s_nop 1" : "+v"(ones));
    mf32z(gram, fr[0], fr[0]); mf32z(ssum, ones, fr[0]);
    mf32c(gram, fr[1], fr[1]); mf32c(ssum, ones, fr[1]);
    asm volatile("s_nop 7" : "+v"(gram), "+v"(ssum));
    float q, t;
    asm volatile("v_cndmask_b32 %0, %2, %3, %6\n\tv_cndmask_b32 %1, %4, %5, %6\n\tv_cndmask_b32 %0, %0, %1, %7"
                 : "=&v"(q), "=&v"(t) : "v"(gram[0]), "v"(gram[1]), "v"(gram[2]), "v"(gram[3]), "s"(m1), "s"(m2));
    if (g == (tok >> 2)) st128(ctx, v4u{asu(ssum[0]), ctx.T, asu(q), ctx.T}, tok * 16, ssb);
    WAIT_VM(1);
    BAR();
    flag_store(ctx, WS_FX + ((ODD ? 32 : 0) + c) * 128);
    if (first) part_step<P_BQ>(ctx, L_CS + (w * 6 + 3) * 64);
  }
  asm volatile("" : "+a"(xacc[0]), "+a"(xacc[1]), "+a"(xacc[2]), "+a"(xacc[3]));
  STAMP(L * 8 + 7);
}

__global__ void __launch_bounds__(NT, 1) __attribute__((amdgpu_num_vgpr(88)))
fused_kernel(const unsigned char* __restrict__ x_, const unsigned char* __restrict__ wa_, const unsigned char* __restrict__ wphi_,
             const unsigned char* __restrict__ woa_, const unsigned char* __restrict__ wb_, const unsigned char* __restrict__ wob_,
             unsigned char* out_, unsigned char* ws_) {
  extern __shared__ __attribute__((aligned(16))) unsigned char smem[];
  asm volatile("" ::: "v255", "a255");
  const GAS unsigned char* x = (const GAS unsigned char*)x_;
  Ctx ctx;
  ctx.ws = (GAS unsigned char*)ws_;
  ctx.rs = __builtin_amdgcn_make_buffer_rsrc((void*)ws_, 0, WS_END, 0x00020000);
  ctx.L = (lds_t*)smem;
  ctx.Lb = (unsigned)(size_t)ctx.L;
  const int b = blockIdx.x, idx = b >> 3;
  ctx.c = (b & 7) * 4 + (idx >> 3);
  ctx.m = idx & 7;
  ctx.w = __builtin_amdgcn_readfirstlane(threadIdx.x >> 6);
  ctx.budget = SPIN_CAP;
  fresh(ctx);
  const int c = ctx.c, m = ctx.m, w = ctx.w, lane = ctx.lane, g = ctx.g, tok = ctx.tok;
  STAMP(40); CSTAMP(0);
  const rsrc_t rsa = __builtin_amdgcn_make_buffer_rsrc((void*)wa_, 0, 512 * 3072, 0x00020000);
  const rsrc_t rsb = __builtin_amdgcn_make_buffer_rsrc((void*)wb_, 0, 512 * 3072, 0x00020000);
  const rsrc_t rwa = __builtin_amdgcn_make_buffer_rsrc((void*)woa_, 0, 512 * 1024, 0x00020000);
  const rsrc_t rwb = __builtin_amdgcn_make_buffer_rsrc((void*)wob_, 0, 512 * 1024, 0x00020000);
  {
    u64 pc; unsigned len;
    asm volatile("s_getpc_b64 %0\n.Lfk_here:\n\ts_mov_b32 %1, .Lfk_end-.Lfk_here" : "=s"(pc), "=s"(len));
    const rsrc_t rc = __builtin_amdgcn_make_buffer_rsrc((void*)pc, 0, len, 0x00020000);
    asm volatile("s_nop 4" ::: "memory");
    pf1<0>(lane * 16, rc, (idx * 4 + w) * 1024);
  }
  unsigned epoch, ph0, ph1, ph2, ph3;
  {
    const GAS unsigned char* pp = (const GAS unsigned char*)wphi_ + (4 * m + w) * 512;
    const unsigned pv = (unsigned)(4 * g * 16 + tok) * 2, zero = 0;
    asm volatile("s_nop 4\n\tglobal_load_dword %0, %5, %6 sc1\n\t"
                 "global_load_ushort %1, %7, %8\n\tglobal_load_ushort %2, %7, %8 offset:32\n\t"
                 "global_load_ushort %3, %7, %8 offset:64\n\tglobal_load_ushort %4, %7, %8 offset:96"
                 : "=&v"(epoch), "=&v"(ph0), "=&v"(ph1), "=&v"(ph2), "=&v"(ph3)
                 : "v"(zero), "s"(ctx.ws), "v"(pv), "s"(pp) : "memory");
  }
  const unsigned lv = lane * 16;
  const int pfr = (16 * idx + 4 * w) * 3072;
  sfor<0, 4>([&](auto i) { pf1<2048>(lv, rsa, pfr + i * 3072); });
  {
    const ImgDma kd = img_prep<3072>(ctx);
    sfor<0, 4>([&](auto j) { img_chunk<3072, 1024, L_RQ, j>(ctx, kd, rsa); });
  }
  {
    const GAS unsigned char* sb = x + (size_t)(64 * c + 16 * w) * 1024;
    const unsigned xv = (unsigned)((lane >> 2) * 1024 + (((lane & 3) ^ ((4 - (lane >> 4)) & 3)) << 4));
    const unsigned ldsbase = ctx.Lb + L_XT + (1 + w) * 16384;
#define XD(i) "global_load_lds_dwordx4 %0, %1 offset:" #i "\n\ts_add_u32 m0, m0, 0x3c0\n\ts_nop 0\n\t"
    asm volatile("s_mov_b32 m0, %2\n\ts_nop 0\n\t"
                 XD(0) XD(64) XD(128) XD(192) XD(256) XD(320) XD(384) XD(448)
                 XD(512) XD(576) XD(640) XD(704) XD(768) XD(832) XD(896) XD(960)
                 :: "v"(xv), "s"(sb), "s"(ldsbase) : "memory", "scc");
#undef XD
  }
  STAMP(42);
  asm volatile("s_waitcnt vmcnt(16)" : "+v"(epoch), "+v"(ph0), "+v"(ph1), "+v"(ph2), "+v"(ph3) :: "memory");
  RAWBAR();
  STAMP(43);
  const unsigned rvo = (unsigned)((lane >> 3) * 3072 + (lane & 7) * 16);
  const int rvb = 2048 + m * 128 + 32 * w * 3072;
  const unsigned trb = ctx.Lb + L_RQ + tr_base(lane, w);
  sfor<0, 8>([&](auto i) {
    raw_ld1<P_AV + i>(rvo, rsa, rvb + (128 * (i >> 2) + 8 * (i & 3)) * 3072);
    wtr<P_AK + 2 * i, 2 * i * 4096>(trb); wtr<P_AK + 2 * i + 1, (2 * i + 1) * 4096>(trb);
  });
  ctx.T = __builtin_amdgcn_readfirstlane(epoch) + 1u;
  v2u wphi = v2u{ph0 | (ph1 << 16), ph2 | (ph3 << 16)};
  const int csa = L_CS + (w * 6) * 64;
  unsigned wp0, wp1;
  {
    const int ch = lane & 7, rb1 = (lane >> 4) & 1;
    const unsigned base = ctx.Lb + L_RQ + (32 * w + (lane >> 3)) * 128 + (ch & 1) * 16;
    wp0 = base + (((ch >> 1) ^ rb1) << 5); wp1 = base + (((ch >> 1) ^ (rb1 | 2)) << 5);
  }
  f4 xacc[4];
  WAIT_VM(8);
  STAMP(44);
  {
    lds_t* tb = ctx.L + opq(L_XT + (1 + w) * 16384 + x0_lane(tok, g));
    v4u xs[16];
#pragma unroll
    for (int ks = 0; ks < 16; ++ks) xs[ks] = lds_ld128(tb, ks * 1024);
    v2u xr[4];
#pragma unroll
    for (int nt = 0; nt < 4; ++nt)
      xr[nt] = *(LAS v2u*)(ctx.L + L_XT + (1 + w) * 16384 + (2 * m + (nt >> 1)) * 1024 + x0_lane(tok, 2 * (nt & 1) + (g >> 1)) + (g & 1) * 8);
    v4u ones = {mk1(), mk1(), mk1(), mk1()};
    BAR();
#pragma unroll
    for (int ks = 0; ks < 16; ++ks) asm volatile("" : "+a"(xs[ks]));
    f4 gram = {0.f, 0.f, 0.f, 0.f}, ssum = {0.f, 0.f, 0.f, 0.f};
    asm volatile("s_nop 1" : "+v"(gram), "+v"(ssum), "+v"(ones));
    sfor<0, 16>([&](auto ks) {
      mf_aa(gram, xs[ks], xs[ks]); VNOP();
      if constexpr (ks < 8) raw_ld1<P_AV + 8 + ks>(rvo, rsa, rvb + (128 * ((8 + ks) >> 2) + 8 * (ks & 3)) * 3072);
      if constexpr (ks == 8) WAIT_VM(8);
      if constexpr (ks >= 8) raw_st1<P_AV + ks - 8, ((ks - 8) >> 2) * 16384 + (ks & 3) * 1024>((ks & 1) ? wp1 : wp0);
      mf_va(ssum, ones, xs[ks]); VNOP();
    });
#pragma unroll
    for (int nt = 0; nt < 4; ++nt)
      xacc[nt] = f4{asf(xr[nt].x << 16), asf(xr[nt].x & 0xffff0000u), asf(xr[nt].y << 16), asf(xr[nt].y & 0xffff0000u)};
    asm volatile("" : "+a"(xacc[0]), "+a"(xacc[1]), "+a"(xacc[2]), "+a"(xacc[3]));
    BAR();
    STAMP(45);
    f4 c0, c1;
    sfor<0, 8>([&](auto k) {
      wtr<P_AV + k, k * 4096>(trb);
      wcs<P_AK + 2 * k, k == 0>(c0, ones);
      if constexpr (k < 4) pf1<0>(lv, rsa, pfr + k * 3072);
      wcs<P_AK + 2 * k + 1, k == 0>(c1, ones);
    });
    asm volatile("" : "+v"(gram), "+v"(ssum));
    const float q = (tok & 2) ? ((tok & 1) ? gram[3] : gram[2]) : ((tok & 1) ? gram[1] : gram[0]);
    const float mean = ssum[0] * (1.0f / 512.0f);
    const float var = fmaxf(q * (1.0f / 512.0f) - mean * mean, 0.0f);
    const float rstd = __builtin_amdgcn_rsqf(var + 1e-5f);
    WAIT_VM(4);
    sfor<8, 16>([&](auto i) { raw_st1<P_AV + i, (i >> 2) * 16384 + (i & 3) * 1024>((i & 1) ? wp1 : wp0); });
    if (g == (tok >> 2)) {
      *(LAS f4*)(ctx.L + L_REC + ((1 + w) * 16 + tok) * 16) = f4{-mean, rstd, rstd, 0.f};
      *(LAS float*)(ctx.L + L_NM + ((1 + w) * 16 + tok) * 4) = -mean;
      *(LAS float*)(ctx.L + L_RS + ((1 + w) * 16 + tok) * 4) = rstd;
    }
    asm volatile("" : "+v"(c0), "+v"(c1));
    if (g == 0) *(LAS float*)(ctx.L + csa + 64 + tok * 4) = c0[0] + c1[0];
    BAR();
    STAMP(46);
    sfor<8, 16>([&](auto k) { wtr<P_AV + k, k * 4096>(trb); });
    sfor<0, 8>([&](auto k) {
      if constexpr (k == 4) asm volatile("s_waitcnt lgkmcnt(0)" ::: "memory");
      wcs<P_AV + 2 * k, k == 0>(c0, ones);
      if constexpr (k < 6) pf1<(k % 3) * 1024>(lv, rsb, pfr + (k / 3) * 3072);
      wcs<P_AV + 2 * k + 1, k == 0>(c1, ones);
    });
    asm volatile("s_nop 7" : "+v"(c0), "+v"(c1));
    if (g == 0) *(LAS float*)(ctx.L + csa + 128 + tok * 4) = c0[0] + c1[0];
  }
  asm volatile("" : "+a"(xacc[0]), "+a"(xacc[1]), "+a"(xacc[2]), "+a"(xacc[3]), "+a"(wphi));
  STAMP(1);
  const rsrc_t out = __builtin_amdgcn_make_buffer_rsrc((void*)out_, 0, 2048 * 1024, 0x00020000);

#pragma nounroll
  for (int lp = 0; lp < 2; ++lp) {
    asm volatile("" : "+a"(xacc[0]), "+a"(xacc[1]), "+a"(xacc[2]), "+a"(xacc[3]), "+a"(wphi));
    layer_A(ctx, lp, wphi, rwa, rsa, rsb);
    out_proj<false>(ctx, lp, xacc, out, rsb);
    layer_B(ctx, 2 * lp + 1, rwb);
    out_proj<true>(ctx, lp, xacc, out, rsb);
  }
  asm volatile(".Lfk_end:");
  if (b == 0) { ctx.m = 0; flag_store(ctx, WS_EPOCH); }
  STAMP(41); CSTAMP(1);
  STAMP_FLUSH();
}

extern "C" void kernel_launch(void* const* d_in, const int* in_sizes, int n_in, void* d_out, int out_size, void* d_ws,
                              size_t ws_size, hipStream_t stream) {
  (void)in_sizes; (void)n_in; (void)out_size; (void)ws_size;
  (void)hipFuncSetAttribute((const void*)fused_kernel, hipFuncAttributeMaxDynamicSharedMemorySize, LDS_BYTES);
  hipLaunchKernelGGL(fused_kernel, dim3(NWG), dim3(NT), LDS_BYTES, stream,
                     (const unsigned char*)d_in[0], (const unsigned char*)d_in[1], (const unsigned char*)d_in[2],
                     (const unsigned char*)d_in[3], (const unsigned char*)d_in[5], (const unsigned char*)d_in[6],
                     (unsigned char*)d_out, (unsigned char*)d_ws);
}
```

```cpp
#include <hip/hip_runtime.h>
#include <hip/hip_bf16.h>
#include <type_traits>

#define GAS __attribute__((address_space(1)))
#define LAS __attribute__((address_space(3)))
#define DEV __device__ __forceinline__
typedef short s4 __attribute__((ext_vector_type(4)));
typedef float f4 __attribute__((ext_vector_type(4)));
typedef float f2 __attribute__((ext_vector_type(2)));
typedef __bf16 bf2 __attribute__((ext_vector_type(2)));
typedef unsigned v4u __attribute__((ext_vector_type(4)));
typedef unsigned v2u __attribute__((ext_vector_type(2)));
typedef unsigned long long u64;
typedef GAS unsigned gu32;
typedef LAS unsigned char lds_t;
typedef __amdgpu_buffer_rsrc_t rsrc_t;
#define RLX_AGENT __ATOMIC_RELAXED, __HIP_MEMORY_SCOPE_AGENT

namespace {
constexpr int NWG = 256, NT = 256;
constexpr int WS_EPOCH = 0;
constexpr int WS_FX = 4096;
constexpr int WS_FO = WS_FX + 2 * 32 * 128;
constexpr int WS_KV2 = 1 << 16;
constexpr int WS_KVZ = 1 << 18;
constexpr int WS_STAT = 2 << 18;
constexpr int STAT_L = 32 * 8 * 64 * 16;
constexpr int WS_STAMP = 4 << 18;
constexpr int WS_X = 2 << 20;
constexpr int ACT_L = 2 << 20;
constexpr int WS_O = WS_X + 2 * ACT_L;
constexpr int WS_KV1 = WS_O + ACT_L;
constexpr int WS_END = WS_KV1 + 8 * 64 * 32 * 256;
constexpr int L_XT = 0;
constexpr int L_RQ = 81920;
constexpr int L_REC = 147456 + 256;
constexpr int L_NM = L_REC + 1280;
constexpr int L_RS = L_NM + 320;
constexpr int L_CS = L_RS + 320;
constexpr int L_END = L_CS + 4 * 2 * 3 * 64;
constexpr int LDS_BYTES = 158 * 1024;
static_assert(L_END <= LDS_BYTES, "lds");
constexpr int SPIN_CAP = 1 << 18;
constexpr float LOG2E = 1.4426950408889634f;
#define RB 0
#define PF_U 1
#define PF_B 0
#define PF_KV 0
#define PF_LATE 1
#define PRE0 20
#define QPOLL 6
#define S2POLL 0
}

DEV float asf(unsigned u) { return __builtin_bit_cast(float, u); }
DEV unsigned asu(float f) { return __builtin_bit_cast(unsigned, f); }
DEV unsigned pk2(float a, float b) { unsigned r; asm("v_cvt_pk_bf16_f32 %0, %1, %2" : "=v"(r) : "v"(a), "v"(b)); return r; }
DEV float xg_sum(float v) {
  auto r = __builtin_amdgcn_permlane16_swap(asu(v), asu(v), false, false);
  v = asf(r[0]) + asf(r[1]);
  auto q = __builtin_amdgcn_permlane32_swap(asu(v), asu(v), false, false);
  return asf(q[0]) + asf(q[1]);
}
DEV float row_sum(float v) {
  v += asf(__builtin_amdgcn_update_dpp(0u, asu(v), 0x128, 0xf, 0xf, false));
  v += asf(__builtin_amdgcn_update_dpp(0u, asu(v), 0x124, 0xf, 0xf, false));
  v += asf(__builtin_amdgcn_update_dpp(0u, asu(v), 0x122, 0xf, 0xf, false));
  v += asf(__builtin_amdgcn_update_dpp(0u, asu(v), 0x121, 0xf, 0xf, false));
  return v;
}
DEV float sum32(float v) {
  v = row_sum(v);
  auto r = __builtin_amdgcn_permlane16_swap(asu(v), asu(v), false, false);
  return asf(r[0]) + asf(r[1]);
}
#define BAR() asm volatile("s_waitcnt lgkmcnt(0)\n\ts_barrier" ::: "memory")
#define RAWBAR() do { asm volatile("" ::: "memory"); __builtin_amdgcn_s_barrier(); asm volatile("" ::: "memory"); } while (0)
#define DRAIN() asm volatile("s_waitcnt vmcnt(0)" ::: "memory")
#define WAIT_VM(n) asm volatile("s_waitcnt vmcnt(%0)" :: "n"(n) : "memory")
#define WAIT_LG(n) __builtin_amdgcn_s_waitcnt(0xC07F | ((n) << 8))
#define WAIT_VMV(n) __builtin_amdgcn_s_waitcnt(0x0F70 | ((n) & 15) | (((n) >> 4) << 14))
#define VNOP() __builtin_amdgcn_s_setprio(0)
#define SB() __builtin_amdgcn_sched_barrier(0)
#define NOP(n) asm volatile("s_nop %0" :: "n"(n))
#define SC1 16
#define ONE2 0x3f803f80u
template <int N> using IC = std::integral_constant<int, N>;

DEV unsigned mk1() { unsigned r; asm volatile("v_mov_b32 %0, 0x3f803f80" : "=v"(r)); return r; }
DEV unsigned mk0() { unsigned r; asm volatile("v_mov_b32 %0, 0" : "=v"(r)); return r; }
DEV int opq(int v) { asm volatile("" : "+v"(v)); return v; }
DEV v4u lds_ld128(lds_t* L, int off) { return *(LAS v4u*)(L + off); }
DEV f4 lds_ldf4(lds_t* L, int off) { return *(LAS f4*)(L + off); }
DEV float lds_ldf(lds_t* L, int off) { return *(LAS float*)(L + off); }
DEV v4u tr_frag(lds_t* L, int off) {
  s4 a = __builtin_amdgcn_ds_read_tr16_b64_v4i16((LAS s4*)(L + off));
  s4 b = __builtin_amdgcn_ds_read_tr16_b64_v4i16((LAS s4*)(L + off + 512));
  v2u ua = __builtin_bit_cast(v2u, a), ub = __builtin_bit_cast(v2u, b);
  return v4u{ua.x, ua.y, ub.x, ub.y};
}
template <int I, int N, class F> DEV void sfor(F&& f) { if constexpr (I < N) { f(IC<I>{}); sfor<I + 1, N>(f); } }

constexpr int P_AK = 0, P_AV = 16, P_BQ = 32, P_BK = 48, P_BV = 64;
#define MF32 "v_mfma_f32_16x16x32_bf16 "
#define MF16 "v_mfma_f32_16x16x16_bf16 "
#define MM_CASE(WC, XC)                                                                                                         \
  if constexpr (WA) {                                                                                                           \
    if constexpr (FIRST) asm volatile(MF32 "%0, " WC "[%c1:%c2], " XC "[%c3:%c4], 0" : "=v"(acc) : "n"(R), "n"(R + 3), "n"(X), "n"(X + 3)); \
    else asm volatile(MF32 "%0, " WC "[%c1:%c2], " XC "[%c3:%c4], %0" : "+v"(acc) : "n"(R), "n"(R + 3), "n"(X), "n"(X + 3));     \
  } else {                                                                                                                      \
    if constexpr (FIRST) asm volatile(MF32 "%0, " XC "[%c3:%c4], " WC "[%c1:%c2], 0" : "=v"(acc) : "n"(R), "n"(R + 3), "n"(X), "n"(X + 3)); \
    else asm volatile(MF32 "%0, " XC "[%c3:%c4], " WC "[%c1:%c2], %0" : "+v"(acc) : "n"(R), "n"(R + 3), "n"(X), "n"(X + 3));     \
  }
template <int F, int RS, bool WA, bool FIRST>
DEV void mm(f4& acc) {
  constexpr int R = 96 + 4 * (F < 40 ? F : F - 40), X = 88 + 4 * (RS & 1);
  if constexpr (F < 40) { if constexpr (RS < 2) { MM_CASE("v", "v") } else { MM_CASE("v", "a") } }
  else { if constexpr (RS < 2) { MM_CASE("a", "v") } else { MM_CASE("a", "a") } }
}
template <int RS, int OFF>
DEV void ring_rd(unsigned addr) {
  constexpr int X = 88 + 4 * (RS & 1);
  if constexpr (RS < 2) asm volatile("ds_read_b128 v[%c1:%c2], %0 offset:%c3" :: "v"(addr), "n"(X), "n"(X + 3), "n"(OFF) : "memory");
  else asm volatile("ds_read_b128 a[%c1:%c2], %0 offset:%c3" :: "v"(addr), "n"(X), "n"(X + 3), "n"(OFF) : "memory");
}
template <int F, bool FIRST>
DEV void wcs(f4& acc, v4u x) {
  constexpr int R = 96 + 4 * (F < 40 ? F : F - 40);
  if constexpr (F < 40) {
    if constexpr (FIRST) asm volatile(MF32 "%0, %1, v[%c2:%c3], 0" : "=&v"(acc) : "v"(x), "n"(R), "n"(R + 3));
    else asm volatile(MF32 "%0, %1, v[%c2:%c3], %0" : "+v"(acc) : "v"(x), "n"(R), "n"(R + 3));
  } else {
    if constexpr (FIRST) asm volatile(MF32 "%0, %1, a[%c2:%c3], 0" : "=&v"(acc) : "v"(x), "n"(R), "n"(R + 3));
    else asm volatile(MF32 "%0, %1, a[%c2:%c3], %0" : "+v"(acc) : "v"(x), "n"(R), "n"(R + 3));
  }
}
template <int F, bool FIRST>
DEV void wxa(f4& acc, v4u x) {
  constexpr int R = 96 + 4 * (F < 40 ? F : F - 40);
  static_assert(F < 40, "");
  if constexpr (FIRST) asm volatile(MF32 "%0, v[%c2:%c3], %1, 0" : "=v"(acc) : "a"(x), "n"(R), "n"(R + 3));
  else asm volatile(MF32 "%0, v[%c2:%c3], %1, %0" : "+v"(acc) : "a"(x), "n"(R), "n"(R + 3));
}
template <int F, bool FIRST>
DEV void wxb(f4& acc, v4u x) {
  constexpr int R = 96 + 4 * (F < 40 ? F : F - 40);
  static_assert(F < 40, "");
  if constexpr (FIRST) asm volatile(MF32 "%0, %1, v[%c2:%c3], 0" : "=v"(acc) : "a"(x), "n"(R), "n"(R + 3));
  else asm volatile(MF32 "%0, %1, v[%c2:%c3], %0" : "+v"(acc) : "a"(x), "n"(R), "n"(R + 3));
}
template <bool FIRST>
DEV void mf_aaz(f4& acc, v4u a, v4u b) {
  if constexpr (FIRST) asm volatile(MF32 "%0, %1, %2, 0" : "=v"(acc) : "a"(a), "a"(b));
  else asm volatile(MF32 "%0, %1, %2, %0" : "+v"(acc) : "a"(a), "a"(b));
}
template <int RS, bool FIRST>
DEV void mmq(f4& acc, v4u a) {
  constexpr int X = 88 + 4 * (RS & 1);
  if constexpr (RS < 2) {
    if constexpr (FIRST) asm volatile(MF32 "%0, %1, v[%c2:%c3], 0" : "=v"(acc) : "a"(a), "n"(X), "n"(X + 3));
    else asm volatile(MF32 "%0, %1, v[%c2:%c3], %0" : "+v"(acc) : "a"(a), "n"(X), "n"(X + 3));
  } else {
    if constexpr (FIRST) asm volatile(MF32 "%0, %1, a[%c2:%c3], 0" : "=v"(acc) : "a"(a), "n"(X), "n"(X + 3));
    else asm volatile(MF32 "%0, %1, a[%c2:%c3], %0" : "+v"(acc) : "a"(a), "n"(X), "n"(X + 3));
  }
}
template <bool FIRST>
DEV void mf_av(f4& acc, v4u a, v4u b) {
  if constexpr (FIRST) asm volatile(MF32 "%0, %1, %2, 0" : "=&v"(acc) : "a"(a), "v"(b));
  else asm volatile(MF32 "%0, %1, %2, %0" : "+v"(acc) : "a"(a), "v"(b));
}
DEV void mf_va(f4& acc, v4u a, v4u b) { asm volatile(MF32 "%0, %1, %2, %0" : "+v"(acc) : "v"(a), "a"(b)); }
DEV void mf_aa(f4& acc, v4u a, v4u b) { asm volatile(MF32 "%0, %1, %2, %0" : "+v"(acc) : "a"(a), "a"(b)); }
DEV void mf32z(f4& d, v4u a, v4u b) { asm volatile(MF32 "%0, %1, %2, 0" : "=&v"(d) : "v"(a), "v"(b)); VNOP(); }
DEV void mf32c(f4& d, v4u a, v4u b) { asm volatile(MF32 "%0, %1, %2, %0" : "+v"(d) : "v"(a), "v"(b)); VNOP(); }
DEV void mf16z(f4& d, v2u a, v2u b) { asm volatile(MF16 "%0, %1, %2, 0" : "=&v"(d) : "v"(a), "v"(b)); VNOP(); }
DEV void mf16c(f4& d, v2u a, v2u b) { asm volatile(MF16 "%0, %1, %2, %0" : "+v"(d) : "v"(a), "v"(b)); VNOP(); }
DEV void mf16z_a(f4& d, v2u a, v2u b) { asm volatile(MF16 "%0, %1, %2, 0" : "=&v"(d) : "a"(a), "v"(b)); VNOP(); }
DEV void mf16c_a(f4& d, v2u a, v2u b) { asm volatile(MF16 "%0, %1, %2, %0" : "+v"(d) : "a"(a), "v"(b)); VNOP(); }
DEV void mf16z_b(f4& d, v2u a, v2u b) { asm volatile(MF16 "%0, %1, %2, 0" : "=&v"(d) : "v"(a), "a"(b)); VNOP(); }
template <int F, int OFF>
DEV void wtr(unsigned addr) {
  constexpr int R = 96 + 4 * (F < 40 ? F : F - 40);
  if constexpr (F < 40)
    asm volatile("ds_read_b64_tr_b16 v[%c1:%c2], %0 offset:%c5\n\tds_read_b64_tr_b16 v[%c3:%c4], %0 offset:%c6"
                 :: "v"(addr), "n"(R), "n"(R + 1), "n"(R + 2), "n"(R + 3), "n"(OFF), "n"(OFF + 512) : "memory");
  else
    asm volatile("ds_read_b64_tr_b16 a[%c1:%c2], %0 offset:%c5\n\tds_read_b64_tr_b16 a[%c3:%c4], %0 offset:%c6"
                 :: "v"(addr), "n"(R), "n"(R + 1), "n"(R + 2), "n"(R + 3), "n"(OFF), "n"(OFF + 512) : "memory");
}
#define A_FMA(d, a, b, c) asm volatile("v_fma_f32 %0, %1, %2, %3" : "=v"(d) : "v"(a), "v"(b), "v"(c))
#define A_FMAN(d, a, b, c) asm volatile("v_fma_f32 %0, %1, -%2, %3" : "=v"(d) : "v"(a), "v"(b), "v"(c))
#define A_MUL(d, a, b) asm volatile("v_mul_f32 %0, %1, %2" : "=v"(d) : "v"(a), "v"(b))
#define A_MULS(d, k, b) asm volatile("v_mul_f32 %0, %1, %2" : "=v"(d) : "s"(k), "v"(b))
#define A_MAX(d, a, b) asm volatile("v_max_f32 %0, %1, %2" : "=v"(d) : "v"(a), "v"(b))
#define A_SUB(d, a, b) asm volatile("v_sub_f32 %0, %1, %2" : "=v"(d) : "v"(a), "v"(b))
#define A_CVT(d, a, b) asm volatile("v_cvt_pk_bf16_f32 %0, %1, %2" : "=v"(d) : "v"(a), "v"(b))
#define A_EXP(d, a) asm volatile("v_exp_f32 %0, %1" : "=v"(d) : "v"(a))
#define A_RCP(d, x4) asm volatile("v_rcp_f32 %0, %1" : "=&v"(d) : "v"(x4[0]), "v"(x4))
#define A_SEL(d, f, t, m) asm volatile("v_cndmask_b32 %0, %1, %2, %3" : "=v"(d) : "v"(f), "v"(t), "s"(m))
#define A_PERM(d, x, sel) asm volatile("v_perm_b32 %0, %1, %1, %2" : "=v"(d) : "v"(x), "v"(sel))
#define A_SW16(a, b) asm volatile("v_permlane16_swap_b32 %0, %1" : "+v"(a), "+v"(b))
#define A_SW32(a, b) asm volatile("v_permlane32_swap_b32 %0, %1" : "+v"(a), "+v"(b))
#define A_LD64(d, addr, off) asm volatile("ds_read_b64 %0, %1 offset:%2" : "=v"(d) : "v"(addr), "n"(off) : "memory")
#define A_LD128A(d, addr, off) asm volatile("ds_read_b128 %0, %1 offset:%2" : "=a"(d) : "v"(addr), "n"(off) : "memory")
#define A_LD128(d, addr, off) asm volatile("ds_read_b128 %0, %1 offset:%2" : "=v"(d) : "v"(addr), "n"(off) : "memory")

#define STAMP(i) do {} while (0)
#define CSTAMP(i) do {} while (0)
#define STAMP_FLUSH() do {} while (0)
#define TS(k) do {} while (0)
#define TS_DECL
#define TS_FLUSH(base, n) do {} while (0)

struct Ctx {
  GAS unsigned char* ws;
  rsrc_t rs;
  lds_t* L;
  unsigned Lb, Lw;
  int c, m, w;
  int lane, tok, g;
  unsigned T;
  int budget;
};
DEV void fresh(Ctx& ctx) {
  int l;
  asm volatile("v_mbcnt_lo_u32_b32 %0, -1, 0\n\tv_mbcnt_hi_u32_b32 %0, -1, %0" : "=v"(l));
  ctx.lane = l; ctx.tok = l & 15; ctx.g = l >> 4;
  asm volatile("" : "+s"(ctx.c), "+s"(ctx.m), "+s"(ctx.w), "+s"(ctx.Lb));
  ctx.Lw = ctx.Lb + 4096 * ctx.w;
}
DEV v4u ld128(Ctx& ctx, int voff, int soff) { return __builtin_amdgcn_raw_buffer_load_b128(ctx.rs, voff, soff, SC1); }
DEV void st128(Ctx& ctx, v4u v, int voff, int soff) { __builtin_amdgcn_raw_buffer_store_b128(v, ctx.rs, voff, soff, SC1); }
DEV void st64(Ctx& ctx, v2u v, int voff, int soff) { __builtin_amdgcn_raw_buffer_store_b64(v, ctx.rs, voff, soff, SC1); }

DEV void wait_flags(Ctx& ctx, int off0, int off1, bool two) {
  if (ctx.w == 0) {
    const int l = ctx.lane & (two ? 15 : 7);
    const int vo = (l < 8 ? off0 : off1 - 128) + l * 16;
    for (;;) {
      asm volatile("" ::: "memory");
      const unsigned v = __builtin_amdgcn_raw_buffer_load_b32(ctx.rs, vo, 0, SC1);
      if (__all(v == ctx.T) || --ctx.budget < 0) break;
    }
    __builtin_amdgcn_fence(__ATOMIC_ACQUIRE, "wavefront");
  }
  BAR();
}
DEV void flag_store(Ctx& ctx, int off) {
  if (ctx.w == 0) {
    unsigned tv, zv;
    asm volatile("v_mov_b32 %0, %2\n\tv_mov_b32 %1, 0" : "=v"(tv), "=v"(zv) : "s"(ctx.T));
    __builtin_amdgcn_raw_buffer_store_b32(tv, ctx.rs, zv, off + ctx.m * 16, SC1);
  }
}

DEV int x0_lane(int tok, int g) { return tok * 64 + ((g ^ ((4 - (tok >> 2)) & 3)) << 4); }
DEV int tr_base(int lane, int j) {
  const int g = lane >> 4, q = (lane & 15) >> 2, p = lane & 3;
  const int s = ((q >> 1) & 1) | ((g & 1) << 1);
  return (8 * g + q) * 128 + ((j ^ s) << 5) + p * 8;
}
template <int F>
DEV void raw_ld1(unsigned voff, rsrc_t rs, int soff) {
  constexpr int R = 96 + 4 * (F < 40 ? F : F - 40);
  if constexpr (F < 40) asm volatile("buffer_load_dwordx4 v[%c3:%c4], %0, %1, %2 offen" :: "v"(voff), "s"(rs), "s"(soff), "n"(R), "n"(R + 3) : "memory");
  else asm volatile("buffer_load_dwordx4 a[%c3:%c4], %0, %1, %2 offen" :: "v"(voff), "s"(rs), "s"(soff), "n"(R), "n"(R + 3) : "memory");
}
template <int P, int I0, int I1>
DEV void raw_load(Ctx& ctx, rsrc_t rs, int colbyte) {
  const unsigned vo = (unsigned)((ctx.lane >> 3) * 3072 + (ctx.lane & 7) * 16);
  const int sb = colbyte + ctx.m * 128 + 32 * ctx.w * 3072;
  sfor<I0, I1>([&](auto i) { raw_ld1<P + i>(vo, rs, sb + (128 * (i >> 2) + 8 * (i & 3)) * 3072); });
}
template <int F, int OFF>
DEV void raw_st1(unsigned addr) {
  constexpr int R = 96 + 4 * (F < 40 ? F : F - 40);
  if constexpr (F < 40) asm volatile("ds_write_b128 %0, v[%c1:%c2] offset:%c3" :: "v"(addr), "n"(R), "n"(R + 3), "n"(OFF) : "memory");
  else asm volatile("ds_write_b128 %0, a[%c1:%c2] offset:%c3" :: "v"(addr), "n"(R), "n"(R + 3), "n"(OFF) : "memory");
}
template <int P, int I0, int I1>
DEV void raw_store(Ctx& ctx) {
  const int lane = ctx.lane, ch = lane & 7, rb1 = (lane >> 4) & 1;
  const unsigned base = ctx.Lb + L_RQ + (32 * ctx.w + (lane >> 3)) * 128 + (ch & 1) * 16;
  const unsigned p0 = base + (((ch >> 1) ^ rb1) << 5), p1 = base + (((ch >> 1) ^ (rb1 | 2)) << 5);
  sfor<I0, I1>([&](auto i) { raw_st1<P + i, (i >> 2) * 16384 + (i & 3) * 1024>((i & 1) ? p1 : p0); });
}
template <int P, int K0, int K1>
DEV void part_tr(Ctx& ctx) {
  const unsigned tb = ctx.Lb + L_RQ + tr_base(ctx.lane, ctx.w);
  sfor<K0, K1>([&](auto ks) { wtr<P + ks, ks * 4096>(tb); });
}
template <int P>
DEV void part_cs(Ctx& ctx, int csoff) {
  v4u ones = {mk1(), mk1(), mk1(), mk1()};
  asm volatile("s_waitcnt lgkmcnt(0)\n\ts_nop 1" : "+v"(ones) :: "memory");
  f4 c0, c1;
  sfor<0, 8>([&](auto k) { wcs<P + 2 * k, k == 0>(c0, ones); wcs<P + 2 * k + 1, k == 0>(c1, ones); });
  asm volatile("s_nop 7" : "+v"(c0), "+v"(c1));
  if (ctx.g == 0) *(LAS float*)(ctx.L + csoff + ctx.tok * 4) = c0[0] + c1[0];
}

template <int P>
DEV void part_step(Ctx& ctx, int csoff) {
  raw_store<P, 0, 8>(ctx);
  BAR();
  part_tr<P, 0, 8>(ctx);
  raw_store<P, 8, 16>(ctx);
  BAR();
  part_tr<P, 8, 16>(ctx);
  part_cs<P>(ctx, csoff);
}

template <int P, int PN>
DEV void part_step_ld(Ctx& ctx, int csoff, rsrc_t rs, int colbyte) {
  raw_load<PN, 0, 4>(ctx, rs, colbyte);
  raw_store<P, 0, 8>(ctx);
  raw_load<PN, 4, 8>(ctx, rs, colbyte);
  BAR();
  part_tr<P, 0, 8>(ctx);
  raw_load<PN, 8, 12>(ctx, rs, colbyte);
  raw_store<P, 8, 16>(ctx);
  raw_load<PN, 12, 16>(ctx, rs, colbyte);
  BAR();
  part_tr<P, 8, 16>(ctx);
  part_cs<P>(ctx, csoff);
}
template <int OFF>
DEV void pf1(unsigned lv, rsrc_t rs, int so) {
  asm volatile("buffer_load_dwordx4 v[224:227], %0, %1, %2 offen offset:%3" :: "v"(lv), "s"(rs), "s"(so), "n"(OFF) : "memory");
}

DEV void pfw(unsigned vo, rsrc_t rs, int so) {
  so = __builtin_amdgcn_readfirstlane(so);
  asm volatile("s_nop 4\n\t" "buffer_load_dword v224, %0, %1, %2 offen" :: "v"(vo), "s"(rs), "s"(so) : "memory");
}
DEV void readback(Ctx& ctx, int vo, int so) {
  asm volatile("buffer_load_dword v88, %0, %1, %2 offen sc1" :: "v"(vo), "s"(ctx.rs), "s"(so) : "memory");
}
template <int ROWB0, int ROWB1>
DEV void pf_pair(Ctx& ctx, rsrc_t rs0, int col0, rsrc_t rs1, int col1) {
  const int row = 16 * (int)(blockIdx.x >> 3) + 8 * (ctx.w & 1);
  if (ctx.w < 2) pfw((unsigned)((ctx.lane >> 3) * ROWB0 + (ctx.lane & 7) * 128), rs0, col0 + row * ROWB0);
  else pfw((unsigned)((ctx.lane >> 3) * ROWB1 + (ctx.lane & 7) * 128), rs1, col1 + row * ROWB1);
}

struct ImgDma { unsigned v[4]; int sb; };
template <int ROWB>
DEV ImgDma img_prep(Ctx& ctx) {
  const int lane = ctx.lane, cp = lane & 7, rb1 = (lane >> 4) & 1;
  const unsigned vb = (unsigned)((lane >> 3) * ROWB + (cp & 1) * 16);
  const unsigned v0 = vb + (((cp >> 1) ^ rb1) << 5), v1 = vb + (((cp >> 1) ^ (rb1 | 2)) << 5);
  ImgDma d;
  d.v[0] = v0; d.v[1] = v1 + (8 * ROWB - 1024); d.v[2] = v0 + 2 * (8 * ROWB - 1024); d.v[3] = v1 + 3 * (8 * ROWB - 1024);
  d.sb = ctx.m * 128 + 32 * ctx.w * ROWB;
  return d;
}
template <int LOFF> DEV void dma_m0(unsigned lbase) { asm volatile("s_add_u32 m0, %0, %1" :: "s"(lbase), "n"(LOFF) : "scc"); }
template <int SOFF> DEV void dma_so(int& t, int sbase) { asm volatile("s_add_u32 %0, %1, %2" : "=s"(t) : "s"(sbase), "n"(SOFF) : "scc"); }
template <int I> DEV void dma_go(unsigned voff, rsrc_t rs, int so) {
  asm volatile("buffer_load_dwordx4 %0, %1, %2 offen offset:%3 lds" :: "v"(voff), "s"(rs), "s"(so), "n"(I * 1024) : "memory");
}
template <int I> DEV void dma_go_sc1(unsigned voff, rsrc_t rs, int so) {
  asm volatile("buffer_load_dwordx4 %0, %1, %2 offen offset:%3 sc1 lds" :: "v"(voff), "s"(rs), "s"(so), "n"(I * 1024) : "memory");
}
template <int SOFF, int LOFF>
DEV void dma1(unsigned voff, rsrc_t rs, int sbase, unsigned lbase) {
  int t;
  asm volatile("s_add_u32 m0, %4, %6\n\ts_add_u32 %0, %3, %5\n\tbuffer_load_dwordx4 %1, %2, %0 offen lds"
               : "=&s"(t) : "v"(voff), "s"(rs), "s"(sbase), "s"(lbase), "n"(SOFF), "n"(LOFF) : "memory", "scc");
}
template <int ROWB, int COLB, int LDSOFF, int J>
DEV void img_chunk(Ctx& ctx, const ImgDma& d, rsrc_t rs) {
  int so;
  dma_m0<LDSOFF + J * 16384>(ctx.Lw); dma_so<COLB + 128 * J * ROWB>(so, d.sb);
  sfor<0, 4>([&](auto i) { dma_go<i>(d.v[i], rs, so); });
}
template <int TT>
DEV void x_chunk(Ctx& ctx, unsigned lv, int xs) {
  int so;
  dma_m0<L_XT + (TT + 1) * 16384>(ctx.Lw); dma_so<(TT + 1) * 16384>(so, xs);
  sfor<0, 4>([&](auto i) { dma_go_sc1<i>(lv, ctx.rs, so); });
}

struct ST {
  v4u d[4];
  float s, q, t, bias;
  unsigned bad, sv, wrec, wnm;
  int so;
};
template <bool HALO>
DEV void st_prep(Ctx& ctx, ST& st, int L) {
  (void)L;
  const int c = ctx.c, w = ctx.w, r32 = ctx.lane & 31, hf = ctx.lane >> 5;
  const bool halo = HALO && w >= 2;
  const int cc = (halo && c > 0) ? c - 1 : c;
  const int row0 = halo ? (c > 0 ? 48 : 0) : 32 * (w & 1);
  const int r = halo ? (r32 & 15) : r32;
  st.so = WS_STAT + (HALO ? 0 : STAT_L) + (cc * 8 * 64 + row0) * 16;
  st.sv = hf * 4096 + r * 16;
  const int lrow = halo ? r : 16 + row0 + r;
  st.wrec = ctx.Lb + L_REC + lrow * 16;
  st.wnm = ctx.Lb + L_NM + lrow * 4;
  st.bias = (halo && c == 0) ? -__builtin_inff() : 0.f;
  asm volatile("" : "+v"(st.sv), "+v"(st.wrec), "+v"(st.wnm), "+v"(st.bias));
}
DEV void st_issue(Ctx& ctx, ST& st) {
  asm volatile("buffer_load_dwordx4 %0, %4, %5, %6 offen sc1\n\t"
               "buffer_load_dwordx4 %1, %4, %5, %6 offen offset:1024 sc1\n\t"
               "buffer_load_dwordx4 %2, %4, %5, %6 offen offset:2048 sc1\n\t"
               "buffer_load_dwordx4 %3, %4, %5, %6 offen offset:3072 sc1"
               : "=&v"(st.d[0]), "=&v"(st.d[1]), "=&v"(st.d[2]), "=&v"(st.d[3]) : "v"(st.sv), "s"(ctx.rs), "s"(st.so) : "memory");
}
DEV void st_retry(Ctx& ctx, ST& st) {
  for (;;) {
    asm volatile("" ::: "memory");
    unsigned bad = 0;
#pragma unroll
    for (int i = 0; i < 4; ++i) { st.d[i] = ld128(ctx, st.sv + i * 1024, st.so); }
#pragma unroll
    for (int i = 0; i < 4; ++i) bad |= (st.d[i].y ^ ctx.T) | (st.d[i].w ^ ctx.T);
    if (__all(bad == 0) || --ctx.budget < 0) break;
  }
}
DEV void st_check(Ctx& ctx, ST& st) {
  asm volatile("" : "+v"(st.d[0]), "+v"(st.d[1]), "+v"(st.d[2]), "+v"(st.d[3]));
  const unsigned T = ctx.T;
  const bool ok = (st.d[0].y == T) & (st.d[0].w == T) & (st.d[1].y == T) & (st.d[1].w == T) &
                  (st.d[2].y == T) & (st.d[2].w == T) & (st.d[3].y == T) & (st.d[3].w == T);
  if (__builtin_expect(!__all(ok), 0)) st_retry(ctx, st);
}
template <int K>
DEV void st_step(ST& st) {
  if constexpr (K == 0) asm volatile("v_add_f32 %0, %2, %3\n\tv_add_f32 %1, %4, %5" : "=&v"(st.s), "=&v"(st.q) : "v"(st.d[0].x), "v"(st.d[1].x), "v"(st.d[0].z), "v"(st.d[1].z));
  if constexpr (K == 1) asm volatile("v_add_f32 %0, %0, %2\n\tv_add_f32 %1, %1, %3" : "+v"(st.s), "+v"(st.q) : "v"(st.d[2].x), "v"(st.d[2].z));
  if constexpr (K == 2) asm volatile("v_add_f32 %0, %0, %2\n\tv_add_f32 %1, %1, %3" : "+v"(st.s), "+v"(st.q) : "v"(st.d[3].x), "v"(st.d[3].z));
  if constexpr (K == 3) A_SW32(st.s, st.q);
  if constexpr (K == 4) { float x = st.s, y = st.q; asm volatile("v_add_f32 %0, %2, %3\n\tv_add_f32 %1, %2, %3" : "=&v"(st.s), "=&v"(st.q) : "v"(x), "v"(y)); }
  if constexpr (K == 5) A_SW32(st.s, st.q);
  if constexpr (K == 6) asm volatile("v_mul_f32 %0, %2, %0\n\tv_mul_f32 %1, %3, %1" : "+v"(st.s), "+v"(st.q) : "s"(-1.0f / 512.0f), "s"(1.0f / 512.0f));
  if constexpr (K == 7) asm volatile("v_fma_f32 %0, -%1, %1, %0\n\tv_max_f32 %0, 0, %0" : "+v"(st.q) : "v"(st.s));
  if constexpr (K == 8) asm volatile("v_add_f32 %0, %1, %0" : "+v"(st.q) : "s"(1e-5f));
  if constexpr (K == 9) asm volatile("v_rsq_f32 %0, %0" : "+v"(st.q));
  if constexpr (K == 10) asm volatile("v_mov_b32 %0, %1" : "=v"(st.t) : "v"(st.q));
  if constexpr (K == 11) asm volatile("ds_write_b128 %0, %1" :: "v"(st.wrec), "v"(f4{st.s, st.q, st.t, st.bias}) : "memory");
  if constexpr (K == 12) asm volatile("ds_write2_b32 %0, %1, %2 offset1:80" :: "v"(st.wnm), "v"(st.s), "v"(st.q) : "memory");
}

template <bool HALO>
DEV void gather_start(Ctx& ctx, ST& st, int L, unsigned lv, int xs) {
  const int c = ctx.c;
  const int cprev = c > 0 ? c - 1 : 0;
  constexpr int T0 = HALO ? -1 : 0;
  const int xs0 = (HALO && c == 0) ? xs + 16384 : xs;
  st_prep<HALO>(ctx, st, L);
  wait_flags(ctx, WS_FX + ((HALO ? 0 : 32) + c) * 128, WS_FX + ((HALO ? 0 : 32) + cprev) * 128, HALO);
  STAMP(L * 8 + 0);
  x_chunk<T0>(ctx, lv, xs0);
  st_issue(ctx, st);
  x_chunk<T0 + 1>(ctx, lv, xs);
  WAIT_VM(8);
  RAWBAR();
  STAMP(L * 8 + 1);
}
template <int H>
DEV void st_gaps(Ctx& ctx, ST& st) {
  if constexpr (H == 4 * 2 + 2) st_check(ctx, st);
  if constexpr (H == 4 * 3 + 2) st_step<0>(st);
  if constexpr (H == 4 * 4 + 2) st_step<1>(st);
  if constexpr (H == 4 * 5) st_step<2>(st);
  if constexpr (H == 4 * 5 + 2) st_step<3>(st);
  if constexpr (H == 4 * 6) st_step<4>(st);
  if constexpr (H == 4 * 6 + 2) st_step<5>(st);
  if constexpr (H == 4 * 7 + 2) st_step<6>(st);
  if constexpr (H == 4 * 8) st_step<7>(st);
  if constexpr (H == 4 * 8 + 2) { st_step<8>(st); }
  if constexpr (H == 4 * 9) st_step<9>(st);
  if constexpr (H == 4 * 9 + 2) { st_step<10>(st); st_step<12>(st); }
  if constexpr (H == 4 * 10) st_step<11>(st);
}

template <int OFF> DEV void ring_prime(unsigned ab) { ring_rd<0, OFF>(ab); ring_rd<1, OFF + 1024>(ab); ring_rd<2, OFF + 2048>(ab); WAIT_LG(2); }
template <int PQ, int PK, int PV, int OFF, bool CINIT, class XL, class SH, class GP>
DEV void gemm_tile(unsigned ab, f4& aq, f4& ak, f4& av, XL&& xl, SH&& sh, GP&& gp) {
  sfor<0, 16>([&](auto ks) {
    constexpr bool F = ks == 0 && !CINIT;
    if constexpr (PQ >= 0) {
      mm<(PQ >= 0 ? PQ : 0) + ks, ks & 3, true, F>(aq);
      if (!sh(IC<4 * ks>{})) VNOP();
      ring_rd<(ks + 3) & 3, OFF + (ks + 3) * 1024>(ab);
      gp(IC<4 * ks>{});
      mm<PK + ks, ks & 3, true, F>(ak);
      if (!sh(IC<4 * ks + 1>{})) VNOP();
      gp(IC<4 * ks + 1>{});
    } else {
      mm<PK + ks, ks & 3, true, F>(ak);
      if (!sh(IC<4 * ks>{})) VNOP();
      ring_rd<(ks + 3) & 3, OFF + (ks + 3) * 1024>(ab);
      gp(IC<4 * ks>{});
    }
    mm<PV + ks, ks & 3, false, F>(av);
    WAIT_LG(2 + decltype(xl(ks))::value);
    gp(IC<4 * ks + 2>{});
    SB();
  });
}
#define GA(k) (4 * (k))
#define GB(k) (4 * (k) + 1)
#define GC(k) (4 * (k) + 2)

struct BE {
  f4 aq, ak, av;
  f2 st, ka, kb2; f4 nm4;
  f2 nst, nka, nkb; f4 nnm4;
  f4 csq, csk; float csv;
  float q[4], k[4], v[4];
  unsigned qb[2], kb[2], vb[2], vb2[2], kprev[2], vprev[2], P[4], pk0, pk, ob[2];
  f4 sp, sc, num, den;
  float a, b, t0, t1, m, m2, ft, fa, fb, pa, pb;
  v2u ones;
  unsigned sPA, sPB, sCA, sCB;
  u64 modd, mA, mB;
  unsigned aT, aF, aKa, aKb;
  int ovo;
};

DEV void layer_B(Ctx& ctx, int L, rsrc_t rwo) {
  fresh(ctx);
  const int c = ctx.c, w = ctx.w, g = ctx.g, tok = ctx.tok;
  const int csb = L_CS + (w * 6 + 3) * 64;
  const unsigned lv = ctx.lane * 16;
  BE e;
  TS_DECL;
  unsigned ab0, ab1;
  ImgDma wd = img_prep<1024>(ctx);
  {
    const bool odd = tok & 1;
    const int ja = 4 * g + (odd ? 1 : 0), jb = ja + 2;
    const bool curA = ja <= tok, curB = jb <= tok;
    const unsigned selA = odd ? 0x01000c0cu : 0x0c0c0100u, selB = odd ? 0x03020c0cu : 0x0c0c0302u, selZ = 0x0c0c0c0cu;
    e.sPA = curA ? selZ : selA; e.sPB = curB ? selZ : selB; e.sCA = curA ? selA : selZ; e.sCB = curB ? selB : selZ;
    e.modd = __ballot(odd); e.mA = __ballot(curA); e.mB = __ballot(curB);
    e.aT = ctx.Lb + L_REC + tok * 16;
    e.aF = ctx.Lb + L_NM + g * 16;
    e.aKa = ctx.Lb + L_REC + (ja - (curA ? 0 : 16)) * 16 + 8;
    e.aKb = ctx.Lb + L_REC + (jb - (curB ? 0 : 16)) * 16 + 8;
    e.csq = lds_ldf4(ctx.L, csb + g * 16); e.csk = lds_ldf4(ctx.L, csb + 64 + g * 16); e.csv = lds_ldf(ctx.L, csb + 128 + tok * 4);
    e.ones = v2u{mk1(), mk0()};
    e.ovo = ((g >> 1) * 16 + tok) * 16 + (g & 1) * 8;
    ab0 = ctx.Lb + L_XT + lv; ab1 = ab0 + 49152;
    asm volatile("" : "+v"(e.sPA), "+v"(e.sPB), "+v"(e.sCA), "+v"(e.sCB), "+v"(e.csq), "+v"(e.csk), "+v"(e.csv), "+a"(e.ones), "+v"(e.ovo),
                      "+v"(e.aT), "+v"(e.aF), "+v"(e.aKa), "+v"(e.aKb), "+v"(ab0), "+v"(ab1), "+v"(wd.v[0]), "+v"(wd.v[1]), "+v"(wd.v[2]), "+v"(wd.v[3]));
  }
  const int xs = WS_X + c * 65536 + w * 4096 - 16384;
  const int osb = WS_O + (4 * c * 16 + 2 * ctx.m + (w >> 1)) * 1024 + (w & 1) * 512;
  ST st;
  gather_start<true>(ctx, st, L, lv, xs);
  ring_prime<0>(ab0);
  f4 xq, xk, xv, yq, yk, yv;
  int so, oso;
  auto stat_rd = [&](auto S, auto h) {
    (void)e;
    if constexpr (h == GC(6)) { A_LD64(e.nst, e.aT, S * 256); A_LD128(e.nnm4, e.aF, S * 64); }
    if constexpr (h == GA(7)) A_LD64(e.nka, e.aKa, S * 256);
    if constexpr (h == GA(8)) A_LD64(e.nkb, e.aKb, S * 256);
  };
  auto xl = [](auto ks) { return IC<(ks == 7 ? 3 : ks == 8 ? 4 : ks == 9 ? 2 : ks == 10 ? 1 : 0)>{}; };
  auto next = [&](f4& q, f4& k, f4& v) { e.aq = q; e.ak = k; e.av = v; e.st = e.nst; e.ka = e.nka; e.kb2 = e.nkb; e.nm4 = e.nnm4; };
  auto epi = [&](auto h) {
    (void)e;
    if constexpr (h == GA(0)) A_FMA(e.q[0], e.st[0], e.csq[0], e.aq[0]);
    if constexpr (h == GB(0)) { A_FMA(e.q[1], e.st[0], e.csq[1], e.aq[1]); A_FMA(e.q[2], e.st[0], e.csq[2], e.aq[2]); }
    if constexpr (h == GC(0)) { A_FMA(e.q[3], e.st[0], e.csq[3], e.aq[3]); A_FMA(e.k[0], e.st[0], e.csk[0], e.ak[0]); }
    if constexpr (h == GA(1)) A_FMA(e.k[1], e.st[0], e.csk[1], e.ak[1]);
    if constexpr (h == GB(1)) { A_FMA(e.k[2], e.st[0], e.csk[2], e.ak[2]); A_FMA(e.k[3], e.st[0], e.csk[3], e.ak[3]); }
    if constexpr (h == GC(1)) { A_CVT(e.qb[0], e.q[0], e.q[1]); A_CVT(e.qb[1], e.q[2], e.q[3]); }
    if constexpr (h == GB(2)) { A_CVT(e.kb[0], e.k[0], e.k[1]); A_CVT(e.kb[1], e.k[2], e.k[3]); }
    if constexpr (h == GC(2)) {
      A_FMA(e.v[0], e.nm4[0], e.csv, e.av[0]); A_FMA(e.v[1], e.nm4[1], e.csv, e.av[1]);
      mf16z(e.sp, v2u{e.kprev[0], e.kprev[1]}, v2u{e.qb[0], e.qb[1]});
      A_FMA(e.v[2], e.nm4[2], e.csv, e.av[2]); A_FMA(e.v[3], e.nm4[3], e.csv, e.av[3]);
      mf16z(e.sc, v2u{e.kb[0], e.kb[1]}, v2u{e.qb[0], e.qb[1]});
      A_CVT(e.vb[0], e.v[0], e.v[1]); A_CVT(e.vb[1], e.v[2], e.v[3]);
    }
    if constexpr (h == GB(3)) { A_CVT(e.vb2[0], e.v[0], e.v[1]); A_CVT(e.vb2[1], e.v[2], e.v[3]); }
    if constexpr (h == GC(3)) A_MULS(e.ft, 0.25f * LOG2E, e.st[1]);
    if constexpr (h == GA(4)) A_MUL(e.fa, e.ka[0], e.ft);
    if constexpr (h == GB(4)) { A_SEL(e.a, e.sc[0], e.sc[1], e.modd); A_SEL(e.t0, e.sp[0], e.sp[1], e.modd); }
    if constexpr (h == GC(4)) { A_SEL(e.b, e.sc[2], e.sc[3], e.modd); A_SEL(e.t1, e.sp[2], e.sp[3], e.modd); }
    if constexpr (h == GB(5)) { A_SEL(e.a, e.t0, e.a, e.mA); A_SEL(e.b, e.t1, e.b, e.mB); }
    if constexpr (h == GC(5)) { A_MUL(e.fb, e.kb2[0], e.ft); A_FMA(e.a, e.a, e.fa, e.ka[1]); }
    if constexpr (h == GB(6)) {
      asm volatile("v_fma_f32 %0, %0, %3, %4\n\tv_max_f32 %1, %2, %0" : "+v"(e.b), "=&v"(e.m) : "v"(e.a), "v"(e.fb), "v"(e.kb2[1]));
    }
    if constexpr (h == GA(7)) A_MAX(e.m2, e.a, e.b);
    if constexpr (h == GC(7)) A_SW16(e.m, e.m2);
    if constexpr (h == GB(8)) { float x = e.m, y = e.m2; A_MAX(e.m, x, y); A_MAX(e.m2, x, y); }
    if constexpr (h == GA(9)) A_SW32(e.m, e.m2);
    if constexpr (h == GB(9)) asm volatile("v_max_f32 %0, %0, %2\n\tv_sub_f32 %1, %1, %0" : "+v"(e.m), "+v"(e.a) : "v"(e.m2));
    if constexpr (h == GC(9)) { A_SUB(e.b, e.b, e.m); }
    if constexpr (h == GB(10)) A_EXP(e.a, e.a);
    if constexpr (h == GC(10)) A_EXP(e.b, e.b);
    if constexpr (h == GB(11)) { A_MUL(e.pa, e.a, e.ka[0]); A_MUL(e.pb, e.b, e.kb2[0]); }
    if constexpr (h == GC(11)) { A_CVT(e.pk, e.pa, e.pb); A_CVT(e.pk0, e.a, e.b); }
    if constexpr (h == GB(12)) { A_PERM(e.P[0], e.pk, e.sPA); A_PERM(e.P[1], e.pk, e.sPB); }
    if constexpr (h == GC(12)) { A_PERM(e.P[2], e.pk, e.sCA); A_PERM(e.P[3], e.pk, e.sCB); }
    if constexpr (h == GA(13)) {
      mf16z_a(e.den, e.ones, v2u{e.pk0, e.pk});
      mf32z(e.num, v4u{e.vprev[0], e.vprev[1], e.vb[0], e.vb[1]}, v4u{e.P[0], e.P[1], e.P[2], e.P[3]});
      e.kprev[0] = e.kb[0]; e.kprev[1] = e.kb[1]; e.vprev[0] = e.vb2[0]; e.vprev[1] = e.vb2[1];
    }
    if constexpr (h == GB(14)) A_RCP(e.m, e.den);
    if constexpr (h == GC(14)) { A_MUL(e.q[0], e.num[0], e.m); A_MUL(e.q[1], e.num[1], e.m); }
    if constexpr (h == GA(15)) A_MUL(e.q[2], e.num[2], e.m);
    if constexpr (h == GB(15)) { A_MUL(e.q[3], e.num[3], e.m); A_CVT(e.ob[0], e.q[0], e.q[1]); }
    if constexpr (h == GC(15)) { A_CVT(e.ob[1], e.q[2], e.q[3]); st64(ctx, v2u{e.ob[0], e.ob[1]}, e.ovo, oso); }
  };
  auto x_sh = [&](auto TT, auto h) {
    if constexpr (h == GB(1)) dma_so<(TT + 1) * 16384>(so, xs);
    if constexpr (h == GA(2)) dma_m0<L_XT + (TT + 1) * 16384>(ctx.Lw);
    return h == GB(1) || h == GA(2);
  };
  auto x_gp = [&](auto h) {
    if constexpr (h == GA(2)) dma_go_sc1<0>(lv, ctx.rs, so);
    if constexpr (h == GA(3)) dma_go_sc1<1>(lv, ctx.rs, so);
    if constexpr (h == GA(5)) dma_go_sc1<2>(lv, ctx.rs, so);
    if constexpr (h == GA(6)) dma_go_sc1<3>(lv, ctx.rs, so);
  };
  auto w_sh = [&](auto J, auto N, auto h) {
    if constexpr (h == GA(7)) WAIT_VMV(N.value);
    if constexpr (h == GB(7)) RAWBAR();
    if constexpr (h == GB(9)) dma_so<128 * J * 1024>(so, wd.sb);
    if constexpr (h == GA(10)) dma_m0<L_XT + J * 16384>(ctx.Lw);
    if constexpr (h == GB(13) && J > 0) dma_so<(J - 1) * 16384>(oso, osb);
    return h == GA(7) || h == GB(7) || h == GB(9) || h == GA(10) || (h == GB(13) && J > 0);
  };
  auto w_gp = [&](auto h) {
    if constexpr (h == GA(10)) dma_go<0>(wd.v[0], rwo, so);
    if constexpr (h == GA(11)) dma_go<1>(wd.v[1], rwo, so);
    if constexpr (h == GA(12)) dma_go<2>(wd.v[2], rwo, so);
    if constexpr (h == GA(14)) dma_go<3>(wd.v[3], rwo, so);
  };
  TS(0);
  gemm_tile<-1, P_BK, P_BV, 0, false>(ab0, xq, xk, xv,
    [](auto ks) { return IC<(ks == 10 ? 2 : ks == 11 ? -1 : (ks == 12 || ks == 13 || ks == 14) ? 2 : 0)>{}; },
    [&](auto h) {
      if constexpr (h == GA(0)) dma_so<2 * 16384>(so, xs);
      if constexpr (h == GA(1)) dma_m0<L_XT + 2 * 16384>(ctx.Lw);
      if constexpr (h == GA(2)) WAIT_VMV(5);
      if constexpr (h == GA(11)) WAIT_VMV(4);
      if constexpr (h == GA(12)) RAWBAR();
      return h == GA(0) || h == GA(1) || h == GA(2) || h == GA(11) || h == GA(12);
    },
    [&](auto h) {
      (void)e;
      if constexpr (h == GA(1)) dma_go_sc1<0>(lv, ctx.rs, so);
      if constexpr (h == GA(4)) dma_go_sc1<1>(lv, ctx.rs, so);
      if constexpr (h == GA(7)) dma_go_sc1<2>(lv, ctx.rs, so);
      if constexpr (h == GC(10)) dma_go_sc1<3>(lv, ctx.rs, so);
      st_gaps<h>(ctx, st);
      if constexpr (h == GA(12)) { A_LD64(e.nst, e.aT, 0); A_LD128(e.nnm4, e.aF, 0); }
    });
  TS(1);
  gemm_tile<P_BQ, P_BK, P_BV, 16384, false>(ab0, yq, yk, yv, xl,
    [&](auto h) { return x_sh(IC<2>{}, h) | w_sh(IC<0>{}, IC<4>{}, h); },
    [&](auto h) {
      (void)e; (void)xk; (void)xv;
      if constexpr (h == GB(1)) { A_FMA(e.k[0], e.nst[0], e.csk[0], xk[0]); A_FMA(e.k[1], e.nst[0], e.csk[1], xk[1]); }
      if constexpr (h == GC(1)) { A_FMA(e.k[2], e.nst[0], e.csk[2], xk[2]); A_FMA(e.k[3], e.nst[0], e.csk[3], xk[3]); }
      if constexpr (h == GB(2)) { A_FMA(e.v[0], e.nnm4[0], e.csv, xv[0]); A_FMA(e.v[1], e.nnm4[1], e.csv, xv[1]); }
      if constexpr (h == GC(2)) { A_FMA(e.v[2], e.nnm4[2], e.csv, xv[2]); A_FMA(e.v[3], e.nnm4[3], e.csv, xv[3]); }
      if constexpr (h == GB(3)) { A_CVT(e.kprev[0], e.k[0], e.k[1]); A_CVT(e.kprev[1], e.k[2], e.k[3]); }
      if constexpr (h == GC(3)) { A_CVT(e.vprev[0], e.v[0], e.v[1]); A_CVT(e.vprev[1], e.v[2], e.v[3]); }
      if constexpr (h == GC(15)) NOP(1);
      x_gp(h); stat_rd(IC<1>{}, h); w_gp(h);
    });
  next(yq, yk, yv);
  TS(2);
  gemm_tile<P_BQ, P_BK, P_BV, 32768, false>(ab0, xq, xk, xv, xl,
    [&](auto h) { return x_sh(IC<3>{}, h) | w_sh(IC<1>{}, IC<8>{}, h); },
    [&](auto h) { x_gp(h); stat_rd(IC<2>{}, h); w_gp(h); epi(h); });
  next(xq, xk, xv);
  TS(3);
  gemm_tile<P_BQ, P_BK, P_BV, 0, false>(ab1, yq, yk, yv, xl,
    [&](auto h) { return w_sh(IC<2>{}, IC<5>{}, h); },
    [&](auto h) { stat_rd(IC<3>{}, h); w_gp(h); epi(h); });
  next(yq, yk, yv);
  TS(4);
  gemm_tile<P_BQ, P_BK, P_BV, 16384, false>(ab1, xq, xk, xv, xl,
    [&](auto h) { return w_sh(IC<3>{}, IC<63>{}, h); },
    [&](auto h) { stat_rd(IC<4>{}, h); w_gp(h); epi(h); });
  next(xq, xk, xv);
  TS(5);
  {
    dma_so<3 * 16384>(oso, osb);
    A_MULS(e.ft, 0.25f * LOG2E, e.st[1]);
    A_FMA(e.q[0], e.st[0], e.csq[0], e.aq[0]); A_FMA(e.q[1], e.st[0], e.csq[1], e.aq[1]);
    A_FMA(e.q[2], e.st[0], e.csq[2], e.aq[2]); A_FMA(e.q[3], e.st[0], e.csq[3], e.aq[3]);
    A_FMA(e.k[0], e.st[0], e.csk[0], e.ak[0]); A_FMA(e.k[1], e.st[0], e.csk[1], e.ak[1]);
    A_CVT(e.qb[0], e.q[0], e.q[1]); A_CVT(e.qb[1], e.q[2], e.q[3]);
    A_FMA(e.k[2], e.st[0], e.csk[2], e.ak[2]); A_FMA(e.k[3], e.st[0], e.csk[3], e.ak[3]);
    mf16z(e.sp, v2u{e.kprev[0], e.kprev[1]}, v2u{e.qb[0], e.qb[1]});
    A_CVT(e.kb[0], e.k[0], e.k[1]); A_CVT(e.kb[1], e.k[2], e.k[3]);
    A_MUL(e.fa, e.ka[0], e.ft); A_MUL(e.fb, e.kb2[0], e.ft);
    mf16z(e.sc, v2u{e.kb[0], e.kb[1]}, v2u{e.qb[0], e.qb[1]});
    A_FMA(e.v[0], e.nm4[0], e.csv, e.av[0]); A_FMA(e.v[1], e.nm4[1], e.csv, e.av[1]);
    A_FMA(e.v[2], e.nm4[2], e.csv, e.av[2]); A_FMA(e.v[3], e.nm4[3], e.csv, e.av[3]);
    A_SEL(e.t0, e.sp[0], e.sp[1], e.modd); A_SEL(e.t1, e.sp[2], e.sp[3], e.modd);
    A_CVT(e.vb[0], e.v[0], e.v[1]); A_CVT(e.vb[1], e.v[2], e.v[3]);
    asm volatile("v_cndmask_b32 %0, %4, %5, %10\n\tv_cndmask_b32 %1, %6, %7, %10\n\t"
                 "v_cndmask_b32 %0, %8, %0, %11\n\tv_cndmask_b32 %1, %9, %1, %12\n\t"
                 "v_fma_f32 %0, %0, %13, %15\n\tv_fma_f32 %1, %1, %14, %16\n\t"
                 "v_max_f32 %2, %0, %1\n\tv_max_f32 %3, %0, %1\n\ts_nop 1\n\t"
                 "v_permlane16_swap_b32 %2, %3\n\tv_max_f32 %2, %2, %3\n\tv_mov_b32 %3, %2\n\ts_nop 1\n\t"
                 "v_permlane32_swap_b32 %2, %3\n\tv_max_f32 %2, %2, %3\n\tv_sub_f32 %0, %0, %2\n\tv_sub_f32 %1, %1, %2\n\t"
                 "v_exp_f32 %0, %0\n\tv_exp_f32 %1, %1"
                 : "=&v"(e.a), "=&v"(e.b), "=&v"(e.m), "=&v"(e.m2)
                 : "v"(e.sc[0]), "v"(e.sc[1]), "v"(e.sc[2]), "v"(e.sc[3]), "v"(e.t0), "v"(e.t1), "s"(e.modd), "s"(e.mA), "s"(e.mB),
                   "v"(e.fa), "v"(e.fb), "v"(e.ka[1]), "v"(e.kb2[1]));
    asm volatile("v_mul_f32 %4, %7, %9\n\tv_mul_f32 %5, %8, %10\n\tv_cvt_pk_bf16_f32 %6, %7, %8\n\tv_cvt_pk_bf16_f32 %4, %4, %5\n\t"
                 "v_perm_b32 %0, %4, %4, %11\n\tv_perm_b32 %1, %4, %4, %12\n\tv_perm_b32 %2, %4, %4, %13\n\tv_perm_b32 %3, %4, %4, %14"
                 : "=&v"(e.P[0]), "=&v"(e.P[1]), "=&v"(e.P[2]), "=&v"(e.P[3]), "=&v"(e.pk), "=&v"(e.pb), "=&v"(e.pk0)
                 : "v"(e.a), "v"(e.b), "v"(e.ka[0]), "v"(e.kb2[0]), "v"(e.sPA), "v"(e.sPB), "v"(e.sCA), "v"(e.sCB));
    mf16z_a(e.den, e.ones, v2u{e.pk0, e.pk});
    NOP(0);
    mf32z(e.num, v4u{e.vprev[0], e.vprev[1], e.vb[0], e.vb[1]}, v4u{e.P[0], e.P[1], e.P[2], e.P[3]});
    NOP(5);
    asm volatile("v_rcp_f32 %2, %7\n\ts_nop 0\n\t"
                 "v_mul_f32 %3, %8, %2\n\tv_mul_f32 %4, %9, %2\n\tv_mul_f32 %5, %10, %2\n\tv_mul_f32 %6, %11, %2\n\t"
                 "v_cvt_pk_bf16_f32 %0, %3, %4\n\tv_cvt_pk_bf16_f32 %1, %5, %6"
                 : "=&v"(e.ob[0]), "=&v"(e.ob[1]), "=&v"(e.m), "=&v"(e.q[0]), "=&v"(e.q[1]), "=&v"(e.q[2]), "=&v"(e.q[3])
                 : "v"(e.den[0]), "v"(e.num[0]), "v"(e.num[1]), "v"(e.num[2]), "v"(e.num[3]), "v"(e.den));
    st64(ctx, v2u{e.ob[0], e.ob[1]}, e.ovo, oso);
  }
  TS(6);
  STAMP(L * 8 + 4);
  TS_FLUSH(L * 8 + 2, 7);
  DRAIN();
  BAR();
  flag_store(ctx, WS_FO + (32 + c) * 128);
}

struct FM {
  f4 p, sp, sn;
  float ep[4], en[4], m, m2, c;
  unsigned eb[2], nb[2];
};
#define FM_MM1(f) asm volatile("v_max3_f32 %0, %2, %3, %4\n\tv_max3_f32 %1, -%2, -%3, -%4" : "=&v"(f.m), "=&v"(f.m2) : "v"(f.p[0]), "v"(f.p[1]), "v"(f.p[2]))
#define FM_MM2(f) asm volatile("v_max_f32 %0, %0, %2\n\tv_max_f32 %1, %1, -%2" : "+v"(f.m), "+v"(f.m2) : "v"(f.p[3]))
#define FM_MX2(f) do { float x_ = f.m, y_ = f.m2; A_MAX(f.m, x_, y_); A_MAX(f.m2, x_, y_); } while (0)
#define FM_MXC(f) asm volatile("v_max_f32 %0, %0, %1\n\tv_mul_f32 %1, %0, -%2\n\tv_mul_f32 %0, %0, -%2" : "+v"(f.m), "+v"(f.m2) : "v"(f.c))

struct AE {
  f4 ak, av;
  f2 st, nst; f4 nm4, rs4, nnm4, nrs4;
  f4 csk; float csv;
  float k[4], v[4];
  unsigned kb[2], vb[2], vbo[2], php[2], phm[2];
  v2u wphi, ident, ones, lo1, hi1;
  f4 tp, tm, kvp, kvm, zz;
  FM f;
  unsigned aT, aF;
};

DEV void layer_A(Ctx& ctx, int lp, v2u wphi, rsrc_t rwo, rsrc_t rsa, rsrc_t rsb, rsrc_t rwn) {
  fresh(ctx);
  const int L = 2 * lp;
  const int c = ctx.c, m = ctx.m, w = ctx.w, lane = ctx.lane, g = ctx.g, tok = ctx.tok;
  const int csb = L_CS + (w * 6 + 0) * 64;
  const unsigned lv = lane * 16;
  const unsigned T = ctx.T;
  AE e;
  TS_DECL;
  unsigned ab0, ab1;
  ImgDma wd = img_prep<1024>(ctx), qd = img_prep<3072>(ctx);
  {
    const int d = tok - 4 * g;
    e.ident = v2u{d == 0 ? 0x3f80u : d == 1 ? 0x3f800000u : 0u, d == 2 ? 0x3f80u : d == 3 ? 0x3f800000u : 0u};
    const unsigned lo = tok < 8 ? ONE2 : 0u, hi = tok < 8 ? 0u : ONE2;
    e.lo1 = v2u{lo, lo}; e.hi1 = v2u{hi, hi}; e.ones = v2u{mk1(), mk1()}; e.wphi = wphi;
    e.aT = ctx.Lb + L_REC + tok * 16;
    e.aF = ctx.Lb + L_NM + g * 16;
    e.csk = lds_ldf4(ctx.L, csb + 64 + g * 16); e.csv = lds_ldf(ctx.L, csb + 128 + tok * 4);
    ab0 = ctx.Lb + L_XT + (lp == 0 ? x0_lane(tok, g) : lv); ab1 = ab0 + 49152;
    asm volatile("" : "+a"(e.ident), "+a"(e.lo1), "+a"(e.hi1), "+a"(e.ones), "+a"(e.wphi), "+v"(e.csk), "+v"(e.csv), "+v"(e.aT), "+v"(e.aF),
                      "+v"(ab0), "+v"(ab1), "+v"(qd.v[0]), "+v"(qd.v[1]), "+v"(qd.v[2]), "+v"(qd.v[3]));
  }
  const int xs = WS_X + ACT_L + c * 65536 + w * 4096 - 16384;
  f4 xk, xv, yk, yv;
  int so;
  auto stat_rd = [&](auto S, auto h) {
    (void)e;
    if constexpr (h == GA(12)) A_LD64(e.nst, e.aT, S * 256);
    if constexpr (h == GC(12)) { A_LD128(e.nnm4, e.aF, S * 64); A_LD128(e.nrs4, e.aF, S * 64 + 320); }
  };
  auto xl = [](auto ks) { return IC<(ks == 12 ? 1 : (ks == 13 || ks == 14) ? 3 : 0)>{}; };
  auto next = [&](f4& k, f4& v) { e.ak = k; e.av = v; e.st = e.nst; e.nm4 = e.nnm4; e.rs4 = e.nrs4; };
  auto epi = [&](auto h) {
    (void)e;
    if constexpr (h == GC(0)) { A_FMA(e.k[0], e.st[0], e.csk[0], e.ak[0]); A_FMA(e.k[1], e.st[0], e.csk[1], e.ak[1]); }
    if constexpr (h == GA(1)) { A_FMA(e.k[2], e.st[0], e.csk[2], e.ak[2]); A_FMA(e.k[3], e.st[0], e.csk[3], e.ak[3]); }
    if constexpr (h == GC(1)) { A_CVT(e.kb[0], e.k[0], e.k[1]); A_CVT(e.kb[1], e.k[2], e.k[3]); A_FMA(e.v[0], e.nm4[0], e.csv, e.av[0]); }
    if constexpr (h == GA(2)) {
      A_FMA(e.v[1], e.nm4[1], e.csv, e.av[1]);
      mf16z_a(e.f.p, e.wphi, v2u{e.kb[0], e.kb[1]});
      A_FMA(e.v[2], e.nm4[2], e.csv, e.av[2]); A_FMA(e.v[3], e.nm4[3], e.csv, e.av[3]);
    }
    if constexpr (h == GC(2)) { A_MUL(e.v[0], e.v[0], e.rs4[0]); A_MUL(e.v[1], e.v[1], e.rs4[1]); A_MUL(e.v[2], e.v[2], e.rs4[2]); }
    if constexpr (h == GA(3)) { A_MUL(e.v[3], e.v[3], e.rs4[3]); A_MULS(e.f.c, LOG2E, e.st[1]); }
    if constexpr (h == GC(3)) { FM_MM1(e.f); A_CVT(e.vb[0], e.v[0], e.v[1]); }
    if constexpr (h == GA(4)) FM_MM2(e.f);
    if constexpr (h == GC(4)) { A_CVT(e.vb[1], e.v[2], e.v[3]); A_SW16(e.f.m, e.f.m2); }
    if constexpr (h == GA(5)) FM_MX2(e.f);
    if constexpr (h == GC(5)) A_SW32(e.f.m, e.f.m2);
    if constexpr (h == GA(6)) FM_MXC(e.f);
    if constexpr (h == GC(6)) A_SW16(e.f.m, e.f.m2);
    if constexpr (h == GA(7)) { A_FMA(e.f.ep[0], e.f.p[0], e.f.c, e.f.m); A_FMA(e.f.ep[1], e.f.p[1], e.f.c, e.f.m); }
    if constexpr (h == GC(7)) { A_FMA(e.f.ep[2], e.f.p[2], e.f.c, e.f.m); A_FMA(e.f.ep[3], e.f.p[3], e.f.c, e.f.m); A_FMAN(e.f.en[0], e.f.p[0], e.f.c, e.f.m2); }
    if constexpr (h == GA(8)) { A_FMAN(e.f.en[1], e.f.p[1], e.f.c, e.f.m2); A_FMAN(e.f.en[2], e.f.p[2], e.f.c, e.f.m2); }
    if constexpr (h == GC(8)) { A_FMAN(e.f.en[3], e.f.p[3], e.f.c, e.f.m2); A_EXP(e.f.ep[0], e.f.ep[0]); A_EXP(e.f.ep[1], e.f.ep[1]); }
    if constexpr (h == GA(9)) A_EXP(e.f.ep[2], e.f.ep[2]);
    if constexpr (h == GC(9)) { A_EXP(e.f.ep[3], e.f.ep[3]); A_EXP(e.f.en[0], e.f.en[0]); }
    if constexpr (h == GA(10)) A_EXP(e.f.en[1], e.f.en[1]);
    if constexpr (h == GC(10)) { A_EXP(e.f.en[2], e.f.en[2]); A_EXP(e.f.en[3], e.f.en[3]); }
    if constexpr (h == GA(11)) { A_CVT(e.f.eb[0], e.f.ep[0], e.f.ep[1]); A_CVT(e.f.eb[1], e.f.ep[2], e.f.ep[3]); }
    if constexpr (h == GC(11)) {
      A_CVT(e.f.nb[0], e.f.en[0], e.f.en[1]); A_CVT(e.f.nb[1], e.f.en[2], e.f.en[3]);
      mf16z_a(e.f.sp, e.ones, v2u{e.f.eb[0], e.f.eb[1]});
    }
    if constexpr (h == GA(12)) mf16z_a(e.f.sn, e.ones, v2u{e.f.nb[0], e.f.nb[1]});
    if constexpr (h == GA(13)) A_RCP(e.f.m, e.f.sp);
    if constexpr (h == GC(13)) { A_MUL(e.f.ep[0], e.f.ep[0], e.f.m); A_RCP(e.f.m2, e.f.sn); A_MUL(e.f.ep[1], e.f.ep[1], e.f.m); }
    if constexpr (h == GA(14)) { A_MUL(e.f.ep[2], e.f.ep[2], e.f.m); A_MUL(e.f.ep[3], e.f.ep[3], e.f.m); }
    if constexpr (h == GC(14)) { A_MUL(e.f.en[0], e.f.en[0], e.f.m2); A_MUL(e.f.en[1], e.f.en[1], e.f.m2); A_CVT(e.f.eb[0], e.f.ep[0], e.f.ep[1]); }
    if constexpr (h == GA(15)) { A_CVT(e.f.eb[1], e.f.ep[2], e.f.ep[3]); A_MUL(e.f.en[2], e.f.en[2], e.f.m2); }
    if constexpr (h == GC(15)) {
      A_MUL(e.f.en[3], e.f.en[3], e.f.m2); A_CVT(e.f.nb[0], e.f.en[0], e.f.en[1]);
      mf16z_b(e.tp, v2u{e.f.eb[0], e.f.eb[1]}, e.ident);
      A_CVT(e.f.nb[1], e.f.en[2], e.f.en[3]);
      e.vbo[0] = e.vb[0]; e.vbo[1] = e.vb[1];
    }
  };
  auto epi2 = [&](auto FT, auto h) {
    (void)e;
    if constexpr (h == GA(0)) mf16z_b(e.tm, v2u{e.f.nb[0], e.f.nb[1]}, e.ident);
    if constexpr (h == GC(5)) { A_CVT(e.php[0], e.tp[0], e.tp[1]); }
    if constexpr (h == GC(6)) { A_CVT(e.php[1], e.tp[2], e.tp[3]); }
    if constexpr (h == GA(9)) {
      if constexpr (FT) mf16z(e.kvp, v2u{e.php[0], e.php[1]}, v2u{e.vbo[0], e.vbo[1]}); else mf16c(e.kvp, v2u{e.php[0], e.php[1]}, v2u{e.vbo[0], e.vbo[1]});
      A_CVT(e.phm[0], e.tm[0], e.tm[1]); A_CVT(e.phm[1], e.tm[2], e.tm[3]);
      if constexpr (FT) mf16z_a(e.zz, e.lo1, v2u{e.php[0], e.php[1]}); else mf16c_a(e.zz, e.lo1, v2u{e.php[0], e.php[1]});
    }
    if constexpr (h == GA(10)) {
      if constexpr (FT) mf16z(e.kvm, v2u{e.phm[0], e.phm[1]}, v2u{e.vbo[0], e.vbo[1]}); else mf16c(e.kvm, v2u{e.phm[0], e.phm[1]}, v2u{e.vbo[0], e.vbo[1]});
    }
    if constexpr (h == GA(13)) mf16c_a(e.zz, e.hi1, v2u{e.phm[0], e.phm[1]});
  };
  auto c_sh = [&](auto K0, auto SOFF, auto LOFF, int sb, auto h) {
    if constexpr (h == GA(K0 - 1)) dma_so<SOFF>(so, sb);
    if constexpr (h == GA(K0)) dma_m0<LOFF>(ctx.Lw);
    return h == GA(K0 - 1) || h == GA(K0);
  };
  auto c_gp = [&](auto K0, const ImgDma& d, rsrc_t rs, auto h) {
    if constexpr (h == GC(K0)) dma_go<0>(d.v[0], rs, so);
    if constexpr (h == GC(K0 + 2)) dma_go<1>(d.v[1], rs, so);
    if constexpr (h == GC(K0 + 4)) dma_go<2>(d.v[2], rs, so);
    if constexpr (h == GC(K0 + 6)) dma_go<3>(d.v[3], rs, so);
  };
  constexpr int QS = 128 * 3072;
  constexpr int SL = 32 * 256;
  const int kblk = WS_KV1 + ((m * 64 + w * 16) * 32 + c) * 256;
  const int zblk = WS_KVZ + ((m * 4 + w) * 32 + c) * 256;
  int kvo1 = g * 2 * SL + tok * 16, zvo1 = ((g >> 1) * 16 + tok) * 8;
  asm volatile("" : "+v"(kvo1), "+v"(zvo1));
  auto kv_pass = [&](auto FIRST) {
    ST st;
    gather_start<false>(ctx, st, L, lv, xs);
    auto r_sh = [&](auto) {};
    auto r_gp = [&](auto) {};
    ring_prime<16384>(ab0);
    TS(0);
    gemm_tile<-1, P_AK, P_AV, 16384, false>(ab0, xk, xk, xv,
      [&](auto ks) { return IC<(FIRST ? 0 : ks == 10 ? 2 : ks == 11 ? -1 : 0) + decltype(xl(ks))::value>{}; },
      [&](auto h) {
        if constexpr (FIRST) {
          return c_sh(IC<9>{}, IC<0>{}, IC<L_RQ>{}, qd.sb, h);
        } else {
          if constexpr (h == GA(0)) dma_so<3 * 16384>(so, xs);
          if constexpr (h == GA(1)) dma_m0<L_XT + 3 * 16384>(ctx.Lw);
          if constexpr (h == GA(2)) WAIT_VMV(5);
          if constexpr (h == GA(11)) WAIT_VMV(4);
          if constexpr (h == GA(12)) RAWBAR();
          if constexpr (h == GA(13)) dma_so<4 * 16384>(so, xs);
          if constexpr (h == GA(14)) dma_m0<L_XT + 4 * 16384>(ctx.Lw);
          return h == GA(0) || h == GA(1) || h == GA(2) || (h >= GA(11) && h <= GA(14));
        }
      },
      [&](auto h) {
        if constexpr (FIRST) {
          c_gp(IC<9>{}, qd, rsa, h);
        } else {
          if constexpr (h == GA(1)) dma_go_sc1<0>(lv, ctx.rs, so);
          if constexpr (h == GA(4)) dma_go_sc1<1>(lv, ctx.rs, so);
          if constexpr (h == GA(7)) dma_go_sc1<2>(lv, ctx.rs, so);
          if constexpr (h == GC(10)) dma_go_sc1<3>(lv, ctx.rs, so);
          if constexpr (h == GC(14)) dma_go_sc1<0>(lv, ctx.rs, so);
          st_gaps<h>(ctx, st);
        }
        if constexpr (h == GC(15)) NOP(1);
        stat_rd(IC<1>{}, h);
      });
    next(xk, xv);
    TS(1);
    gemm_tile<-1, P_AK, P_AV, 32768, false>(ab0, yk, yk, yv, xl,
      [&](auto h) {
        if constexpr (FIRST) {
          return c_sh(IC<1>{}, IC<QS>{}, IC<L_RQ + 16384>{}, qd.sb, h) | c_sh(IC<9>{}, IC<2 * QS>{}, IC<L_RQ + 32768>{}, qd.sb, h);
        } else {
          if constexpr (h == GA(11)) WAIT_VMV(6);
          if constexpr (h == GA(12)) RAWBAR();
          return c_sh(IC<7>{}, IC<0>{}, IC<L_RQ>{}, qd.sb, h) | (h == GA(11) || h == GA(12));
        }
      },
      [&](auto h) {
        if constexpr (FIRST) {
          c_gp(IC<1>{}, qd, rsa, h); c_gp(IC<9>{}, qd, rsa, h);
        } else {
          if constexpr (h == GA(0)) dma_go_sc1<1>(lv, ctx.rs, so);
          if constexpr (h == GC(3)) dma_go_sc1<2>(lv, ctx.rs, so);
          if constexpr (h == GC(5)) dma_go_sc1<3>(lv, ctx.rs, so);
          c_gp(IC<7>{}, qd, rsa, h);
        }
        stat_rd(IC<2>{}, h); epi(h);
      });
    next(yk, yv);
    TS(2);
    gemm_tile<-1, P_AK, P_AV, 0, false>(ab1, xk, xk, xv, xl,
      [&](auto h) {
        if constexpr (FIRST) {
          if constexpr (h / 4 >= 8) { r_sh(IC<h / 4 - 8>{}); return true; }
          else return c_sh(IC<1>{}, IC<3 * QS>{}, IC<L_RQ + 49152>{}, qd.sb, h);
        } else {
          if constexpr (h == GA(11)) WAIT_VMV(8);
          if constexpr (h == GA(12)) RAWBAR();
          return c_sh(IC<1>{}, IC<QS>{}, IC<L_RQ + 16384>{}, qd.sb, h) | c_sh(IC<9>{}, IC<2 * QS>{}, IC<L_RQ + 32768>{}, qd.sb, h) | (h == GA(11) || h == GA(12));
        }
      },
      [&](auto h) {
        if constexpr (FIRST) { c_gp(IC<1>{}, qd, rsa, h); if constexpr ((h & 3) == 2 && h / 4 >= 8) r_gp(IC<h / 4 - 8>{}); }
        else { c_gp(IC<1>{}, qd, rsa, h); c_gp(IC<9>{}, qd, rsa, h); }
        stat_rd(IC<3>{}, h); epi2(IC<1>{}, h); epi(h);
      });
    next(xk, xv);
    TS(3);
    gemm_tile<-1, P_AK, P_AV, 16384, false>(ab1, yk, yk, yv, xl,
      [&](auto h) {
        if constexpr (FIRST) {
          if constexpr (h / 4 < 8) { r_sh(IC<8 + h / 4>{}); return true; }
          else return c_sh(IC<9>{}, IC<0>{}, IC<L_XT>{}, wd.sb, h);
        } else {
          return c_sh(IC<1>{}, IC<3 * QS>{}, IC<L_RQ + 49152>{}, qd.sb, h) | c_sh(IC<9>{}, IC<0>{}, IC<L_XT>{}, wd.sb, h);
        }
      },
      [&](auto h) {
        if constexpr (FIRST) { if constexpr ((h & 3) == 2 && h / 4 < 8) r_gp(IC<8 + h / 4>{}); }
        else c_gp(IC<1>{}, qd, rsa, h);
        c_gp(IC<9>{}, wd, rwo, h); stat_rd(IC<4>{}, h); epi2(IC<0>{}, h); epi(h);
      });
    next(yk, yv);
  TS(4);
  {
    FM& f = e.f;
    A_MULS(f.c, LOG2E, e.st[1]);
    NOP(0);
    mf16z_b(e.tm, v2u{f.nb[0], f.nb[1]}, e.ident);
    A_FMA(e.k[0], e.st[0], e.csk[0], e.ak[0]); A_FMA(e.k[1], e.st[0], e.csk[1], e.ak[1]);
    A_FMA(e.k[2], e.st[0], e.csk[2], e.ak[2]); A_FMA(e.k[3], e.st[0], e.csk[3], e.ak[3]);
    A_CVT(e.kb[0], e.k[0], e.k[1]); A_CVT(e.php[0], e.tp[0], e.tp[1]); A_CVT(e.kb[1], e.k[2], e.k[3]); A_CVT(e.php[1], e.tp[2], e.tp[3]);
    A_CVT(e.phm[0], e.tm[0], e.tm[1]);
    mf16z_a(f.p, e.wphi, v2u{e.kb[0], e.kb[1]});
    A_CVT(e.phm[1], e.tm[2], e.tm[3]); A_FMA(e.v[0], e.nm4[0], e.csv, e.av[0]);
    mf16c(e.kvp, v2u{e.php[0], e.php[1]}, v2u{e.vbo[0], e.vbo[1]});
    A_FMA(e.v[1], e.nm4[1], e.csv, e.av[1]); A_FMA(e.v[2], e.nm4[2], e.csv, e.av[2]);
    mf16c(e.kvm, v2u{e.phm[0], e.phm[1]}, v2u{e.vbo[0], e.vbo[1]});
    A_FMA(e.v[3], e.nm4[3], e.csv, e.av[3]); A_MUL(e.v[0], e.v[0], e.rs4[0]);
    FM_MM1(f);
    mf16c_a(e.zz, e.lo1, v2u{e.php[0], e.php[1]});
    FM_MM2(f);
    A_MUL(e.v[1], e.v[1], e.rs4[1]); A_MUL(e.v[2], e.v[2], e.rs4[2]);
    A_SW16(f.m, f.m2);
    mf16c_a(e.zz, e.hi1, v2u{e.phm[0], e.phm[1]});
    FM_MX2(f);
    A_MUL(e.v[3], e.v[3], e.rs4[3]); A_CVT(e.vb[0], e.v[0], e.v[1]);
    A_SW32(f.m, f.m2);
    FM_MXC(f);
    A_CVT(e.vb[1], e.v[2], e.v[3]);
    NOP(0);
    A_SW16(f.m, f.m2);
#pragma unroll
    for (int r = 0; r < 4; ++r) A_FMA(f.ep[r], f.p[r], f.c, f.m);
#pragma unroll
    for (int r = 0; r < 4; ++r) A_FMAN(f.en[r], f.p[r], f.c, f.m2);
#pragma unroll
    for (int r = 0; r < 4; ++r) A_EXP(f.ep[r], f.ep[r]);
    A_EXP(f.en[0], f.en[0]);
    A_CVT(f.eb[0], f.ep[0], f.ep[1]); A_CVT(f.eb[1], f.ep[2], f.ep[3]);
    A_EXP(f.en[1], f.en[1]); A_EXP(f.en[2], f.en[2]);
    mf16z_a(f.sp, e.ones, v2u{f.eb[0], f.eb[1]});
    A_EXP(f.en[3], f.en[3]);
    A_CVT(f.nb[0], f.en[0], f.en[1]); A_CVT(f.nb[1], f.en[2], f.en[3]);
    NOP(1);
    mf16z_a(f.sn, e.ones, v2u{f.nb[0], f.nb[1]});
    NOP(1);
    A_RCP(f.m, f.sp);
    NOP(0);
#pragma unroll
    for (int r = 0; r < 4; ++r) A_MUL(f.ep[r], f.ep[r], f.m);
    A_RCP(f.m2, f.sn);
    A_CVT(f.eb[0], f.ep[0], f.ep[1]); A_CVT(f.eb[1], f.ep[2], f.ep[3]);
    A_MUL(f.en[0], f.en[0], f.m2); A_MUL(f.en[1], f.en[1], f.m2);
    mf16z_b(e.tp, v2u{f.eb[0], f.eb[1]}, e.ident);
    A_MUL(f.en[2], f.en[2], f.m2); A_MUL(f.en[3], f.en[3], f.m2);
    A_CVT(f.nb[0], f.en[0], f.en[1]); A_CVT(f.nb[1], f.en[2], f.en[3]);
    NOP(1);
    mf16z_b(e.tm, v2u{f.nb[0], f.nb[1]}, e.ident);
    NOP(0);
    A_CVT(e.php[0], e.tp[0], e.tp[1]); A_CVT(e.php[1], e.tp[2], e.tp[3]);
    NOP(1);
    mf16c(e.kvp, v2u{e.php[0], e.php[1]}, v2u{e.vb[0], e.vb[1]});
    mf16c_a(e.zz, e.lo1, v2u{e.php[0], e.php[1]});
    NOP(0);
    A_CVT(e.phm[0], e.tm[0], e.tm[1]); A_CVT(e.phm[1], e.tm[2], e.tm[3]);
    NOP(1);
    mf16c(e.kvm, v2u{e.phm[0], e.phm[1]}, v2u{e.vb[0], e.vb[1]});
    mf16c_a(e.zz, e.hi1, v2u{e.phm[0], e.phm[1]});
    asm volatile("s_nop 0" : "+v"(e.kvp));
  }
  TS(5);
  {
    const int blk = kblk, vo = kvo1;
    st128(ctx, v4u{asu(e.kvp[0]), T, asu(e.kvp[1]), T}, vo, blk);
    st128(ctx, v4u{asu(e.kvp[2]), T, asu(e.kvp[3]), T}, vo, blk + SL);
    asm volatile("s_nop 3" : "+v"(e.kvm), "+v"(e.zz));
    st128(ctx, v4u{asu(e.kvm[0]), T, asu(e.kvm[1]), T}, vo, blk + 8 * SL);
    st128(ctx, v4u{asu(e.kvm[2]), T, asu(e.kvm[3]), T}, vo, blk + 9 * SL);
    st64(ctx, v2u{asu(e.zz[0]), T}, zvo1, zblk);
  }
  };
  if (lp > 0) kv_pass(std::false_type{});
  STAMP(L * 8 + 2);
  if (lp) TS_FLUSH(L * 8 + 2, 6);
  f4 aq[4];
  v4u dk[4], dz;
  const int S64 = 2 * c + (w >> 1);
  const int rblk = WS_KV1 + ((m * 64 + S64) * 32) * 256 + (w & 1) * 128;
  const int rzb = WS_KVZ + ((m * 4 + (S64 >> 4)) * 32) * 256 + (S64 & 15) * 16;
  int kvo = (lane >> 3) * 256 + (lane & 7) * 16, zvo = (lane & 31) * 256;
  {
    lds_t* qt = ctx.L + opq(L_RQ + tr_base(lane, w));
    asm volatile("" : "+v"(kvo), "+v"(zvo));
    TS(0);
    WAIT_VM(8);
    RAWBAR();
    TS(1);
    v4u wq[16];
#pragma unroll
    for (int ks = 0; ks < 16; ++ks) wq[ks] = tr_frag(qt, ks * 4096);
    if (lp == 0) {
      { Ctx c2 = ctx; fresh(c2); raw_load<P_BK, 0, 16>(c2, rsb, 1024); }
      f4 c0, c1;
      v4u ones = {mk1(), mk1(), mk1(), mk1()};
      asm volatile("s_nop 1" : "+v"(ones));
      sfor<0, 8>([&](auto k) { mf_av<k == 0>(c0, wq[2 * k], ones); mf_av<k == 0>(c1, wq[2 * k + 1], ones); });
      asm volatile("s_nop 7" : "+v"(c0), "+v"(c1));
      if (tok == 0) *(LAS f4*)(ctx.L + csb + g * 16) = c0 + c1;
    }
    v4u a1[4];
    auto rd = [&](auto k) {
      (void)a1;
      if constexpr (k < 16) { ring_rd<k & 3, 16384 + k * 1024>(ab0); } else if constexpr (k < 32) { ring_rd<k & 3, (k - 16) * 1024>(ab1); }
    };
    auto rd1 = [&](auto k) {
      (void)a1; (void)ab0; (void)ab1;
      if constexpr (k < 16) { A_LD128(a1[k & 3], ab0, 32768 + k * 1024); } else if constexpr (k < 32) { A_LD128(a1[k & 3], ab1, 16384 + (k - 16) * 1024); }
    };
    sfor<0, 3>([&](auto k) { rd(k); rd1(k); });
#pragma unroll
    for (int ks = 0; ks < 16; ++ks) asm volatile("" : "+a"(wq[ks]));
    WAIT_LG(4);
    TS(2);
    sfor<0, 32>([&](auto k) {
      constexpr int ks = k & 15, u = k >> 4;
      (void)dk; (void)dz; (void)kvo; (void)zvo; (void)rblk; (void)rzb;
      mmq<k & 3, ks == 0>(aq[2 * u], wq[ks]);
      VNOP();
      rd(IC<k + 3>{});
      if constexpr (k >= QPOLL && k < QPOLL + 4) {
        constexpr int i = k - QPOLL;
        asm volatile("buffer_load_dwordx4 %0, %1, %2, %3 offen offset:%4 sc1" : "=v"(dk[i]) : "v"(kvo), "s"(ctx.rs), "s"(i < 2 ? rblk : rblk + 4096), "n"((i & 1) * 2048) : "memory");
      }
      if constexpr (k == QPOLL + 4) asm volatile("buffer_load_dwordx4 %0, %1, %2, %3 offen sc1" : "=v"(dz) : "v"(zvo), "s"(ctx.rs), "s"(rzb) : "memory");
      mf_av<ks == 0>(aq[2 * u + 1], wq[ks], a1[k & 3]);
      WAIT_LG(k + 3 < 32 ? 3 : k == 29 ? 2 : 0);
      rd1(IC<k + 3>{});
      SB();
    });
  }
  TS(6);
  TS_FLUSH(34 + lp * 8, 7);
  STAMP(48 + lp * 4);
  v4u phq[4], d2[4];
  const int b2 = WS_KV2 + (m * 64 + w * 16) * 256;
  int vo2 = g * 512 + tok * 16;
  {
    const f4 csq = lds_ldf4(ctx.L, csb + g * 16);
    f2 st[4];
#pragma unroll
    for (int t = 0; t < 4; ++t) st[t] = *(LAS f2*)(ctx.L + L_REC + ((1 + t) * 16 + tok) * 16);
    int svo = (lane & 7) * 16 + ((lane >> 4) & 1) * 8;
    const u64 mhi = __ballot(lane >= 32);
    const int sblk = WS_KV2 + (m * 64 + S64) * 256 + (w & 1) * 128;
    asm volatile("s_nop 3" : "+v"(aq[0]), "+v"(aq[1]), "+v"(aq[2]), "+v"(aq[3]), "+v"(svo), "+v"(vo2));
    FM f[4];
    unsigned qb[4][2];
#pragma unroll
    for (int t = 0; t < 4; ++t) {
      float q[4];
#pragma unroll
      for (int r = 0; r < 4; ++r) A_FMA(q[r], st[t][0], csq[r], aq[t][r]);
      A_MULS(f[t].c, LOG2E, st[t][1]);
      VNOP();
      A_CVT(qb[t][0], q[0], q[1]); A_CVT(qb[t][1], q[2], q[3]);
      if (t > 0) mf16z_a(f[t - 1].p, e.wphi, v2u{qb[t - 1][0], qb[t - 1][1]});
    }
    FM_MM1(f[0]);
    mf16z_a(f[3].p, e.wphi, v2u{qb[3][0], qb[3][1]});
    FM_MM1(f[1]); FM_MM2(f[0]); FM_MM2(f[1]); FM_MM1(f[2]);
    A_SW16(f[0].m, f[0].m2); FM_MM2(f[2]); FM_MM1(f[3]); A_SW16(f[1].m, f[1].m2); FM_MM2(f[3]);
    FM_MX2(f[0]); A_SW16(f[2].m, f[2].m2); FM_MX2(f[1]); A_SW16(f[3].m, f[3].m2);
    A_SW32(f[0].m, f[0].m2); FM_MX2(f[2]); A_SW32(f[1].m, f[1].m2); FM_MX2(f[3]);
    FM_MXC(f[0]); A_SW32(f[2].m, f[2].m2); FM_MXC(f[1]); A_SW32(f[3].m, f[3].m2);
    A_SW16(f[0].m, f[0].m2); FM_MXC(f[2]); A_SW16(f[1].m, f[1].m2); FM_MXC(f[3]);
    {
      asm volatile("s_waitcnt vmcnt(0)" : "+v"(dk[0]), "+v"(dk[1]), "+v"(dk[2]), "+v"(dk[3]), "+v"(dz) :: "memory");
      STAMP(49 + lp * 4);
      for (;;) {
        unsigned bad;
        asm volatile("v_sad_u32 %0, %1, %11, 0 clamp\n\tv_sad_u32 %0, %2, %11, %0 clamp\n\tv_sad_u32 %0, %3, %11, %0 clamp\n\tv_sad_u32 %0, %4, %11, %0 clamp\n\t"
                     "v_sad_u32 %0, %5, %11, %0 clamp\n\tv_sad_u32 %0, %6, %11, %0 clamp\n\tv_sad_u32 %0, %7, %11, %0 clamp\n\tv_sad_u32 %0, %8, %11, %0 clamp\n\t"
                     "v_sad_u32 %0, %9, %11, %0 clamp\n\tv_sad_u32 %0, %10, %11, %0 clamp"
                     : "=&v"(bad) : "v"(dk[0].y), "v"(dk[0].w), "v"(dk[1].y), "v"(dk[1].w), "v"(dk[2].y), "v"(dk[2].w), "v"(dk[3].y), "v"(dk[3].w),
                       "v"(dz.y), "v"(dz.w), "s"(T));
        if (__builtin_expect(__all(bad == 0), 1) || --ctx.budget < 0) break;
        asm volatile("" ::: "memory");
#pragma unroll
        for (int i = 0; i < 4; ++i) dk[i] = ld128(ctx, kvo, rblk + i * 2048);
        dz = ld128(ctx, zvo, rzb);
      }
      float K, K1, Z; unsigned pk, tg;
      asm volatile("v_add_f32 %0, %5, %6\n\tv_add_f32 %1, %9, %10\n\tv_add_f32 %2, %7, %8\n\tv_add_f32 %3, %11, %12\n\t"
                   "v_add_f32 %0, %0, %2\n\tv_add_f32 %1, %1, %3\n\tv_cndmask_b32 %2, %13, %14, %15\n\tv_mov_b32 %4, %16\n\t"
                   "v_permlane32_swap_b32 %0, %1\n\tv_add_f32 %0, %0, %1\n\ts_nop 1\n\t"
                   "v_permlane16_swap_b32 %0, %2\n\tv_add_f32 %0, %0, %2\n\ts_nop 1\n\t"
                   "v_add_f32_dpp %0, %0, %0 row_ror:8 row_mask:0xf bank_mask:0xf\n\ts_nop 1\n\t"
                   "v_add_f32_dpp %0, %0, %0 row_ror:4 row_mask:0xa bank_mask:0xf\n\ts_nop 1\n\t"
                   "v_add_f32_dpp %0, %0, %0 row_ror:2 row_mask:0xa bank_mask:0xf\n\ts_nop 1\n\t"
                   "v_add_f32_dpp %0, %0, %0 row_ror:1 row_mask:0xa bank_mask:0xf\n\t"
                   "v_mov_b32 %1, %0\n\ts_nop 1\n\tv_permlane32_swap_b32 %0, %1\n\tv_cvt_pk_bf16_f32 %3, %0, %1"
                   : "=&v"(K), "=&v"(K1), "=&v"(Z), "=&v"(pk), "=&v"(tg)
                   : "v"(dk[0].x), "v"(dk[1].x), "v"(dk[2].x), "v"(dk[3].x), "v"(dk[0].z), "v"(dk[1].z), "v"(dk[2].z), "v"(dk[3].z),
                     "v"(dz.x), "v"(dz.z), "s"(mhi), "s"(T));
      st64(ctx, v2u{pk, tg}, svo, sblk);
      STAMP(L * 8 + 3);
    }
    RAWBAR();
    sfor<0, 4>([&](auto t) {
      (void)f; (void)e;
      auto piece = [&](auto p) {
        if constexpr ((p & 3) == 0) { dma_so<128 * 1024 * (1 + (p >> 2))>(so, wd.sb); dma_m0<L_XT + 16384 * (1 + (p >> 2))>(ctx.Lw); VNOP(); }
        dma_go<p & 3>(wd.v[p & 3], rwo, so);
      };
      piece(IC<3 * t>{});
#pragma unroll
      for (int r = 0; r < 4; ++r) A_FMA(f[t].ep[r], f[t].p[r], f[t].c, f[t].m);
      if constexpr (t < 2) A_SW16(f[t + 2].m, f[t + 2].m2);
#pragma unroll
      for (int r = 0; r < 4; ++r) A_FMAN(f[t].en[r], f[t].p[r], f[t].c, f[t].m2);
      if constexpr (t > 0) mf16z_a(f[t - 1].sn, e.ones, v2u{f[t - 1].nb[0], f[t - 1].nb[1]}); else VNOP();
      piece(IC<3 * t + 1>{});
#pragma unroll
      for (int r = 0; r < 4; ++r) A_EXP(f[t].ep[r], f[t].ep[r]);
      A_EXP(f[t].en[0], f[t].en[0]);
      piece(IC<3 * t + 2>{});
      A_CVT(f[t].eb[0], f[t].ep[0], f[t].ep[1]); A_CVT(f[t].eb[1], f[t].ep[2], f[t].ep[3]);
      A_EXP(f[t].en[1], f[t].en[1]); A_EXP(f[t].en[2], f[t].en[2]);
      mf16z_a(f[t].sp, e.ones, v2u{f[t].eb[0], f[t].eb[1]});
      A_EXP(f[t].en[3], f[t].en[3]);
      A_CVT(f[t].nb[0], f[t].en[0], f[t].en[1]); A_CVT(f[t].nb[1], f[t].en[2], f[t].en[3]);
    });
    if (lp == 0) { Ctx c2 = ctx; fresh(c2); if (PF_B) pf_pair<3072, 1024>(c2, rsb, 0, rwn, 0); part_step_ld<P_BK, P_BV>(c2, L_CS + (w * 6 + 4) * 64, rsb, 2048); }
#pragma unroll
    for (int t = 0; t < 4; ++t) {
      A_RCP(f[t].m, f[t].sp); A_RCP(f[t].m2, f[t].sn);
      if (t == 0) mf16z_a(f[3].sn, e.ones, v2u{f[3].nb[0], f[3].nb[1]});
      if (t == S2POLL) {
        asm volatile("buffer_load_dwordx4 %0, %4, %5, %6 offen sc1\n\tbuffer_load_dwordx4 %1, %4, %5, %6 offen offset:256 sc1\n\t"
                     "buffer_load_dwordx4 %2, %4, %5, %6 offen offset:2048 sc1\n\tbuffer_load_dwordx4 %3, %4, %5, %6 offen offset:2304 sc1"
                     : "=&v"(d2[0]), "=&v"(d2[1]), "=&v"(d2[2]), "=&v"(d2[3]) : "v"(vo2), "s"(ctx.rs), "s"(b2) : "memory");
      }
#pragma unroll
      for (int r = 0; r < 4; ++r) A_MUL(f[t].ep[r], f[t].ep[r], f[t].m);
#pragma unroll
      for (int r = 0; r < 4; ++r) A_MUL(f[t].en[r], f[t].en[r], f[t].m2);
      unsigned x0, x1, x2, x3;
      A_CVT(x0, f[t].ep[0], f[t].ep[1]); A_CVT(x1, f[t].ep[2], f[t].ep[3]); A_CVT(x2, f[t].en[0], f[t].en[1]); A_CVT(x3, f[t].en[2], f[t].en[3]);
      phq[t] = v4u{x0, x1, x2, x3};
    }
  }
  STAMP(50 + lp * 4);
  {
    int ovo = ((g >> 1) * 16 + tok) * 16 + (g & 1) * 8;
    const int osb = WS_O + (4 * c * 16 + 2 * m + (w >> 1)) * 1024 + (w & 1) * 512;
    asm volatile("s_waitcnt vmcnt(0)" : "+v"(d2[0]), "+v"(d2[1]), "+v"(d2[2]), "+v"(d2[3]), "+v"(ovo) :: "memory");
    for (;;) {
      unsigned bad;
      asm volatile("v_sad_u32 %0, %1, %9, 0 clamp\n\tv_sad_u32 %0, %2, %9, %0 clamp\n\tv_sad_u32 %0, %3, %9, %0 clamp\n\tv_sad_u32 %0, %4, %9, %0 clamp\n\t"
                   "v_sad_u32 %0, %5, %9, %0 clamp\n\tv_sad_u32 %0, %6, %9, %0 clamp\n\tv_sad_u32 %0, %7, %9, %0 clamp\n\tv_sad_u32 %0, %8, %9, %0 clamp"
                   : "=&v"(bad) : "v"(d2[0].y), "v"(d2[0].w), "v"(d2[1].y), "v"(d2[1].w), "v"(d2[2].y), "v"(d2[2].w), "v"(d2[3].y), "v"(d2[3].w), "s"(T));
      if (__builtin_expect(__all(bad == 0), 1) || --ctx.budget < 0) break;
      asm volatile("" ::: "memory");
      d2[0] = ld128(ctx, vo2, b2); d2[1] = ld128(ctx, vo2 + 256, b2); d2[2] = ld128(ctx, vo2 + 2048, b2); d2[3] = ld128(ctx, vo2 + 2304, b2);
    }
    STAMP(L * 8 + 4);
    v4u akv = {d2[0].x, d2[1].x, d2[2].x, d2[3].x}, az = {d2[0].z, d2[1].z, d2[2].z, d2[3].z};
    f4 num[4], den[4];
    asm volatile("s_nop 1" : "+v"(akv), "+v"(az));
#pragma unroll
    for (int t = 0; t < 4; ++t) { mf32z(den[t], az, phq[t]); mf32z(num[t], akv, phq[t]); }
#pragma unroll
    for (int t = 0; t < 4; ++t) {
      float r, q0, q1, q2, q3; unsigned o0, o1;
      asm volatile("v_add_f32 %2, %12, %7\n\tv_rcp_f32 %2, %2\n\ts_nop 0\n\t"
                   "v_mul_f32 %3, %8, %2\n\tv_mul_f32 %4, %9, %2\n\tv_mul_f32 %5, %10, %2\n\tv_mul_f32 %6, %11, %2\n\t"
                   "v_cvt_pk_bf16_f32 %0, %3, %4\n\tv_cvt_pk_bf16_f32 %1, %5, %6"
                   : "=&v"(o0), "=&v"(o1), "=&v"(r), "=&v"(q0), "=&v"(q1), "=&v"(q2), "=&v"(q3)
                   : "v"(den[t][0]), "v"(num[t][0]), "v"(num[t][1]), "v"(num[t][2]), "v"(num[t][3]), "s"(1e-6f), "v"(den[t]));
      st64(ctx, v2u{o0, o1}, ovo, osb + t * 16384);
    }
  }
  DRAIN();
  BAR();
  flag_store(ctx, WS_FO + c * 128);
}

template <bool ODD>
DEV void out_proj(Ctx& ctx, int lp, f4 (&xacc)[4], rsrc_t out, rsrc_t rsb) {
  fresh(ctx);
  const int L = 2 * lp + (ODD ? 1 : 0);
  const bool last = ODD && lp == 1, first = !ODD && lp == 0;
  const int c = ctx.c, m = ctx.m, w = ctx.w, lane = ctx.lane, g = ctx.g, tok = ctx.tok;
  if (first) part_step_ld<P_BV, P_BQ>(ctx, L_CS + (w * 6 + 5) * 64, rsb, 0);
  lds_t* tb[4];
#pragma unroll
  for (int nt = 0; nt < 4; ++nt) tb[nt] = ctx.L + opq(L_XT + tr_base(lane, nt));
  v4u wf[4][4];
#pragma unroll
  for (int i = 0; i < 4; ++i)
#pragma unroll
    for (int nt = 0; nt < 4; ++nt) wf[i][nt] = tr_frag(tb[nt], i * 4096);
  const int G = ((g & 1) << 1) | (g >> 1);
  int xvo = (G * 16 + tok) * 16;
  u64 m1 = __ballot(tok & 1), m2 = __ballot(tok & 2);
  const int osb = WS_O + (4 * c + w) * 16384;
  const int xsb = WS_X + (ODD ? ACT_L : 0) + ((4 * c + w) * 16 + 2 * m) * 1024;
  const int ssb = WS_STAT + (ODD ? STAT_L : 0) + ((c * 8 + m) * 64 + 16 * w) * 16;
  asm volatile("" : "+v"(xvo), "+v"(xacc[0]), "+v"(xacc[1]), "+v"(xacc[2]), "+v"(xacc[3]));
#pragma unroll
  for (int nt = 0; nt < 4; ++nt) asm volatile("" : "+v"(wf[0][nt]), "+v"(wf[1][nt]), "+v"(wf[2][nt]), "+a"(wf[3][nt]));
  wait_flags(ctx, WS_FO + ((ODD ? 32 : 0) + c) * 128, 0, false);
  if (ODD) ctx.T += 1u;
  STAMP(L * 8 + 5);
  v4u of[16];
  constexpr int PRE = 8;
#pragma unroll
  for (int ks = 0; ks < PRE; ++ks) of[ks] = ld128(ctx, lane * 16 + (ks & 3) * 1024, osb + (ks & ~3) * 1024);
  SB();
  sfor<0, 16>([&](auto ks) {
    if constexpr (ks + PRE < 16) { of[ks + PRE] = ld128(ctx, lane * 16 + ((ks + PRE) & 3) * 1024, osb + ((ks + PRE) & ~3) * 1024); SB(); }
#pragma unroll
    for (int nt = 0; nt < 4; ++nt) {
      if constexpr ((ks & 3) < 3) mf_va(xacc[nt], wf[ks & 3][nt], of[ks]); else mf_aa(xacc[nt], wf[ks & 3][nt], of[ks]);
      if constexpr (ks + 4 < 16) wf[ks & 3][nt] = tr_frag(tb[nt], (ks + 4) * 4096);
      SB();
    }
  });
  NOP(4);
  STAMP(L * 8 + 6);
  v4u fr[2];
#pragma unroll
  for (int u = 0; u < 2; ++u) {
    unsigned a0, a1, b0, b1;
    const f4 a = xacc[2 * u], b = xacc[2 * u + 1];
    asm volatile("v_cvt_pk_bf16_f32 %0, %4, %5\n\tv_cvt_pk_bf16_f32 %1, %6, %7\n\tv_cvt_pk_bf16_f32 %2, %8, %9\n\tv_cvt_pk_bf16_f32 %3, %10, %11\n\t"
                 "s_nop 0\n\tv_permlane16_swap_b32 %0, %2\n\tv_permlane16_swap_b32 %1, %3"
                 : "=&v"(a0), "=&v"(a1), "=&v"(b0), "=&v"(b1)
                 : "v"(a[0]), "v"(a[1]), "v"(a[2]), "v"(a[3]), "v"(b[0]), "v"(b[1]), "v"(b[2]), "v"(b[3]));
    fr[u] = v4u{a0, a1, b0, b1};
    if (!last) st128(ctx, fr[u], xvo + u * 1024, xsb);
  }
  if (last) {
#pragma unroll
    for (int u = 0; u < 2; ++u)
      __builtin_amdgcn_raw_buffer_store_b128(fr[u], out, tok * 1024 + G * 16 + u * 64, (64 * c + 16 * w) * 1024 + 128 * m, 0);
  } else {
    f4 ssum, gram;
    v4u ones = {mk1(), mk1(), mk1(), mk1()};
    asm volatile("s_nop 1" : "+v"(ones));
    mf32z(gram, fr[0], fr[0]); mf32z(ssum, ones, fr[0]);
    mf32c(gram, fr[1], fr[1]); mf32c(ssum, ones, fr[1]);
    asm volatile("s_nop 7" : "+v"(gram), "+v"(ssum));
    float q, t;
    asm volatile("v_cndmask_b32 %0, %2, %3, %6\n\tv_cndmask_b32 %1, %4, %5, %6\n\tv_cndmask_b32 %0, %0, %1, %7"
                 : "=&v"(q), "=&v"(t) : "v"(gram[0]), "v"(gram[1]), "v"(gram[2]), "v"(gram[3]), "s"(m1), "s"(m2));
    if (g == (tok >> 2)) st128(ctx, v4u{asu(ssum[0]), ctx.T, asu(q), ctx.T}, tok * 16, ssb);
    WAIT_VM(1);
    BAR();
    flag_store(ctx, WS_FX + ((ODD ? 32 : 0) + c) * 128);
    if (first) part_step<P_BQ>(ctx, L_CS + (w * 6 + 3) * 64);
  }
  asm volatile("" : "+a"(xacc[0]), "+a"(xacc[1]), "+a"(xacc[2]), "+a"(xacc[3]));
  STAMP(L * 8 + 7);
}

__global__ void __launch_bounds__(NT, 1) __attribute__((amdgpu_num_vgpr(88)))
fused_kernel(const unsigned char* __restrict__ x_, const unsigned char* __restrict__ wa_, const unsigned char* __restrict__ wphi_,
             const unsigned char* __restrict__ woa_, const unsigned char* __restrict__ wb_, const unsigned char* __restrict__ wob_,
             unsigned char* out_, unsigned char* ws_) {
  extern __shared__ __attribute__((aligned(16))) unsigned char smem[];
  asm volatile("" ::: "v255", "a255");
  Ctx ctx;
  ctx.ws = (GAS unsigned char*)ws_;
  ctx.rs = __builtin_amdgcn_make_buffer_rsrc((void*)ws_, 0, WS_END, 0x00020000);
  ctx.L = (lds_t*)smem;
  ctx.Lb = (unsigned)(size_t)ctx.L;
  const int b = blockIdx.x, idx = b >> 3;
  ctx.c = (b & 7) * 4 + (idx >> 3);
  ctx.m = idx & 7;
  ctx.w = __builtin_amdgcn_readfirstlane(threadIdx.x >> 6);
  ctx.budget = SPIN_CAP;
  fresh(ctx);
  const int c = ctx.c, m = ctx.m, w = ctx.w, lane = ctx.lane, g = ctx.g, tok = ctx.tok;
  STAMP(40); CSTAMP(0);
  const rsrc_t rsa = __builtin_amdgcn_make_buffer_rsrc((void*)wa_, 0, 512 * 3072, 0x00020000);
  const rsrc_t rsb = __builtin_amdgcn_make_buffer_rsrc((void*)wb_, 0, 512 * 3072, 0x00020000);
  const rsrc_t rwa = __builtin_amdgcn_make_buffer_rsrc((void*)woa_, 0, 512 * 1024, 0x00020000);
  const rsrc_t rwb = __builtin_amdgcn_make_buffer_rsrc((void*)wob_, 0, 512 * 1024, 0x00020000);
  {
    u64 pc; unsigned len;
    asm volatile("s_getpc_b64 %0\n.Lfk_here:\n\ts_mov_b32 %1, .Lfk_end-.Lfk_here" : "=s"(pc), "=s"(len));
    const rsrc_t rc = __builtin_amdgcn_make_buffer_rsrc((void*)pc, 0, len, 0x00020000);
    asm volatile("s_nop 4" ::: "memory");
    pf1<0>(lane * 16, rc, (idx * 4 + w) * 1024);
  }
  unsigned epoch, ph0, ph1, ph2, ph3;
  {
    const GAS unsigned char* pp = (const GAS unsigned char*)wphi_ + (4 * m + w) * 512;
    const unsigned pv = (unsigned)(4 * g * 16 + tok) * 2, zero = 0;
    asm volatile("s_nop 4\n\tglobal_load_dword %0, %5, %6 sc1\n\t"
                 "global_load_ushort %1, %7, %8\n\tglobal_load_ushort %2, %7, %8 offset:32\n\t"
                 "global_load_ushort %3, %7, %8 offset:64\n\tglobal_load_ushort %4, %7, %8 offset:96"
                 : "=&v"(epoch), "=&v"(ph0), "=&v"(ph1), "=&v"(ph2), "=&v"(ph3)
                 : "v"(zero), "s"(ctx.ws), "v"(pv), "s"(pp) : "memory");
  }
  ImgDma kd = img_prep<3072>(ctx);
  const rsrc_t rsx = __builtin_amdgcn_make_buffer_rsrc((void*)x_, 0, 2048 * 1024, 0x00020000);
  const unsigned xvo = (unsigned)((lane >> 2) * 1024 + (((lane & 3) ^ ((4 - (lane >> 4)) & 3)) << 4));
  const int xso = (64 * c + 16 * w) * 1024;
  const unsigned xlb = ctx.Lb + L_XT + (1 + w) * 16384;
  const unsigned rvo = (unsigned)((lane >> 3) * 3072 + (lane & 7) * 16);
  constexpr int KLOC[4] = {L_RQ, L_RQ + 32768, L_XT, L_RQ + 16384}, VLOC[4] = {L_RQ + 16384, L_RQ + 49152, L_RQ, L_RQ + 32768};
  auto piece = [&](auto P) {
    constexpr int p = P, q = p & 3, blk = p >> 2, kind = blk % 3, j = blk / 3;
    if constexpr (kind == 0) dma1<(4 * j + q) * 64, (4 * j + q) * 1024>(xvo, rsx, xso, xlb);
    else if constexpr (blk >= 8) raw_ld1<P_BK + (blk == 8 ? 0 : blk == 10 ? 4 : 8) + q>(rvo, rsa, kd.sb + 1024 * kind + (128 * j + 8 * q) * 3072);
    else dma1<1024 * kind + 128 * j * 3072 + q * 1024, (kind == 1 ? KLOC[j] : VLOC[j]) + q * 1024>(kd.v[q], rsa, kd.sb, ctx.Lw);
  };
  unsigned wp0, wp1;
  {
    const int ch = lane & 7, rb1 = (lane >> 4) & 1;
    const unsigned base = ctx.Lb + L_RQ + (32 * w + (lane >> 3)) * 128 + (ch & 1) * 16;
    wp0 = base + (((ch >> 1) ^ rb1) << 5); wp1 = base + (((ch >> 1) ^ (rb1 | 2)) << 5);
  }
  sfor<0, 12>(piece);
  {
    const int u = b * 4 + w;
    const unsigned l32 = lane & 31;
    if (u < 128) pfw((l32 >> 3) * 3072 + (l32 & 7) * 128, rsa, u * 4 * 3072);
    else if (u < 512) pfw(l32 * 128, rsb, (u - 128) * 4096);
    else if (u < 640) pfw(l32 * 128, rwa, (u - 512) * 4096);
    else pfw(l32 * 128, rwb, ((u - 640) & 127) * 4096);
  }
  sfor<12, PRE0>(piece);
  STAMP(42);
  f4 xacc[4];
  v2u wphi;
  {
    unsigned abx = ctx.Lb + L_XT + 16384 + x0_lane(tok, g), abo = abx + w * 16384;
    unsigned trq = ctx.Lb + L_RQ + tr_base(lane, w), tr0 = ctx.Lb + L_XT + tr_base(lane, w);
    v4u ones = {mk1(), mk1(), mk1(), mk1()};
    asm volatile("" : "+v"(abx), "+v"(abo), "+v"(trq), "+v"(tr0), "+a"(ones));
    f4 ak[4], av[4], csk, csv, gram, ssum;
    v4u xf[2][5];
    TS_DECL;
    auto rd1 = [&](auto KS, auto N) {
      (void)xf; (void)abx; (void)abo;
      constexpr int ks = KS, n = N, j = ks >> 2;
      if constexpr (n == 0) { if constexpr (j == 2) wtr<P_AK + ks, (ks & 3) * 4096>(tr0); else wtr<P_AK + ks, KLOC[j] - L_RQ + (ks & 3) * 4096>(trq); }
      if constexpr (n >= 1 && n <= 4) A_LD128A(xf[ks & 1][n - 1], abx, (n - 1) * 16384 + ks * 1024);
      if constexpr (n == 5) wtr<P_AV + ks, VLOC[j] - L_RQ + (ks & 3) * 4096>(trq);
      if constexpr (n == 6) A_LD128A(xf[ks & 1][4], abo, ks * 1024);
    };
    auto qpiece = [&](auto P) {
      constexpr int p = P;
      dma1<128 * (p >> 2) * 3072 + (p & 3) * 1024, L_RQ + (p >> 2) * 16384 + (p & 3) * 1024>(kd.v[p & 3], rsa, kd.sb, ctx.Lw);
    };
    sfor<0, 4>([&](auto j) {
      (void)epoch; (void)ph0; (void)ph1; (void)ph2; (void)ph3;
      if constexpr (j == 0) asm volatile("s_waitcnt vmcnt(%5)" : "+v"(epoch), "+v"(ph0), "+v"(ph1), "+v"(ph2), "+v"(ph3) : "n"(PRE0 - 12 + PF_U) : "memory");
      if constexpr (j == 1) WAIT_VM(12);
      RAWBAR();
      TS(j);
      sfor<0, 7>([&](auto n) { rd1(IC<4 * j>{}, n); });
      WAIT_LG(0);
      sfor<0, 4>([&](auto i) {
        constexpr int ks = 4 * j + i;
        auto slot = [&](auto n) {
          constexpr int k = 6 * i + n;
          if constexpr (j == 0 && PRE0 + k < 36) piece(IC<PRE0 + k>{});
          if constexpr (j == 1 && k < 12) piece(IC<36 + k>{});
          if constexpr (j == 1 && k == 14) WAIT_VM(12);
          if constexpr (j == 1 && k >= 14 && k < 18) raw_st1<P_BK + k - 14, VLOC[2] - L_RQ + (k - 14) * 1024>((k & 1) ? wp1 : wp0);
          if constexpr (j == 2 && k == 2) WAIT_VM(4);
          if constexpr (j == 2 && k >= 2 && k < 6) raw_st1<P_BK + 2 + k, KLOC[3] - L_RQ + (k - 2) * 1024>((k & 1) ? wp1 : wp0);
          if constexpr (j == 2 && k == 8) WAIT_VM(0);
          if constexpr (j == 2 && k >= 8 && k < 12) raw_st1<P_BK + k, VLOC[3] - L_RQ + (k - 8) * 1024>((k & 1) ? wp1 : wp0);
          if constexpr (j == 2 && k >= 12 && (k % 3) == 0) qpiece(IC<12 + (k - 12) / 3>{});
          if constexpr (j == 3 && (k % 6) == 3) qpiece(IC<k / 6>{});
        };
        auto nx = [&](auto n) { if constexpr (i < 3) rd1(IC<ks + 1>{}, n); };
        sfor<0, 4>([&](auto t) { wxa<P_AK + ks, ks == 0>(ak[t], xf[ks & 1][t]); VNOP(); nx(t); });
        sfor<0, 3>([&](auto t) { wxb<P_AV + ks, ks == 0>(av[t], xf[ks & 1][t]); VNOP(); nx(IC<4 + t>{}); });
        wxb<P_AV + ks, ks == 0>(av[3], xf[ks & 1][3]); VNOP(); slot(IC<0>{});
        mf_aaz<ks == 0>(gram, xf[ks & 1][4], xf[ks & 1][4]); VNOP(); slot(IC<1>{}); slot(IC<2>{});
        mf_aaz<ks == 0>(ssum, ones, xf[ks & 1][4]); VNOP(); slot(IC<3>{});
        wxa<P_AK + ks, ks == 0>(csk, ones); VNOP(); slot(IC<4>{});
        wxb<P_AV + ks, ks == 0>(csv, ones);
        slot(IC<5>{});
        WAIT_LG(0);
        SB();
      });
    });
    TS(4);
    TS_FLUSH(2, 5);
    STAMP(43);
    ctx.T = __builtin_amdgcn_readfirstlane(epoch) + 1u;
    wphi = v2u{ph0 | (ph1 << 16), ph2 | (ph3 << 16)};
    const int csa = L_CS + (w * 6) * 64;
    {
      asm volatile("s_nop 3" : "+v"(gram), "+v"(ssum));
      const float q = (tok & 2) ? ((tok & 1) ? gram[3] : gram[2]) : ((tok & 1) ? gram[1] : gram[0]);
      const float mean = ssum[0] * (1.0f / 512.0f);
      const float var = fmaxf(q * (1.0f / 512.0f) - mean * mean, 0.0f);
      const float rstd = __builtin_amdgcn_rsqf(var + 1e-5f);
      if (g == (tok >> 2)) {
        *(LAS f4*)(ctx.L + L_REC + ((1 + w) * 16 + tok) * 16) = f4{-mean, rstd, rstd, 0.f};
        *(LAS float*)(ctx.L + L_NM + ((1 + w) * 16 + tok) * 4) = -mean;
        *(LAS float*)(ctx.L + L_RS + ((1 + w) * 16 + tok) * 4) = rstd;
      }
      asm volatile("" : "+v"(csk), "+v"(csv));
      if (tok == 0) *(LAS f4*)(ctx.L + csa + 64 + g * 16) = csk;
      if (g == 0) *(LAS float*)(ctx.L + csa + 128 + tok * 4) = csv[0];
    }
    v2u ident, lo1, hi1, on2;
    {
      const int d = tok - 4 * g;
      ident = v2u{d == 0 ? 0x3f80u : d == 1 ? 0x3f800000u : 0u, d == 2 ? 0x3f80u : d == 3 ? 0x3f800000u : 0u};
      const unsigned lo = tok < 8 ? ONE2 : 0u, hi = tok < 8 ? 0u : ONE2;
      lo1 = v2u{lo, lo}; hi1 = v2u{hi, hi}; on2 = v2u{mk1(), mk1()};
      asm volatile("" : "+a"(ident), "+a"(lo1), "+a"(hi1), "+a"(on2), "+a"(wphi));
    }
    BAR();
    STAMP(44);
    f2 st[4]; f4 nm4[4], rs4[4];
#pragma unroll
    for (int t = 0; t < 4; ++t) st[t] = *(LAS f2*)(ctx.L + L_REC + ((1 + t) * 16 + tok) * 16);
#pragma unroll
    for (int t = 0; t < 4; ++t) { nm4[t] = lds_ldf4(ctx.L, L_NM + ((1 + t) * 16 + 4 * g) * 4); rs4[t] = lds_ldf4(ctx.L, L_RS + ((1 + t) * 16 + 4 * g) * 4); }
    qpiece(IC<4>{});
    FM f[4];
    unsigned kb[4][2], vb[4][2];
#pragma unroll
    for (int t = 0; t < 4; ++t) {
      float q[4];
#pragma unroll
      for (int r = 0; r < 4; ++r) A_FMA(q[r], st[t][0], csk[r], ak[t][r]);
      A_MULS(f[t].c, LOG2E, st[t][1]);
      VNOP();
      A_CVT(kb[t][0], q[0], q[1]); A_CVT(kb[t][1], q[2], q[3]);
      if (t > 0) mf16z_a(f[t - 1].p, wphi, v2u{kb[t - 1][0], kb[t - 1][1]});
    }
    qpiece(IC<5>{});
#pragma unroll
    for (int t = 0; t < 4; ++t) {
      float v[4];
      if (t == 2) qpiece(IC<6>{});
#pragma unroll
      for (int r = 0; r < 4; ++r) A_FMA(v[r], nm4[t][r], csv[0], av[t][r]);
      if (t == 0) mf16z_a(f[3].p, wphi, v2u{kb[3][0], kb[3][1]}); else VNOP();
#pragma unroll
      for (int r = 0; r < 4; ++r) A_MUL(v[r], v[r], rs4[t][r]);
      FM_MM1(f[t]);
      A_CVT(vb[t][0], v[0], v[1]); A_CVT(vb[t][1], v[2], v[3]);
      FM_MM2(f[t]);
    }
    qpiece(IC<7>{});
    A_SW16(f[0].m, f[0].m2); A_SW16(f[1].m, f[1].m2); A_SW16(f[2].m, f[2].m2); A_SW16(f[3].m, f[3].m2);
    FM_MX2(f[0]); FM_MX2(f[1]); FM_MX2(f[2]); FM_MX2(f[3]);
    A_SW32(f[0].m, f[0].m2); A_SW32(f[1].m, f[1].m2); A_SW32(f[2].m, f[2].m2); A_SW32(f[3].m, f[3].m2);
    FM_MXC(f[0]); FM_MXC(f[1]); FM_MXC(f[2]); FM_MXC(f[3]);
    A_SW16(f[0].m, f[0].m2); A_SW16(f[1].m, f[1].m2); A_SW16(f[2].m, f[2].m2); A_SW16(f[3].m, f[3].m2);
#pragma unroll
    for (int t = 0; t < 4; ++t) {
      if (t == 0) qpiece(IC<8>{}); else if (t == 1) qpiece(IC<9>{}); else if (t == 2) qpiece(IC<10>{}); else qpiece(IC<11>{});
#pragma unroll
      for (int r = 0; r < 4; ++r) A_FMA(f[t].ep[r], f[t].p[r], f[t].c, f[t].m);
#pragma unroll
      for (int r = 0; r < 4; ++r) A_FMAN(f[t].en[r], f[t].p[r], f[t].c, f[t].m2);
      if (t > 0) mf16z_a(f[t - 1].sn, on2, v2u{f[t - 1].nb[0], f[t - 1].nb[1]}); else VNOP();
#pragma unroll
      for (int r = 0; r < 4; ++r) A_EXP(f[t].ep[r], f[t].ep[r]);
      A_EXP(f[t].en[0], f[t].en[0]);
      A_CVT(f[t].eb[0], f[t].ep[0], f[t].ep[1]); A_CVT(f[t].eb[1], f[t].ep[2], f[t].ep[3]);
      A_EXP(f[t].en[1], f[t].en[1]); A_EXP(f[t].en[2], f[t].en[2]);
      mf16z_a(f[t].sp, on2, v2u{f[t].eb[0], f[t].eb[1]});
      A_EXP(f[t].en[3], f[t].en[3]);
      A_CVT(f[t].nb[0], f[t].en[0], f[t].en[1]); A_CVT(f[t].nb[1], f[t].en[2], f[t].en[3]);
    }
    f4 tp[4], tm[4], kvp, kvm, zz;
#pragma unroll
    for (int t = 0; t < 4; ++t) {
      A_RCP(f[t].m, f[t].sp); A_RCP(f[t].m2, f[t].sn);
      if (t == 0) mf16z_a(f[3].sn, on2, v2u{f[3].nb[0], f[3].nb[1]}); else mf16z_b(tm[t - 1], v2u{f[t - 1].nb[0], f[t - 1].nb[1]}, ident);
#pragma unroll
      for (int r = 0; r < 4; ++r) A_MUL(f[t].ep[r], f[t].ep[r], f[t].m);
      A_MUL(f[t].en[0], f[t].en[0], f[t].m2); A_MUL(f[t].en[1], f[t].en[1], f[t].m2);
      A_CVT(f[t].eb[0], f[t].ep[0], f[t].ep[1]); A_CVT(f[t].eb[1], f[t].ep[2], f[t].ep[3]);
      A_MUL(f[t].en[2], f[t].en[2], f[t].m2); A_MUL(f[t].en[3], f[t].en[3], f[t].m2);
      mf16z_b(tp[t], v2u{f[t].eb[0], f[t].eb[1]}, ident);
      A_CVT(f[t].nb[0], f[t].en[0], f[t].en[1]); A_CVT(f[t].nb[1], f[t].en[2], f[t].en[3]);
    }
#pragma unroll
    for (int t = 0; t < 4; ++t) {
      unsigned pp[2], pm[2];
      A_CVT(pp[0], tp[t][0], tp[t][1]); A_CVT(pp[1], tp[t][2], tp[t][3]);
      if (t == 0) mf16z_b(tm[3], v2u{f[3].nb[0], f[3].nb[1]}, ident);
      A_CVT(pm[0], tm[t][0], tm[t][1]); A_CVT(pm[1], tm[t][2], tm[t][3]);
      if (t == 0) { mf16z(kvp, v2u{pp[0], pp[1]}, v2u{vb[0][0], vb[0][1]}); mf16z_a(zz, lo1, v2u{pp[0], pp[1]}); mf16z(kvm, v2u{pm[0], pm[1]}, v2u{vb[0][0], vb[0][1]}); }
      else { mf16c(kvp, v2u{pp[0], pp[1]}, v2u{vb[t][0], vb[t][1]}); mf16c_a(zz, lo1, v2u{pp[0], pp[1]}); mf16c(kvm, v2u{pm[0], pm[1]}, v2u{vb[t][0], vb[t][1]}); }
      mf16c_a(zz, hi1, v2u{pm[0], pm[1]});
    }
    {
      constexpr int SL = 32 * 256;
      const int blk = WS_KV1 + ((m * 64 + w * 16) * 32 + c) * 256, zblk = WS_KVZ + ((m * 4 + w) * 32 + c) * 256;
      const int vo = g * 2 * SL + tok * 16, zvo1 = ((g >> 1) * 16 + tok) * 8;
      const unsigned T = ctx.T;
      asm volatile("s_nop 1" : "+v"(kvp));
      st128(ctx, v4u{asu(kvp[0]), T, asu(kvp[1]), T}, vo, blk);
      st128(ctx, v4u{asu(kvp[2]), T, asu(kvp[3]), T}, vo, blk + SL);
      asm volatile("s_nop 3" : "+v"(kvm), "+v"(zz));
      st128(ctx, v4u{asu(kvm[0]), T, asu(kvm[1]), T}, vo, blk + 8 * SL);
      st128(ctx, v4u{asu(kvm[2]), T, asu(kvm[3]), T}, vo, blk + 9 * SL);
      st64(ctx, v2u{asu(zz[0]), T}, zvo1, zblk);
    }
    {
      const ImgDma wd = img_prep<1024>(ctx);
      img_chunk<1024, 0, L_XT, 0>(ctx, wd, rwa);
    }
#pragma unroll
    for (int nt = 0; nt < 4; ++nt) {
      const v2u v = *(LAS v2u*)(ctx.L + L_XT + (1 + w) * 16384 + (2 * m + (nt >> 1)) * 1024 + x0_lane(tok, 2 * (nt & 1) + (g >> 1)) + (g & 1) * 8);
      xacc[nt] = f4{asf(v.x << 16), asf(v.x & 0xffff0000u), asf(v.y << 16), asf(v.y & 0xffff0000u)};
    }
  }
  asm volatile("" : "+a"(xacc[0]), "+a"(xacc[1]), "+a"(xacc[2]), "+a"(xacc[3]), "+a"(wphi));
  STAMP(1);
  const rsrc_t out = __builtin_amdgcn_make_buffer_rsrc((void*)out_, 0, 2048 * 1024, 0x00020000);

#pragma nounroll
  for (int lp = 0; lp < 2; ++lp) {
    asm volatile("" : "+a"(xacc[0]), "+a"(xacc[1]), "+a"(xacc[2]), "+a"(xacc[3]), "+a"(wphi));
    layer_A(ctx, lp, wphi, rwa, rsa, rsb, rwb);
    out_proj<false>(ctx, lp, xacc, out, rsb);
    layer_B(ctx, 2 * lp + 1, rwb);
    out_proj<true>(ctx, lp, xacc, out, rsb);
  }
  asm volatile(".Lfk_end:");
  if (b == 0) { ctx.m = 0; flag_store(ctx, WS_EPOCH); }
  STAMP(41); CSTAMP(1);
  STAMP_FLUSH();
}

extern "C" void kernel_launch(void* const* d_in, const int* in_sizes, int n_in, void* d_out, int out_size, void* d_ws,
                              size_t ws_size, hipStream_t stream) {
  (void)in_sizes; (void)n_in; (void)out_size; (void)ws_size;
  (void)hipFuncSetAttribute((const void*)fused_kernel, hipFuncAttributeMaxDynamicSharedMemorySize, LDS_BYTES);
  hipLaunchKernelGGL(fused_kernel, dim3(NWG), dim3(NT), LDS_BYTES, stream,
                     (const unsigned char*)d_in[0], (const unsigned char*)d_in[1], (const unsigned char*)d_in[2],
                     (const unsigned char*)d_in[3], (const unsigned char*)d_in[5], (const unsigned char*)d_in[6],
                     (unsigned char*)d_out, (unsigned char*)d_ws);
}
```

```cpp
#include <hip/hip_runtime.h>
#include <hip/hip_bf16.h>
#include <type_traits>

#define GAS __attribute__((address_space(1)))
#define LAS __attribute__((address_space(3)))
#define DEV __device__ __forceinline__
typedef short s4 __attribute__((ext_vector_type(4)));
typedef float f4 __attribute__((ext_vector_type(4)));
typedef float f2 __attribute__((ext_vector_type(2)));
typedef __bf16 bf2 __attribute__((ext_vector_type(2)));
typedef unsigned v4u __attribute__((ext_vector_type(4)));
typedef unsigned v2u __attribute__((ext_vector_type(2)));
typedef unsigned long long u64;
typedef GAS unsigned gu32;
typedef LAS unsigned char lds_t;
typedef __amdgpu_buffer_rsrc_t rsrc_t;
#define RLX_AGENT __ATOMIC_RELAXED, __HIP_MEMORY_SCOPE_AGENT

namespace {
constexpr int NWG = 256, NT = 256;
constexpr int WS_EPOCH = 0;
constexpr int WS_FX = 4096;
constexpr int WS_FO = WS_FX + 2 * 32 * 128;
constexpr int WS_KV2 = 1 << 16;
constexpr int WS_KVZ = 1 << 18;
constexpr int WS_STAT = 2 << 18;
constexpr int STAT_L = 32 * 8 * 64 * 16;
constexpr int WS_STAMP = 4 << 18;
constexpr int WS_X = 2 << 20;
constexpr int ACT_L = 2 << 20;
constexpr int WS_O = WS_X + 2 * ACT_L;
constexpr int WS_KV1 = WS_O + ACT_L;
constexpr int WS_END = WS_KV1 + 8 * 64 * 32 * 256;
constexpr int L_XT = 0;
constexpr int L_RQ = 81920;
constexpr int L_REC = 147456 + 256;
constexpr int L_NM = L_REC + 1280;
constexpr int L_RS = L_NM + 320;
constexpr int L_CS = L_RS + 320;
constexpr int L_END = L_CS + 4 * 2 * 3 * 64;
constexpr int LDS_BYTES = 158 * 1024;
static_assert(L_END <= LDS_BYTES, "lds");
constexpr int SPIN_CAP = 1 << 18;
constexpr float LOG2E = 1.4426950408889634f;
#define RB 0
#define ST_DLY 4
#define UPOS 12
#define PF_U 1
#define PF_B 0
#define PF_KV 0
#define PF_LATE 1
#define PRE0 16
#define QPOLL 6
#define S2POLL 0
}

DEV float asf(unsigned u) { return __builtin_bit_cast(float, u); }
DEV unsigned asu(float f) { return __builtin_bit_cast(unsigned, f); }
DEV unsigned pk2(float a, float b) { unsigned r; asm("v_cvt_pk_bf16_f32 %0, %1, %2" : "=v"(r) : "v"(a), "v"(b)); return r; }
DEV float xg_sum(float v) {
  auto r = __builtin_amdgcn_permlane16_swap(asu(v), asu(v), false, false);
  v = asf(r[0]) + asf(r[1]);
  auto q = __builtin_amdgcn_permlane32_swap(asu(v), asu(v), false, false);
  return asf(q[0]) + asf(q[1]);
}
DEV float row_sum(float v) {
  v += asf(__builtin_amdgcn_update_dpp(0u, asu(v), 0x128, 0xf, 0xf, false));
  v += asf(__builtin_amdgcn_update_dpp(0u, asu(v), 0x124, 0xf, 0xf, false));
  v += asf(__builtin_amdgcn_update_dpp(0u, asu(v), 0x122, 0xf, 0xf, false));
  v += asf(__builtin_amdgcn_update_dpp(0u, asu(v), 0x121, 0xf, 0xf, false));
  return v;
}
DEV float sum32(float v) {
  v = row_sum(v);
  auto r = __builtin_amdgcn_permlane16_swap(asu(v), asu(v), false, false);
  return asf(r[0]) + asf(r[1]);
}
#define BAR() asm volatile("s_waitcnt lgkmcnt(0)\n\ts_barrier" ::: "memory")
#define RAWBAR() do { asm volatile("" ::: "memory"); __builtin_amdgcn_s_barrier(); asm volatile("" ::: "memory"); } while (0)
#define DRAIN() asm volatile("s_waitcnt vmcnt(0)" ::: "memory")
#define WAIT_VM(n) asm volatile("s_waitcnt vmcnt(%0)" :: "n"(n) : "memory")
#define WAIT_LG(n) __builtin_amdgcn_s_waitcnt(0xC07F | ((n) << 8))
#define WAIT_VMV(n) __builtin_amdgcn_s_waitcnt(0x0F70 | ((n) & 15) | (((n) >> 4) << 14))
#define VNOP() __builtin_amdgcn_s_setprio(0)
#define SB() __builtin_amdgcn_sched_barrier(0)
#define NOP(n) asm volatile("s_nop %0" :: "n"(n))
#define SC1 16
#define ONE2 0x3f803f80u
template <int N> using IC = std::integral_constant<int, N>;

DEV unsigned mk1() { unsigned r; asm volatile("v_mov_b32 %0, 0x3f803f80" : "=v"(r)); return r; }
DEV unsigned mk0() { unsigned r; asm volatile("v_mov_b32 %0, 0" : "=v"(r)); return r; }
DEV int opq(int v) { asm volatile("" : "+v"(v)); return v; }
DEV v4u lds_ld128(lds_t* L, int off) { return *(LAS v4u*)(L + off); }
DEV f4 lds_ldf4(lds_t* L, int off) { return *(LAS f4*)(L + off); }
DEV float lds_ldf(lds_t* L, int off) { return *(LAS float*)(L + off); }
DEV v4u tr_frag(lds_t* L, int off) {
  s4 a = __builtin_amdgcn_ds_read_tr16_b64_v4i16((LAS s4*)(L + off));
  s4 b = __builtin_amdgcn_ds_read_tr16_b64_v4i16((LAS s4*)(L + off + 512));
  v2u ua = __builtin_bit_cast(v2u, a), ub = __builtin_bit_cast(v2u, b);
  return v4u{ua.x, ua.y, ub.x, ub.y};
}
template <int I, int N, class F> DEV void sfor(F&& f) { if constexpr (I < N) { f(IC<I>{}); sfor<I + 1, N>(f); } }

constexpr int P_AK = 0, P_AV = 16, P_BQ = 32, P_BK = 48, P_BV = 64;
#define MF32 "v_mfma_f32_16x16x32_bf16 "
#define MF16 "v_mfma_f32_16x16x16_bf16 "
#define MM_CASE(WC, XC)                                                                                                         \
  if constexpr (WA) {                                                                                                           \
    if constexpr (FIRST) asm volatile(MF32 "%0, " WC "[%c1:%c2], " XC "[%c3:%c4], 0" : "=v"(acc) : "n"(R), "n"(R + 3), "n"(X), "n"(X + 3)); \
    else asm volatile(MF32 "%0, " WC "[%c1:%c2], " XC "[%c3:%c4], %0" : "+v"(acc) : "n"(R), "n"(R + 3), "n"(X), "n"(X + 3));     \
  } else {                                                                                                                      \
    if constexpr (FIRST) asm volatile(MF32 "%0, " XC "[%c3:%c4], " WC "[%c1:%c2], 0" : "=v"(acc) : "n"(R), "n"(R + 3), "n"(X), "n"(X + 3)); \
    else asm volatile(MF32 "%0, " XC "[%c3:%c4], " WC "[%c1:%c2], %0" : "+v"(acc) : "n"(R), "n"(R + 3), "n"(X), "n"(X + 3));     \
  }
template <int F, int RS, bool WA, bool FIRST>
DEV void mm(f4& acc) {
  constexpr int R = 96 + 4 * (F < 40 ? F : F - 40), X = 88 + 4 * (RS & 1);
  if constexpr (F < 40) { if constexpr (RS < 2) { MM_CASE("v", "v") } else { MM_CASE("v", "a") } }
  else { if constexpr (RS < 2) { MM_CASE("a", "v") } else { MM_CASE("a", "a") } }
}
template <int RS, int OFF>
DEV void ring_rd(unsigned addr) {
  constexpr int X = 88 + 4 * (RS & 1);
  if constexpr (RS < 2) asm volatile("ds_read_b128 v[%c1:%c2], %0 offset:%c3" :: "v"(addr), "n"(X), "n"(X + 3), "n"(OFF) : "memory");
  else asm volatile("ds_read_b128 a[%c1:%c2], %0 offset:%c3" :: "v"(addr), "n"(X), "n"(X + 3), "n"(OFF) : "memory");
}
template <int F, bool FIRST>
DEV void wcs(f4& acc, v4u x) {
  constexpr int R = 96 + 4 * (F < 40 ? F : F - 40);
  if constexpr (F < 40) {
    if constexpr (FIRST) asm volatile(MF32 "%0, %1, v[%c2:%c3], 0" : "=&v"(acc) : "v"(x), "n"(R), "n"(R + 3));
    else asm volatile(MF32 "%0, %1, v[%c2:%c3], %0" : "+v"(acc) : "v"(x), "n"(R), "n"(R + 3));
  } else {
    if constexpr (FIRST) asm volatile(MF32 "%0, %1, a[%c2:%c3], 0" : "=&v"(acc) : "v"(x), "n"(R), "n"(R + 3));
    else asm volatile(MF32 "%0, %1, a[%c2:%c3], %0" : "+v"(acc) : "v"(x), "n"(R), "n"(R + 3));
  }
}
template <int F, bool FIRST>
DEV void wxa(f4& acc, v4u x) {
  constexpr int R = 96 + 4 * (F < 40 ? F : F - 40);
  static_assert(F < 40, "");
  if constexpr (FIRST) asm volatile(MF32 "%0, v[%c2:%c3], %1, 0" : "=v"(acc) : "a"(x), "n"(R), "n"(R + 3));
  else asm volatile(MF32 "%0, v[%c2:%c3], %1, %0" : "+v"(acc) : "a"(x), "n"(R), "n"(R + 3));
}
template <int F, bool FIRST>
DEV void wxb(f4& acc, v4u x) {
  constexpr int R = 96 + 4 * (F < 40 ? F : F - 40);
  static_assert(F < 40, "");
  if constexpr (FIRST) asm volatile(MF32 "%0, %1, v[%c2:%c3], 0" : "=v"(acc) : "a"(x), "n"(R), "n"(R + 3));
  else asm volatile(MF32 "%0, %1, v[%c2:%c3], %0" : "+v"(acc) : "a"(x), "n"(R), "n"(R + 3));
}
template <bool FIRST>
DEV void mf_aaz(f4& acc, v4u a, v4u b) {
  if constexpr (FIRST) asm volatile(MF32 "%0, %1, %2, 0" : "=v"(acc) : "a"(a), "a"(b));
  else asm volatile(MF32 "%0, %1, %2, %0" : "+v"(acc) : "a"(a), "a"(b));
}
template <int RS, bool FIRST>
DEV void mmq(f4& acc, v4u a) {
  constexpr int X = 88 + 4 * (RS & 1);
  if constexpr (RS < 2) {
    if constexpr (FIRST) asm volatile(MF32 "%0, %1, v[%c2:%c3], 0" : "=v"(acc) : "a"(a), "n"(X), "n"(X + 3));
    else asm volatile(MF32 "%0, %1, v[%c2:%c3], %0" : "+v"(acc) : "a"(a), "n"(X), "n"(X + 3));
  } else {
    if constexpr (FIRST) asm volatile(MF32 "%0, %1, a[%c2:%c3], 0" : "=v"(acc) : "a"(a), "n"(X), "n"(X + 3));
    else asm volatile(MF32 "%0, %1, a[%c2:%c3], %0" : "+v"(acc) : "a"(a), "n"(X), "n"(X + 3));
  }
}
template <bool FIRST>
DEV void mf_av(f4& acc, v4u a, v4u b) {
  if constexpr (FIRST) asm volatile(MF32 "%0, %1, %2, 0" : "=&v"(acc) : "a"(a), "v"(b));
  else asm volatile(MF32 "%0, %1, %2, %0" : "+v"(acc) : "a"(a), "v"(b));
}
DEV void mf_va(f4& acc, v4u a, v4u b) { asm volatile(MF32 "%0, %1, %2, %0" : "+v"(acc) : "v"(a), "a"(b)); }
DEV void mf_aa(f4& acc, v4u a, v4u b) { asm volatile(MF32 "%0, %1, %2, %0" : "+v"(acc) : "a"(a), "a"(b)); }
DEV void mf32z(f4& d, v4u a, v4u b) { asm volatile(MF32 "%0, %1, %2, 0" : "=&v"(d) : "v"(a), "v"(b)); VNOP(); }
DEV void mf32c(f4& d, v4u a, v4u b) { asm volatile(MF32 "%0, %1, %2, %0" : "+v"(d) : "v"(a), "v"(b)); VNOP(); }
DEV void mf16z(f4& d, v2u a, v2u b) { asm volatile(MF16 "%0, %1, %2, 0" : "=&v"(d) : "v"(a), "v"(b)); VNOP(); }
DEV void mf16c(f4& d, v2u a, v2u b) { asm volatile(MF16 "%0, %1, %2, %0" : "+v"(d) : "v"(a), "v"(b)); VNOP(); }
DEV void mf16z_a(f4& d, v2u a, v2u b) { asm volatile(MF16 "%0, %1, %2, 0" : "=&v"(d) : "a"(a), "v"(b)); VNOP(); }
DEV void mf16c_a(f4& d, v2u a, v2u b) { asm volatile(MF16 "%0, %1, %2, %0" : "+v"(d) : "a"(a), "v"(b)); VNOP(); }
DEV void mf16z_b(f4& d, v2u a, v2u b) { asm volatile(MF16 "%0, %1, %2, 0" : "=&v"(d) : "v"(a), "a"(b)); VNOP(); }
template <int F, int OFF>
DEV void wtr(unsigned addr) {
  constexpr int R = 96 + 4 * (F < 40 ? F : F - 40);
  if constexpr (F < 40)
    asm volatile("ds_read_b64_tr_b16 v[%c1:%c2], %0 offset:%c5\n\tds_read_b64_tr_b16 v[%c3:%c4], %0 offset:%c6"
                 :: "v"(addr), "n"(R), "n"(R + 1), "n"(R + 2), "n"(R + 3), "n"(OFF), "n"(OFF + 512) : "memory");
  else
    asm volatile("ds_read_b64_tr_b16 a[%c1:%c2], %0 offset:%c5\n\tds_read_b64_tr_b16 a[%c3:%c4], %0 offset:%c6"
                 :: "v"(addr), "n"(R), "n"(R + 1), "n"(R + 2), "n"(R + 3), "n"(OFF), "n"(OFF + 512) : "memory");
}
#define A_FMA(d, a, b, c) asm volatile("v_fma_f32 %0, %1, %2, %3" : "=v"(d) : "v"(a), "v"(b), "v"(c))
#define A_FMAN(d, a, b, c) asm volatile("v_fma_f32 %0, %1, -%2, %3" : "=v"(d) : "v"(a), "v"(b), "v"(c))
#define A_MUL(d, a, b) asm volatile("v_mul_f32 %0, %1, %2" : "=v"(d) : "v"(a), "v"(b))
#define A_MULS(d, k, b) asm volatile("v_mul_f32 %0, %1, %2" : "=v"(d) : "s"(k), "v"(b))
#define A_MAX(d, a, b) asm volatile("v_max_f32 %0, %1, %2" : "=v"(d) : "v"(a), "v"(b))
#define A_SUB(d, a, b) asm volatile("v_sub_f32 %0, %1, %2" : "=v"(d) : "v"(a), "v"(b))
#define A_CVT(d, a, b) asm volatile("v_cvt_pk_bf16_f32 %0, %1, %2" : "=v"(d) : "v"(a), "v"(b))
#define A_EXP(d, a) asm volatile("v_exp_f32 %0, %1" : "=v"(d) : "v"(a))
#define A_RCP(d, x4) asm volatile("v_rcp_f32 %0, %1" : "=&v"(d) : "v"(x4[0]), "v"(x4))
#define A_SEL(d, f, t, m) asm volatile("v_cndmask_b32 %0, %1, %2, %3" : "=v"(d) : "v"(f), "v"(t), "s"(m))
#define A_PERM(d, x, sel) asm volatile("v_perm_b32 %0, %1, %1, %2" : "=v"(d) : "v"(x), "v"(sel))
#define A_SW16(a, b) asm volatile("v_permlane16_swap_b32 %0, %1" : "+v"(a), "+v"(b))
#define A_SW32(a, b) asm volatile("v_permlane32_swap_b32 %0, %1" : "+v"(a), "+v"(b))
#define A_LD64(d, addr, off) asm volatile("ds_read_b64 %0, %1 offset:%2" : "=v"(d) : "v"(addr), "n"(off) : "memory")
#define A_LD128A(d, addr, off) asm volatile("ds_read_b128 %0, %1 offset:%2" : "=a"(d) : "v"(addr), "n"(off) : "memory")
#define A_LD128(d, addr, off) asm volatile("ds_read_b128 %0, %1 offset:%2" : "=v"(d) : "v"(addr), "n"(off) : "memory")

#define STAMP(i) do {} while (0)
#define CSTAMP(i) do {} while (0)
#define STAMP_FLUSH() do {} while (0)
#define TS(k) do {} while (0)
#define TS_DECL
#define TS_FLUSH(base, n) do {} while (0)

struct Ctx {
  GAS unsigned char* ws;
  rsrc_t rs;
  lds_t* L;
  unsigned Lb, Lw;
  int c, m, w;
  int lane, tok, g;
  unsigned T;
  int budget;
};
DEV void fresh(Ctx& ctx) {
  int l;
  asm volatile("v_mbcnt_lo_u32_b32 %0, -1, 0\n\tv_mbcnt_hi_u32_b32 %0, -1, %0" : "=v"(l));
  ctx.lane = l; ctx.tok = l & 15; ctx.g = l >> 4;
  asm volatile("" : "+s"(ctx.c), "+s"(ctx.m), "+s"(ctx.w), "+s"(ctx.Lb));
  ctx.Lw = ctx.Lb + 4096 * ctx.w;
}
DEV v4u ld128(Ctx& ctx, int voff, int soff) { return __builtin_amdgcn_raw_buffer_load_b128(ctx.rs, voff, soff, SC1); }
DEV void st128(Ctx& ctx, v4u v, int voff, int soff) { __builtin_amdgcn_raw_buffer_store_b128(v, ctx.rs, voff, soff, SC1); }
DEV void st64(Ctx& ctx, v2u v, int voff, int soff) { __builtin_amdgcn_raw_buffer_store_b64(v, ctx.rs, voff, soff, SC1); }

DEV void wait_flags(Ctx& ctx, int off0, int off1, bool two) {
  if (ctx.w == 0) {
    const int l = ctx.lane & (two ? 15 : 7);
    const int vo = (l < 8 ? off0 : off1 - 128) + l * 16;
    for (;;) {
      asm volatile("" ::: "memory");
      const unsigned v = __builtin_amdgcn_raw_buffer_load_b32(ctx.rs, vo, 0, SC1);
      if (__all(v == ctx.T) || --ctx.budget < 0) break;
    }
    __builtin_amdgcn_fence(__ATOMIC_ACQUIRE, "wavefront");
  }
  BAR();
}
DEV void flag_store(Ctx& ctx, int off) {
  if (ctx.w == 0) {
    unsigned tv, zv;
    asm volatile("v_mov_b32 %0, %2\n\tv_mov_b32 %1, 0" : "=v"(tv), "=v"(zv) : "s"(ctx.T));
    __builtin_amdgcn_raw_buffer_store_b32(tv, ctx.rs, zv, off + ctx.m * 16, SC1);
  }
}

DEV int x0_lane(int tok, int g) { return tok * 64 + ((g ^ ((4 - (tok >> 2)) & 3)) << 4); }
DEV int tr_base(int lane, int j) {
  const int g = lane >> 4, q = (lane & 15) >> 2, p = lane & 3;
  const int s = ((q >> 1) & 1) | ((g & 1) << 1);
  return (8 * g + q) * 128 + ((j ^ s) << 5) + p * 8;
}
template <int F>
DEV void raw_ld1(unsigned voff, rsrc_t rs, int soff) {
  constexpr int R = 96 + 4 * (F < 40 ? F : F - 40);
  if constexpr (F < 40) asm volatile("buffer_load_dwordx4 v[%c3:%c4], %0, %1, %2 offen" :: "v"(voff), "s"(rs), "s"(soff), "n"(R), "n"(R + 3) : "memory");
  else asm volatile("buffer_load_dwordx4 a[%c3:%c4], %0, %1, %2 offen" :: "v"(voff), "s"(rs), "s"(soff), "n"(R), "n"(R + 3) : "memory");
}
template <int P, int I0, int I1>
DEV void raw_load(Ctx& ctx, rsrc_t rs, int colbyte) {
  const unsigned vo = (unsigned)((ctx.lane >> 3) * 3072 + (ctx.lane & 7) * 16);
  const int sb = colbyte + ctx.m * 128 + 32 * ctx.w * 3072;
  sfor<I0, I1>([&](auto i) { raw_ld1<P + i>(vo, rs, sb + (128 * (i >> 2) + 8 * (i & 3)) * 3072); });
}
template <int F, int OFF>
DEV void raw_st1(unsigned addr) {
  constexpr int R = 96 + 4 * (F < 40 ? F : F - 40);
  if constexpr (F < 40) asm volatile("ds_write_b128 %0, v[%c1:%c2] offset:%c3" :: "v"(addr), "n"(R), "n"(R + 3), "n"(OFF) : "memory");
  else asm volatile("ds_write_b128 %0, a[%c1:%c2] offset:%c3" :: "v"(addr), "n"(R), "n"(R + 3), "n"(OFF) : "memory");
}
template <int P, int I0, int I1>
DEV void raw_store(Ctx& ctx) {
  const int lane = ctx.lane, ch = lane & 7, rb1 = (lane >> 4) & 1;
  const unsigned base = ctx.Lb + L_RQ + (32 * ctx.w + (lane >> 3)) * 128 + (ch & 1) * 16;
  const unsigned p0 = base + (((ch >> 1) ^ rb1) << 5), p1 = base + (((ch >> 1) ^ (rb1 | 2)) << 5);
  sfor<I0, I1>([&](auto i) { raw_st1<P + i, (i >> 2) * 16384 + (i & 3) * 1024>((i & 1) ? p1 : p0); });
}
template <int P, int K0, int K1>
DEV void part_tr(Ctx& ctx) {
  const unsigned tb = ctx.Lb + L_RQ + tr_base(ctx.lane, ctx.w);
  sfor<K0, K1>([&](auto ks) { wtr<P + ks, ks * 4096>(tb); });
}
template <int P>
DEV void part_cs(Ctx& ctx, int csoff) {
  v4u ones = {mk1(), mk1(), mk1(), mk1()};
  asm volatile("s_waitcnt lgkmcnt(0)\n\ts_nop 1" : "+v"(ones) :: "memory");
  f4 c0, c1;
  sfor<0, 8>([&](auto k) { wcs<P + 2 * k, k == 0>(c0, ones); wcs<P + 2 * k + 1, k == 0>(c1, ones); });
  asm volatile("s_nop 7" : "+v"(c0), "+v"(c1));
  if (ctx.g == 0) *(LAS float*)(ctx.L + csoff + ctx.tok * 4) = c0[0] + c1[0];
}

template <int P>
DEV void part_step(Ctx& ctx, int csoff) {
  raw_store<P, 0, 8>(ctx);
  BAR();
  part_tr<P, 0, 8>(ctx);
  raw_store<P, 8, 16>(ctx);
  BAR();
  part_tr<P, 8, 16>(ctx);
  part_cs<P>(ctx, csoff);
}

template <int P, int PN>
DEV void part_step_ld(Ctx& ctx, int csoff, rsrc_t rs, int colbyte) {
  raw_load<PN, 0, 4>(ctx, rs, colbyte);
  raw_store<P, 0, 8>(ctx);
  raw_load<PN, 4, 8>(ctx, rs, colbyte);
  BAR();
  part_tr<P, 0, 8>(ctx);
  raw_load<PN, 8, 12>(ctx, rs, colbyte);
  raw_store<P, 8, 16>(ctx);
  raw_load<PN, 12, 16>(ctx, rs, colbyte);
  BAR();
  part_tr<P, 8, 16>(ctx);
  part_cs<P>(ctx, csoff);
}
template <int OFF>
DEV void pf1(unsigned lv, rsrc_t rs, int so) {
  asm volatile("buffer_load_dwordx4 v[224:227], %0, %1, %2 offen offset:%3" :: "v"(lv), "s"(rs), "s"(so), "n"(OFF) : "memory");
}

template <int SINK = 224>
DEV void pfw(unsigned vo, rsrc_t rs, int so) {
  so = __builtin_amdgcn_readfirstlane(so);
  asm volatile("s_nop 4\n\t" "buffer_load_dword v%c3, %0, %1, %2 offen" :: "v"(vo), "s"(rs), "s"(so), "n"(SINK) : "memory");
}
DEV void readback(Ctx& ctx, int vo, int so) {
  asm volatile("buffer_load_dword v88, %0, %1, %2 offen sc1" :: "v"(vo), "s"(ctx.rs), "s"(so) : "memory");
}
template <int ROWB, int SINK = 224>
DEV void pf32(Ctx& ctx, rsrc_t rs, int col) {
  const int row = 16 * (int)(blockIdx.x >> 3) + 4 * ctx.w;
  pfw<SINK>((unsigned)(((ctx.lane >> 3) & 3) * ROWB + (ctx.lane & 7) * 128), rs, col + row * ROWB);
}
template <int ROWB0, int ROWB1>
DEV void pf_pair(Ctx& ctx, rsrc_t rs0, int col0, rsrc_t rs1, int col1) {
  const int row = 16 * (int)(blockIdx.x >> 3) + 8 * (ctx.w & 1);
  if (ctx.w < 2) pfw((unsigned)((ctx.lane >> 3) * ROWB0 + (ctx.lane & 7) * 128), rs0, col0 + row * ROWB0);
  else pfw((unsigned)((ctx.lane >> 3) * ROWB1 + (ctx.lane & 7) * 128), rs1, col1 + row * ROWB1);
}

struct ImgDma { unsigned v[4]; int sb; };
template <int ROWB>
DEV ImgDma img_prep(Ctx& ctx) {
  const int lane = ctx.lane, cp = lane & 7, rb1 = (lane >> 4) & 1;
  const unsigned vb = (unsigned)((lane >> 3) * ROWB + (cp & 1) * 16);
  const unsigned v0 = vb + (((cp >> 1) ^ rb1) << 5), v1 = vb + (((cp >> 1) ^ (rb1 | 2)) << 5);
  ImgDma d;
  d.v[0] = v0; d.v[1] = v1 + (8 * ROWB - 1024); d.v[2] = v0 + 2 * (8 * ROWB - 1024); d.v[3] = v1 + 3 * (8 * ROWB - 1024);
  d.sb = ctx.m * 128 + 32 * ctx.w * ROWB;
  return d;
}
template <int LOFF> DEV void dma_m0(unsigned lbase) { asm volatile("s_add_u32 m0, %0, %1" :: "s"(lbase), "n"(LOFF) : "scc"); }
template <int SOFF> DEV void dma_so(int& t, int sbase) { asm volatile("s_add_u32 %0, %1, %2" : "=s"(t) : "s"(sbase), "n"(SOFF) : "scc"); }
template <int I> DEV void dma_go(unsigned voff, rsrc_t rs, int so) {
  asm volatile("buffer_load_dwordx4 %0, %1, %2 offen offset:%3 lds" :: "v"(voff), "s"(rs), "s"(so), "n"(I * 1024) : "memory");
}
template <int I> DEV void dma_go_sc1(unsigned voff, rsrc_t rs, int so) {
  asm volatile("buffer_load_dwordx4 %0, %1, %2 offen offset:%3 sc1 lds" :: "v"(voff), "s"(rs), "s"(so), "n"(I * 1024) : "memory");
}
template <int SOFF, int LOFF>
DEV void dma1(unsigned voff, rsrc_t rs, int sbase, unsigned lbase) {
  int t;
  asm volatile("s_add_u32 m0, %4, %6\n\ts_add_u32 %0, %3, %5\n\tbuffer_load_dwordx4 %1, %2, %0 offen lds"
               : "=&s"(t) : "v"(voff), "s"(rs), "s"(sbase), "s"(lbase), "n"(SOFF), "n"(LOFF) : "memory", "scc");
}
template <int ROWB, int COLB, int LDSOFF, int J>
DEV void img_chunk(Ctx& ctx, const ImgDma& d, rsrc_t rs) {
  int so;
  dma_m0<LDSOFF + J * 16384>(ctx.Lw); dma_so<COLB + 128 * J * ROWB>(so, d.sb);
  sfor<0, 4>([&](auto i) { dma_go<i>(d.v[i], rs, so); });
}
template <int TT>
DEV void x_chunk(Ctx& ctx, unsigned lv, int xs) {
  int so;
  dma_m0<L_XT + (TT + 1) * 16384>(ctx.Lw); dma_so<(TT + 1) * 16384>(so, xs);
  sfor<0, 4>([&](auto i) { dma_go_sc1<i>(lv, ctx.rs, so); });
}

struct ST {
  v4u d[4];
  float s, q, t, bias;
  unsigned bad, sv, wrec, wnm;
  int so;
};
template <bool HALO>
DEV void st_prep(Ctx& ctx, ST& st, int L) {
  (void)L;
  const int c = ctx.c, w = ctx.w, r32 = ctx.lane & 31, hf = ctx.lane >> 5;
  const bool halo = HALO && w == 3;
  const int cc = (halo && c > 0) ? c - 1 : c;
  const int row0 = halo ? (c > 0 ? 48 : 0) : 32 * (w & 1);
  const int r = halo ? (r32 & 15) : r32;
  st.so = WS_STAT + (HALO ? 0 : STAT_L) + (cc * 8 * 64 + row0) * 16;
  st.sv = hf * 4096 + r * 16;
  const int lrow = halo ? r : 16 + row0 + r;
  st.wrec = ctx.Lb + L_REC + lrow * 16;
  st.wnm = ctx.Lb + L_NM + lrow * 4;
  st.bias = (halo && c == 0) ? -__builtin_inff() : 0.f;
  asm volatile("" : "+v"(st.sv), "+v"(st.wrec), "+v"(st.wnm), "+v"(st.bias));
}
DEV void st_issue(Ctx& ctx, ST& st) {
  asm volatile("buffer_load_dwordx4 %0, %4, %5, %6 offen sc1\n\t"
               "buffer_load_dwordx4 %1, %4, %5, %6 offen offset:1024 sc1\n\t"
               "buffer_load_dwordx4 %2, %4, %5, %6 offen offset:2048 sc1\n\t"
               "buffer_load_dwordx4 %3, %4, %5, %6 offen offset:3072 sc1"
               : "=&v"(st.d[0]), "=&v"(st.d[1]), "=&v"(st.d[2]), "=&v"(st.d[3]) : "v"(st.sv), "s"(ctx.rs), "s"(st.so) : "memory");
}
DEV void st_retry(Ctx& ctx, ST& st) {
  for (;;) {
    asm volatile("" ::: "memory");
    unsigned bad = 0;
#pragma unroll
    for (int i = 0; i < 4; ++i) { st.d[i] = ld128(ctx, st.sv + i * 1024, st.so); }
#pragma unroll
    for (int i = 0; i < 4; ++i) bad |= (st.d[i].y ^ ctx.T) | (st.d[i].w ^ ctx.T);
    if (__all(bad == 0) || --ctx.budget < 0) break;
  }
}
DEV void st_check(Ctx& ctx, ST& st) {
  asm volatile("" : "+v"(st.d[0]), "+v"(st.d[1]), "+v"(st.d[2]), "+v"(st.d[3]));
  const unsigned T = ctx.T;
  const bool ok = (st.d[0].y == T) & (st.d[0].w == T) & (st.d[1].y == T) & (st.d[1].w == T) &
                  (st.d[2].y == T) & (st.d[2].w == T) & (st.d[3].y == T) & (st.d[3].w == T);
  if (__builtin_expect(!__all(ok), 0)) st_retry(ctx, st);
}
template <int K>
DEV void st_step(ST& st) {
  if constexpr (K == 0) asm volatile("v_add_f32 %0, %2, %3\n\tv_add_f32 %1, %4, %5" : "=&v"(st.s), "=&v"(st.q) : "v"(st.d[0].x), "v"(st.d[1].x), "v"(st.d[0].z), "v"(st.d[1].z));
  if constexpr (K == 1) asm volatile("v_add_f32 %0, %0, %2\n\tv_add_f32 %1, %1, %3" : "+v"(st.s), "+v"(st.q) : "v"(st.d[2].x), "v"(st.d[2].z));
  if constexpr (K == 2) asm volatile("v_add_f32 %0, %0, %2\n\tv_add_f32 %1, %1, %3" : "+v"(st.s), "+v"(st.q) : "v"(st.d[3].x), "v"(st.d[3].z));
  if constexpr (K == 3) A_SW32(st.s, st.q);
  if constexpr (K == 4) { float x = st.s, y = st.q; asm volatile("v_add_f32 %0, %2, %3\n\tv_add_f32 %1, %2, %3" : "=&v"(st.s), "=&v"(st.q) : "v"(x), "v"(y)); }
  if constexpr (K == 5) A_SW32(st.s, st.q);
  if constexpr (K == 6) asm volatile("v_mul_f32 %0, %2, %0\n\tv_mul_f32 %1, %3, %1" : "+v"(st.s), "+v"(st.q) : "s"(-1.0f / 512.0f), "s"(1.0f / 512.0f));
  if constexpr (K == 7) asm volatile("v_fma_f32 %0, -%1, %1, %0\n\tv_max_f32 %0, 0, %0" : "+v"(st.q) : "v"(st.s));
  if constexpr (K == 8) asm volatile("v_add_f32 %0, %1, %0" : "+v"(st.q) : "s"(1e-5f));
  if constexpr (K == 9) asm volatile("v_rsq_f32 %0, %0" : "+v"(st.q));
  if constexpr (K == 10) asm volatile("v_mov_b32 %0, %1" : "=v"(st.t) : "v"(st.q));
  if constexpr (K == 11) asm volatile("ds_write_b128 %0, %1" :: "v"(st.wrec), "v"(f4{st.s, st.q, st.t, st.bias}) : "memory");
  if constexpr (K == 12) asm volatile("ds_write2_b32 %0, %1, %2 offset1:80" :: "v"(st.wnm), "v"(st.s), "v"(st.q) : "memory");
}

template <bool HALO>
DEV void st_begin(Ctx& ctx, ST& st) {
  st_prep<HALO>(ctx, st, 0);
  if (ctx.w != 0) st_issue(ctx, st);
}
template <bool HALO>
DEV void gather_start(Ctx& ctx, ST& st, int L, unsigned lv, int xs) {
  const int c = ctx.c;
  const int cprev = c > 0 ? c - 1 : 0;
  constexpr int T0 = HALO ? -1 : 0;
  const int xs0 = (HALO && c == 0) ? xs + 16384 : xs;
  if (ctx.w != 0) {
    DRAIN();
    st_check(ctx, st);
    st_step<0>(st); st_step<1>(st); st_step<2>(st); NOP(1); st_step<3>(st); st_step<4>(st); NOP(1); st_step<5>(st);
    st_step<6>(st); st_step<7>(st); st_step<8>(st); st_step<9>(st); NOP(0); st_step<10>(st); st_step<12>(st); st_step<11>(st);
  }
  wait_flags(ctx, WS_FX + ((HALO ? 0 : 32) + c) * 128, WS_FX + ((HALO ? 0 : 32) + cprev) * 128, HALO);
  STAMP(L * 8 + 0);
  x_chunk<T0>(ctx, lv, xs0);
  x_chunk<T0 + 1>(ctx, lv, xs);
  WAIT_VM(4);
  RAWBAR();
  STAMP(L * 8 + 1);
}

template <int OFF> DEV void ring_prime(unsigned ab) { ring_rd<0, OFF>(ab); ring_rd<1, OFF + 1024>(ab); ring_rd<2, OFF + 2048>(ab); WAIT_LG(2); }
template <int PQ, int PK, int PV, int OFF, bool CINIT, class XL, class SH, class GP>
DEV void gemm_tile(unsigned ab, f4& aq, f4& ak, f4& av, XL&& xl, SH&& sh, GP&& gp) {
  sfor<0, 16>([&](auto ks) {
    constexpr bool F = ks == 0 && !CINIT;
    if constexpr (PQ >= 0) {
      mm<(PQ >= 0 ? PQ : 0) + ks, ks & 3, true, F>(aq);
      if (!sh(IC<4 * ks>{})) VNOP();
      ring_rd<(ks + 3) & 3, OFF + (ks + 3) * 1024>(ab);
      gp(IC<4 * ks>{});
      mm<PK + ks, ks & 3, true, F>(ak);
      if (!sh(IC<4 * ks + 1>{})) VNOP();
      gp(IC<4 * ks + 1>{});
    } else {
      mm<PK + ks, ks & 3, true, F>(ak);
      if (!sh(IC<4 * ks>{})) VNOP();
      ring_rd<(ks + 3) & 3, OFF + (ks + 3) * 1024>(ab);
      gp(IC<4 * ks>{});
    }
    mm<PV + ks, ks & 3, false, F>(av);
    WAIT_LG(2 + decltype(xl(ks))::value);
    gp(IC<4 * ks + 2>{});
    SB();
  });
}
#define GA(k) (4 * (k))
#define GB(k) (4 * (k) + 1)
#define GC(k) (4 * (k) + 2)

struct BE {
  f4 aq, ak, av;
  f2 st, ka, kb2; f4 nm4;
  f2 nst, nka, nkb; f4 nnm4;
  f4 csq, csk; float csv;
  float q[4], k[4], v[4];
  unsigned qb[2], kb[2], vb[2], vb2[2], kprev[2], vprev[2], P[4], pk0, pk, ob[2];
  f4 sp, sc, num, den;
  float a, b, t0, t1, m, m2, ft, fa, fb, pa, pb;
  v2u ones;
  unsigned sPA, sPB, sCA, sCB;
  u64 modd, mA, mB;
  unsigned aT, aF, aKa, aKb;
  int ovo;
};

DEV void layer_B(Ctx& ctx, int L, rsrc_t rwo) {
  fresh(ctx);
  ST st;
  st_begin<true>(ctx, st);
  const int c = ctx.c, w = ctx.w, g = ctx.g, tok = ctx.tok;
  const int csb = L_CS + (w * 6 + 3) * 64;
  const unsigned lv = ctx.lane * 16;
  BE e;
  TS_DECL;
  unsigned ab0, ab1;
  ImgDma wd = img_prep<1024>(ctx);
  {
    const bool odd = tok & 1;
    const int ja = 4 * g + (odd ? 1 : 0), jb = ja + 2;
    const bool curA = ja <= tok, curB = jb <= tok;
    const unsigned selA = odd ? 0x01000c0cu : 0x0c0c0100u, selB = odd ? 0x03020c0cu : 0x0c0c0302u, selZ = 0x0c0c0c0cu;
    e.sPA = curA ? selZ : selA; e.sPB = curB ? selZ : selB; e.sCA = curA ? selA : selZ; e.sCB = curB ? selB : selZ;
    e.modd = __ballot(odd); e.mA = __ballot(curA); e.mB = __ballot(curB);
    e.aT = ctx.Lb + L_REC + tok * 16;
    e.aF = ctx.Lb + L_NM + g * 16;
    e.aKa = ctx.Lb + L_REC + (ja - (curA ? 0 : 16)) * 16 + 8;
    e.aKb = ctx.Lb + L_REC + (jb - (curB ? 0 : 16)) * 16 + 8;
    e.csq = lds_ldf4(ctx.L, csb + g * 16); e.csk = lds_ldf4(ctx.L, csb + 64 + g * 16); e.csv = lds_ldf(ctx.L, csb + 128 + tok * 4);
    e.ones = v2u{mk1(), mk0()};
    e.ovo = ((g >> 1) * 16 + tok) * 16 + (g & 1) * 8;
    ab0 = ctx.Lb + L_XT + lv; ab1 = ab0 + 49152;
    asm volatile("" : "+v"(e.sPA), "+v"(e.sPB), "+v"(e.sCA), "+v"(e.sCB), "+v"(e.csq), "+v"(e.csk), "+v"(e.csv), "+a"(e.ones), "+v"(e.ovo),
                      "+v"(e.aT), "+v"(e.aF), "+v"(e.aKa), "+v"(e.aKb), "+v"(ab0), "+v"(ab1), "+v"(wd.v[0]), "+v"(wd.v[1]), "+v"(wd.v[2]), "+v"(wd.v[3]));
  }
  const int xs = WS_X + c * 65536 + w * 4096 - 16384;
  const int osb = WS_O + (4 * c * 16 + 2 * ctx.m + (w >> 1)) * 1024 + (w & 1) * 512;
  gather_start<true>(ctx, st, L, lv, xs);
  ring_prime<0>(ab0);
  f4 xq, xk, xv, yq, yk, yv;
  int so, oso;
  auto stat_rd = [&](auto S, auto h) {
    (void)e;
    if constexpr (h == GC(6)) { A_LD64(e.nst, e.aT, S * 256); A_LD128(e.nnm4, e.aF, S * 64); }
    if constexpr (h == GA(7)) A_LD64(e.nka, e.aKa, S * 256);
    if constexpr (h == GA(8)) A_LD64(e.nkb, e.aKb, S * 256);
  };
  auto xl = [](auto ks) { return IC<(ks == 7 ? 3 : ks == 8 ? 4 : ks == 9 ? 2 : ks == 10 ? 1 : 0)>{}; };
  auto next = [&](f4& q, f4& k, f4& v) { e.aq = q; e.ak = k; e.av = v; e.st = e.nst; e.ka = e.nka; e.kb2 = e.nkb; e.nm4 = e.nnm4; };
  auto epi = [&](auto h) {
    (void)e;
    if constexpr (h == GA(0)) A_FMA(e.q[0], e.st[0], e.csq[0], e.aq[0]);
    if constexpr (h == GB(0)) { A_FMA(e.q[1], e.st[0], e.csq[1], e.aq[1]); A_FMA(e.q[2], e.st[0], e.csq[2], e.aq[2]); }
    if constexpr (h == GC(0)) { A_FMA(e.q[3], e.st[0], e.csq[3], e.aq[3]); A_FMA(e.k[0], e.st[0], e.csk[0], e.ak[0]); }
    if constexpr (h == GA(1)) A_FMA(e.k[1], e.st[0], e.csk[1], e.ak[1]);
    if constexpr (h == GB(1)) { A_FMA(e.k[2], e.st[0], e.csk[2], e.ak[2]); A_FMA(e.k[3], e.st[0], e.csk[3], e.ak[3]); }
    if constexpr (h == GC(1)) { A_CVT(e.qb[0], e.q[0], e.q[1]); A_CVT(e.qb[1], e.q[2], e.q[3]); }
    if constexpr (h == GB(2)) { A_CVT(e.kb[0], e.k[0], e.k[1]); A_CVT(e.kb[1], e.k[2], e.k[3]); }
    if constexpr (h == GC(2)) {
      A_FMA(e.v[0], e.nm4[0], e.csv, e.av[0]); A_FMA(e.v[1], e.nm4[1], e.csv, e.av[1]);
      mf16z(e.sp, v2u{e.kprev[0], e.kprev[1]}, v2u{e.qb[0], e.qb[1]});
      A_FMA(e.v[2], e.nm4[2], e.csv, e.av[2]); A_FMA(e.v[3], e.nm4[3], e.csv, e.av[3]);
      mf16z(e.sc, v2u{e.kb[0], e.kb[1]}, v2u{e.qb[0], e.qb[1]});
      A_CVT(e.vb[0], e.v[0], e.v[1]); A_CVT(e.vb[1], e.v[2], e.v[3]);
    }
    if constexpr (h == GB(3)) { A_CVT(e.vb2[0], e.v[0], e.v[1]); A_CVT(e.vb2[1], e.v[2], e.v[3]); }
    if constexpr (h == GC(3)) A_MULS(e.ft, 0.25f * LOG2E, e.st[1]);
    if constexpr (h == GA(4)) A_MUL(e.fa, e.ka[0], e.ft);
    if constexpr (h == GB(4)) { A_SEL(e.a, e.sc[0], e.sc[1], e.modd); A_SEL(e.t0, e.sp[0], e.sp[1], e.modd); }
    if constexpr (h == GC(4)) { A_SEL(e.b, e.sc[2], e.sc[3], e.modd); A_SEL(e.t1, e.sp[2], e.sp[3], e.modd); }
    if constexpr (h == GB(5)) { A_SEL(e.a, e.t0, e.a, e.mA); A_SEL(e.b, e.t1, e.b, e.mB); }
    if constexpr (h == GC(5)) { A_MUL(e.fb, e.kb2[0], e.ft); A_FMA(e.a, e.a, e.fa, e.ka[1]); }
    if constexpr (h == GB(6)) {
      asm volatile("v_fma_f32 %0, %0, %3, %4\n\tv_max_f32 %1, %2, %0" : "+v"(e.b), "=&v"(e.m) : "v"(e.a), "v"(e.fb), "v"(e.kb2[1]));
    }
    if constexpr (h == GA(7)) A_MAX(e.m2, e.a, e.b);
    if constexpr (h == GC(7)) A_SW16(e.m, e.m2);
    if constexpr (h == GB(8)) { float x = e.m, y = e.m2; A_MAX(e.m, x, y); A_MAX(e.m2, x, y); }
    if constexpr (h == GA(9)) A_SW32(e.m, e.m2);
    if constexpr (h == GB(9)) asm volatile("v_max_f32 %0, %0, %2\n\tv_sub_f32 %1, %1, %0" : "+v"(e.m), "+v"(e.a) : "v"(e.m2));
    if constexpr (h == GC(9)) { A_SUB(e.b, e.b, e.m); }
    if constexpr (h == GB(10)) A_EXP(e.a, e.a);
    if constexpr (h == GC(10)) A_EXP(e.b, e.b);
    if constexpr (h == GB(11)) { A_MUL(e.pa, e.a, e.ka[0]); A_MUL(e.pb, e.b, e.kb2[0]); }
    if constexpr (h == GC(11)) { A_CVT(e.pk, e.pa, e.pb); A_CVT(e.pk0, e.a, e.b); }
    if constexpr (h == GB(12)) { A_PERM(e.P[0], e.pk, e.sPA); A_PERM(e.P[1], e.pk, e.sPB); }
    if constexpr (h == GC(12)) { A_PERM(e.P[2], e.pk, e.sCA); A_PERM(e.P[3], e.pk, e.sCB); }
    if constexpr (h == GA(13)) {
      mf16z_a(e.den, e.ones, v2u{e.pk0, e.pk});
      mf32z(e.num, v4u{e.vprev[0], e.vprev[1], e.vb[0], e.vb[1]}, v4u{e.P[0], e.P[1], e.P[2], e.P[3]});
      e.kprev[0] = e.kb[0]; e.kprev[1] = e.kb[1]; e.vprev[0] = e.vb2[0]; e.vprev[1] = e.vb2[1];
    }
    if constexpr (h == GB(14)) A_RCP(e.m, e.den);
    if constexpr (h == GC(14)) { A_MUL(e.q[0], e.num[0], e.m); A_MUL(e.q[1], e.num[1], e.m); }
    if constexpr (h == GA(15)) A_MUL(e.q[2], e.num[2], e.m);
    if constexpr (h == GB(15)) { A_MUL(e.q[3], e.num[3], e.m); A_CVT(e.ob[0], e.q[0], e.q[1]); }
    if constexpr (h == GC(15)) { A_CVT(e.ob[1], e.q[2], e.q[3]); st64(ctx, v2u{e.ob[0], e.ob[1]}, e.ovo, oso); }
  };
  auto x_sh = [&](auto TT, auto h) {
    if constexpr (h == GB(1)) dma_so<(TT + 1) * 16384>(so, xs);
    if constexpr (h == GA(2)) dma_m0<L_XT + (TT + 1) * 16384>(ctx.Lw);
    return h == GB(1) || h == GA(2);
  };
  auto x_gp = [&](auto h) {
    if constexpr (h == GA(2)) dma_go_sc1<0>(lv, ctx.rs, so);
    if constexpr (h == GA(3)) dma_go_sc1<1>(lv, ctx.rs, so);
    if constexpr (h == GA(5)) dma_go_sc1<2>(lv, ctx.rs, so);
    if constexpr (h == GA(6)) dma_go_sc1<3>(lv, ctx.rs, so);
  };
  auto w_sh = [&](auto J, auto N, auto h) {
    if constexpr (h == GA(7)) WAIT_VMV(N.value);
    if constexpr (h == GB(7)) RAWBAR();
    if constexpr (h == GB(9)) dma_so<128 * J * 1024>(so, wd.sb);
    if constexpr (h == GA(10)) dma_m0<L_XT + J * 16384>(ctx.Lw);
    if constexpr (h == GB(13) && J > 0) dma_so<(J - 1) * 16384>(oso, osb);
    return h == GA(7) || h == GB(7) || h == GB(9) || h == GA(10) || (h == GB(13) && J > 0);
  };
  auto w_gp = [&](auto h) {
    if constexpr (h == GA(10)) dma_go<0>(wd.v[0], rwo, so);
    if constexpr (h == GA(11)) dma_go<1>(wd.v[1], rwo, so);
    if constexpr (h == GA(12)) dma_go<2>(wd.v[2], rwo, so);
    if constexpr (h == GA(14)) dma_go<3>(wd.v[3], rwo, so);
  };
  TS(0);
  gemm_tile<-1, P_BK, P_BV, 0, false>(ab0, xq, xk, xv,
    [](auto ks) { return IC<((ks == 12 || ks == 13 || ks == 14) ? 2 : 0)>{}; },
    [&](auto h) {
      if constexpr (h == GA(0)) dma_so<2 * 16384>(so, xs);
      if constexpr (h == GA(1)) dma_m0<L_XT + 2 * 16384>(ctx.Lw);
      if constexpr (h == GA(11)) WAIT_VMV(4);
      if constexpr (h == GA(12)) RAWBAR();
      return h == GA(0) || h == GA(1) || h == GA(11) || h == GA(12);
    },
    [&](auto h) {
      (void)e;
      if constexpr (h == GA(1)) dma_go_sc1<0>(lv, ctx.rs, so);
      if constexpr (h == GA(4)) dma_go_sc1<1>(lv, ctx.rs, so);
      if constexpr (h == GA(7)) dma_go_sc1<2>(lv, ctx.rs, so);
      if constexpr (h == GC(10)) dma_go_sc1<3>(lv, ctx.rs, so);
      if constexpr (h == GA(12)) { A_LD64(e.nst, e.aT, 0); A_LD128(e.nnm4, e.aF, 0); }
    });
  TS(1);
  gemm_tile<P_BQ, P_BK, P_BV, 16384, false>(ab0, yq, yk, yv, xl,
    [&](auto h) { return x_sh(IC<2>{}, h) | w_sh(IC<0>{}, IC<4>{}, h); },
    [&](auto h) {
      (void)e; (void)xk; (void)xv;
      if constexpr (h == GB(1)) { A_FMA(e.k[0], e.nst[0], e.csk[0], xk[0]); A_FMA(e.k[1], e.nst[0], e.csk[1], xk[1]); }
      if constexpr (h == GC(1)) { A_FMA(e.k[2], e.nst[0], e.csk[2], xk[2]); A_FMA(e.k[3], e.nst[0], e.csk[3], xk[3]); }
      if constexpr (h == GB(2)) { A_FMA(e.v[0], e.nnm4[0], e.csv, xv[0]); A_FMA(e.v[1], e.nnm4[1], e.csv, xv[1]); }
      if constexpr (h == GC(2)) { A_FMA(e.v[2], e.nnm4[2], e.csv, xv[2]); A_FMA(e.v[3], e.nnm4[3], e.csv, xv[3]); }
      if constexpr (h == GB(3)) { A_CVT(e.kprev[0], e.k[0], e.k[1]); A_CVT(e.kprev[1], e.k[2], e.k[3]); }
      if constexpr (h == GC(3)) { A_CVT(e.vprev[0], e.v[0], e.v[1]); A_CVT(e.vprev[1], e.v[2], e.v[3]); }
      if constexpr (h == GC(15)) NOP(1);
      x_gp(h); stat_rd(IC<1>{}, h); w_gp(h);
    });
  next(yq, yk, yv);
  TS(2);
  gemm_tile<P_BQ, P_BK, P_BV, 32768, false>(ab0, xq, xk, xv, xl,
    [&](auto h) { return x_sh(IC<3>{}, h) | w_sh(IC<1>{}, IC<8>{}, h); },
    [&](auto h) { x_gp(h); stat_rd(IC<2>{}, h); w_gp(h); epi(h); });
  next(xq, xk, xv);
  TS(3);
  gemm_tile<P_BQ, P_BK, P_BV, 0, false>(ab1, yq, yk, yv, xl,
    [&](auto h) { return w_sh(IC<2>{}, IC<5>{}, h); },
    [&](auto h) { stat_rd(IC<3>{}, h); w_gp(h); epi(h); });
  next(yq, yk, yv);
  TS(4);
  gemm_tile<P_BQ, P_BK, P_BV, 16384, false>(ab1, xq, xk, xv, xl,
    [&](auto h) { return w_sh(IC<3>{}, IC<63>{}, h); },
    [&](auto h) { stat_rd(IC<4>{}, h); w_gp(h); epi(h); });
  next(xq, xk, xv);
  TS(5);
  {
    dma_so<3 * 16384>(oso, osb);
    A_MULS(e.ft, 0.25f * LOG2E, e.st[1]);
    A_FMA(e.q[0], e.st[0], e.csq[0], e.aq[0]); A_FMA(e.q[1], e.st[0], e.csq[1], e.aq[1]);
    A_FMA(e.q[2], e.st[0], e.csq[2], e.aq[2]); A_FMA(e.q[3], e.st[0], e.csq[3], e.aq[3]);
    A_FMA(e.k[0], e.st[0], e.csk[0], e.ak[0]); A_FMA(e.k[1], e.st[0], e.csk[1], e.ak[1]);
    A_CVT(e.qb[0], e.q[0], e.q[1]); A_CVT(e.qb[1], e.q[2], e.q[3]);
    A_FMA(e.k[2], e.st[0], e.csk[2], e.ak[2]); A_FMA(e.k[3], e.st[0], e.csk[3], e.ak[3]);
    mf16z(e.sp, v2u{e.kprev[0], e.kprev[1]}, v2u{e.qb[0], e.qb[1]});
    A_CVT(e.kb[0], e.k[0], e.k[1]); A_CVT(e.kb[1], e.k[2], e.k[3]);
    A_MUL(e.fa, e.ka[0], e.ft); A_MUL(e.fb, e.kb2[0], e.ft);
    mf16z(e.sc, v2u{e.kb[0], e.kb[1]}, v2u{e.qb[0], e.qb[1]});
    A_FMA(e.v[0], e.nm4[0], e.csv, e.av[0]); A_FMA(e.v[1], e.nm4[1], e.csv, e.av[1]);
    A_FMA(e.v[2], e.nm4[2], e.csv, e.av[2]); A_FMA(e.v[3], e.nm4[3], e.csv, e.av[3]);
    A_SEL(e.t0, e.sp[0], e.sp[1], e.modd); A_SEL(e.t1, e.sp[2], e.sp[3], e.modd);
    A_CVT(e.vb[0], e.v[0], e.v[1]); A_CVT(e.vb[1], e.v[2], e.v[3]);
    asm volatile("v_cndmask_b32 %0, %4, %5, %10\n\tv_cndmask_b32 %1, %6, %7, %10\n\t"
                 "v_cndmask_b32 %0, %8, %0, %11\n\tv_cndmask_b32 %1, %9, %1, %12\n\t"
                 "v_fma_f32 %0, %0, %13, %15\n\tv_fma_f32 %1, %1, %14, %16\n\t"
                 "v_max_f32 %2, %0, %1\n\tv_max_f32 %3, %0, %1\n\ts_nop 1\n\t"
                 "v_permlane16_swap_b32 %2, %3\n\tv_max_f32 %2, %2, %3\n\tv_mov_b32 %3, %2\n\ts_nop 1\n\t"
                 "v_permlane32_swap_b32 %2, %3\n\tv_max_f32 %2, %2, %3\n\tv_sub_f32 %0, %0, %2\n\tv_sub_f32 %1, %1, %2\n\t"
                 "v_exp_f32 %0, %0\n\tv_exp_f32 %1, %1"
                 : "=&v"(e.a), "=&v"(e.b), "=&v"(e.m), "=&v"(e.m2)
                 : "v"(e.sc[0]), "v"(e.sc[1]), "v"(e.sc[2]), "v"(e.sc[3]), "v"(e.t0), "v"(e.t1), "s"(e.modd), "s"(e.mA), "s"(e.mB),
                   "v"(e.fa), "v"(e.fb), "v"(e.ka[1]), "v"(e.kb2[1]));
    asm volatile("v_mul_f32 %4, %7, %9\n\tv_mul_f32 %5, %8, %10\n\tv_cvt_pk_bf16_f32 %6, %7, %8\n\tv_cvt_pk_bf16_f32 %4, %4, %5\n\t"
                 "v_perm_b32 %0, %4, %4, %11\n\tv_perm_b32 %1, %4, %4, %12\n\tv_perm_b32 %2, %4, %4, %13\n\tv_perm_b32 %3, %4, %4, %14"
                 : "=&v"(e.P[0]), "=&v"(e.P[1]), "=&v"(e.P[2]), "=&v"(e.P[3]), "=&v"(e.pk), "=&v"(e.pb), "=&v"(e.pk0)
                 : "v"(e.a), "v"(e.b), "v"(e.ka[0]), "v"(e.kb2[0]), "v"(e.sPA), "v"(e.sPB), "v"(e.sCA), "v"(e.sCB));
    mf16z_a(e.den, e.ones, v2u{e.pk0, e.pk});
    NOP(0);
    mf32z(e.num, v4u{e.vprev[0], e.vprev[1], e.vb[0], e.vb[1]}, v4u{e.P[0], e.P[1], e.P[2], e.P[3]});
    NOP(5);
    asm volatile("v_rcp_f32 %2, %7\n\ts_nop 0\n\t"
                 "v_mul_f32 %3, %8, %2\n\tv_mul_f32 %4, %9, %2\n\tv_mul_f32 %5, %10, %2\n\tv_mul_f32 %6, %11, %2\n\t"
                 "v_cvt_pk_bf16_f32 %0, %3, %4\n\tv_cvt_pk_bf16_f32 %1, %5, %6"
                 : "=&v"(e.ob[0]), "=&v"(e.ob[1]), "=&v"(e.m), "=&v"(e.q[0]), "=&v"(e.q[1]), "=&v"(e.q[2]), "=&v"(e.q[3])
                 : "v"(e.den[0]), "v"(e.num[0]), "v"(e.num[1]), "v"(e.num[2]), "v"(e.num[3]), "v"(e.den));
    st64(ctx, v2u{e.ob[0], e.ob[1]}, e.ovo, oso);
  }
  TS(6);
  STAMP(L * 8 + 4);
  TS_FLUSH(L * 8 + 2, 7);
  DRAIN();
  BAR();
  flag_store(ctx, WS_FO + (32 + c) * 128);
}

constexpr int KLOC[4] = {L_RQ, L_RQ + 32768, L_XT, L_RQ + 16384}, VLOC[4] = {L_RQ + 16384, L_RQ + 49152, L_RQ, L_RQ + 32768};
template <int P0, int P1>
DEV void first_pieces(Ctx& ctx, rsrc_t rsx, rsrc_t rsa) {
  const int lane = ctx.lane, w = ctx.w;
  const ImgDma kd = img_prep<3072>(ctx);
  const unsigned xvo = (unsigned)((lane >> 2) * 1024 + (((lane & 3) ^ ((4 - (lane >> 4)) & 3)) << 4));
  const int xso = (64 * ctx.c + 16 * w) * 1024;
  const unsigned xlb = ctx.Lb + L_XT + (1 + w) * 16384;
  const unsigned rvo = (unsigned)((lane >> 3) * 3072 + (lane & 7) * 16);
  sfor<P0, P1>([&](auto P) {
    constexpr int p = P, q = p & 3, blk = p >> 2, kind = blk % 3, j = blk / 3;
    if constexpr (kind == 0) dma1<(4 * j + q) * 64, (4 * j + q) * 1024>(xvo, rsx, xso, xlb);
    else if constexpr (blk >= 8) raw_ld1<P_BK + (blk == 8 ? 0 : blk == 10 ? 4 : 8) + q>(rvo, rsa, kd.sb + 1024 * kind + (128 * j + 8 * q) * 3072);
    else dma1<1024 * kind + 128 * j * 3072 + q * 1024, (kind == 1 ? KLOC[j] : VLOC[j]) + q * 1024>(kd.v[q], rsa, kd.sb, ctx.Lw);
  });
}
DEV void first_stage(Ctx& ctx, rsrc_t rsx, rsrc_t rsa) {
  fresh(ctx);
  const int lane = ctx.lane, w = ctx.w, g = ctx.g, tok = ctx.tok;
  unsigned wp0, wp1;
  {
    const int ch = lane & 7, rb1 = (lane >> 4) & 1;
    const unsigned base = ctx.Lb + L_RQ + (32 * w + (lane >> 3)) * 128 + (ch & 1) * 16;
    wp0 = base + (((ch >> 1) ^ rb1) << 5); wp1 = base + (((ch >> 1) ^ (rb1 | 2)) << 5);
  }
  unsigned abo = ctx.Lb + L_XT + 16384 + x0_lane(tok, g) + w * 16384;
  unsigned trq = ctx.Lb + L_RQ + tr_base(lane, w), tr0 = ctx.Lb + L_XT + tr_base(lane, w);
  v4u ones = {mk1(), mk1(), mk1(), mk1()};
  unsigned wrec = ctx.Lb + L_REC + ((1 + w) * 16 + tok) * 16, wnm = ctx.Lb + L_NM + ((1 + w) * 16 + tok) * 4;
  unsigned wck = ctx.Lb + L_CS + (w * 6) * 64 + 64 + g * 16, wcv = ctx.Lb + L_CS + (w * 6) * 64 + 128 + tok * 4;
  const u64 m1 = __ballot(tok & 1), m2 = __ballot(tok & 2), mdiag = __ballot(g == (tok >> 2));
  asm volatile("" : "+v"(abo), "+v"(trq), "+v"(tr0), "+a"(ones), "+v"(wrec), "+v"(wnm), "+v"(wck), "+v"(wcv), "+v"(wp0), "+v"(wp1));
  first_pieces<PRE0, 24>(ctx, rsx, rsa);
  STAMP(42);
  f4 csk, csv, gram, ssum;
  v4u xf[4];
  sfor<0, 4>([&](auto j) {
    (void)xf; (void)abo;
    if constexpr (j < 2) WAIT_VM(12);
    RAWBAR();
    sfor<0, 4>([&](auto i) {
      constexpr int ks = 4 * j + i;
      (void)xf; (void)abo;
      A_LD128A(xf[i], abo, ks * 1024);
      if constexpr (j == 2) wtr<P_AK + ks, i * 4096>(tr0); else wtr<P_AK + ks, KLOC[j] - L_RQ + i * 4096>(trq);
      wtr<P_AV + ks, VLOC[j] - L_RQ + i * 4096>(trq);
    });
    if constexpr (j < 2) first_pieces<24 + 12 * j, 30 + 12 * j>(ctx, rsx, rsa);
    if constexpr (j == 2) {
      WAIT_VM(4);
      sfor<0, 4>([&](auto q) { raw_st1<P_BK + 4 + q, KLOC[3] - L_RQ + q * 1024>((q & 1) ? wp1 : wp0); });
      WAIT_VM(0);
      sfor<0, 4>([&](auto q) { raw_st1<P_BK + 8 + q, VLOC[3] - L_RQ + q * 1024>((q & 1) ? wp1 : wp0); });
      pf32<3072>(ctx, rsa, 0);
      WAIT_LG(8);
    } else {
      WAIT_LG(0);
    }
    sfor<0, 4>([&](auto i) {
      constexpr int ks = 4 * j + i;
      mf_aaz<ks == 0>(gram, xf[i], xf[i]); VNOP();
      mf_aaz<ks == 0>(ssum, ones, xf[i]); VNOP();
    });
    sfor<0, 4>([&](auto i) {
      constexpr int ks = 4 * j + i;
      wxa<P_AK + ks, ks == 0>(csk, ones); VNOP();
      wxb<P_AV + ks, ks == 0>(csv, ones); VNOP();
    });
    if constexpr (j < 2) first_pieces<30 + 12 * j, 36 + 12 * j>(ctx, rsx, rsa);
    if constexpr (j == 1) {
      WAIT_VM(12);
      sfor<0, 4>([&](auto q) { raw_st1<P_BK + q, VLOC[2] - L_RQ + q * 1024>((q & 1) ? wp1 : wp0); });
    }
    WAIT_LG(0);
    SB();
  });
  STAMP(43);
  float q, t, nm, z0;
  asm volatile("v_cndmask_b32 %0, %4, %5, %9\n\tv_cndmask_b32 %1, %6, %7, %9\n\tv_mul_f32 %2, %11, %8\n\tv_cndmask_b32 %0, %0, %1, %10\n\t"
               "v_mul_f32 %0, %12, %0\n\tv_fma_f32 %0, -%2, %2, %0\n\tv_max_f32 %0, 0, %0\n\tv_add_f32 %0, %13, %0\n\tv_rsq_f32 %0, %0\n\tv_mov_b32 %3, 0\n\tv_mov_b32 %1, %0"
               : "=&v"(q), "=&v"(t), "=&v"(nm), "=&v"(z0)
               : "v"(gram[0]), "v"(gram[1]), "v"(gram[2]), "v"(gram[3]), "v"(ssum[0]), "s"(m1), "s"(m2), "s"(-1.0f / 512.0f), "s"(1.0f / 512.0f), "s"(1e-5f), "v"(ssum));
  u64 sv;
  asm volatile("s_mov_b64 %0, exec\n\ts_mov_b64 exec, %6\n\tds_write_b128 %1, %2\n\tds_write2_b32 %3, %4, %5 offset1:80\n\ts_mov_b64 exec, %0"
               : "=&s"(sv) : "v"(wrec), "v"(f4{nm, q, t, z0}), "v"(wnm), "v"(nm), "v"(q), "s"(mdiag) : "memory");
  asm volatile("ds_write_b128 %0, %1\n\tds_write_b32 %2, %3" :: "v"(wck), "v"(csk), "v"(wcv), "v"(csv[0]), "v"(csv) : "memory");
}

struct FM {
  f4 p, sp, sn;
  float ep[4], en[4], m, m2, c;
  unsigned eb[2], nb[2];
};
#define FM_MM1(f) asm volatile("v_max3_f32 %0, %2, %3, %4\n\tv_max3_f32 %1, -%2, -%3, -%4" : "=&v"(f.m), "=&v"(f.m2) : "v"(f.p[0]), "v"(f.p[1]), "v"(f.p[2]))
#define FM_MM2(f) asm volatile("v_max_f32 %0, %0, %2\n\tv_max_f32 %1, %1, -%2" : "+v"(f.m), "+v"(f.m2) : "v"(f.p[3]))
#define FM_MX2(f) do { float x_ = f.m, y_ = f.m2; A_MAX(f.m, x_, y_); A_MAX(f.m2, x_, y_); } while (0)
#define FM_MXC(f) asm volatile("v_max_f32 %0, %0, %1\n\tv_mul_f32 %1, %0, -%2\n\tv_mul_f32 %0, %0, -%2" : "+v"(f.m), "+v"(f.m2) : "v"(f.c))

struct AE {
  f4 ak, av;
  f2 st, nst; f4 nm4, rs4, nnm4, nrs4;
  f4 csk; float csv;
  float k[4], v[4];
  unsigned kb[2], vb[2], vbo[2], php[2], phm[2];
  v2u wphi, ident, ones, lo1, hi1;
  f4 tp, tm, kvp, kvm, zz;
  FM f;
  unsigned aT, aF;
};

DEV void layer_A(Ctx& ctx, int lp, v2u wphi, rsrc_t rwo, rsrc_t rsa, rsrc_t rsb, rsrc_t rsx) {
  fresh(ctx);
  const int L = 2 * lp;
  const int c = ctx.c, m = ctx.m, w = ctx.w, lane = ctx.lane, g = ctx.g, tok = ctx.tok;
  const int csb = L_CS + (w * 6 + 0) * 64;
  const unsigned lv = lane * 16;
  const unsigned T = ctx.T;
  AE e;
  TS_DECL;
  unsigned ab0, ab1;
  ImgDma wd = img_prep<1024>(ctx), qd = img_prep<3072>(ctx);
  {
    const int d = tok - 4 * g;
    e.ident = v2u{d == 0 ? 0x3f80u : d == 1 ? 0x3f800000u : 0u, d == 2 ? 0x3f80u : d == 3 ? 0x3f800000u : 0u};
    const unsigned lo = tok < 8 ? ONE2 : 0u, hi = tok < 8 ? 0u : ONE2;
    e.lo1 = v2u{lo, lo}; e.hi1 = v2u{hi, hi}; e.ones = v2u{mk1(), mk1()}; e.wphi = wphi;
    e.aT = ctx.Lb + L_REC + tok * 16;
    e.aF = ctx.Lb + L_NM + g * 16;
    ab0 = ctx.Lb + L_XT + (lp == 0 ? x0_lane(tok, g) : lv); ab1 = ab0 + 49152;
    asm volatile("" : "+a"(e.ident), "+a"(e.lo1), "+a"(e.hi1), "+a"(e.ones), "+a"(e.wphi), "+v"(e.aT), "+v"(e.aF),
                      "+v"(ab0), "+v"(ab1), "+v"(qd.v[0]), "+v"(qd.v[1]), "+v"(qd.v[2]), "+v"(qd.v[3]));
  }
  const int xs = WS_X + ACT_L + c * 65536 + w * 4096 - 16384;
  f4 xk, xv, yk, yv;
  int so;
  auto stat_rd = [&](auto S, auto h) {
    (void)e;
    if constexpr (h == GA(12)) A_LD64(e.nst, e.aT, S * 256);
    if constexpr (h == GC(12)) { A_LD128(e.nnm4, e.aF, S * 64); A_LD128(e.nrs4, e.aF, S * 64 + 320); }
  };
  auto xl = [](auto ks) { return IC<(ks == 12 ? 1 : (ks == 13 || ks == 14) ? 3 : 0)>{}; };
  auto next = [&](f4& k, f4& v) { e.ak = k; e.av = v; e.st = e.nst; e.nm4 = e.nnm4; e.rs4 = e.nrs4; };
  auto epi = [&](auto h) {
    (void)e;
    if constexpr (h == GC(0)) { A_FMA(e.k[0], e.st[0], e.csk[0], e.ak[0]); A_FMA(e.k[1], e.st[0], e.csk[1], e.ak[1]); }
    if constexpr (h == GA(1)) { A_FMA(e.k[2], e.st[0], e.csk[2], e.ak[2]); A_FMA(e.k[3], e.st[0], e.csk[3], e.ak[3]); }
    if constexpr (h == GC(1)) { A_CVT(e.kb[0], e.k[0], e.k[1]); A_CVT(e.kb[1], e.k[2], e.k[3]); A_FMA(e.v[0], e.nm4[0], e.csv, e.av[0]); }
    if constexpr (h == GA(2)) {
      A_FMA(e.v[1], e.nm4[1], e.csv, e.av[1]);
      mf16z_a(e.f.p, e.wphi, v2u{e.kb[0], e.kb[1]});
      A_FMA(e.v[2], e.nm4[2], e.csv, e.av[2]); A_FMA(e.v[3], e.nm4[3], e.csv, e.av[3]);
    }
    if constexpr (h == GC(2)) { A_MUL(e.v[0], e.v[0], e.rs4[0]); A_MUL(e.v[1], e.v[1], e.rs4[1]); A_MUL(e.v[2], e.v[2], e.rs4[2]); }
    if constexpr (h == GA(3)) { A_MUL(e.v[3], e.v[3], e.rs4[3]); A_MULS(e.f.c, LOG2E, e.st[1]); }
    if constexpr (h == GC(3)) { FM_MM1(e.f); A_CVT(e.vb[0], e.v[0], e.v[1]); }
    if constexpr (h == GA(4)) FM_MM2(e.f);
    if constexpr (h == GC(4)) { A_CVT(e.vb[1], e.v[2], e.v[3]); A_SW16(e.f.m, e.f.m2); }
    if constexpr (h == GA(5)) FM_MX2(e.f);
    if constexpr (h == GC(5)) A_SW32(e.f.m, e.f.m2);
    if constexpr (h == GA(6)) FM_MXC(e.f);
    if constexpr (h == GC(6)) A_SW16(e.f.m, e.f.m2);
    if constexpr (h == GA(7)) { A_FMA(e.f.ep[0], e.f.p[0], e.f.c, e.f.m); A_FMA(e.f.ep[1], e.f.p[1], e.f.c, e.f.m); }
    if constexpr (h == GC(7)) { A_FMA(e.f.ep[2], e.f.p[2], e.f.c, e.f.m); A_FMA(e.f.ep[3], e.f.p[3], e.f.c, e.f.m); A_FMAN(e.f.en[0], e.f.p[0], e.f.c, e.f.m2); }
    if constexpr (h == GA(8)) { A_FMAN(e.f.en[1], e.f.p[1], e.f.c, e.f.m2); A_FMAN(e.f.en[2], e.f.p[2], e.f.c, e.f.m2); }
    if constexpr (h == GC(8)) { A_FMAN(e.f.en[3], e.f.p[3], e.f.c, e.f.m2); A_EXP(e.f.ep[0], e.f.ep[0]); A_EXP(e.f.ep[1], e.f.ep[1]); }
    if constexpr (h == GA(9)) A_EXP(e.f.ep[2], e.f.ep[2]);
    if constexpr (h == GC(9)) { A_EXP(e.f.ep[3], e.f.ep[3]); A_EXP(e.f.en[0], e.f.en[0]); }
    if constexpr (h == GA(10)) A_EXP(e.f.en[1], e.f.en[1]);
    if constexpr (h == GC(10)) { A_EXP(e.f.en[2], e.f.en[2]); A_EXP(e.f.en[3], e.f.en[3]); }
    if constexpr (h == GA(11)) { A_CVT(e.f.eb[0], e.f.ep[0], e.f.ep[1]); A_CVT(e.f.eb[1], e.f.ep[2], e.f.ep[3]); }
    if constexpr (h == GC(11)) {
      A_CVT(e.f.nb[0], e.f.en[0], e.f.en[1]); A_CVT(e.f.nb[1], e.f.en[2], e.f.en[3]);
      mf16z_a(e.f.sp, e.ones, v2u{e.f.eb[0], e.f.eb[1]});
    }
    if constexpr (h == GA(12)) mf16z_a(e.f.sn, e.ones, v2u{e.f.nb[0], e.f.nb[1]});
    if constexpr (h == GA(13)) A_RCP(e.f.m, e.f.sp);
    if constexpr (h == GC(13)) { A_MUL(e.f.ep[0], e.f.ep[0], e.f.m); A_RCP(e.f.m2, e.f.sn); A_MUL(e.f.ep[1], e.f.ep[1], e.f.m); }
    if constexpr (h == GA(14)) { A_MUL(e.f.ep[2], e.f.ep[2], e.f.m); A_MUL(e.f.ep[3], e.f.ep[3], e.f.m); }
    if constexpr (h == GC(14)) { A_MUL(e.f.en[0], e.f.en[0], e.f.m2); A_MUL(e.f.en[1], e.f.en[1], e.f.m2); A_CVT(e.f.eb[0], e.f.ep[0], e.f.ep[1]); }
    if constexpr (h == GA(15)) { A_CVT(e.f.eb[1], e.f.ep[2], e.f.ep[3]); A_MUL(e.f.en[2], e.f.en[2], e.f.m2); }
    if constexpr (h == GC(15)) {
      A_MUL(e.f.en[3], e.f.en[3], e.f.m2); A_CVT(e.f.nb[0], e.f.en[0], e.f.en[1]);
      mf16z_b(e.tp, v2u{e.f.eb[0], e.f.eb[1]}, e.ident);
      A_CVT(e.f.nb[1], e.f.en[2], e.f.en[3]);
      e.vbo[0] = e.vb[0]; e.vbo[1] = e.vb[1];
    }
  };
  auto epi2 = [&](auto FT, auto h) {
    (void)e;
    if constexpr (h == GA(0)) mf16z_b(e.tm, v2u{e.f.nb[0], e.f.nb[1]}, e.ident);
    if constexpr (h == GC(5)) { A_CVT(e.php[0], e.tp[0], e.tp[1]); }
    if constexpr (h == GC(6)) { A_CVT(e.php[1], e.tp[2], e.tp[3]); }
    if constexpr (h == GA(9)) {
      if constexpr (FT) mf16z(e.kvp, v2u{e.php[0], e.php[1]}, v2u{e.vbo[0], e.vbo[1]}); else mf16c(e.kvp, v2u{e.php[0], e.php[1]}, v2u{e.vbo[0], e.vbo[1]});
      A_CVT(e.phm[0], e.tm[0], e.tm[1]); A_CVT(e.phm[1], e.tm[2], e.tm[3]);
      if constexpr (FT) mf16z_a(e.zz, e.lo1, v2u{e.php[0], e.php[1]}); else mf16c_a(e.zz, e.lo1, v2u{e.php[0], e.php[1]});
    }
    if constexpr (h == GA(10)) {
      if constexpr (FT) mf16z(e.kvm, v2u{e.phm[0], e.phm[1]}, v2u{e.vbo[0], e.vbo[1]}); else mf16c(e.kvm, v2u{e.phm[0], e.phm[1]}, v2u{e.vbo[0], e.vbo[1]});
    }
    if constexpr (h == GA(13)) mf16c_a(e.zz, e.hi1, v2u{e.phm[0], e.phm[1]});
  };
  auto c_sh = [&](auto K0, auto SOFF, auto LOFF, int sb, auto h) {
    if constexpr (h == GA(K0 - 1)) dma_so<SOFF>(so, sb);
    if constexpr (h == GA(K0)) dma_m0<LOFF>(ctx.Lw);
    return h == GA(K0 - 1) || h == GA(K0);
  };
  auto c_gp = [&](auto K0, const ImgDma& d, rsrc_t rs, auto h) {
    if constexpr (h == GC(K0)) dma_go<0>(d.v[0], rs, so);
    if constexpr (h == GC(K0 + 2)) dma_go<1>(d.v[1], rs, so);
    if constexpr (h == GC(K0 + 4)) dma_go<2>(d.v[2], rs, so);
    if constexpr (h == GC(K0 + 6)) dma_go<3>(d.v[3], rs, so);
  };
  constexpr int QS = 128 * 3072;
  constexpr int SL = 32 * 256;
  const int kblk = WS_KV1 + ((m * 64 + w * 16) * 32 + c) * 256;
  const int zblk = WS_KVZ + ((m * 4 + w) * 32 + c) * 256;
  int kvo1 = g * 2 * SL + tok * 16, zvo1 = ((g >> 1) * 16 + tok) * 8;
  asm volatile("" : "+v"(kvo1), "+v"(zvo1));
  {
    const rsrc_t rnull = __builtin_amdgcn_make_buffer_rsrc((void*)ctx.ws, 0, 0, 0x00020000);
    const rsrc_t rx = lp ? ctx.rs : rnull, pb = lp ? rnull : rsb, pw = lp ? rnull : rwo;
    const unsigned lb3 = lp ? ctx.Lw + 3 * 16384 : ctx.Lw, lb4 = lp ? ctx.Lw + 4 * 16384 : ctx.Lw;
    unsigned pv3 = (unsigned)(((lane >> 3) & 3) * 3072 + (lane & 7) * 128), pv1 = (unsigned)((lane & 31) * 128), sink = 0;
    const int prow = 16 * (int)(blockIdx.x >> 3) + 4 * w;
    const int ps3 = __builtin_amdgcn_readfirstlane(prow * 3072), ps1 = __builtin_amdgcn_readfirstlane(prow * 1024);
    unsigned ack = ctx.Lb + csb + 64 + g * 16, acv = ctx.Lb + csb + 128 + tok * 4;
    asm volatile("" : "+v"(pv3), "+v"(pv1), "+v"(sink), "+v"(ack), "+v"(acv));
    auto pfs = [&](unsigned vo, rsrc_t rs, int so_, auto OFF) {
      (void)sink;
      asm volatile("buffer_load_dword %0, %1, %2, %3 offen offset:%4" : "+v"(sink) : "v"(vo), "s"(rs), "s"(so_), "n"(OFF.value) : "memory");
    };
    if (lp > 0) {
      ST st;
      st_begin<false>(ctx, st);
      gather_start<false>(ctx, st, L, lv, xs);
    } else {
      Ctx c2 = ctx;
      first_stage(c2, rsx, rsa);
      STAMP(1);
    }
    A_LD128(e.csk, ack, 0);
    asm volatile("ds_read_b32 %0, %1" : "=v"(e.csv) : "v"(acv) : "memory");
    ring_prime<16384>(ab0);
    TS(0);
    gemm_tile<-1, P_AK, P_AV, 16384, false>(ab0, xk, xk, xv, xl,
      [&](auto h) {
        if constexpr (h == GA(0)) dma_so<3 * 16384>(so, xs);
        if constexpr (h == GA(1)) dma_m0<L_XT>(lb3);
        if constexpr (h == GA(11)) WAIT_VMV(4);
        if constexpr (h == GA(12)) RAWBAR();
        if constexpr (h == GA(13)) dma_so<4 * 16384>(so, xs);
        if constexpr (h == GA(14)) dma_m0<L_XT>(lb4);
        return h == GA(0) || h == GA(1) || (h >= GA(11) && h <= GA(14));
      },
      [&](auto h) {
        if constexpr (h == GA(1)) dma_go_sc1<0>(lv, rx, so);
        if constexpr (h == GA(4)) dma_go_sc1<1>(lv, rx, so);
        if constexpr (h == GA(7)) dma_go_sc1<2>(lv, rx, so);
        if constexpr (h == GC(10)) dma_go_sc1<3>(lv, rx, so);
        if constexpr (h == GC(14)) dma_go_sc1<0>(lv, rx, so);
        if constexpr (h == GC(15)) NOP(1);
        stat_rd(IC<1>{}, h);
      });
    next(xk, xv);
    TS(1);
    gemm_tile<-1, P_AK, P_AV, 32768, false>(ab0, yk, yk, yv, xl,
      [&](auto h) {
        if constexpr (h == GA(11)) WAIT_VMV(6);
        if constexpr (h == GA(12)) RAWBAR();
        return c_sh(IC<7>{}, IC<0>{}, IC<L_RQ>{}, qd.sb, h) | (h == GA(11) || h == GA(12));
      },
      [&](auto h) {
        if constexpr (h == GA(0)) dma_go_sc1<1>(lv, rx, so);
        if constexpr (h == GC(3)) dma_go_sc1<2>(lv, rx, so);
        if constexpr (h == GC(5)) dma_go_sc1<3>(lv, rx, so);
        c_gp(IC<7>{}, qd, rsa, h);
        if constexpr (h == GC(12)) pfs(pv1, pw, ps1, IC<0>{});
        stat_rd(IC<2>{}, h); epi(h);
      });
    next(yk, yv);
    TS(2);
    gemm_tile<-1, P_AK, P_AV, 0, false>(ab1, xk, xk, xv, xl,
      [&](auto h) {
        if constexpr (h == GA(11)) WAIT_VMV(10);
        if constexpr (h == GA(12)) RAWBAR();
        return c_sh(IC<1>{}, IC<QS>{}, IC<L_RQ + 16384>{}, qd.sb, h) | c_sh(IC<9>{}, IC<2 * QS>{}, IC<L_RQ + 32768>{}, qd.sb, h) | (h == GA(11) || h == GA(12));
      },
      [&](auto h) {
        c_gp(IC<1>{}, qd, rsa, h); c_gp(IC<9>{}, qd, rsa, h);
        if constexpr (h == GC(12)) pfs(pv3, pb, ps3, IC<1024>{});
        stat_rd(IC<3>{}, h); epi2(IC<1>{}, h); epi(h);
      });
    next(xk, xv);
    TS(3);
    gemm_tile<-1, P_AK, P_AV, 16384, false>(ab1, yk, yk, yv, xl,
      [&](auto h) {
        return c_sh(IC<1>{}, IC<3 * QS>{}, IC<L_RQ + 49152>{}, qd.sb, h) | c_sh(IC<9>{}, IC<0>{}, IC<L_XT>{}, wd.sb, h);
      },
      [&](auto h) {
        c_gp(IC<1>{}, qd, rsa, h);
        if constexpr (h == GC(8)) pfs(pv3, pb, ps3, IC<2048>{});
        c_gp(IC<9>{}, wd, rwo, h); stat_rd(IC<4>{}, h); epi2(IC<0>{}, h); epi(h);
      });
    next(yk, yv);
  TS(4);
  {
    FM& f = e.f;
    A_MULS(f.c, LOG2E, e.st[1]);
    NOP(0);
    mf16z_b(e.tm, v2u{f.nb[0], f.nb[1]}, e.ident);
    A_FMA(e.k[0], e.st[0], e.csk[0], e.ak[0]); A_FMA(e.k[1], e.st[0], e.csk[1], e.ak[1]);
    A_FMA(e.k[2], e.st[0], e.csk[2], e.ak[2]); A_FMA(e.k[3], e.st[0], e.csk[3], e.ak[3]);
    A_CVT(e.kb[0], e.k[0], e.k[1]); A_CVT(e.php[0], e.tp[0], e.tp[1]); A_CVT(e.kb[1], e.k[2], e.k[3]); A_CVT(e.php[1], e.tp[2], e.tp[3]);
    A_CVT(e.phm[0], e.tm[0], e.tm[1]);
    mf16z_a(f.p, e.wphi, v2u{e.kb[0], e.kb[1]});
    A_CVT(e.phm[1], e.tm[2], e.tm[3]); A_FMA(e.v[0], e.nm4[0], e.csv, e.av[0]);
    mf16c(e.kvp, v2u{e.php[0], e.php[1]}, v2u{e.vbo[0], e.vbo[1]});
    A_FMA(e.v[1], e.nm4[1], e.csv, e.av[1]); A_FMA(e.v[2], e.nm4[2], e.csv, e.av[2]);
    mf16c(e.kvm, v2u{e.phm[0], e.phm[1]}, v2u{e.vbo[0], e.vbo[1]});
    A_FMA(e.v[3], e.nm4[3], e.csv, e.av[3]); A_MUL(e.v[0], e.v[0], e.rs4[0]);
    FM_MM1(f);
    mf16c_a(e.zz, e.lo1, v2u{e.php[0], e.php[1]});
    FM_MM2(f);
    A_MUL(e.v[1], e.v[1], e.rs4[1]); A_MUL(e.v[2], e.v[2], e.rs4[2]);
    A_SW16(f.m, f.m2);
    mf16c_a(e.zz, e.hi1, v2u{e.phm[0], e.phm[1]});
    FM_MX2(f);
    A_MUL(e.v[3], e.v[3], e.rs4[3]); A_CVT(e.vb[0], e.v[0], e.v[1]);
    A_SW32(f.m, f.m2);
    FM_MXC(f);
    A_CVT(e.vb[1], e.v[2], e.v[3]);
    NOP(0);
    A_SW16(f.m, f.m2);
#pragma unroll
    for (int r = 0; r < 4; ++r) A_FMA(f.ep[r], f.p[r], f.c, f.m);
#pragma unroll
    for (int r = 0; r < 4; ++r) A_FMAN(f.en[r], f.p[r], f.c, f.m2);
#pragma unroll
    for (int r = 0; r < 4; ++r) A_EXP(f.ep[r], f.ep[r]);
    A_EXP(f.en[0], f.en[0]);
    A_CVT(f.eb[0], f.ep[0], f.ep[1]); A_CVT(f.eb[1], f.ep[2], f.ep[3]);
    A_EXP(f.en[1], f.en[1]); A_EXP(f.en[2], f.en[2]);
    mf16z_a(f.sp, e.ones, v2u{f.eb[0], f.eb[1]});
    A_EXP(f.en[3], f.en[3]);
    A_CVT(f.nb[0], f.en[0], f.en[1]); A_CVT(f.nb[1], f.en[2], f.en[3]);
    NOP(1);
    mf16z_a(f.sn, e.ones, v2u{f.nb[0], f.nb[1]});
    NOP(1);
    A_RCP(f.m, f.sp);
    NOP(0);
#pragma unroll
    for (int r = 0; r < 4; ++r) A_MUL(f.ep[r], f.ep[r], f.m);
    A_RCP(f.m2, f.sn);
    A_CVT(f.eb[0], f.ep[0], f.ep[1]); A_CVT(f.eb[1], f.ep[2], f.ep[3]);
    A_MUL(f.en[0], f.en[0], f.m2); A_MUL(f.en[1], f.en[1], f.m2);
    mf16z_b(e.tp, v2u{f.eb[0], f.eb[1]}, e.ident);
    A_MUL(f.en[2], f.en[2], f.m2); A_MUL(f.en[3], f.en[3], f.m2);
    A_CVT(f.nb[0], f.en[0], f.en[1]); A_CVT(f.nb[1], f.en[2], f.en[3]);
    NOP(1);
    mf16z_b(e.tm, v2u{f.nb[0], f.nb[1]}, e.ident);
    NOP(0);
    A_CVT(e.php[0], e.tp[0], e.tp[1]); A_CVT(e.php[1], e.tp[2], e.tp[3]);
    NOP(1);
    mf16c(e.kvp, v2u{e.php[0], e.php[1]}, v2u{e.vb[0], e.vb[1]});
    mf16c_a(e.zz, e.lo1, v2u{e.php[0], e.php[1]});
    NOP(0);
    A_CVT(e.phm[0], e.tm[0], e.tm[1]); A_CVT(e.phm[1], e.tm[2], e.tm[3]);
    NOP(1);
    mf16c(e.kvm, v2u{e.phm[0], e.phm[1]}, v2u{e.vb[0], e.vb[1]});
    mf16c_a(e.zz, e.hi1, v2u{e.phm[0], e.phm[1]});
    asm volatile("s_nop 0" : "+v"(e.kvp));
  }
  TS(5);
  {
    const int blk = kblk, vo = kvo1;
    st128(ctx, v4u{asu(e.kvp[0]), T, asu(e.kvp[1]), T}, vo, blk);
    st128(ctx, v4u{asu(e.kvp[2]), T, asu(e.kvp[3]), T}, vo, blk + SL);
    asm volatile("s_nop 3" : "+v"(e.kvm), "+v"(e.zz));
    st128(ctx, v4u{asu(e.kvm[0]), T, asu(e.kvm[1]), T}, vo, blk + 8 * SL);
    st128(ctx, v4u{asu(e.kvm[2]), T, asu(e.kvm[3]), T}, vo, blk + 9 * SL);
    st64(ctx, v2u{asu(e.zz[0]), T}, zvo1, zblk);
  }
    asm volatile("s_waitcnt vmcnt(8)" : "+v"(sink) :: "memory");
  }
  STAMP(L * 8 + 2);
  TS_FLUSH(L * 8 + 2, 6);
  f4 aq[4];
  v4u dk[4], dz;
  const int S64 = 2 * c + (w >> 1);
  const int rblk = WS_KV1 + ((m * 64 + S64) * 32) * 256 + (w & 1) * 128;
  const int rzb = WS_KVZ + ((m * 4 + (S64 >> 4)) * 32) * 256 + (S64 & 15) * 16;
  int kvo = (lane >> 3) * 256 + (lane & 7) * 16, zvo = (lane & 31) * 256;
  {
    lds_t* qt = ctx.L + opq(L_RQ + tr_base(lane, w));
    asm volatile("" : "+v"(kvo), "+v"(zvo));
    TS(0);
    RAWBAR();
    TS(1);
    v4u wq[16];
#pragma unroll
    for (int ks = 0; ks < 16; ++ks) wq[ks] = tr_frag(qt, ks * 4096);
    if (lp == 0) {
      { Ctx c2 = ctx; fresh(c2); raw_load<P_BK, 0, 16>(c2, rsb, 1024); }
      f4 c0, c1;
      v4u ones = {mk1(), mk1(), mk1(), mk1()};
      asm volatile("s_nop 1" : "+v"(ones));
      sfor<0, 8>([&](auto k) { mf_av<k == 0>(c0, wq[2 * k], ones); mf_av<k == 0>(c1, wq[2 * k + 1], ones); });
      asm volatile("s_nop 7" : "+v"(c0), "+v"(c1));
      if (tok == 0) *(LAS f4*)(ctx.L + csb + g * 16) = c0 + c1;
    }
    v4u a1[4];
    auto rd = [&](auto k) {
      (void)a1;
      if constexpr (k < 16) { ring_rd<k & 3, 16384 + k * 1024>(ab0); } else if constexpr (k < 32) { ring_rd<k & 3, (k - 16) * 1024>(ab1); }
    };
    auto rd1 = [&](auto k) {
      (void)a1; (void)ab0; (void)ab1;
      if constexpr (k < 16) { A_LD128(a1[k & 3], ab0, 32768 + k * 1024); } else if constexpr (k < 32) { A_LD128(a1[k & 3], ab1, 16384 + (k - 16) * 1024); }
    };
    sfor<0, 3>([&](auto k) { rd(k); rd1(k); });
#pragma unroll
    for (int ks = 0; ks < 16; ++ks) asm volatile("" : "+a"(wq[ks]));
    WAIT_LG(4);
    TS(2);
    sfor<0, 32>([&](auto k) {
      constexpr int ks = k & 15, u = k >> 4;
      (void)dk; (void)dz; (void)kvo; (void)zvo; (void)rblk; (void)rzb;
      mmq<k & 3, ks == 0>(aq[2 * u], wq[ks]);
      VNOP();
      rd(IC<k + 3>{});
      if constexpr (k >= QPOLL && k < QPOLL + 4) {
        constexpr int i = k - QPOLL;
        asm volatile("buffer_load_dwordx4 %0, %1, %2, %3 offen offset:%4 sc1" : "=v"(dk[i]) : "v"(kvo), "s"(ctx.rs), "s"(i < 2 ? rblk : rblk + 4096), "n"((i & 1) * 2048) : "memory");
      }
      if constexpr (k == QPOLL + 4) asm volatile("buffer_load_dwordx4 %0, %1, %2, %3 offen sc1" : "=v"(dz) : "v"(zvo), "s"(ctx.rs), "s"(rzb) : "memory");
      mf_av<ks == 0>(aq[2 * u + 1], wq[ks], a1[k & 3]);
      WAIT_LG(k + 3 < 32 ? 3 : k == 29 ? 2 : 0);
      rd1(IC<k + 3>{});
      SB();
    });
  }
  TS(6);
  TS_FLUSH(34 + lp * 8, 7);
  STAMP(48 + lp * 4);
  v4u phq[4], d2[4];
  const int b2 = WS_KV2 + (m * 64 + w * 16) * 256;
  int vo2 = g * 512 + tok * 16;
  {
    const f4 csq = lds_ldf4(ctx.L, csb + g * 16);
    f2 st[4];
#pragma unroll
    for (int t = 0; t < 4; ++t) st[t] = *(LAS f2*)(ctx.L + L_REC + ((1 + t) * 16 + tok) * 16);
    int svo = (lane & 7) * 16 + ((lane >> 4) & 1) * 8;
    const u64 mhi = __ballot(lane >= 32);
    const int sblk = WS_KV2 + (m * 64 + S64) * 256 + (w & 1) * 128;
    asm volatile("s_nop 3" : "+v"(aq[0]), "+v"(aq[1]), "+v"(aq[2]), "+v"(aq[3]), "+v"(svo), "+v"(vo2));
    {
      asm volatile("s_waitcnt vmcnt(0)" : "+v"(dk[0]), "+v"(dk[1]), "+v"(dk[2]), "+v"(dk[3]), "+v"(dz) :: "memory");
      STAMP(49 + lp * 4);
      for (;;) {
        unsigned bad;
        asm volatile("v_sad_u32 %0, %1, %11, 0 clamp\n\tv_sad_u32 %0, %2, %11, %0 clamp\n\tv_sad_u32 %0, %3, %11, %0 clamp\n\tv_sad_u32 %0, %4, %11, %0 clamp\n\t"
                     "v_sad_u32 %0, %5, %11, %0 clamp\n\tv_sad_u32 %0, %6, %11, %0 clamp\n\tv_sad_u32 %0, %7, %11, %0 clamp\n\tv_sad_u32 %0, %8, %11, %0 clamp\n\t"
                     "v_sad_u32 %0, %9, %11, %0 clamp\n\tv_sad_u32 %0, %10, %11, %0 clamp"
                     : "=&v"(bad) : "v"(dk[0].y), "v"(dk[0].w), "v"(dk[1].y), "v"(dk[1].w), "v"(dk[2].y), "v"(dk[2].w), "v"(dk[3].y), "v"(dk[3].w),
                       "v"(dz.y), "v"(dz.w), "s"(T));
        if (__builtin_expect(__all(bad == 0), 1) || --ctx.budget < 0) break;
        asm volatile("" ::: "memory");
#pragma unroll
        for (int i = 0; i < 4; ++i) dk[i] = ld128(ctx, kvo, rblk + i * 2048);
        dz = ld128(ctx, zvo, rzb);
      }
      float K, K1, Z; unsigned pk, tg;
      asm volatile("v_add_f32 %0, %5, %6\n\tv_add_f32 %1, %9, %10\n\tv_add_f32 %2, %7, %8\n\tv_add_f32 %3, %11, %12\n\t"
                   "v_add_f32 %0, %0, %2\n\tv_add_f32 %1, %1, %3\n\tv_cndmask_b32 %2, %13, %14, %15\n\tv_mov_b32 %4, %16\n\t"
                   "v_permlane32_swap_b32 %0, %1\n\tv_add_f32 %0, %0, %1\n\ts_nop 1\n\t"
                   "v_permlane16_swap_b32 %0, %2\n\tv_add_f32 %0, %0, %2\n\ts_nop 1\n\t"
                   "v_add_f32_dpp %0, %0, %0 row_ror:8 row_mask:0xf bank_mask:0xf\n\ts_nop 1\n\t"
                   "v_add_f32_dpp %0, %0, %0 row_ror:4 row_mask:0xa bank_mask:0xf\n\ts_nop 1\n\t"
                   "v_add_f32_dpp %0, %0, %0 row_ror:2 row_mask:0xa bank_mask:0xf\n\ts_nop 1\n\t"
                   "v_add_f32_dpp %0, %0, %0 row_ror:1 row_mask:0xa bank_mask:0xf\n\t"
                   "v_mov_b32 %1, %0\n\ts_nop 1\n\tv_permlane32_swap_b32 %0, %1\n\tv_cvt_pk_bf16_f32 %3, %0, %1"
                   : "=&v"(K), "=&v"(K1), "=&v"(Z), "=&v"(pk), "=&v"(tg)
                   : "v"(dk[0].x), "v"(dk[1].x), "v"(dk[2].x), "v"(dk[3].x), "v"(dk[0].z), "v"(dk[1].z), "v"(dk[2].z), "v"(dk[3].z),
                     "v"(dz.x), "v"(dz.z), "s"(mhi), "s"(T));
      st64(ctx, v2u{pk, tg}, svo, sblk);
      STAMP(L * 8 + 3);
    }
    FM f[4];
    unsigned qb[4][2];
#pragma unroll
    for (int t = 0; t < 4; ++t) {
      float q[4];
#pragma unroll
      for (int r = 0; r < 4; ++r) A_FMA(q[r], st[t][0], csq[r], aq[t][r]);
      A_MULS(f[t].c, LOG2E, st[t][1]);
      VNOP();
      A_CVT(qb[t][0], q[0], q[1]); A_CVT(qb[t][1], q[2], q[3]);
      if (t > 0) mf16z_a(f[t - 1].p, e.wphi, v2u{qb[t - 1][0], qb[t - 1][1]});
    }
    FM_MM1(f[0]);
    mf16z_a(f[3].p, e.wphi, v2u{qb[3][0], qb[3][1]});
    FM_MM1(f[1]); FM_MM2(f[0]); FM_MM2(f[1]); FM_MM1(f[2]);
    A_SW16(f[0].m, f[0].m2); FM_MM2(f[2]); FM_MM1(f[3]); A_SW16(f[1].m, f[1].m2); FM_MM2(f[3]);
    FM_MX2(f[0]); A_SW16(f[2].m, f[2].m2); FM_MX2(f[1]); A_SW16(f[3].m, f[3].m2);
    A_SW32(f[0].m, f[0].m2); FM_MX2(f[2]); A_SW32(f[1].m, f[1].m2); FM_MX2(f[3]);
    FM_MXC(f[0]); A_SW32(f[2].m, f[2].m2); FM_MXC(f[1]); A_SW32(f[3].m, f[3].m2);
    A_SW16(f[0].m, f[0].m2); FM_MXC(f[2]); A_SW16(f[1].m, f[1].m2); FM_MXC(f[3]);
    RAWBAR();
    sfor<0, 4>([&](auto t) {
      (void)f; (void)e;
      auto piece = [&](auto p) {
        if constexpr ((p & 3) == 0) { dma_so<128 * 1024 * (1 + (p >> 2))>(so, wd.sb); dma_m0<L_XT + 16384 * (1 + (p >> 2))>(ctx.Lw); VNOP(); }
        dma_go<p & 3>(wd.v[p & 3], rwo, so);
      };
      piece(IC<3 * t>{});
#pragma unroll
      for (int r = 0; r < 4; ++r) A_FMA(f[t].ep[r], f[t].p[r], f[t].c, f[t].m);
      if constexpr (t < 2) A_SW16(f[t + 2].m, f[t + 2].m2);
#pragma unroll
      for (int r = 0; r < 4; ++r) A_FMAN(f[t].en[r], f[t].p[r], f[t].c, f[t].m2);
      if constexpr (t > 0) mf16z_a(f[t - 1].sn, e.ones, v2u{f[t - 1].nb[0], f[t - 1].nb[1]}); else VNOP();
      piece(IC<3 * t + 1>{});
#pragma unroll
      for (int r = 0; r < 4; ++r) A_EXP(f[t].ep[r], f[t].ep[r]);
      A_EXP(f[t].en[0], f[t].en[0]);
      piece(IC<3 * t + 2>{});
      A_CVT(f[t].eb[0], f[t].ep[0], f[t].ep[1]); A_CVT(f[t].eb[1], f[t].ep[2], f[t].ep[3]);
      A_EXP(f[t].en[1], f[t].en[1]); A_EXP(f[t].en[2], f[t].en[2]);
      mf16z_a(f[t].sp, e.ones, v2u{f[t].eb[0], f[t].eb[1]});
      A_EXP(f[t].en[3], f[t].en[3]);
      A_CVT(f[t].nb[0], f[t].en[0], f[t].en[1]); A_CVT(f[t].nb[1], f[t].en[2], f[t].en[3]);
    });
    if (lp == 0) { Ctx c2 = ctx; fresh(c2); pf32<3072>(c2, rsb, 0); part_step_ld<P_BK, P_BV>(c2, L_CS + (w * 6 + 4) * 64, rsb, 2048); }
#pragma unroll
    for (int t = 0; t < 4; ++t) {
      A_RCP(f[t].m, f[t].sp); A_RCP(f[t].m2, f[t].sn);
      if (t == 0) mf16z_a(f[3].sn, e.ones, v2u{f[3].nb[0], f[3].nb[1]});
      if (t == S2POLL) {
        asm volatile("buffer_load_dwordx4 %0, %4, %5, %6 offen sc1\n\tbuffer_load_dwordx4 %1, %4, %5, %6 offen offset:256 sc1\n\t"
                     "buffer_load_dwordx4 %2, %4, %5, %6 offen offset:2048 sc1\n\tbuffer_load_dwordx4 %3, %4, %5, %6 offen offset:2304 sc1"
                     : "=&v"(d2[0]), "=&v"(d2[1]), "=&v"(d2[2]), "=&v"(d2[3]) : "v"(vo2), "s"(ctx.rs), "s"(b2) : "memory");
      }
#pragma unroll
      for (int r = 0; r < 4; ++r) A_MUL(f[t].ep[r], f[t].ep[r], f[t].m);
#pragma unroll
      for (int r = 0; r < 4; ++r) A_MUL(f[t].en[r], f[t].en[r], f[t].m2);
      unsigned x0, x1, x2, x3;
      A_CVT(x0, f[t].ep[0], f[t].ep[1]); A_CVT(x1, f[t].ep[2], f[t].ep[3]); A_CVT(x2, f[t].en[0], f[t].en[1]); A_CVT(x3, f[t].en[2], f[t].en[3]);
      phq[t] = v4u{x0, x1, x2, x3};
    }
  }
  STAMP(50 + lp * 4);
  {
    int ovo = ((g >> 1) * 16 + tok) * 16 + (g & 1) * 8;
    const int osb = WS_O + (4 * c * 16 + 2 * m + (w >> 1)) * 1024 + (w & 1) * 512;
    asm volatile("s_waitcnt vmcnt(0)" : "+v"(d2[0]), "+v"(d2[1]), "+v"(d2[2]), "+v"(d2[3]), "+v"(ovo) :: "memory");
    for (;;) {
      unsigned bad;
      asm volatile("v_sad_u32 %0, %1, %9, 0 clamp\n\tv_sad_u32 %0, %2, %9, %0 clamp\n\tv_sad_u32 %0, %3, %9, %0 clamp\n\tv_sad_u32 %0, %4, %9, %0 clamp\n\t"
                   "v_sad_u32 %0, %5, %9, %0 clamp\n\tv_sad_u32 %0, %6, %9, %0 clamp\n\tv_sad_u32 %0, %7, %9, %0 clamp\n\tv_sad_u32 %0, %8, %9, %0 clamp"
                   : "=&v"(bad) : "v"(d2[0].y), "v"(d2[0].w), "v"(d2[1].y), "v"(d2[1].w), "v"(d2[2].y), "v"(d2[2].w), "v"(d2[3].y), "v"(d2[3].w), "s"(T));
      if (__builtin_expect(__all(bad == 0), 1) || --ctx.budget < 0) break;
      asm volatile("" ::: "memory");
      d2[0] = ld128(ctx, vo2, b2); d2[1] = ld128(ctx, vo2 + 256, b2); d2[2] = ld128(ctx, vo2 + 2048, b2); d2[3] = ld128(ctx, vo2 + 2304, b2);
    }
    STAMP(L * 8 + 4);
    v4u akv = {d2[0].x, d2[1].x, d2[2].x, d2[3].x}, az = {d2[0].z, d2[1].z, d2[2].z, d2[3].z};
    f4 num[4], den[4];
    asm volatile("s_nop 1" : "+v"(akv), "+v"(az));
#pragma unroll
    for (int t = 0; t < 4; ++t) { mf32z(den[t], az, phq[t]); mf32z(num[t], akv, phq[t]); }
#pragma unroll
    for (int t = 0; t < 4; ++t) {
      float r, q0, q1, q2, q3; unsigned o0, o1;
      asm volatile("v_add_f32 %2, %12, %7\n\tv_rcp_f32 %2, %2\n\ts_nop 0\n\t"
                   "v_mul_f32 %3, %8, %2\n\tv_mul_f32 %4, %9, %2\n\tv_mul_f32 %5, %10, %2\n\tv_mul_f32 %6, %11, %2\n\t"
                   "v_cvt_pk_bf16_f32 %0, %3, %4\n\tv_cvt_pk_bf16_f32 %1, %5, %6"
                   : "=&v"(o0), "=&v"(o1), "=&v"(r), "=&v"(q0), "=&v"(q1), "=&v"(q2), "=&v"(q3)
                   : "v"(den[t][0]), "v"(num[t][0]), "v"(num[t][1]), "v"(num[t][2]), "v"(num[t][3]), "s"(1e-6f), "v"(den[t]));
      st64(ctx, v2u{o0, o1}, ovo, osb + t * 16384);
    }
  }
  DRAIN();
  BAR();
  flag_store(ctx, WS_FO + c * 128);
}

template <bool ODD>
DEV void out_proj(Ctx& ctx, int lp, f4 (&xacc)[4], rsrc_t out, rsrc_t rsb, rsrc_t rwn, rsrc_t rsx) {
  fresh(ctx);
  const int L = 2 * lp + (ODD ? 1 : 0);
  const bool last = ODD && lp == 1, first = !ODD && lp == 0;
  const int c = ctx.c, m = ctx.m, w = ctx.w, lane = ctx.lane, g = ctx.g, tok = ctx.tok;
  if (first) {
    v2u xr[4];
#pragma unroll
    for (int nt = 0; nt < 4; ++nt)
      xr[nt] = __builtin_amdgcn_raw_buffer_load_b64(rsx, tok * 1024 + g * 8 + nt * 32, (64 * c + 16 * w) * 1024 + 128 * m, 0);
    SB();
    part_step_ld<P_BV, P_BQ>(ctx, L_CS + (w * 6 + 5) * 64, rsb, 0);
#pragma unroll
    for (int nt = 0; nt < 4; ++nt)
      xacc[nt] = f4{asf(xr[nt].x << 16), asf(xr[nt].x & 0xffff0000u), asf(xr[nt].y << 16), asf(xr[nt].y & 0xffff0000u)};
  }
  lds_t* tb[4];
#pragma unroll
  for (int nt = 0; nt < 4; ++nt) tb[nt] = ctx.L + opq(L_XT + tr_base(lane, nt));
  v4u wf[4][4];
#pragma unroll
  for (int i = 0; i < 4; ++i)
#pragma unroll
    for (int nt = 0; nt < 4; ++nt) wf[i][nt] = tr_frag(tb[nt], i * 4096);
  const int G = ((g & 1) << 1) | (g >> 1);
  int xvo = (G * 16 + tok) * 16;
  u64 m1 = __ballot(tok & 1), m2 = __ballot(tok & 2);
  const int osb = WS_O + (4 * c + w) * 16384;
  const int xsb = WS_X + (ODD ? ACT_L : 0) + ((4 * c + w) * 16 + 2 * m) * 1024;
  const int ssb = WS_STAT + (ODD ? STAT_L : 0) + ((c * 8 + m) * 64 + 16 * w) * 16;
  asm volatile("" : "+v"(xvo), "+v"(xacc[0]), "+v"(xacc[1]), "+v"(xacc[2]), "+v"(xacc[3]));
#pragma unroll
  for (int nt = 0; nt < 4; ++nt) asm volatile("" : "+v"(wf[0][nt]), "+v"(wf[1][nt]), "+v"(wf[2][nt]), "+a"(wf[3][nt]));
  wait_flags(ctx, WS_FO + ((ODD ? 32 : 0) + c) * 128, 0, false);
  if (ODD) ctx.T += 1u;
  STAMP(L * 8 + 5);
  v4u of[16];
  constexpr int PRE = 8;
#pragma unroll
  for (int ks = 0; ks < PRE; ++ks) of[ks] = ld128(ctx, lane * 16 + (ks & 3) * 1024, osb + (ks & ~3) * 1024);
  SB();
  sfor<0, 16>([&](auto ks) {
    if constexpr (ks + PRE < 16) { of[ks + PRE] = ld128(ctx, lane * 16 + ((ks + PRE) & 3) * 1024, osb + ((ks + PRE) & ~3) * 1024); SB(); }
#pragma unroll
    for (int nt = 0; nt < 4; ++nt) {
      if constexpr ((ks & 3) < 3) mf_va(xacc[nt], wf[ks & 3][nt], of[ks]); else mf_aa(xacc[nt], wf[ks & 3][nt], of[ks]);
      if constexpr (ks + 4 < 16) wf[ks & 3][nt] = tr_frag(tb[nt], (ks + 4) * 4096);
      SB();
    }
  });
  NOP(4);
  STAMP(L * 8 + 6);
  v4u fr[2];
#pragma unroll
  for (int u = 0; u < 2; ++u) {
    unsigned a0, a1, b0, b1;
    const f4 a = xacc[2 * u], b = xacc[2 * u + 1];
    asm volatile("v_cvt_pk_bf16_f32 %0, %4, %5\n\tv_cvt_pk_bf16_f32 %1, %6, %7\n\tv_cvt_pk_bf16_f32 %2, %8, %9\n\tv_cvt_pk_bf16_f32 %3, %10, %11\n\t"
                 "s_nop 0\n\tv_permlane16_swap_b32 %0, %2\n\tv_permlane16_swap_b32 %1, %3"
                 : "=&v"(a0), "=&v"(a1), "=&v"(b0), "=&v"(b1)
                 : "v"(a[0]), "v"(a[1]), "v"(a[2]), "v"(a[3]), "v"(b[0]), "v"(b[1]), "v"(b[2]), "v"(b[3]));
    fr[u] = v4u{a0, a1, b0, b1};
    if (!last) st128(ctx, fr[u], xvo + u * 1024, xsb);
  }
  if (last) {
#pragma unroll
    for (int u = 0; u < 2; ++u)
      __builtin_amdgcn_raw_buffer_store_b128(fr[u], out, tok * 1024 + G * 16 + u * 64, (64 * c + 16 * w) * 1024 + 128 * m, 0);
  } else {
    f4 ssum, gram;
    v4u ones = {mk1(), mk1(), mk1(), mk1()};
    asm volatile("s_nop 1" : "+v"(ones));
    mf32z(gram, fr[0], fr[0]); mf32z(ssum, ones, fr[0]);
    mf32c(gram, fr[1], fr[1]); mf32c(ssum, ones, fr[1]);
    asm volatile("s_nop 7" : "+v"(gram), "+v"(ssum));
    float q, t;
    asm volatile("v_cndmask_b32 %0, %2, %3, %6\n\tv_cndmask_b32 %1, %4, %5, %6\n\tv_cndmask_b32 %0, %0, %1, %7"
                 : "=&v"(q), "=&v"(t) : "v"(gram[0]), "v"(gram[1]), "v"(gram[2]), "v"(gram[3]), "s"(m1), "s"(m2));
    if (g == (tok >> 2)) st128(ctx, v4u{asu(ssum[0]), ctx.T, asu(q), ctx.T}, tok * 16, ssb);
    WAIT_VM(1);
    BAR();
    flag_store(ctx, WS_FX + ((ODD ? 32 : 0) + c) * 128);
    if (first) { pf32<1024, 88>(ctx, rwn, 0); part_step<P_BQ>(ctx, L_CS + (w * 6 + 3) * 64); }
  }
  asm volatile("" : "+a"(xacc[0]), "+a"(xacc[1]), "+a"(xacc[2]), "+a"(xacc[3]));
  STAMP(L * 8 + 7);
}

__global__ void __launch_bounds__(NT, 1) __attribute__((amdgpu_num_vgpr(88)))
fused_kernel(const unsigned char* __restrict__ x_, const unsigned char* __restrict__ wa_, const unsigned char* __restrict__ wphi_,
             const unsigned char* __restrict__ woa_, const unsigned char* __restrict__ wb_, const unsigned char* __restrict__ wob_,
             unsigned char* out_, unsigned char* ws_) {
  extern __shared__ __attribute__((aligned(16))) unsigned char smem[];
  asm volatile("" ::: "v255", "a255");
  Ctx ctx;
  ctx.ws = (GAS unsigned char*)ws_;
  ctx.rs = __builtin_amdgcn_make_buffer_rsrc((void*)ws_, 0, WS_END, 0x00020000);
  ctx.L = (lds_t*)smem;
  ctx.Lb = (unsigned)(size_t)ctx.L;
  const int b = blockIdx.x, idx = b >> 3;
  ctx.c = (b & 7) * 4 + (idx >> 3);
  ctx.m = idx & 7;
  ctx.w = __builtin_amdgcn_readfirstlane(threadIdx.x >> 6);
  ctx.budget = SPIN_CAP;
  fresh(ctx);
  const int c = ctx.c, m = ctx.m, w = ctx.w, lane = ctx.lane, g = ctx.g, tok = ctx.tok;
  STAMP(40); CSTAMP(0);
  const rsrc_t rsa = __builtin_amdgcn_make_buffer_rsrc((void*)wa_, 0, 512 * 3072, 0x00020000);
  const rsrc_t rsb = __builtin_amdgcn_make_buffer_rsrc((void*)wb_, 0, 512 * 3072, 0x00020000);
  const rsrc_t rwa = __builtin_amdgcn_make_buffer_rsrc((void*)woa_, 0, 512 * 1024, 0x00020000);
  const rsrc_t rwb = __builtin_amdgcn_make_buffer_rsrc((void*)wob_, 0, 512 * 1024, 0x00020000);
  {
    u64 pc; unsigned len;
    asm volatile("s_getpc_b64 %0\n.Lfk_here:\n\ts_mov_b32 %1, .Lfk_end-.Lfk_here" : "=s"(pc), "=s"(len));
    const rsrc_t rc = __builtin_amdgcn_make_buffer_rsrc((void*)pc, 0, len, 0x00020000);
    asm volatile("s_nop 4" ::: "memory");
    pf1<0>(lane * 16, rc, (idx * 4 + w) * 1024);
  }
  unsigned epoch, ph0, ph1, ph2, ph3;
  {
    const GAS unsigned char* pp = (const GAS unsigned char*)wphi_ + (4 * m + w) * 512;
    const unsigned pv = (unsigned)(4 * g * 16 + tok) * 2, zero = 0;
    asm volatile("s_nop 4\n\tglobal_load_dword %0, %5, %6 sc1\n\t"
                 "global_load_ushort %1, %7, %8\n\tglobal_load_ushort %2, %7, %8 offset:32\n\t"
                 "global_load_ushort %3, %7, %8 offset:64\n\tglobal_load_ushort %4, %7, %8 offset:96"
                 : "=&v"(epoch), "=&v"(ph0), "=&v"(ph1), "=&v"(ph2), "=&v"(ph3)
                 : "v"(zero), "s"(ctx.ws), "v"(pv), "s"(pp) : "memory");
  }
  const rsrc_t rsx = __builtin_amdgcn_make_buffer_rsrc((void*)x_, 0, 2048 * 1024, 0x00020000);
  first_pieces<0, 12>(ctx, rsx, rsa);
  {
    const int u = b * 4 + w;
    const unsigned l32 = lane & 31;
    if (u < 384) pfw(l32 * 128, rsa, u * 4096);
    else if (u < 768) pfw(l32 * 128, rsb, (u - 384) * 4096);
    else if (u < 896) pfw(l32 * 128, rwa, (u - 768) * 4096);
    else pfw(l32 * 128, rwb, (u - 896) * 4096);
  }
  first_pieces<12, PRE0>(ctx, rsx, rsa);
  asm volatile("s_waitcnt vmcnt(%5)" : "+v"(epoch), "+v"(ph0), "+v"(ph1), "+v"(ph2), "+v"(ph3) : "n"(PRE0 + 1) : "memory");
  ctx.T = __builtin_amdgcn_readfirstlane(epoch) + 1u;
  v2u wphi = v2u{ph0 | (ph1 << 16), ph2 | (ph3 << 16)};
  f4 xacc[4] = {{0.f, 0.f, 0.f, 0.f}, {0.f, 0.f, 0.f, 0.f}, {0.f, 0.f, 0.f, 0.f}, {0.f, 0.f, 0.f, 0.f}};
  asm volatile("" : "+a"(xacc[0]), "+a"(xacc[1]), "+a"(xacc[2]), "+a"(xacc[3]), "+a"(wphi));
  const rsrc_t out = __builtin_amdgcn_make_buffer_rsrc((void*)out_, 0, 2048 * 1024, 0x00020000);

#pragma nounroll
  for (int lp = 0; lp < 2; ++lp) {
    asm volatile("" : "+a"(xacc[0]), "+a"(xacc[1]), "+a"(xacc[2]), "+a"(xacc[3]), "+a"(wphi));
    layer_A(ctx, lp, wphi, rwa, rsa, rsb, rsx);
    out_proj<false>(ctx, lp, xacc, out, rsb, rwb, rsx);
    layer_B(ctx, 2 * lp + 1, rwb);
    out_proj<true>(ctx, lp, xacc, out, rsb, rwb, rsx);
  }
  asm volatile(".Lfk_end:");
  if (b == 0) { ctx.m = 0; flag_store(ctx, WS_EPOCH); }
  STAMP(41); CSTAMP(1);
  STAMP_FLUSH();
}

extern "C" void kernel_launch(void* const* d_in, const int* in_sizes, int n_in, void* d_out, int out_size, void* d_ws,
                              size_t ws_size, hipStream_t stream) {
  (void)in_sizes; (void)n_in; (void)out_size; (void)ws_size;
  (void)hipFuncSetAttribute((const void*)fused_kernel, hipFuncAttributeMaxDynamicSharedMemorySize, LDS_BYTES);
  hipLaunchKernelGGL(fused_kernel, dim3(NWG), dim3(NT), LDS_BYTES, stream,
                     (const unsigned char*)d_in[0], (const unsigned char*)d_in[1], (const unsigned char*)d_in[2],
                     (const unsigned char*)d_in[3], (const unsigned char*)d_in[5], (const unsigned char*)d_in[6],
                     (unsigned char*)d_out, (unsigned char*)d_ws);
}
```

```cpp
#include <hip/hip_runtime.h>
#include <hip/hip_bf16.h>
#include <type_traits>

#define GAS __attribute__((address_space(1)))
#define LAS __attribute__((address_space(3)))
#define DEV __device__ __forceinline__
typedef short s4 __attribute__((ext_vector_type(4)));
typedef float f4 __attribute__((ext_vector_type(4)));
typedef float f2 __attribute__((ext_vector_type(2)));
typedef __bf16 bf2 __attribute__((ext_vector_type(2)));
typedef unsigned v4u __attribute__((ext_vector_type(4)));
typedef unsigned v2u __attribute__((ext_vector_type(2)));
typedef unsigned long long u64;
typedef GAS unsigned gu32;
typedef LAS unsigned char lds_t;
typedef __amdgpu_buffer_rsrc_t rsrc_t;
#define RLX_AGENT __ATOMIC_RELAXED, __HIP_MEMORY_SCOPE_AGENT

namespace {
constexpr int NWG = 256, NT = 256;
constexpr int WS_EPOCH = 0;
constexpr int WS_FX = 4096;
constexpr int WS_FO = WS_FX + 2 * 32 * 128;
constexpr int WS_FW = WS_FO + 2 * 32 * 128;
constexpr int WS_KV2 = 1 << 16;
constexpr int WS_KVZ = 1 << 18;
constexpr int WS_STAT = 2 << 18;
constexpr int STAT_L = 32 * 8 * 64 * 16;
constexpr int WS_STAMP = 4 << 18;
constexpr int WS_X = 2 << 20;
constexpr int ACT_L = 2 << 20;
constexpr int WS_O = WS_X + 2 * ACT_L;
constexpr int WS_KV1 = WS_O + ACT_L;
constexpr int WS_FR = WS_KV1 + 8 * 64 * 32 * 256;
constexpr int WS_END = WS_FR + 8 * 4 * 48 * 1024;
constexpr int L_XT = 0;
constexpr int L_RQ = 81920;
constexpr int L_REC = 147456 + 256;
constexpr int L_NM = L_REC + 1280;
constexpr int L_RS = L_NM + 320;
constexpr int L_CS = L_RS + 320;
constexpr int L_END = L_CS + 4 * 2 * 3 * 64;
constexpr int L_IMG = 151552;
constexpr int LDS_BYTES = 160 * 1024;
static_assert(L_END <= L_IMG, "lds");
constexpr int SPIN_CAP = 1 << 18;
constexpr float LOG2E = 1.4426950408889634f;
#define RB 0
#define ST_DLY 4
#define UPOS 12
#define PF_U 1
#define PF_B 0
#define PF_KV 0
#define PF_LATE 1
#define PRE0 16
#define OPRE 8
#define QPOLL 6
#define S2POLL 0
}

DEV float asf(unsigned u) { return __builtin_bit_cast(float, u); }
DEV unsigned asu(float f) { return __builtin_bit_cast(unsigned, f); }
DEV unsigned pk2(float a, float b) { unsigned r; asm("v_cvt_pk_bf16_f32 %0, %1, %2" : "=v"(r) : "v"(a), "v"(b)); return r; }
DEV float xg_sum(float v) {
  auto r = __builtin_amdgcn_permlane16_swap(asu(v), asu(v), false, false);
  v = asf(r[0]) + asf(r[1]);
  auto q = __builtin_amdgcn_permlane32_swap(asu(v), asu(v), false, false);
  return asf(q[0]) + asf(q[1]);
}
DEV float row_sum(float v) {
  v += asf(__builtin_amdgcn_update_dpp(0u, asu(v), 0x128, 0xf, 0xf, false));
  v += asf(__builtin_amdgcn_update_dpp(0u, asu(v), 0x124, 0xf, 0xf, false));
  v += asf(__builtin_amdgcn_update_dpp(0u, asu(v), 0x122, 0xf, 0xf, false));
  v += asf(__builtin_amdgcn_update_dpp(0u, asu(v), 0x121, 0xf, 0xf, false));
  return v;
}
DEV float sum32(float v) {
  v = row_sum(v);
  auto r = __builtin_amdgcn_permlane16_swap(asu(v), asu(v), false, false);
  return asf(r[0]) + asf(r[1]);
}
#define BAR() asm volatile("s_waitcnt lgkmcnt(0)\n\ts_barrier" ::: "memory")
#define RAWBAR() do { asm volatile("" ::: "memory"); __builtin_amdgcn_s_barrier(); asm volatile("" ::: "memory"); } while (0)
#define DRAIN() asm volatile("s_waitcnt vmcnt(0)" ::: "memory")
#define WAIT_VM(n) asm volatile("s_waitcnt vmcnt(%0)" :: "n"(n) : "memory")
#define WAIT_LG(n) __builtin_amdgcn_s_waitcnt(0xC07F | ((n) << 8))
#define WAIT_VMV(n) __builtin_amdgcn_s_waitcnt(0x0F70 | ((n) & 15) | (((n) >> 4) << 14))
#define VNOP() __builtin_amdgcn_s_setprio(0)
#define SB() __builtin_amdgcn_sched_barrier(0)
#define NOP(n) asm volatile("s_nop %0" :: "n"(n))
#define SC1 16
#define ONE2 0x3f803f80u
template <int N> using IC = std::integral_constant<int, N>;

DEV unsigned mk1() { unsigned r; asm volatile("v_mov_b32 %0, 0x3f803f80" : "=v"(r)); return r; }
DEV unsigned mk0() { unsigned r; asm volatile("v_mov_b32 %0, 0" : "=v"(r)); return r; }
DEV int opq(int v) { asm volatile("" : "+v"(v)); return v; }
DEV v4u lds_ld128(lds_t* L, int off) { return *(LAS v4u*)(L + off); }
DEV f4 lds_ldf4(lds_t* L, int off) { return *(LAS f4*)(L + off); }
DEV float lds_ldf(lds_t* L, int off) { return *(LAS float*)(L + off); }
DEV v4u tr_frag(lds_t* L, int off) {
  s4 a = __builtin_amdgcn_ds_read_tr16_b64_v4i16((LAS s4*)(L + off));
  s4 b = __builtin_amdgcn_ds_read_tr16_b64_v4i16((LAS s4*)(L + off + 512));
  v2u ua = __builtin_bit_cast(v2u, a), ub = __builtin_bit_cast(v2u, b);
  return v4u{ua.x, ua.y, ub.x, ub.y};
}
template <int I, int N, class F> DEV void sfor(F&& f) { if constexpr (I < N) { f(IC<I>{}); sfor<I + 1, N>(f); } }

constexpr int P_AK = 0, P_AV = 16, P_BQ = 32, P_BK = 48, P_BV = 64;
#define MF32 "v_mfma_f32_16x16x32_bf16 "
#define MF16 "v_mfma_f32_16x16x16_bf16 "
#define MM_CASE(WC, XC)                                                                                                         \
  if constexpr (WA) {                                                                                                           \
    if constexpr (FIRST) asm volatile(MF32 "%0, " WC "[%c1:%c2], " XC "[%c3:%c4], 0" : "=v"(acc) : "n"(R), "n"(R + 3), "n"(X), "n"(X + 3)); \
    else asm volatile(MF32 "%0, " WC "[%c1:%c2], " XC "[%c3:%c4], %0" : "+v"(acc) : "n"(R), "n"(R + 3), "n"(X), "n"(X + 3));     \
  } else {                                                                                                                      \
    if constexpr (FIRST) asm volatile(MF32 "%0, " XC "[%c3:%c4], " WC "[%c1:%c2], 0" : "=v"(acc) : "n"(R), "n"(R + 3), "n"(X), "n"(X + 3)); \
    else asm volatile(MF32 "%0, " XC "[%c3:%c4], " WC "[%c1:%c2], %0" : "+v"(acc) : "n"(R), "n"(R + 3), "n"(X), "n"(X + 3));     \
  }
template <int F, int RS, bool WA, bool FIRST>
DEV void mm(f4& acc) {
  constexpr int R = 96 + 4 * (F < 40 ? F : F - 40), X = 88 + 4 * (RS & 1);
  if constexpr (F < 40) { if constexpr (RS < 2) { MM_CASE("v", "v") } else { MM_CASE("v", "a") } }
  else { if constexpr (RS < 2) { MM_CASE("a", "v") } else { MM_CASE("a", "a") } }
}
template <int RS, int OFF>
DEV void ring_rd(unsigned addr) {
  constexpr int X = 88 + 4 * (RS & 1);
  if constexpr (RS < 2) asm volatile("ds_read_b128 v[%c1:%c2], %0 offset:%c3" :: "v"(addr), "n"(X), "n"(X + 3), "n"(OFF) : "memory");
  else asm volatile("ds_read_b128 a[%c1:%c2], %0 offset:%c3" :: "v"(addr), "n"(X), "n"(X + 3), "n"(OFF) : "memory");
}
template <int F, bool FIRST>
DEV void wcs(f4& acc, v4u x) {
  constexpr int R = 96 + 4 * (F < 40 ? F : F - 40);
  if constexpr (F < 40) {
    if constexpr (FIRST) asm volatile(MF32 "%0, %1, v[%c2:%c3], 0" : "=&v"(acc) : "v"(x), "n"(R), "n"(R + 3));
    else asm volatile(MF32 "%0, %1, v[%c2:%c3], %0" : "+v"(acc) : "v"(x), "n"(R), "n"(R + 3));
  } else {
    if constexpr (FIRST) asm volatile(MF32 "%0, %1, a[%c2:%c3], 0" : "=&v"(acc) : "v"(x), "n"(R), "n"(R + 3));
    else asm volatile(MF32 "%0, %1, a[%c2:%c3], %0" : "+v"(acc) : "v"(x), "n"(R), "n"(R + 3));
  }
}
template <int F, bool FIRST>
DEV void wxa(f4& acc, v4u x) {
  constexpr int R = 96 + 4 * (F < 40 ? F : F - 40);
  static_assert(F < 40, "");
  if constexpr (FIRST) asm volatile(MF32 "%0, v[%c2:%c3], %1, 0" : "=v"(acc) : "a"(x), "n"(R), "n"(R + 3));
  else asm volatile(MF32 "%0, v[%c2:%c3], %1, %0" : "+v"(acc) : "a"(x), "n"(R), "n"(R + 3));
}
template <int F, bool FIRST>
DEV void wxb(f4& acc, v4u x) {
  constexpr int R = 96 + 4 * (F < 40 ? F : F - 40);
  static_assert(F < 40, "");
  if constexpr (FIRST) asm volatile(MF32 "%0, %1, v[%c2:%c3], 0" : "=v"(acc) : "a"(x), "n"(R), "n"(R + 3));
  else asm volatile(MF32 "%0, %1, v[%c2:%c3], %0" : "+v"(acc) : "a"(x), "n"(R), "n"(R + 3));
}
template <bool FIRST>
DEV void mf_aaz(f4& acc, v4u a, v4u b) {
  if constexpr (FIRST) asm volatile(MF32 "%0, %1, %2, 0" : "=v"(acc) : "a"(a), "a"(b));
  else asm volatile(MF32 "%0, %1, %2, %0" : "+v"(acc) : "a"(a), "a"(b));
}
template <int RS, bool FIRST>
DEV void mmq(f4& acc, v4u a) {
  constexpr int X = 88 + 4 * (RS & 1);
  if constexpr (RS < 2) {
    if constexpr (FIRST) asm volatile(MF32 "%0, %1, v[%c2:%c3], 0" : "=v"(acc) : "a"(a), "n"(X), "n"(X + 3));
    else asm volatile(MF32 "%0, %1, v[%c2:%c3], %0" : "+v"(acc) : "a"(a), "n"(X), "n"(X + 3));
  } else {
    if constexpr (FIRST) asm volatile(MF32 "%0, %1, a[%c2:%c3], 0" : "=v"(acc) : "a"(a), "n"(X), "n"(X + 3));
    else asm volatile(MF32 "%0, %1, a[%c2:%c3], %0" : "+v"(acc) : "a"(a), "n"(X), "n"(X + 3));
  }
}
template <bool FIRST>
DEV void mf_av(f4& acc, v4u a, v4u b) {
  if constexpr (FIRST) asm volatile(MF32 "%0, %1, %2, 0" : "=&v"(acc) : "a"(a), "v"(b));
  else asm volatile(MF32 "%0, %1, %2, %0" : "+v"(acc) : "a"(a), "v"(b));
}
DEV void mf_va(f4& acc, v4u a, v4u b) { asm volatile(MF32 "%0, %1, %2, %0" : "+v"(acc) : "v"(a), "a"(b)); }
DEV void mf_aa(f4& acc, v4u a, v4u b) { asm volatile(MF32 "%0, %1, %2, %0" : "+v"(acc) : "a"(a), "a"(b)); }
DEV void mf32z(f4& d, v4u a, v4u b) { asm volatile(MF32 "%0, %1, %2, 0" : "=&v"(d) : "v"(a), "v"(b)); VNOP(); }
DEV void mf32c(f4& d, v4u a, v4u b) { asm volatile(MF32 "%0, %1, %2, %0" : "+v"(d) : "v"(a), "v"(b)); VNOP(); }
DEV void mf16z(f4& d, v2u a, v2u b) { asm volatile(MF16 "%0, %1, %2, 0" : "=&v"(d) : "v"(a), "v"(b)); VNOP(); }
DEV void mf16c(f4& d, v2u a, v2u b) { asm volatile(MF16 "%0, %1, %2, %0" : "+v"(d) : "v"(a), "v"(b)); VNOP(); }
DEV void mf16z_a(f4& d, v2u a, v2u b) { asm volatile(MF16 "%0, %1, %2, 0" : "=&v"(d) : "a"(a), "v"(b)); VNOP(); }
DEV void mf16c_a(f4& d, v2u a, v2u b) { asm volatile(MF16 "%0, %1, %2, %0" : "+v"(d) : "a"(a), "v"(b)); VNOP(); }
DEV void mf16z_b(f4& d, v2u a, v2u b) { asm volatile(MF16 "%0, %1, %2, 0" : "=&v"(d) : "v"(a), "a"(b)); VNOP(); }
template <int F, int OFF>
DEV void wtr(unsigned addr) {
  constexpr int R = 96 + 4 * (F < 40 ? F : F - 40);
  if constexpr (F < 40)
    asm volatile("ds_read_b64_tr_b16 v[%c1:%c2], %0 offset:%c5\n\tds_read_b64_tr_b16 v[%c3:%c4], %0 offset:%c6"
                 :: "v"(addr), "n"(R), "n"(R + 1), "n"(R + 2), "n"(R + 3), "n"(OFF), "n"(OFF + 512) : "memory");
  else
    asm volatile("ds_read_b64_tr_b16 a[%c1:%c2], %0 offset:%c5\n\tds_read_b64_tr_b16 a[%c3:%c4], %0 offset:%c6"
                 :: "v"(addr), "n"(R), "n"(R + 1), "n"(R + 2), "n"(R + 3), "n"(OFF), "n"(OFF + 512) : "memory");
}
#define A_FMA(d, a, b, c) asm volatile("v_fma_f32 %0, %1, %2, %3" : "=v"(d) : "v"(a), "v"(b), "v"(c))
#define A_FMAN(d, a, b, c) asm volatile("v_fma_f32 %0, %1, -%2, %3" : "=v"(d) : "v"(a), "v"(b), "v"(c))
#define A_MUL(d, a, b) asm volatile("v_mul_f32 %0, %1, %2" : "=v"(d) : "v"(a), "v"(b))
#define A_MULS(d, k, b) asm volatile("v_mul_f32 %0, %1, %2" : "=v"(d) : "s"(k), "v"(b))
#define A_MAX(d, a, b) asm volatile("v_max_f32 %0, %1, %2" : "=v"(d) : "v"(a), "v"(b))
#define A_SUB(d, a, b) asm volatile("v_sub_f32 %0, %1, %2" : "=v"(d) : "v"(a), "v"(b))
#define A_CVT(d, a, b) asm volatile("v_cvt_pk_bf16_f32 %0, %1, %2" : "=v"(d) : "v"(a), "v"(b))
#define A_EXP(d, a) asm volatile("v_exp_f32 %0, %1" : "=v"(d) : "v"(a))
#define A_RCP(d, x4) asm volatile("v_rcp_f32 %0, %1" : "=&v"(d) : "v"(x4[0]), "v"(x4))
#define A_SEL(d, f, t, m) asm volatile("v_cndmask_b32 %0, %1, %2, %3" : "=v"(d) : "v"(f), "v"(t), "s"(m))
#define A_PERM(d, x, sel) asm volatile("v_perm_b32 %0, %1, %1, %2" : "=v"(d) : "v"(x), "v"(sel))
#define A_SW16(a, b) asm volatile("v_permlane16_swap_b32 %0, %1" : "+v"(a), "+v"(b))
#define A_SW32(a, b) asm volatile("v_permlane32_swap_b32 %0, %1" : "+v"(a), "+v"(b))
#define A_LD64(d, addr, off) asm volatile("ds_read_b64 %0, %1 offset:%2" : "=v"(d) : "v"(addr), "n"(off) : "memory")
#define A_LD128A(d, addr, off) asm volatile("ds_read_b128 %0, %1 offset:%2" : "=a"(d) : "v"(addr), "n"(off) : "memory")
#define A_LD128(d, addr, off) asm volatile("ds_read_b128 %0, %1 offset:%2" : "=v"(d) : "v"(addr), "n"(off) : "memory")

#define STAMP(i) do {} while (0)
#define CSTAMP(i) do {} while (0)
#define STAMP_FLUSH() do {} while (0)
#define TS(k) do {} while (0)
#define TS_DECL
#define TS_FLUSH(base, n) do {} while (0)

struct Ctx {
  GAS unsigned char* ws;
  rsrc_t rs;
  lds_t* L;
  unsigned Lb, Lw;
  int c, m, w;
  int lane, tok, g;
  unsigned T;
  int budget;
};
DEV void fresh(Ctx& ctx) {
  int l;
  asm volatile("v_mbcnt_lo_u32_b32 %0, -1, 0\n\tv_mbcnt_hi_u32_b32 %0, -1, %0" : "=v"(l));
  ctx.lane = l; ctx.tok = l & 15; ctx.g = l >> 4;
  asm volatile("" : "+s"(ctx.c), "+s"(ctx.m), "+s"(ctx.w), "+s"(ctx.Lb));
  ctx.Lw = ctx.Lb + 4096 * ctx.w;
}
DEV v4u ld128(Ctx& ctx, int voff, int soff) { return __builtin_amdgcn_raw_buffer_load_b128(ctx.rs, voff, soff, SC1); }
DEV void st128(Ctx& ctx, v4u v, int voff, int soff) { __builtin_amdgcn_raw_buffer_store_b128(v, ctx.rs, voff, soff, SC1); }
DEV void st64(Ctx& ctx, v2u v, int voff, int soff) { __builtin_amdgcn_raw_buffer_store_b64(v, ctx.rs, voff, soff, SC1); }

DEV unsigned bld32(Ctx& ctx, int voff, int soff) {
  unsigned r;
  asm volatile("s_nop 4\n\tbuffer_load_dword %0, %1, %2, %3 offen sc1\n\ts_waitcnt vmcnt(0)" : "=&v"(r) : "v"(voff), "s"(ctx.rs), "s"(soff) : "memory");
  return r;
}
template <int O1, int O2, int O3>
DEV void bld128x4(Ctx& ctx, v4u (&d)[4], int voff, int soff) {
  asm volatile("s_nop 4\n\tbuffer_load_dwordx4 %0, %4, %5, %6 offen sc1\n\tbuffer_load_dwordx4 %1, %4, %5, %6 offen offset:%7 sc1\n\t"
               "buffer_load_dwordx4 %2, %4, %5, %6 offen offset:%8 sc1\n\tbuffer_load_dwordx4 %3, %4, %5, %6 offen offset:%9 sc1\n\ts_waitcnt vmcnt(0)"
               : "=&v"(d[0]), "=&v"(d[1]), "=&v"(d[2]), "=&v"(d[3]) : "v"(voff), "s"(ctx.rs), "s"(soff), "n"(O1), "n"(O2), "n"(O3) : "memory");
}
DEV void wait_flags(Ctx& ctx, int off0, int off1, bool two) {
  if (ctx.w == 0) {
    const int l = ctx.lane & (two ? 15 : 7);
    const int vo = (l < 8 ? off0 : off1 - 128) + l * 16;
    for (;;) {
      const unsigned v = bld32(ctx, vo, 0);
      if (__all(v == ctx.T) || --ctx.budget < 0) break;
    }
    __builtin_amdgcn_fence(__ATOMIC_ACQUIRE, "wavefront");
  }
  BAR();
}
DEV void flag_store(Ctx& ctx, int off) {
  if (ctx.w == 0) {
    unsigned tv, zv;
    asm volatile("v_mov_b32 %0, %2\n\tv_mov_b32 %1, 0" : "=v"(tv), "=v"(zv) : "s"(ctx.T));
    __builtin_amdgcn_raw_buffer_store_b32(tv, ctx.rs, zv, off + ctx.m * 16, SC1);
  }
}

DEV int x0_lane(int tok, int g) { return tok * 64 + ((g ^ ((4 - (tok >> 2)) & 3)) << 4); }
DEV int tr_base(int lane, int j) {
  const int g = lane >> 4, q = (lane & 15) >> 2, p = lane & 3;
  const int s = ((q >> 1) & 1) | ((g & 1) << 1);
  return (8 * g + q) * 128 + ((j ^ s) << 5) + p * 8;
}
template <int F>
DEV void raw_ld1(unsigned voff, rsrc_t rs, int soff) {
  constexpr int R = 96 + 4 * (F < 40 ? F : F - 40);
  if constexpr (F < 40) asm volatile("buffer_load_dwordx4 v[%c3:%c4], %0, %1, %2 offen" :: "v"(voff), "s"(rs), "s"(soff), "n"(R), "n"(R + 3) : "memory");
  else asm volatile("buffer_load_dwordx4 a[%c3:%c4], %0, %1, %2 offen" :: "v"(voff), "s"(rs), "s"(soff), "n"(R), "n"(R + 3) : "memory");
}
template <int P, int I0, int I1>
DEV void raw_load(Ctx& ctx, rsrc_t rs, int colbyte) {
  const unsigned vo = (unsigned)((ctx.lane >> 3) * 3072 + (ctx.lane & 7) * 16);
  const int sb = colbyte + ctx.m * 128 + 32 * ctx.w * 3072;
  sfor<I0, I1>([&](auto i) { raw_ld1<P + i>(vo, rs, sb + (128 * (i >> 2) + 8 * (i & 3)) * 3072); });
}
template <int F, int OFF>
DEV void raw_st1(unsigned addr) {
  constexpr int R = 96 + 4 * (F < 40 ? F : F - 40);
  if constexpr (F < 40) asm volatile("ds_write_b128 %0, v[%c1:%c2] offset:%c3" :: "v"(addr), "n"(R), "n"(R + 3), "n"(OFF) : "memory");
  else asm volatile("ds_write_b128 %0, a[%c1:%c2] offset:%c3" :: "v"(addr), "n"(R), "n"(R + 3), "n"(OFF) : "memory");
}
template <int P, int I0, int I1>
DEV void raw_store(Ctx& ctx) {
  const int lane = ctx.lane, ch = lane & 7, rb1 = (lane >> 4) & 1;
  const unsigned base = ctx.Lb + L_RQ + (32 * ctx.w + (lane >> 3)) * 128 + (ch & 1) * 16;
  const unsigned p0 = base + (((ch >> 1) ^ rb1) << 5), p1 = base + (((ch >> 1) ^ (rb1 | 2)) << 5);
  sfor<I0, I1>([&](auto i) { raw_st1<P + i, (i >> 2) * 16384 + (i & 3) * 1024>((i & 1) ? p1 : p0); });
}
template <int P, int K0, int K1>
DEV void part_tr(Ctx& ctx) {
  const unsigned tb = ctx.Lb + L_RQ + tr_base(ctx.lane, ctx.w);
  sfor<K0, K1>([&](auto ks) { wtr<P + ks, ks * 4096>(tb); });
}
template <int P>
DEV void part_cs(Ctx& ctx, int csoff) {
  v4u ones = {mk1(), mk1(), mk1(), mk1()};
  asm volatile("s_waitcnt lgkmcnt(0)\n\ts_nop 1" : "+v"(ones) :: "memory");
  f4 c0, c1;
  sfor<0, 8>([&](auto k) { wcs<P + 2 * k, k == 0>(c0, ones); wcs<P + 2 * k + 1, k == 0>(c1, ones); });
  asm volatile("s_nop 7" : "+v"(c0), "+v"(c1));
  if (ctx.g == 0) *(LAS float*)(ctx.L + csoff + ctx.tok * 4) = c0[0] + c1[0];
}

template <int P>
DEV void part_step(Ctx& ctx, int csoff) {
  raw_store<P, 0, 8>(ctx);
  BAR();
  part_tr<P, 0, 8>(ctx);
  raw_store<P, 8, 16>(ctx);
  BAR();
  part_tr<P, 8, 16>(ctx);
  part_cs<P>(ctx, csoff);
}

template <int P, int PN>
DEV void part_step_ld(Ctx& ctx, int csoff, rsrc_t rs, int colbyte) {
  raw_load<PN, 0, 4>(ctx, rs, colbyte);
  raw_store<P, 0, 8>(ctx);
  raw_load<PN, 4, 8>(ctx, rs, colbyte);
  BAR();
  part_tr<P, 0, 8>(ctx);
  raw_load<PN, 8, 12>(ctx, rs, colbyte);
  raw_store<P, 8, 16>(ctx);
  raw_load<PN, 12, 16>(ctx, rs, colbyte);
  BAR();
  part_tr<P, 8, 16>(ctx);
  part_cs<P>(ctx, csoff);
}
template <int IM>
DEV void fr_ld(unsigned lv, rsrc_t rs, int so) {
  constexpr int F = P_BQ + IM, R = 96 + 4 * (F < 40 ? F : F - 40);
  if constexpr (F < 40) asm volatile("buffer_load_dwordx4 v[%c3:%c4], %0, %1, %2 offen offset:%5 sc1" :: "v"(lv), "s"(rs), "s"(so), "n"(R), "n"(R + 3), "n"((IM & 3) * 1024) : "memory");
  else asm volatile("buffer_load_dwordx4 a[%c3:%c4], %0, %1, %2 offen offset:%5 sc1" :: "v"(lv), "s"(rs), "s"(so), "n"(R), "n"(R + 3), "n"((IM & 3) * 1024) : "memory");
}
template <int I0, int I1>
DEV void fr_load(Ctx& ctx) {
  const int fb = WS_FR + (ctx.m * 4 + ctx.w) * 49152;
  const unsigned lv = ctx.lane * 16;
  sfor<I0, I1>([&](auto i) { fr_ld<i>(lv, ctx.rs, fb + (i & ~3) * 1024); });
}
template <int OFF>
DEV void pf1(unsigned lv, rsrc_t rs, int so) {
  asm volatile("buffer_load_dwordx4 v[224:227], %0, %1, %2 offen offset:%3" :: "v"(lv), "s"(rs), "s"(so), "n"(OFF) : "memory");
}

template <int SINK = 224>
DEV void pfw(unsigned vo, rsrc_t rs, int so) {
  so = __builtin_amdgcn_readfirstlane(so);
  asm volatile("s_nop 4\n\t" "buffer_load_dword v%c3, %0, %1, %2 offen" :: "v"(vo), "s"(rs), "s"(so), "n"(SINK) : "memory");
}
DEV void readback(Ctx& ctx, int vo, int so) {
  asm volatile("buffer_load_dword v88, %0, %1, %2 offen sc1" :: "v"(vo), "s"(ctx.rs), "s"(so) : "memory");
}
template <int ROWB, int SINK = 224>
DEV void pf32(Ctx& ctx, rsrc_t rs, int col) {
  const int row = 16 * (int)(blockIdx.x >> 3) + 4 * ctx.w;
  pfw<SINK>((unsigned)(((ctx.lane >> 3) & 3) * ROWB + (ctx.lane & 7) * 128), rs, col + row * ROWB);
}
template <int ROWB0, int ROWB1>
DEV void pf_pair(Ctx& ctx, rsrc_t rs0, int col0, rsrc_t rs1, int col1) {
  const int row = 16 * (int)(blockIdx.x >> 3) + 8 * (ctx.w & 1);
  if (ctx.w < 2) pfw((unsigned)((ctx.lane >> 3) * ROWB0 + (ctx.lane & 7) * 128), rs0, col0 + row * ROWB0);
  else pfw((unsigned)((ctx.lane >> 3) * ROWB1 + (ctx.lane & 7) * 128), rs1, col1 + row * ROWB1);
}

struct ImgDma { unsigned v[4]; int sb; };
template <int ROWB>
DEV ImgDma img_prep(Ctx& ctx) {
  const int lane = ctx.lane, cp = lane & 7, rb1 = (lane >> 4) & 1;
  const unsigned vb = (unsigned)((lane >> 3) * ROWB + (cp & 1) * 16);
  const unsigned v0 = vb + (((cp >> 1) ^ rb1) << 5), v1 = vb + (((cp >> 1) ^ (rb1 | 2)) << 5);
  ImgDma d;
  d.v[0] = v0; d.v[1] = v1 + (8 * ROWB - 1024); d.v[2] = v0 + 2 * (8 * ROWB - 1024); d.v[3] = v1 + 3 * (8 * ROWB - 1024);
  d.sb = ctx.m * 128 + 32 * ctx.w * ROWB;
  return d;
}
template <int LOFF> DEV void dma_m0(unsigned lbase) { asm volatile("s_add_u32 m0, %0, %1" :: "s"(lbase), "n"(LOFF) : "scc"); }
template <int SOFF> DEV void dma_so(int& t, int sbase) { asm volatile("s_add_u32 %0, %1, %2" : "=s"(t) : "s"(sbase), "n"(SOFF) : "scc"); }
template <int I> DEV void dma_go(unsigned voff, rsrc_t rs, int so) {
  asm volatile("buffer_load_dwordx4 %0, %1, %2 offen offset:%3 lds" :: "v"(voff), "s"(rs), "s"(so), "n"(I * 1024) : "memory");
}
template <int I> DEV void dma_go_sc1(unsigned voff, rsrc_t rs, int so) {
  asm volatile("buffer_load_dwordx4 %0, %1, %2 offen offset:%3 sc1 lds" :: "v"(voff), "s"(rs), "s"(so), "n"(I * 1024) : "memory");
}
template <int SOFF, int LOFF>
DEV void dma1(unsigned voff, rsrc_t rs, int sbase, unsigned lbase) {
  int t;
  asm volatile("s_add_u32 m0, %4, %6\n\ts_add_u32 %0, %3, %5\n\tbuffer_load_dwordx4 %1, %2, %0 offen lds"
               : "=&s"(t) : "v"(voff), "s"(rs), "s"(sbase), "s"(lbase), "n"(SOFF), "n"(LOFF) : "memory", "scc");
}
template <int ROWB, int COLB, int LDSOFF, int J>
DEV void img_chunk(Ctx& ctx, const ImgDma& d, rsrc_t rs) {
  int so;
  dma_m0<LDSOFF + J * 16384>(ctx.Lw); dma_so<COLB + 128 * J * ROWB>(so, d.sb);
  sfor<0, 4>([&](auto i) { dma_go<i>(d.v[i], rs, so); });
}
template <int TT>
DEV void x_chunk(Ctx& ctx, unsigned lv, int xs) {
  int so;
  dma_m0<L_XT + (TT + 1) * 16384>(ctx.Lw); dma_so<(TT + 1) * 16384>(so, xs);
  sfor<0, 4>([&](auto i) { dma_go_sc1<i>(lv, ctx.rs, so); });
}

struct ST {
  v4u d[4];
  float s, q, t, bias;
  unsigned bad, sv, wrec, wnm;
  int so;
};
template <bool HALO>
DEV void st_prep(Ctx& ctx, ST& st, int L) {
  (void)L;
  const int c = ctx.c, w = ctx.w, r32 = ctx.lane & 31, hf = ctx.lane >> 5;
  const bool halo = HALO && w == 3;
  const int cc = (halo && c > 0) ? c - 1 : c;
  const int row0 = halo ? (c > 0 ? 48 : 0) : 32 * (w & 1);
  const int r = halo ? (r32 & 15) : r32;
  st.so = WS_STAT + (HALO ? 0 : STAT_L) + (cc * 8 * 64 + row0) * 16;
  st.sv = hf * 4096 + r * 16;
  const int lrow = halo ? r : 16 + row0 + r;
  st.wrec = ctx.Lb + L_REC + lrow * 16;
  st.wnm = ctx.Lb + L_NM + lrow * 4;
  st.bias = (halo && c == 0) ? -__builtin_inff() : 0.f;
  asm volatile("" : "+v"(st.sv), "+v"(st.wrec), "+v"(st.wnm), "+v"(st.bias));
}
DEV void st_issue(Ctx& ctx, ST& st) {
  asm volatile("buffer_load_dwordx4 %0, %4, %5, %6 offen sc1\n\t"
               "buffer_load_dwordx4 %1, %4, %5, %6 offen offset:1024 sc1\n\t"
               "buffer_load_dwordx4 %2, %4, %5, %6 offen offset:2048 sc1\n\t"
               "buffer_load_dwordx4 %3, %4, %5, %6 offen offset:3072 sc1"
               : "=&v"(st.d[0]), "=&v"(st.d[1]), "=&v"(st.d[2]), "=&v"(st.d[3]) : "v"(st.sv), "s"(ctx.rs), "s"(st.so) : "memory");
}
DEV void st_retry(Ctx& ctx, ST& st) {
  for (;;) {
    unsigned bad = 0;
    bld128x4<1024, 2048, 3072>(ctx, st.d, st.sv, st.so);
#pragma unroll
    for (int i = 0; i < 4; ++i) bad |= (st.d[i].y ^ ctx.T) | (st.d[i].w ^ ctx.T);
    if (__all(bad == 0) || --ctx.budget < 0) break;
  }
}
DEV void st_check(Ctx& ctx, ST& st) {
  asm volatile("" : "+v"(st.d[0]), "+v"(st.d[1]), "+v"(st.d[2]), "+v"(st.d[3]));
  const unsigned T = ctx.T;
  const bool ok = (st.d[0].y == T) & (st.d[0].w == T) & (st.d[1].y == T) & (st.d[1].w == T) &
                  (st.d[2].y == T) & (st.d[2].w == T) & (st.d[3].y == T) & (st.d[3].w == T);
  if (__builtin_expect(!__all(ok), 0)) st_retry(ctx, st);
}
template <int K>
DEV void st_step(ST& st) {
  if constexpr (K == 0) asm volatile("v_add_f32 %0, %2, %3\n\tv_add_f32 %1, %4, %5" : "=&v"(st.s), "=&v"(st.q) : "v"(st.d[0].x), "v"(st.d[1].x), "v"(st.d[0].z), "v"(st.d[1].z));
  if constexpr (K == 1) asm volatile("v_add_f32 %0, %0, %2\n\tv_add_f32 %1, %1, %3" : "+v"(st.s), "+v"(st.q) : "v"(st.d[2].x), "v"(st.d[2].z));
  if constexpr (K == 2) asm volatile("v_add_f32 %0, %0, %2\n\tv_add_f32 %1, %1, %3" : "+v"(st.s), "+v"(st.q) : "v"(st.d[3].x), "v"(st.d[3].z));
  if constexpr (K == 3) A_SW32(st.s, st.q);
  if constexpr (K == 4) { float x = st.s, y = st.q; asm volatile("v_add_f32 %0, %2, %3\n\tv_add_f32 %1, %2, %3" : "=&v"(st.s), "=&v"(st.q) : "v"(x), "v"(y)); }
  if constexpr (K == 5) A_SW32(st.s, st.q);
  if constexpr (K == 6) asm volatile("v_mul_f32 %0, %2, %0\n\tv_mul_f32 %1, %3, %1" : "+v"(st.s), "+v"(st.q) : "s"(-1.0f / 512.0f), "s"(1.0f / 512.0f));
  if constexpr (K == 7) asm volatile("v_fma_f32 %0, -%1, %1, %0\n\tv_max_f32 %0, 0, %0" : "+v"(st.q) : "v"(st.s));
  if constexpr (K == 8) asm volatile("v_add_f32 %0, %1, %0" : "+v"(st.q) : "s"(1e-5f));
  if constexpr (K == 9) asm volatile("v_rsq_f32 %0, %0" : "+v"(st.q));
  if constexpr (K == 10) asm volatile("v_mov_b32 %0, %1" : "=v"(st.t) : "v"(st.q));
  if constexpr (K == 11) asm volatile("ds_write_b128 %0, %1" :: "v"(st.wrec), "v"(f4{st.s, st.q, st.t, st.bias}) : "memory");
  if constexpr (K == 12) asm volatile("ds_write2_b32 %0, %1, %2 offset1:80" :: "v"(st.wnm), "v"(st.s), "v"(st.q) : "memory");
}

template <bool HALO>
DEV void st_begin(Ctx& ctx, ST& st) {
  st_prep<HALO>(ctx, st, 0);
  if (ctx.w != 0) st_issue(ctx, st);
}
template <bool HALO>
DEV void gather_start(Ctx& ctx, ST& st, int L, unsigned lv, int xs) {
  const int c = ctx.c;
  const int cprev = c > 0 ? c - 1 : 0;
  constexpr int T0 = HALO ? -1 : 0;
  const int xs0 = (HALO && c == 0) ? xs + 16384 : xs;
  if (ctx.w != 0) {
    DRAIN();
    st_check(ctx, st);
    st_step<0>(st); st_step<1>(st); st_step<2>(st); NOP(1); st_step<3>(st); st_step<4>(st); NOP(1); st_step<5>(st);
    st_step<6>(st); st_step<7>(st); st_step<8>(st); st_step<9>(st); NOP(0); st_step<10>(st); st_step<12>(st); st_step<11>(st);
  }
  wait_flags(ctx, WS_FX + ((HALO ? 0 : 32) + c) * 128, WS_FX + ((HALO ? 0 : 32) + cprev) * 128, HALO);
  STAMP(L * 8 + 0);
  x_chunk<T0>(ctx, lv, xs0);
  x_chunk<T0 + 1>(ctx, lv, xs);
  WAIT_VM(4);
  RAWBAR();
  STAMP(L * 8 + 1);
}

template <int OFF> DEV void ring_prime(unsigned ab) { ring_rd<0, OFF>(ab); ring_rd<1, OFF + 1024>(ab); ring_rd<2, OFF + 2048>(ab); WAIT_LG(2); }
template <int PQ, int PK, int PV, int OFF, bool CINIT, class XL, class SH, class GP>
DEV void gemm_tile(unsigned ab, f4& aq, f4& ak, f4& av, XL&& xl, SH&& sh, GP&& gp) {
  sfor<0, 16>([&](auto ks) {
    constexpr bool F = ks == 0 && !CINIT;
    if constexpr (PQ >= 0) {
      mm<(PQ >= 0 ? PQ : 0) + ks, ks & 3, true, F>(aq);
      if (!sh(IC<4 * ks>{})) VNOP();
      ring_rd<(ks + 3) & 3, OFF + (ks + 3) * 1024>(ab);
      gp(IC<4 * ks>{});
      mm<PK + ks, ks & 3, true, F>(ak);
      if (!sh(IC<4 * ks + 1>{})) VNOP();
      gp(IC<4 * ks + 1>{});
    } else {
      mm<PK + ks, ks & 3, true, F>(ak);
      if (!sh(IC<4 * ks>{})) VNOP();
      ring_rd<(ks + 3) & 3, OFF + (ks + 3) * 1024>(ab);
      gp(IC<4 * ks>{});
    }
    mm<PV + ks, ks & 3, false, F>(av);
    WAIT_LG(2 + decltype(xl(ks))::value);
    gp(IC<4 * ks + 2>{});
    SB();
  });
}
#define GA(k) (4 * (k))
#define GB(k) (4 * (k) + 1)
#define GC(k) (4 * (k) + 2)

struct BE {
  f4 aq, ak, av;
  f2 st, ka, kb2; f4 nm4;
  f2 nst, nka, nkb; f4 nnm4;
  f4 csq, csk; float csv;
  float q[4], k[4], v[4];
  unsigned qb[2], kb[2], vb[2], vb2[2], kprev[2], vprev[2], P[4], pk0, pk, ob[2];
  f4 sp, sc, num, den;
  float a, b, t0, t1, m, m2, ft, fa, fb, pa, pb;
  v2u ones;
  unsigned sPA, sPB, sCA, sCB;
  u64 modd, mA, mB;
  unsigned aT, aF, aKa, aKb;
  int ovo;
};

DEV void layer_B(Ctx& ctx, int L, rsrc_t rwo) {
  fresh(ctx);
  ST st;
  st_begin<true>(ctx, st);
  const int c = ctx.c, w = ctx.w, g = ctx.g, tok = ctx.tok;
  const int csb = L_CS + (w * 6 + 3) * 64;
  const unsigned lv = ctx.lane * 16;
  BE e;
  TS_DECL;
  unsigned ab0, ab1;
  ImgDma wd = img_prep<1024>(ctx);
  {
    const bool odd = tok & 1;
    const int ja = 4 * g + (odd ? 1 : 0), jb = ja + 2;
    const bool curA = ja <= tok, curB = jb <= tok;
    const unsigned selA = odd ? 0x01000c0cu : 0x0c0c0100u, selB = odd ? 0x03020c0cu : 0x0c0c0302u, selZ = 0x0c0c0c0cu;
    e.sPA = curA ? selZ : selA; e.sPB = curB ? selZ : selB; e.sCA = curA ? selA : selZ; e.sCB = curB ? selB : selZ;
    e.modd = __ballot(odd); e.mA = __ballot(curA); e.mB = __ballot(curB);
    e.aT = ctx.Lb + L_REC + tok * 16;
    e.aF = ctx.Lb + L_NM + g * 16;
    e.aKa = ctx.Lb + L_REC + (ja - (curA ? 0 : 16)) * 16 + 8;
    e.aKb = ctx.Lb + L_REC + (jb - (curB ? 0 : 16)) * 16 + 8;
    e.csq = lds_ldf4(ctx.L, csb + g * 16); e.csk = lds_ldf4(ctx.L, csb + 64 + g * 16); e.csv = lds_ldf(ctx.L, csb + 128 + tok * 4);
    e.ones = v2u{mk1(), mk0()};
    e.ovo = ((g >> 1) * 16 + tok) * 16 + (g & 1) * 8;
    ab0 = ctx.Lb + L_XT + lv; ab1 = ab0 + 49152;
    asm volatile("" : "+v"(e.sPA), "+v"(e.sPB), "+v"(e.sCA), "+v"(e.sCB), "+v"(e.csq), "+v"(e.csk), "+v"(e.csv), "+a"(e.ones), "+v"(e.ovo),
                      "+v"(e.aT), "+v"(e.aF), "+v"(e.aKa), "+v"(e.aKb), "+v"(ab0), "+v"(ab1), "+v"(wd.v[0]), "+v"(wd.v[1]), "+v"(wd.v[2]), "+v"(wd.v[3]));
  }
  const int xs = WS_X + c * 65536 + w * 4096 - 16384;
  const int osb = WS_O + (4 * c * 16 + 2 * ctx.m + (w >> 1)) * 1024 + (w & 1) * 512;
  gather_start<true>(ctx, st, L, lv, xs);
  ring_prime<0>(ab0);
  f4 xq, xk, xv, yq, yk, yv;
  int so, oso;
  auto stat_rd = [&](auto S, auto h) {
    (void)e;
    if constexpr (h == GC(6)) { A_LD64(e.nst, e.aT, S * 256); A_LD128(e.nnm4, e.aF, S * 64); }
    if constexpr (h == GA(7)) A_LD64(e.nka, e.aKa, S * 256);
    if constexpr (h == GA(8)) A_LD64(e.nkb, e.aKb, S * 256);
  };
  auto xl = [](auto ks) { return IC<(ks == 7 ? 3 : ks == 8 ? 4 : ks == 9 ? 2 : ks == 10 ? 1 : 0)>{}; };
  auto next = [&](f4& q, f4& k, f4& v) { e.aq = q; e.ak = k; e.av = v; e.st = e.nst; e.ka = e.nka; e.kb2 = e.nkb; e.nm4 = e.nnm4; };
  auto epi = [&](auto h) {
    (void)e;
    if constexpr (h == GA(0)) A_FMA(e.q[0], e.st[0], e.csq[0], e.aq[0]);
    if constexpr (h == GB(0)) { A_FMA(e.q[1], e.st[0], e.csq[1], e.aq[1]); A_FMA(e.q[2], e.st[0], e.csq[2], e.aq[2]); }
    if constexpr (h == GC(0)) { A_FMA(e.q[3], e.st[0], e.csq[3], e.aq[3]); A_FMA(e.k[0], e.st[0], e.csk[0], e.ak[0]); }
    if constexpr (h == GA(1)) A_FMA(e.k[1], e.st[0], e.csk[1], e.ak[1]);
    if constexpr (h == GB(1)) { A_FMA(e.k[2], e.st[0], e.csk[2], e.ak[2]); A_FMA(e.k[3], e.st[0], e.csk[3], e.ak[3]); }
    if constexpr (h == GC(1)) { A_CVT(e.qb[0], e.q[0], e.q[1]); A_CVT(e.qb[1], e.q[2], e.q[3]); }
    if constexpr (h == GB(2)) { A_CVT(e.kb[0], e.k[0], e.k[1]); A_CVT(e.kb[1], e.k[2], e.k[3]); }
    if constexpr (h == GC(2)) {
      A_FMA(e.v[0], e.nm4[0], e.csv, e.av[0]); A_FMA(e.v[1], e.nm4[1], e.csv, e.av[1]);
      mf16z(e.sp, v2u{e.kprev[0], e.kprev[1]}, v2u{e.qb[0], e.qb[1]});
      A_FMA(e.v[2], e.nm4[2], e.csv, e.av[2]); A_FMA(e.v[3], e.nm4[3], e.csv, e.av[3]);
      mf16z(e.sc, v2u{e.kb[0], e.kb[1]}, v2u{e.qb[0], e.qb[1]});
      A_CVT(e.vb[0], e.v[0], e.v[1]); A_CVT(e.vb[1], e.v[2], e.v[3]);
    }
    if constexpr (h == GB(3)) { A_CVT(e.vb2[0], e.v[0], e.v[1]); A_CVT(e.vb2[1], e.v[2], e.v[3]); }
    if constexpr (h == GC(3)) A_MULS(e.ft, 0.25f * LOG2E, e.st[1]);
    if constexpr (h == GA(4)) A_MUL(e.fa, e.ka[0], e.ft);
    if constexpr (h == GB(4)) { A_SEL(e.a, e.sc[0], e.sc[1], e.modd); A_SEL(e.t0, e.sp[0], e.sp[1], e.modd); }
    if constexpr (h == GC(4)) { A_SEL(e.b, e.sc[2], e.sc[3], e.modd); A_SEL(e.t1, e.sp[2], e.sp[3], e.modd); }
    if constexpr (h == GB(5)) { A_SEL(e.a, e.t0, e.a, e.mA); A_SEL(e.b, e.t1, e.b, e.mB); }
    if constexpr (h == GC(5)) { A_MUL(e.fb, e.kb2[0], e.ft); A_FMA(e.a, e.a, e.fa, e.ka[1]); }
    if constexpr (h == GB(6)) {
      asm volatile("v_fma_f32 %0, %0, %3, %4\n\tv_max_f32 %1, %2, %0" : "+v"(e.b), "=&v"(e.m) : "v"(e.a), "v"(e.fb), "v"(e.kb2[1]));
    }
    if constexpr (h == GA(7)) A_MAX(e.m2, e.a, e.b);
    if constexpr (h == GC(7)) A_SW16(e.m, e.m2);
    if constexpr (h == GB(8)) { float x = e.m, y = e.m2; A_MAX(e.m, x, y); A_MAX(e.m2, x, y); }
    if constexpr (h == GA(9)) A_SW32(e.m, e.m2);
    if constexpr (h == GB(9)) asm volatile("v_max_f32 %0, %0, %2\n\tv_sub_f32 %1, %1, %0" : "+v"(e.m), "+v"(e.a) : "v"(e.m2));
    if constexpr (h == GC(9)) { A_SUB(e.b, e.b, e.m); }
    if constexpr (h == GB(10)) A_EXP(e.a, e.a);
    if constexpr (h == GC(10)) A_EXP(e.b, e.b);
    if constexpr (h == GB(11)) { A_MUL(e.pa, e.a, e.ka[0]); A_MUL(e.pb, e.b, e.kb2[0]); }
    if constexpr (h == GC(11)) { A_CVT(e.pk, e.pa, e.pb); A_CVT(e.pk0, e.a, e.b); }
    if constexpr (h == GB(12)) { A_PERM(e.P[0], e.pk, e.sPA); A_PERM(e.P[1], e.pk, e.sPB); }
    if constexpr (h == GC(12)) { A_PERM(e.P[2], e.pk, e.sCA); A_PERM(e.P[3], e.pk, e.sCB); }
    if constexpr (h == GA(13)) {
      mf16z_a(e.den, e.ones, v2u{e.pk0, e.pk});
      mf32z(e.num, v4u{e.vprev[0], e.vprev[1], e.vb[0], e.vb[1]}, v4u{e.P[0], e.P[1], e.P[2], e.P[3]});
      e.kprev[0] = e.kb[0]; e.kprev[1] = e.kb[1]; e.vprev[0] = e.vb2[0]; e.vprev[1] = e.vb2[1];
    }
    if constexpr (h == GB(14)) A_RCP(e.m, e.den);
    if constexpr (h == GC(14)) { A_MUL(e.q[0], e.num[0], e.m); A_MUL(e.q[1], e.num[1], e.m); }
    if constexpr (h == GA(15)) A_MUL(e.q[2], e.num[2], e.m);
    if constexpr (h == GB(15)) { A_MUL(e.q[3], e.num[3], e.m); A_CVT(e.ob[0], e.q[0], e.q[1]); }
    if constexpr (h == GC(15)) { A_CVT(e.ob[1], e.q[2], e.q[3]); st64(ctx, v2u{e.ob[0], e.ob[1]}, e.ovo, oso); }
  };
  auto x_sh = [&](auto TT, auto h) {
    if constexpr (h == GB(1)) dma_so<(TT + 1) * 16384>(so, xs);
    if constexpr (h == GA(2)) dma_m0<L_XT + (TT + 1) * 16384>(ctx.Lw);
    return h == GB(1) || h == GA(2);
  };
  auto x_gp = [&](auto h) {
    if constexpr (h == GA(2)) dma_go_sc1<0>(lv, ctx.rs, so);
    if constexpr (h == GA(3)) dma_go_sc1<1>(lv, ctx.rs, so);
    if constexpr (h == GA(5)) dma_go_sc1<2>(lv, ctx.rs, so);
    if constexpr (h == GA(6)) dma_go_sc1<3>(lv, ctx.rs, so);
  };
  auto w_sh = [&](auto J, auto N, auto h) {
    if constexpr (h == GA(7)) WAIT_VMV(N.value);
    if constexpr (h == GB(7)) RAWBAR();
    if constexpr (h == GB(9)) dma_so<128 * J * 1024>(so, wd.sb);
    if constexpr (h == GA(10)) dma_m0<L_XT + J * 16384>(ctx.Lw);
    if constexpr (h == GB(13) && J > 0) dma_so<(J - 1) * 16384>(oso, osb);
    return h == GA(7) || h == GB(7) || h == GB(9) || h == GA(10) || (h == GB(13) && J > 0);
  };
  auto w_gp = [&](auto h) {
    if constexpr (h == GA(10)) dma_go<0>(wd.v[0], rwo, so);
    if constexpr (h == GA(11)) dma_go<1>(wd.v[1], rwo, so);
    if constexpr (h == GA(12)) dma_go<2>(wd.v[2], rwo, so);
    if constexpr (h == GA(14)) dma_go<3>(wd.v[3], rwo, so);
  };
  TS(0);
  gemm_tile<-1, P_BK, P_BV, 0, false>(ab0, xq, xk, xv,
    [](auto ks) { return IC<((ks == 12 || ks == 13 || ks == 14) ? 2 : 0)>{}; },
    [&](auto h) {
      if constexpr (h == GA(0)) dma_so<2 * 16384>(so, xs);
      if constexpr (h == GA(1)) dma_m0<L_XT + 2 * 16384>(ctx.Lw);
      if constexpr (h == GA(11)) WAIT_VMV(4);
      if constexpr (h == GA(12)) RAWBAR();
      return h == GA(0) || h == GA(1) || h == GA(11) || h == GA(12);
    },
    [&](auto h) {
      (void)e;
      if constexpr (h == GA(1)) dma_go_sc1<0>(lv, ctx.rs, so);
      if constexpr (h == GA(4)) dma_go_sc1<1>(lv, ctx.rs, so);
      if constexpr (h == GA(7)) dma_go_sc1<2>(lv, ctx.rs, so);
      if constexpr (h == GC(10)) dma_go_sc1<3>(lv, ctx.rs, so);
      if constexpr (h == GA(12)) { A_LD64(e.nst, e.aT, 0); A_LD128(e.nnm4, e.aF, 0); }
    });
  TS(1);
  gemm_tile<P_BQ, P_BK, P_BV, 16384, false>(ab0, yq, yk, yv, xl,
    [&](auto h) { return x_sh(IC<2>{}, h) | w_sh(IC<0>{}, IC<4>{}, h); },
    [&](auto h) {
      (void)e; (void)xk; (void)xv;
      if constexpr (h == GB(1)) { A_FMA(e.k[0], e.nst[0], e.csk[0], xk[0]); A_FMA(e.k[1], e.nst[0], e.csk[1], xk[1]); }
      if constexpr (h == GC(1)) { A_FMA(e.k[2], e.nst[0], e.csk[2], xk[2]); A_FMA(e.k[3], e.nst[0], e.csk[3], xk[3]); }
      if constexpr (h == GB(2)) { A_FMA(e.v[0], e.nnm4[0], e.csv, xv[0]); A_FMA(e.v[1], e.nnm4[1], e.csv, xv[1]); }
      if constexpr (h == GC(2)) { A_FMA(e.v[2], e.nnm4[2], e.csv, xv[2]); A_FMA(e.v[3], e.nnm4[3], e.csv, xv[3]); }
      if constexpr (h == GB(3)) { A_CVT(e.kprev[0], e.k[0], e.k[1]); A_CVT(e.kprev[1], e.k[2], e.k[3]); }
      if constexpr (h == GC(3)) { A_CVT(e.vprev[0], e.v[0], e.v[1]); A_CVT(e.vprev[1], e.v[2], e.v[3]); }
      if constexpr (h == GC(15)) NOP(1);
      x_gp(h); stat_rd(IC<1>{}, h); w_gp(h);
    });
  next(yq, yk, yv);
  TS(2);
  gemm_tile<P_BQ, P_BK, P_BV, 32768, false>(ab0, xq, xk, xv, xl,
    [&](auto h) { return x_sh(IC<3>{}, h) | w_sh(IC<1>{}, IC<8>{}, h); },
    [&](auto h) { x_gp(h); stat_rd(IC<2>{}, h); w_gp(h); epi(h); });
  next(xq, xk, xv);
  TS(3);
  gemm_tile<P_BQ, P_BK, P_BV, 0, false>(ab1, yq, yk, yv, xl,
    [&](auto h) { return w_sh(IC<2>{}, IC<5>{}, h); },
    [&](auto h) { stat_rd(IC<3>{}, h); w_gp(h); epi(h); });
  next(yq, yk, yv);
  TS(4);
  gemm_tile<P_BQ, P_BK, P_BV, 16384, false>(ab1, xq, xk, xv, xl,
    [&](auto h) { return w_sh(IC<3>{}, IC<63>{}, h); },
    [&](auto h) { stat_rd(IC<4>{}, h); w_gp(h); epi(h); });
  next(xq, xk, xv);
  TS(5);
  {
    dma_so<3 * 16384>(oso, osb);
    A_MULS(e.ft, 0.25f * LOG2E, e.st[1]);
    A_FMA(e.q[0], e.st[0], e.csq[0], e.aq[0]); A_FMA(e.q[1], e.st[0], e.csq[1], e.aq[1]);
    A_FMA(e.q[2], e.st[0], e.csq[2], e.aq[2]); A_FMA(e.q[3], e.st[0], e.csq[3], e.aq[3]);
    A_FMA(e.k[0], e.st[0], e.csk[0], e.ak[0]); A_FMA(e.k[1], e.st[0], e.csk[1], e.ak[1]);
    A_CVT(e.qb[0], e.q[0], e.q[1]); A_CVT(e.qb[1], e.q[2], e.q[3]);
    A_FMA(e.k[2], e.st[0], e.csk[2], e.ak[2]); A_FMA(e.k[3], e.st[0], e.csk[3], e.ak[3]);
    mf16z(e.sp, v2u{e.kprev[0], e.kprev[1]}, v2u{e.qb[0], e.qb[1]});
    A_CVT(e.kb[0], e.k[0], e.k[1]); A_CVT(e.kb[1], e.k[2], e.k[3]);
    A_MUL(e.fa, e.ka[0], e.ft); A_MUL(e.fb, e.kb2[0], e.ft);
    mf16z(e.sc, v2u{e.kb[0], e.kb[1]}, v2u{e.qb[0], e.qb[1]});
    A_FMA(e.v[0], e.nm4[0], e.csv, e.av[0]); A_FMA(e.v[1], e.nm4[1], e.csv, e.av[1]);
    A_FMA(e.v[2], e.nm4[2], e.csv, e.av[2]); A_FMA(e.v[3], e.nm4[3], e.csv, e.av[3]);
    A_SEL(e.t0, e.sp[0], e.sp[1], e.modd); A_SEL(e.t1, e.sp[2], e.sp[3], e.modd);
    A_CVT(e.vb[0], e.v[0], e.v[1]); A_CVT(e.vb[1], e.v[2], e.v[3]);
    asm volatile("v_cndmask_b32 %0, %4, %5, %10\n\tv_cndmask_b32 %1, %6, %7, %10\n\t"
                 "v_cndmask_b32 %0, %8, %0, %11\n\tv_cndmask_b32 %1, %9, %1, %12\n\t"
                 "v_fma_f32 %0, %0, %13, %15\n\tv_fma_f32 %1, %1, %14, %16\n\t"
                 "v_max_f32 %2, %0, %1\n\tv_max_f32 %3, %0, %1\n\ts_nop 1\n\t"
                 "v_permlane16_swap_b32 %2, %3\n\tv_max_f32 %2, %2, %3\n\tv_mov_b32 %3, %2\n\ts_nop 1\n\t"
                 "v_permlane32_swap_b32 %2, %3\n\tv_max_f32 %2, %2, %3\n\tv_sub_f32 %0, %0, %2\n\tv_sub_f32 %1, %1, %2\n\t"
                 "v_exp_f32 %0, %0\n\tv_exp_f32 %1, %1"
                 : "=&v"(e.a), "=&v"(e.b), "=&v"(e.m), "=&v"(e.m2)
                 : "v"(e.sc[0]), "v"(e.sc[1]), "v"(e.sc[2]), "v"(e.sc[3]), "v"(e.t0), "v"(e.t1), "s"(e.modd), "s"(e.mA), "s"(e.mB),
                   "v"(e.fa), "v"(e.fb), "v"(e.ka[1]), "v"(e.kb2[1]));
    asm volatile("v_mul_f32 %4, %7, %9\n\tv_mul_f32 %5, %8, %10\n\tv_cvt_pk_bf16_f32 %6, %7, %8\n\tv_cvt_pk_bf16_f32 %4, %4, %5\n\t"
                 "v_perm_b32 %0, %4, %4, %11\n\tv_perm_b32 %1, %4, %4, %12\n\tv_perm_b32 %2, %4, %4, %13\n\tv_perm_b32 %3, %4, %4, %14"
                 : "=&v"(e.P[0]), "=&v"(e.P[1]), "=&v"(e.P[2]), "=&v"(e.P[3]), "=&v"(e.pk), "=&v"(e.pb), "=&v"(e.pk0)
                 : "v"(e.a), "v"(e.b), "v"(e.ka[0]), "v"(e.kb2[0]), "v"(e.sPA), "v"(e.sPB), "v"(e.sCA), "v"(e.sCB));
    mf16z_a(e.den, e.ones, v2u{e.pk0, e.pk});
    NOP(0);
    mf32z(e.num, v4u{e.vprev[0], e.vprev[1], e.vb[0], e.vb[1]}, v4u{e.P[0], e.P[1], e.P[2], e.P[3]});
    NOP(5);
    asm volatile("v_rcp_f32 %2, %7\n\ts_nop 0\n\t"
                 "v_mul_f32 %3, %8, %2\n\tv_mul_f32 %4, %9, %2\n\tv_mul_f32 %5, %10, %2\n\tv_mul_f32 %6, %11, %2\n\t"
                 "v_cvt_pk_bf16_f32 %0, %3, %4\n\tv_cvt_pk_bf16_f32 %1, %5, %6"
                 : "=&v"(e.ob[0]), "=&v"(e.ob[1]), "=&v"(e.m), "=&v"(e.q[0]), "=&v"(e.q[1]), "=&v"(e.q[2]), "=&v"(e.q[3])
                 : "v"(e.den[0]), "v"(e.num[0]), "v"(e.num[1]), "v"(e.num[2]), "v"(e.num[3]), "v"(e.den));
    st64(ctx, v2u{e.ob[0], e.ob[1]}, e.ovo, oso);
  }
  TS(6);
  STAMP(L * 8 + 4);
  TS_FLUSH(L * 8 + 2, 7);
  DRAIN();
  BAR();
  flag_store(ctx, WS_FO + (32 + c) * 128);
}

constexpr int KLOC[4] = {L_RQ, L_RQ + 32768, L_XT, L_RQ + 16384}, VLOC[4] = {L_RQ + 16384, L_RQ + 49152, L_RQ, L_RQ + 32768};
template <int P0, int P1>
DEV void first_pieces(Ctx& ctx, rsrc_t rsx, rsrc_t rsa) {
  const int lane = ctx.lane, w = ctx.w;
  const ImgDma kd = img_prep<3072>(ctx);
  const unsigned xvo = (unsigned)((lane >> 2) * 1024 + (((lane & 3) ^ ((4 - (lane >> 4)) & 3)) << 4));
  const int xso = (64 * ctx.c + 16 * w) * 1024;
  const unsigned xlb = ctx.Lb + L_XT + (1 + w) * 16384;
  const unsigned rvo = (unsigned)((lane >> 3) * 3072 + (lane & 7) * 16);
  sfor<P0, P1>([&](auto P) {
    constexpr int p = P, q = p & 3, blk = p >> 2, kind = blk % 3, j = blk / 3;
    if constexpr (kind == 0) dma1<(4 * j + q) * 64, (4 * j + q) * 1024>(xvo, rsx, xso, xlb);
    else if constexpr (blk >= 8) raw_ld1<P_BK + (blk == 8 ? 0 : blk == 10 ? 4 : 8) + q>(rvo, rsa, kd.sb + 1024 * kind + (128 * j + 8 * q) * 3072);
    else dma1<1024 * kind + 128 * j * 3072 + q * 1024, (kind == 1 ? KLOC[j] : VLOC[j]) + q * 1024>(kd.v[q], rsa, kd.sb, ctx.Lw);
  });
}
DEV void first_stage(Ctx& ctx, rsrc_t rsx, rsrc_t rsa, rsrc_t rsb, rsrc_t rwa, rsrc_t rwb) {
  fresh(ctx);
  const int lane = ctx.lane, w = ctx.w, g = ctx.g, tok = ctx.tok;
  unsigned wp0, wp1;
  {
    const int ch = lane & 7, rb1 = (lane >> 4) & 1;
    const unsigned base = ctx.Lb + L_RQ + (32 * w + (lane >> 3)) * 128 + (ch & 1) * 16;
    wp0 = base + (((ch >> 1) ^ rb1) << 5); wp1 = base + (((ch >> 1) ^ (rb1 | 2)) << 5);
  }
  unsigned abo = ctx.Lb + L_XT + 16384 + x0_lane(tok, g) + w * 16384;
  unsigned trq = ctx.Lb + L_RQ + tr_base(lane, w), tr0 = ctx.Lb + L_XT + tr_base(lane, w);
  v4u ones = {mk1(), mk1(), mk1(), mk1()};
  unsigned wrec = ctx.Lb + L_REC + ((1 + w) * 16 + tok) * 16, wnm = ctx.Lb + L_NM + ((1 + w) * 16 + tok) * 4;
  unsigned wck = ctx.Lb + L_CS + (w * 6) * 64 + 64 + g * 16, wcv = ctx.Lb + L_CS + (w * 6) * 64 + 128 + tok * 4;
  const u64 m1 = __ballot(tok & 1), m2 = __ballot(tok & 2), mdiag = __ballot(g == (tok >> 2));
  asm volatile("" : "+v"(abo), "+v"(trq), "+v"(tr0), "+a"(ones), "+v"(wrec), "+v"(wnm), "+v"(wck), "+v"(wcv), "+v"(wp0), "+v"(wp1));
  first_pieces<PRE0, 24>(ctx, rsx, rsa);
  STAMP(42);
  f4 csk, csv, gram, ssum;
  v4u xf[4];
  sfor<0, 4>([&](auto j) {
    (void)xf; (void)abo;
    if constexpr (j == 0) WAIT_VM(13);
    if constexpr (j == 1) WAIT_VM(14);
    RAWBAR();
    sfor<0, 4>([&](auto i) {
      constexpr int ks = 4 * j + i;
      (void)xf; (void)abo;
      A_LD128A(xf[i], abo, ks * 1024);
      if constexpr (j == 2) wtr<P_AK + ks, i * 4096>(tr0); else wtr<P_AK + ks, KLOC[j] - L_RQ + i * 4096>(trq);
      wtr<P_AV + ks, VLOC[j] - L_RQ + i * 4096>(trq);
    });
    if constexpr (j < 2) first_pieces<24 + 12 * j, 30 + 12 * j>(ctx, rsx, rsa);
    if constexpr (j == 2) {
      WAIT_VM(5);
      sfor<0, 4>([&](auto q) { raw_st1<P_BK + 4 + q, KLOC[3] - L_RQ + q * 1024>((q & 1) ? wp1 : wp0); });
      WAIT_VM(1);
      sfor<0, 4>([&](auto q) { raw_st1<P_BK + 8 + q, VLOC[3] - L_RQ + q * 1024>((q & 1) ? wp1 : wp0); });
      pf32<3072>(ctx, rsa, 0);
      WAIT_LG(8);
    } else {
      WAIT_LG(0);
    }
    sfor<0, 4>([&](auto i) {
      constexpr int ks = 4 * j + i;
      mf_aaz<ks == 0>(gram, xf[i], xf[i]); VNOP();
      mf_aaz<ks == 0>(ssum, ones, xf[i]); VNOP();
    });
    sfor<0, 4>([&](auto i) {
      constexpr int ks = 4 * j + i;
      wxa<P_AK + ks, ks == 0>(csk, ones); VNOP();
      wxb<P_AV + ks, ks == 0>(csv, ones); VNOP();
    });
    if constexpr (j < 2) first_pieces<30 + 12 * j, 36 + 12 * j>(ctx, rsx, rsa);
    if constexpr (j == 0) {
      const int c = ctx.c, m = ctx.m;
      const int cp = lane & 7, sw = ((lane >> 4) & 1) | ((w & 1) << 1);
      const unsigned vo = (unsigned)((lane >> 3) * 3072 + (cp & 1) * 16 + (((cp >> 1) ^ sw) << 5));
      const int s0 = (c >> 4) * 1024 + m * 128 + (32 * (c & 15) + 8 * w) * 3072, s1 = 2048 + m * 128 + (32 * (c & 15) + 8 * w) * 3072;
      dma1<0, L_IMG>(vo, rsb, s0, ctx.Lb + w * 1024); dma1<0, L_IMG + 4096>(vo, rsb, s1, ctx.Lb + w * 1024);
    }
    if constexpr (j == 3) {
      lds_t* ib = ctx.L + (L_IMG + tr_base(lane, w));
      const v4u f0 = tr_frag(ib, 0), f1 = tr_frag(ib, 4096);
      const int fb = WS_FR + (ctx.m * 4 + w) * 49152;
      st128(ctx, f0, lane * 16, fb + ctx.c * 1024);
      st128(ctx, f1, lane * 16, fb + (32 + (ctx.c & 15)) * 1024);
      SB();
    }
    if constexpr (j == 1) {
      {
        const int u = blockIdx.x * 4 + w;
        const unsigned l16 = lane & 15;
        if (u < 256) pfw((l16 >> 3) * 3072 + (l16 & 7) * 128, rsa, u * 2 * 3072);
        else if (u < 512) pfw(l16 * 128, rwa, (u - 256) * 2048);
        else pfw(l16 * 128, rwb, ((u - 512) & 255) * 2048);
      }
      WAIT_VM(15);
      sfor<0, 4>([&](auto q) { raw_st1<P_BK + q, VLOC[2] - L_RQ + q * 1024>((q & 1) ? wp1 : wp0); });
    }
    WAIT_LG(0);
    SB();
  });
  STAMP(43);
  float q, t, nm, z0;
  asm volatile("v_cndmask_b32 %0, %4, %5, %9\n\tv_cndmask_b32 %1, %6, %7, %9\n\tv_mul_f32 %2, %11, %8\n\tv_cndmask_b32 %0, %0, %1, %10\n\t"
               "v_mul_f32 %0, %12, %0\n\tv_fma_f32 %0, -%2, %2, %0\n\tv_max_f32 %0, 0, %0\n\tv_add_f32 %0, %13, %0\n\tv_rsq_f32 %0, %0\n\tv_mov_b32 %3, 0\n\tv_mov_b32 %1, %0"
               : "=&v"(q), "=&v"(t), "=&v"(nm), "=&v"(z0)
               : "v"(gram[0]), "v"(gram[1]), "v"(gram[2]), "v"(gram[3]), "v"(ssum[0]), "s"(m1), "s"(m2), "s"(-1.0f / 512.0f), "s"(1.0f / 512.0f), "s"(1e-5f), "v"(ssum));
  u64 sv;
  asm volatile("s_mov_b64 %0, exec\n\ts_mov_b64 exec, %6\n\tds_write_b128 %1, %2\n\tds_write2_b32 %3, %4, %5 offset1:80\n\ts_mov_b64 exec, %0"
               : "=&s"(sv) : "v"(wrec), "v"(f4{nm, q, t, z0}), "v"(wnm), "v"(nm), "v"(q), "s"(mdiag) : "memory");
  asm volatile("ds_write_b128 %0, %1\n\tds_write_b32 %2, %3" :: "v"(wck), "v"(csk), "v"(wcv), "v"(csv[0]), "v"(csv) : "memory");
}

struct FM {
  f4 p, sp, sn;
  float ep[4], en[4], m, m2, c;
  unsigned eb[2], nb[2];
};
#define FM_MM1(f) asm volatile("v_max3_f32 %0, %2, %3, %4\n\tv_max3_f32 %1, -%2, -%3, -%4" : "=&v"(f.m), "=&v"(f.m2) : "v"(f.p[0]), "v"(f.p[1]), "v"(f.p[2]))
#define FM_MM2(f) asm volatile("v_max_f32 %0, %0, %2\n\tv_max_f32 %1, %1, -%2" : "+v"(f.m), "+v"(f.m2) : "v"(f.p[3]))
#define FM_MX2(f) do { float x_ = f.m, y_ = f.m2; A_MAX(f.m, x_, y_); A_MAX(f.m2, x_, y_); } while (0)
#define FM_MXC(f) asm volatile("v_max_f32 %0, %0, %1\n\tv_mul_f32 %1, %0, -%2\n\tv_mul_f32 %0, %0, -%2" : "+v"(f.m), "+v"(f.m2) : "v"(f.c))

struct AE {
  f4 ak, av;
  f2 st, nst; f4 nm4, rs4, nnm4, nrs4;
  f4 csk; float csv;
  float k[4], v[4];
  unsigned kb[2], vb[2], vbo[2], php[2], phm[2];
  v2u wphi, ident, ones, lo1, hi1;
  f4 tp, tm, kvp, kvm, zz;
  FM f;
  unsigned aT, aF;
};

DEV void layer_A(Ctx& ctx, int lp, v2u wphi, rsrc_t rwo, rsrc_t rsa, rsrc_t rsb, rsrc_t rsx, rsrc_t rwn) {
  fresh(ctx);
  const int L = 2 * lp;
  const int c = ctx.c, m = ctx.m, w = ctx.w, lane = ctx.lane, g = ctx.g, tok = ctx.tok;
  const int csb = L_CS + (w * 6 + 0) * 64;
  const unsigned lv = lane * 16;
  const unsigned T = ctx.T;
  AE e;
  TS_DECL;
  unsigned ab0, ab1;
  ImgDma wd = img_prep<1024>(ctx), qd = img_prep<3072>(ctx);
  {
    const int d = tok - 4 * g;
    e.ident = v2u{d == 0 ? 0x3f80u : d == 1 ? 0x3f800000u : 0u, d == 2 ? 0x3f80u : d == 3 ? 0x3f800000u : 0u};
    const unsigned lo = tok < 8 ? ONE2 : 0u, hi = tok < 8 ? 0u : ONE2;
    e.lo1 = v2u{lo, lo}; e.hi1 = v2u{hi, hi}; e.ones = v2u{mk1(), mk1()}; e.wphi = wphi;
    e.aT = ctx.Lb + L_REC + tok * 16;
    e.aF = ctx.Lb + L_NM + g * 16;
    ab0 = ctx.Lb + L_XT + (lp == 0 ? x0_lane(tok, g) : lv); ab1 = ab0 + 49152;
    asm volatile("" : "+a"(e.ident), "+a"(e.lo1), "+a"(e.hi1), "+a"(e.ones), "+a"(e.wphi), "+v"(e.aT), "+v"(e.aF),
                      "+v"(ab0), "+v"(ab1), "+v"(qd.v[0]), "+v"(qd.v[1]), "+v"(qd.v[2]), "+v"(qd.v[3]));
  }
  const int xs = WS_X + ACT_L + c * 65536 + w * 4096 - 16384;
  f4 xk, xv, yk, yv;
  int so;
  auto stat_rd = [&](auto S, auto h) {
    (void)e;
    if constexpr (h == GA(12)) A_LD64(e.nst, e.aT, S * 256);
    if constexpr (h == GC(12)) { A_LD128(e.nnm4, e.aF, S * 64); A_LD128(e.nrs4, e.aF, S * 64 + 320); }
  };
  auto xl = [](auto ks) { return IC<(ks == 12 ? 1 : (ks == 13 || ks == 14) ? 3 : 0)>{}; };
  auto next = [&](f4& k, f4& v) { e.ak = k; e.av = v; e.st = e.nst; e.nm4 = e.nnm4; e.rs4 = e.nrs4; };
  auto epi = [&](auto h) {
    (void)e;
    if constexpr (h == GC(0)) { A_FMA(e.k[0], e.st[0], e.csk[0], e.ak[0]); A_FMA(e.k[1], e.st[0], e.csk[1], e.ak[1]); }
    if constexpr (h == GA(1)) { A_FMA(e.k[2], e.st[0], e.csk[2], e.ak[2]); A_FMA(e.k[3], e.st[0], e.csk[3], e.ak[3]); }
    if constexpr (h == GC(1)) { A_CVT(e.kb[0], e.k[0], e.k[1]); A_CVT(e.kb[1], e.k[2], e.k[3]); A_FMA(e.v[0], e.nm4[0], e.csv, e.av[0]); }
    if constexpr (h == GA(2)) {
      A_FMA(e.v[1], e.nm4[1], e.csv, e.av[1]);
      mf16z_a(e.f.p, e.wphi, v2u{e.kb[0], e.kb[1]});
      A_FMA(e.v[2], e.nm4[2], e.csv, e.av[2]); A_FMA(e.v[3], e.nm4[3], e.csv, e.av[3]);
    }
    if constexpr (h == GC(2)) { A_MUL(e.v[0], e.v[0], e.rs4[0]); A_MUL(e.v[1], e.v[1], e.rs4[1]); A_MUL(e.v[2], e.v[2], e.rs4[2]); }
    if constexpr (h == GA(3)) { A_MUL(e.v[3], e.v[3], e.rs4[3]); A_MULS(e.f.c, LOG2E, e.st[1]); }
    if constexpr (h == GC(3)) { FM_MM1(e.f); A_CVT(e.vb[0], e.v[0], e.v[1]); }
    if constexpr (h == GA(4)) FM_MM2(e.f);
    if constexpr (h == GC(4)) { A_CVT(e.vb[1], e.v[2], e.v[3]); A_SW16(e.f.m, e.f.m2); }
    if constexpr (h == GA(5)) FM_MX2(e.f);
    if constexpr (h == GC(5)) A_SW32(e.f.m, e.f.m2);
    if constexpr (h == GA(6)) FM_MXC(e.f);
    if constexpr (h == GC(6)) A_SW16(e.f.m, e.f.m2);
    if constexpr (h == GA(7)) { A_FMA(e.f.ep[0], e.f.p[0], e.f.c, e.f.m); A_FMA(e.f.ep[1], e.f.p[1], e.f.c, e.f.m); }
    if constexpr (h == GC(7)) { A_FMA(e.f.ep[2], e.f.p[2], e.f.c, e.f.m); A_FMA(e.f.ep[3], e.f.p[3], e.f.c, e.f.m); A_FMAN(e.f.en[0], e.f.p[0], e.f.c, e.f.m2); }
    if constexpr (h == GA(8)) { A_FMAN(e.f.en[1], e.f.p[1], e.f.c, e.f.m2); A_FMAN(e.f.en[2], e.f.p[2], e.f.c, e.f.m2); }
    if constexpr (h == GC(8)) { A_FMAN(e.f.en[3], e.f.p[3], e.f.c, e.f.m2); A_EXP(e.f.ep[0], e.f.ep[0]); A_EXP(e.f.ep[1], e.f.ep[1]); }
    if constexpr (h == GA(9)) A_EXP(e.f.ep[2], e.f.ep[2]);
    if constexpr (h == GC(9)) { A_EXP(e.f.ep[3], e.f.ep[3]); A_EXP(e.f.en[0], e.f.en[0]); }
    if constexpr (h == GA(10)) A_EXP(e.f.en[1], e.f.en[1]);
    if constexpr (h == GC(10)) { A_EXP(e.f.en[2], e.f.en[2]); A_EXP(e.f.en[3], e.f.en[3]); }
    if constexpr (h == GA(11)) { A_CVT(e.f.eb[0], e.f.ep[0], e.f.ep[1]); A_CVT(e.f.eb[1], e.f.ep[2], e.f.ep[3]); }
    if constexpr (h == GC(11)) {
      A_CVT(e.f.nb[0], e.f.en[0], e.f.en[1]); A_CVT(e.f.nb[1], e.f.en[2], e.f.en[3]);
      mf16z_a(e.f.sp, e.ones, v2u{e.f.eb[0], e.f.eb[1]});
    }
    if constexpr (h == GA(12)) mf16z_a(e.f.sn, e.ones, v2u{e.f.nb[0], e.f.nb[1]});
    if constexpr (h == GA(13)) A_RCP(e.f.m, e.f.sp);
    if constexpr (h == GC(13)) { A_MUL(e.f.ep[0], e.f.ep[0], e.f.m); A_RCP(e.f.m2, e.f.sn); A_MUL(e.f.ep[1], e.f.ep[1], e.f.m); }
    if constexpr (h == GA(14)) { A_MUL(e.f.ep[2], e.f.ep[2], e.f.m); A_MUL(e.f.ep[3], e.f.ep[3], e.f.m); }
    if constexpr (h == GC(14)) { A_MUL(e.f.en[0], e.f.en[0], e.f.m2); A_MUL(e.f.en[1], e.f.en[1], e.f.m2); A_CVT(e.f.eb[0], e.f.ep[0], e.f.ep[1]); }
    if constexpr (h == GA(15)) { A_CVT(e.f.eb[1], e.f.ep[2], e.f.ep[3]); A_MUL(e.f.en[2], e.f.en[2], e.f.m2); }
    if constexpr (h == GC(15)) {
      A_MUL(e.f.en[3], e.f.en[3], e.f.m2); A_CVT(e.f.nb[0], e.f.en[0], e.f.en[1]);
      mf16z_b(e.tp, v2u{e.f.eb[0], e.f.eb[1]}, e.ident);
      A_CVT(e.f.nb[1], e.f.en[2], e.f.en[3]);
      e.vbo[0] = e.vb[0]; e.vbo[1] = e.vb[1];
    }
  };
  auto epi2 = [&](auto FT, auto h) {
    (void)e;
    if constexpr (h == GA(0)) mf16z_b(e.tm, v2u{e.f.nb[0], e.f.nb[1]}, e.ident);
    if constexpr (h == GC(5)) { A_CVT(e.php[0], e.tp[0], e.tp[1]); }
    if constexpr (h == GC(6)) { A_CVT(e.php[1], e.tp[2], e.tp[3]); }
    if constexpr (h == GA(9)) {
      if constexpr (FT) mf16z(e.kvp, v2u{e.php[0], e.php[1]}, v2u{e.vbo[0], e.vbo[1]}); else mf16c(e.kvp, v2u{e.php[0], e.php[1]}, v2u{e.vbo[0], e.vbo[1]});
      A_CVT(e.phm[0], e.tm[0], e.tm[1]); A_CVT(e.phm[1], e.tm[2], e.tm[3]);
      if constexpr (FT) mf16z_a(e.zz, e.lo1, v2u{e.php[0], e.php[1]}); else mf16c_a(e.zz, e.lo1, v2u{e.php[0], e.php[1]});
    }
    if constexpr (h == GA(10)) {
      if constexpr (FT) mf16z(e.kvm, v2u{e.phm[0], e.phm[1]}, v2u{e.vbo[0], e.vbo[1]}); else mf16c(e.kvm, v2u{e.phm[0], e.phm[1]}, v2u{e.vbo[0], e.vbo[1]});
    }
    if constexpr (h == GA(13)) mf16c_a(e.zz, e.hi1, v2u{e.phm[0], e.phm[1]});
  };
  auto c_sh = [&](auto K0, auto SOFF, auto LOFF, int sb, auto h) {
    if constexpr (h == GA(K0 - 1)) dma_so<SOFF>(so, sb);
    if constexpr (h == GA(K0)) dma_m0<LOFF>(ctx.Lw);
    return h == GA(K0 - 1) || h == GA(K0);
  };
  auto c_gp = [&](auto K0, const ImgDma& d, rsrc_t rs, auto h) {
    if constexpr (h == GC(K0)) dma_go<0>(d.v[0], rs, so);
    if constexpr (h == GC(K0 + 2)) dma_go<1>(d.v[1], rs, so);
    if constexpr (h == GC(K0 + 4)) dma_go<2>(d.v[2], rs, so);
    if constexpr (h == GC(K0 + 6)) dma_go<3>(d.v[3], rs, so);
  };
  constexpr int QS = 128 * 3072;
  constexpr int SL = 32 * 256;
  const int kblk = WS_KV1 + ((m * 64 + w * 16) * 32 + c) * 256;
  const int zblk = WS_KVZ + ((m * 4 + w) * 32 + c) * 256;
  int kvo1 = g * 2 * SL + tok * 16, zvo1 = ((g >> 1) * 16 + tok) * 8;
  asm volatile("" : "+v"(kvo1), "+v"(zvo1));
  {
    const rsrc_t rnull = __builtin_amdgcn_make_buffer_rsrc((void*)ctx.ws, 0, 0, 0x00020000);
    const rsrc_t rx = lp ? ctx.rs : rnull, pw = lp ? rnull : rwo;
    const unsigned lb3 = lp ? ctx.Lw + 3 * 16384 : ctx.Lw, lb4 = lp ? ctx.Lw + 4 * 16384 : ctx.Lw;
    unsigned pv1 = (unsigned)((lane & 31) * 128), pv3 = (unsigned)((lane & 31) * 4), sink = 0, fw;
    const int prow = 16 * (int)(blockIdx.x >> 3) + 4 * w;
    const int ps1 = __builtin_amdgcn_readfirstlane(prow * 1024);
    int fs[4], fwo = WS_FW + m * 128;
#pragma unroll
    for (int i = 0; i < 4; ++i) fs[i] = WS_FR + (m * 4 + w) * 49152 + (16 + 4 * i) * 1024;
    asm volatile("" : "+s"(fs[0]), "+s"(fs[1]), "+s"(fs[2]), "+s"(fs[3]), "+s"(fwo));
    unsigned ack = ctx.Lb + csb + 64 + g * 16, acv = ctx.Lb + csb + 128 + tok * 4;
    asm volatile("" : "+v"(pv3), "+v"(pv1), "+v"(sink), "+v"(ack), "+v"(acv));
    auto pfs = [&](unsigned vo, rsrc_t rs, int so_, auto OFF) {
      (void)sink;
      asm volatile("buffer_load_dword %0, %1, %2, %3 offen offset:%4" : "+v"(sink) : "v"(vo), "s"(rs), "s"(so_), "n"(OFF.value) : "memory");
    };
    if (lp > 0) {
      ST st;
      st_begin<false>(ctx, st);
      gather_start<false>(ctx, st, L, lv, xs);
    } else {
      Ctx c2 = ctx;
      first_stage(c2, rsx, rsa, rsb, rwo, rwn);
      STAMP(1);
    }
    A_LD128(e.csk, ack, 0);
    asm volatile("ds_read_b32 %0, %1" : "=v"(e.csv) : "v"(acv) : "memory");
    ring_prime<16384>(ab0);
    TS(0);
    gemm_tile<-1, P_AK, P_AV, 16384, false>(ab0, xk, xk, xv, xl,
      [&](auto h) {
        if constexpr (h == GA(0)) dma_so<3 * 16384>(so, xs);
        if constexpr (h == GA(1)) dma_m0<L_XT>(lb3);
        if constexpr (h == GA(11)) WAIT_VMV(4);
        if constexpr (h == GA(12)) RAWBAR();
        if constexpr (h == GA(13)) dma_so<4 * 16384>(so, xs);
        if constexpr (h == GA(14)) dma_m0<L_XT>(lb4);
        return h == GA(0) || h == GA(1) || (h >= GA(11) && h <= GA(14));
      },
      [&](auto h) {
        if constexpr (h == GA(1)) dma_go_sc1<0>(lv, rx, so);
        if constexpr (h == GA(4)) dma_go_sc1<1>(lv, rx, so);
        if constexpr (h == GA(7)) dma_go_sc1<2>(lv, rx, so);
        if constexpr (h == GC(10)) dma_go_sc1<3>(lv, rx, so);
        if constexpr (h == GC(14)) dma_go_sc1<0>(lv, rx, so);
        if constexpr (h == GC(15)) NOP(1);
        stat_rd(IC<1>{}, h);
      });
    next(xk, xv);
    if (lp == 0 && w == 0) {
      unsigned tv, zv;
      asm volatile("v_mov_b32 %0, %2\n\tv_mov_b32 %1, 0" : "=v"(tv), "=v"(zv) : "s"(T));
      __builtin_amdgcn_raw_buffer_store_b32(tv, ctx.rs, zv, fwo + c * 4, SC1);
    }
    TS(1);
    gemm_tile<-1, P_AK, P_AV, 32768, false>(ab0, yk, yk, yv, xl,
      [&](auto h) {
        if constexpr (h == GA(11)) WAIT_VMV(7);
        if constexpr (h == GA(12)) RAWBAR();
        return c_sh(IC<7>{}, IC<0>{}, IC<L_RQ>{}, qd.sb, h) | (h == GA(11) || h == GA(12));
      },
      [&](auto h) {
        if constexpr (h == GA(0)) dma_go_sc1<1>(lv, rx, so);
        if constexpr (h == GC(3)) dma_go_sc1<2>(lv, rx, so);
        if constexpr (h == GC(5)) dma_go_sc1<3>(lv, rx, so);
        c_gp(IC<7>{}, qd, rsa, h);
        if constexpr (h == GC(1)) pfs(pv1, pw, ps1, IC<0>{});
        stat_rd(IC<2>{}, h); epi(h);
      });
    next(yk, yv);
    TS(2);
    gemm_tile<-1, P_AK, P_AV, 0, false>(ab1, xk, xk, xv, xl,
      [&](auto h) {
        if constexpr (h == GA(11)) WAIT_VMV(10);
        if constexpr (h == GA(12)) RAWBAR();
        return c_sh(IC<1>{}, IC<QS>{}, IC<L_RQ + 16384>{}, qd.sb, h) | c_sh(IC<9>{}, IC<2 * QS>{}, IC<L_RQ + 32768>{}, qd.sb, h) | (h == GA(11) || h == GA(12));
      },
      [&](auto h) {
        c_gp(IC<1>{}, qd, rsa, h); c_gp(IC<9>{}, qd, rsa, h);
        if constexpr (h == GC(8)) {
          (void)fw;
          asm volatile("buffer_load_dword %0, %1, %2, %3 offen sc1" : "=v"(fw) : "v"(pv3), "s"(ctx.rs), "s"(fwo) : "memory");
        }
        stat_rd(IC<3>{}, h); epi2(IC<1>{}, h); epi(h);
      });
    next(xk, xv);
    if (lp == 0) {
      unsigned f2;
      asm volatile("s_waitcnt vmcnt(4)\n\tv_mov_b32 %0, %1" : "=v"(f2) : "v"(fw) : "memory");
      while (!__all(f2 == T) && --ctx.budget >= 0) {
        f2 = bld32(ctx, pv3, fwo);
      }
      __builtin_amdgcn_fence(__ATOMIC_ACQUIRE, "wavefront");
      sfor<16, 20>([&](auto i) { fr_ld<i>(lv, ctx.rs, fs[0]); });
    }
    TS(3);
    gemm_tile<-1, P_AK, P_AV, 16384, false>(ab1, yk, yk, yv, xl,
      [&](auto h) {
        return c_sh(IC<1>{}, IC<3 * QS>{}, IC<L_RQ + 49152>{}, qd.sb, h) | c_sh(IC<9>{}, IC<0>{}, IC<L_XT>{}, wd.sb, h);
      },
      [&](auto h) {
        c_gp(IC<1>{}, qd, rsa, h);
        if constexpr (h == GC(8)) { if (lp == 0) sfor<20, 24>([&](auto i) { fr_ld<i>(lv, ctx.rs, fs[1]); }); }
        c_gp(IC<9>{}, wd, rwo, h); stat_rd(IC<4>{}, h); epi2(IC<0>{}, h); epi(h);
      });
    next(yk, yv);
  TS(4);
  {
    FM& f = e.f;
    A_MULS(f.c, LOG2E, e.st[1]);
    NOP(0);
    mf16z_b(e.tm, v2u{f.nb[0], f.nb[1]}, e.ident);
    A_FMA(e.k[0], e.st[0], e.csk[0], e.ak[0]); A_FMA(e.k[1], e.st[0], e.csk[1], e.ak[1]);
    A_FMA(e.k[2], e.st[0], e.csk[2], e.ak[2]); A_FMA(e.k[3], e.st[0], e.csk[3], e.ak[3]);
    A_CVT(e.kb[0], e.k[0], e.k[1]); A_CVT(e.php[0], e.tp[0], e.tp[1]); A_CVT(e.kb[1], e.k[2], e.k[3]); A_CVT(e.php[1], e.tp[2], e.tp[3]);
    A_CVT(e.phm[0], e.tm[0], e.tm[1]);
    mf16z_a(f.p, e.wphi, v2u{e.kb[0], e.kb[1]});
    A_CVT(e.phm[1], e.tm[2], e.tm[3]); A_FMA(e.v[0], e.nm4[0], e.csv, e.av[0]);
    mf16c(e.kvp, v2u{e.php[0], e.php[1]}, v2u{e.vbo[0], e.vbo[1]});
    A_FMA(e.v[1], e.nm4[1], e.csv, e.av[1]); A_FMA(e.v[2], e.nm4[2], e.csv, e.av[2]);
    mf16c(e.kvm, v2u{e.phm[0], e.phm[1]}, v2u{e.vbo[0], e.vbo[1]});
    A_FMA(e.v[3], e.nm4[3], e.csv, e.av[3]); A_MUL(e.v[0], e.v[0], e.rs4[0]);
    FM_MM1(f);
    mf16c_a(e.zz, e.lo1, v2u{e.php[0], e.php[1]});
    FM_MM2(f);
    A_MUL(e.v[1], e.v[1], e.rs4[1]); A_MUL(e.v[2], e.v[2], e.rs4[2]);
    A_SW16(f.m, f.m2);
    mf16c_a(e.zz, e.hi1, v2u{e.phm[0], e.phm[1]});
    FM_MX2(f);
    A_MUL(e.v[3], e.v[3], e.rs4[3]); A_CVT(e.vb[0], e.v[0], e.v[1]);
    A_SW32(f.m, f.m2);
    FM_MXC(f);
    A_CVT(e.vb[1], e.v[2], e.v[3]);
    NOP(0);
    A_SW16(f.m, f.m2);
#pragma unroll
    for (int r = 0; r < 4; ++r) A_FMA(f.ep[r], f.p[r], f.c, f.m);
#pragma unroll
    for (int r = 0; r < 4; ++r) A_FMAN(f.en[r], f.p[r], f.c, f.m2);
#pragma unroll
    for (int r = 0; r < 4; ++r) A_EXP(f.ep[r], f.ep[r]);
    A_EXP(f.en[0], f.en[0]);
    A_CVT(f.eb[0], f.ep[0], f.ep[1]); A_CVT(f.eb[1], f.ep[2], f.ep[3]);
    A_EXP(f.en[1], f.en[1]); A_EXP(f.en[2], f.en[2]);
    mf16z_a(f.sp, e.ones, v2u{f.eb[0], f.eb[1]});
    A_EXP(f.en[3], f.en[3]);
    A_CVT(f.nb[0], f.en[0], f.en[1]); A_CVT(f.nb[1], f.en[2], f.en[3]);
    NOP(1);
    mf16z_a(f.sn, e.ones, v2u{f.nb[0], f.nb[1]});
    NOP(1);
    A_RCP(f.m, f.sp);
    NOP(0);
#pragma unroll
    for (int r = 0; r < 4; ++r) A_MUL(f.ep[r], f.ep[r], f.m);
    A_RCP(f.m2, f.sn);
    A_CVT(f.eb[0], f.ep[0], f.ep[1]); A_CVT(f.eb[1], f.ep[2], f.ep[3]);
    A_MUL(f.en[0], f.en[0], f.m2); A_MUL(f.en[1], f.en[1], f.m2);
    mf16z_b(e.tp, v2u{f.eb[0], f.eb[1]}, e.ident);
    A_MUL(f.en[2], f.en[2], f.m2); A_MUL(f.en[3], f.en[3], f.m2);
    A_CVT(f.nb[0], f.en[0], f.en[1]); A_CVT(f.nb[1], f.en[2], f.en[3]);
    NOP(1);
    mf16z_b(e.tm, v2u{f.nb[0], f.nb[1]}, e.ident);
    NOP(0);
    A_CVT(e.php[0], e.tp[0], e.tp[1]); A_CVT(e.php[1], e.tp[2], e.tp[3]);
    NOP(1);
    mf16c(e.kvp, v2u{e.php[0], e.php[1]}, v2u{e.vb[0], e.vb[1]});
    mf16c_a(e.zz, e.lo1, v2u{e.php[0], e.php[1]});
    NOP(0);
    A_CVT(e.phm[0], e.tm[0], e.tm[1]); A_CVT(e.phm[1], e.tm[2], e.tm[3]);
    NOP(1);
    mf16c(e.kvm, v2u{e.phm[0], e.phm[1]}, v2u{e.vb[0], e.vb[1]});
    mf16c_a(e.zz, e.hi1, v2u{e.phm[0], e.phm[1]});
    asm volatile("s_nop 0" : "+v"(e.kvp));
  }
  TS(5);
  {
    const int blk = kblk, vo = kvo1;
    st128(ctx, v4u{asu(e.kvp[0]), T, asu(e.kvp[1]), T}, vo, blk);
    st128(ctx, v4u{asu(e.kvp[2]), T, asu(e.kvp[3]), T}, vo, blk + SL);
    asm volatile("s_nop 3" : "+v"(e.kvm), "+v"(e.zz));
    st128(ctx, v4u{asu(e.kvm[0]), T, asu(e.kvm[1]), T}, vo, blk + 8 * SL);
    st128(ctx, v4u{asu(e.kvm[2]), T, asu(e.kvm[3]), T}, vo, blk + 9 * SL);
    st64(ctx, v2u{asu(e.zz[0]), T}, zvo1, zblk);
  }
    asm volatile("s_waitcnt vmcnt(9)" : "+v"(sink), "+v"(fw) :: "memory");
  }
  STAMP(L * 8 + 2);
  TS_FLUSH(L * 8 + 2, 6);
  f4 aq[4];
  v4u dk[4], dz;
  const int S64 = 2 * c + (w >> 1);
  const int rblk = WS_KV1 + ((m * 64 + S64) * 32) * 256 + (w & 1) * 128;
  const int rzb = WS_KVZ + ((m * 4 + (S64 >> 4)) * 32) * 256 + (S64 & 15) * 16;
  int kvo = (lane >> 3) * 256 + (lane & 7) * 16, zvo = (lane & 31) * 256;
  {
    lds_t* qt = ctx.L + opq(L_RQ + tr_base(lane, w));
    asm volatile("" : "+v"(kvo), "+v"(zvo));
    TS(0);
    RAWBAR();
    TS(1);
    v4u wq[16];
#pragma unroll
    for (int ks = 0; ks < 16; ++ks) wq[ks] = tr_frag(qt, ks * 4096);
    if (lp == 0) {
      { Ctx c2 = ctx; fresh(c2); fr_load<24, 34>(c2); }
      f4 c0, c1;
      v4u ones = {mk1(), mk1(), mk1(), mk1()};
      asm volatile("s_nop 1" : "+v"(ones));
      sfor<0, 8>([&](auto k) { mf_av<k == 0>(c0, wq[2 * k], ones); mf_av<k == 0>(c1, wq[2 * k + 1], ones); });
      asm volatile("s_nop 7" : "+v"(c0), "+v"(c1));
      if (tok == 0) *(LAS f4*)(ctx.L + csb + g * 16) = c0 + c1;
    }
    v4u a1[4];
    auto rd = [&](auto k) {
      (void)a1;
      if constexpr (k < 16) { ring_rd<k & 3, 16384 + k * 1024>(ab0); } else if constexpr (k < 32) { ring_rd<k & 3, (k - 16) * 1024>(ab1); }
    };
    auto rd1 = [&](auto k) {
      (void)a1; (void)ab0; (void)ab1;
      if constexpr (k < 16) { A_LD128(a1[k & 3], ab0, 32768 + k * 1024); } else if constexpr (k < 32) { A_LD128(a1[k & 3], ab1, 16384 + (k - 16) * 1024); }
    };
    sfor<0, 3>([&](auto k) { rd(k); rd1(k); });
#pragma unroll
    for (int ks = 0; ks < 16; ++ks) asm volatile("" : "+a"(wq[ks]));
    WAIT_LG(4);
    TS(2);
    sfor<0, 32>([&](auto k) {
      constexpr int ks = k & 15, u = k >> 4;
      (void)dk; (void)dz; (void)kvo; (void)zvo; (void)rblk; (void)rzb;
      mmq<k & 3, ks == 0>(aq[2 * u], wq[ks]);
      VNOP();
      rd(IC<k + 3>{});
      if constexpr (k >= QPOLL && k < QPOLL + 4) {
        constexpr int i = k - QPOLL;
        asm volatile("buffer_load_dwordx4 %0, %1, %2, %3 offen offset:%4 sc1" : "=v"(dk[i]) : "v"(kvo), "s"(ctx.rs), "s"(i < 2 ? rblk : rblk + 4096), "n"((i & 1) * 2048) : "memory");
      }
      if constexpr (k == QPOLL + 4) asm volatile("buffer_load_dwordx4 %0, %1, %2, %3 offen sc1" : "=v"(dz) : "v"(zvo), "s"(ctx.rs), "s"(rzb) : "memory");
      mf_av<ks == 0>(aq[2 * u + 1], wq[ks], a1[k & 3]);
      WAIT_LG(k + 3 < 32 ? 3 : k == 29 ? 2 : 0);
      rd1(IC<k + 3>{});
      SB();
    });
  }
  TS(6);
  TS_FLUSH(34 + lp * 8, 7);
  STAMP(48 + lp * 4);
  v4u phq[4], d2[4];
  const int b2 = WS_KV2 + (m * 64 + w * 16) * 256;
  int vo2 = g * 512 + tok * 16;
  {
    const f4 csq = lds_ldf4(ctx.L, csb + g * 16);
    f2 st[4];
#pragma unroll
    for (int t = 0; t < 4; ++t) st[t] = *(LAS f2*)(ctx.L + L_REC + ((1 + t) * 16 + tok) * 16);
    int svo = (lane & 7) * 16 + ((lane >> 4) & 1) * 8;
    const u64 mhi = __ballot(lane >= 32);
    const int sblk = WS_KV2 + (m * 64 + S64) * 256 + (w & 1) * 128;
    asm volatile("s_nop 3" : "+v"(aq[0]), "+v"(aq[1]), "+v"(aq[2]), "+v"(aq[3]), "+v"(svo), "+v"(vo2));
    {
      asm volatile("s_waitcnt vmcnt(0)" : "+v"(dk[0]), "+v"(dk[1]), "+v"(dk[2]), "+v"(dk[3]), "+v"(dz) :: "memory");
      STAMP(49 + lp * 4);
      for (;;) {
        unsigned bad;
        asm volatile("v_sad_u32 %0, %1, %11, 0 clamp\n\tv_sad_u32 %0, %2, %11, %0 clamp\n\tv_sad_u32 %0, %3, %11, %0 clamp\n\tv_sad_u32 %0, %4, %11, %0 clamp\n\t"
                     "v_sad_u32 %0, %5, %11, %0 clamp\n\tv_sad_u32 %0, %6, %11, %0 clamp\n\tv_sad_u32 %0, %7, %11, %0 clamp\n\tv_sad_u32 %0, %8, %11, %0 clamp\n\t"
                     "v_sad_u32 %0, %9, %11, %0 clamp\n\tv_sad_u32 %0, %10, %11, %0 clamp"
                     : "=&v"(bad) : "v"(dk[0].y), "v"(dk[0].w), "v"(dk[1].y), "v"(dk[1].w), "v"(dk[2].y), "v"(dk[2].w), "v"(dk[3].y), "v"(dk[3].w),
                       "v"(dz.y), "v"(dz.w), "s"(T));
        if (__builtin_expect(__all(bad == 0), 1) || --ctx.budget < 0) break;
        asm volatile("s_nop 4\n\tbuffer_load_dwordx4 %0, %5, %7, %8 offen sc1\n\tbuffer_load_dwordx4 %1, %5, %7, %8 offen offset:2048 sc1\n\t"
                     "buffer_load_dwordx4 %2, %5, %7, %9 offen sc1\n\tbuffer_load_dwordx4 %3, %5, %7, %9 offen offset:2048 sc1\n\t"
                     "buffer_load_dwordx4 %4, %6, %7, %10 offen sc1\n\ts_waitcnt vmcnt(0)"
                     : "=&v"(dk[0]), "=&v"(dk[1]), "=&v"(dk[2]), "=&v"(dk[3]), "=&v"(dz)
                     : "v"(kvo), "v"(zvo), "s"(ctx.rs), "s"(rblk), "s"(rblk + 4096), "s"(rzb) : "memory");
      }
      float K, K1, Z; unsigned pk, tg;
      asm volatile("v_add_f32 %0, %5, %6\n\tv_add_f32 %1, %9, %10\n\tv_add_f32 %2, %7, %8\n\tv_add_f32 %3, %11, %12\n\t"
                   "v_add_f32 %0, %0, %2\n\tv_add_f32 %1, %1, %3\n\tv_cndmask_b32 %2, %13, %14, %15\n\tv_mov_b32 %4, %16\n\t"
                   "v_permlane32_swap_b32 %0, %1\n\tv_add_f32 %0, %0, %1\n\ts_nop 1\n\t"
                   "v_permlane16_swap_b32 %0, %2\n\tv_add_f32 %0, %0, %2\n\ts_nop 1\n\t"
                   "v_add_f32_dpp %0, %0, %0 row_ror:8 row_mask:0xf bank_mask:0xf\n\ts_nop 1\n\t"
                   "v_add_f32_dpp %0, %0, %0 row_ror:4 row_mask:0xa bank_mask:0xf\n\ts_nop 1\n\t"
                   "v_add_f32_dpp %0, %0, %0 row_ror:2 row_mask:0xa bank_mask:0xf\n\ts_nop 1\n\t"
                   "v_add_f32_dpp %0, %0, %0 row_ror:1 row_mask:0xa bank_mask:0xf\n\t"
                   "v_mov_b32 %1, %0\n\ts_nop 1\n\tv_permlane32_swap_b32 %0, %1\n\tv_cvt_pk_bf16_f32 %3, %0, %1"
                   : "=&v"(K), "=&v"(K1), "=&v"(Z), "=&v"(pk), "=&v"(tg)
                   : "v"(dk[0].x), "v"(dk[1].x), "v"(dk[2].x), "v"(dk[3].x), "v"(dk[0].z), "v"(dk[1].z), "v"(dk[2].z), "v"(dk[3].z),
                     "v"(dz.x), "v"(dz.z), "s"(mhi), "s"(T));
      st64(ctx, v2u{pk, tg}, svo, sblk);
      STAMP(L * 8 + 3);
    }
    FM f[4];
    unsigned qb[4][2];
#pragma unroll
    for (int t = 0; t < 4; ++t) {
      float q[4];
#pragma unroll
      for (int r = 0; r < 4; ++r) A_FMA(q[r], st[t][0], csq[r], aq[t][r]);
      A_MULS(f[t].c, LOG2E, st[t][1]);
      VNOP();
      A_CVT(qb[t][0], q[0], q[1]); A_CVT(qb[t][1], q[2], q[3]);
      if (t > 0) mf16z_a(f[t - 1].p, e.wphi, v2u{qb[t - 1][0], qb[t - 1][1]});
    }
    FM_MM1(f[0]);
    mf16z_a(f[3].p, e.wphi, v2u{qb[3][0], qb[3][1]});
    FM_MM1(f[1]); FM_MM2(f[0]); FM_MM2(f[1]); FM_MM1(f[2]);
    A_SW16(f[0].m, f[0].m2); FM_MM2(f[2]); FM_MM1(f[3]); A_SW16(f[1].m, f[1].m2); FM_MM2(f[3]);
    FM_MX2(f[0]); A_SW16(f[2].m, f[2].m2); FM_MX2(f[1]); A_SW16(f[3].m, f[3].m2);
    A_SW32(f[0].m, f[0].m2); FM_MX2(f[2]); A_SW32(f[1].m, f[1].m2); FM_MX2(f[3]);
    FM_MXC(f[0]); A_SW32(f[2].m, f[2].m2); FM_MXC(f[1]); A_SW32(f[3].m, f[3].m2);
    A_SW16(f[0].m, f[0].m2); FM_MXC(f[2]); A_SW16(f[1].m, f[1].m2); FM_MXC(f[3]);
    if (lp == 0) { Ctx c2 = ctx; fresh(c2); fr_load<34, 40>(c2); }
    RAWBAR();
    sfor<0, 4>([&](auto t) {
      (void)f; (void)e;
      auto piece = [&](auto p) {
        if constexpr ((p & 3) == 0) { dma_so<128 * 1024 * (1 + (p >> 2))>(so, wd.sb); dma_m0<L_XT + 16384 * (1 + (p >> 2))>(ctx.Lw); VNOP(); }
        dma_go<p & 3>(wd.v[p & 3], rwo, so);
      };
      piece(IC<3 * t>{});
#pragma unroll
      for (int r = 0; r < 4; ++r) A_FMA(f[t].ep[r], f[t].p[r], f[t].c, f[t].m);
      if constexpr (t < 2) A_SW16(f[t + 2].m, f[t + 2].m2);
#pragma unroll
      for (int r = 0; r < 4; ++r) A_FMAN(f[t].en[r], f[t].p[r], f[t].c, f[t].m2);
      if constexpr (t > 0) mf16z_a(f[t - 1].sn, e.ones, v2u{f[t - 1].nb[0], f[t - 1].nb[1]}); else VNOP();
      piece(IC<3 * t + 1>{});
#pragma unroll
      for (int r = 0; r < 4; ++r) A_EXP(f[t].ep[r], f[t].ep[r]);
      A_EXP(f[t].en[0], f[t].en[0]);
      piece(IC<3 * t + 2>{});
      A_CVT(f[t].eb[0], f[t].ep[0], f[t].ep[1]); A_CVT(f[t].eb[1], f[t].ep[2], f[t].ep[3]);
      A_EXP(f[t].en[1], f[t].en[1]); A_EXP(f[t].en[2], f[t].en[2]);
      mf16z_a(f[t].sp, e.ones, v2u{f[t].eb[0], f[t].eb[1]});
      A_EXP(f[t].en[3], f[t].en[3]);
      A_CVT(f[t].nb[0], f[t].en[0], f[t].en[1]); A_CVT(f[t].nb[1], f[t].en[2], f[t].en[3]);
    });
#pragma unroll
    for (int t = 0; t < 4; ++t) {
      A_RCP(f[t].m, f[t].sp); A_RCP(f[t].m2, f[t].sn);
      if (t == 0) mf16z_a(f[3].sn, e.ones, v2u{f[3].nb[0], f[3].nb[1]});
      if (t == S2POLL) {
        asm volatile("buffer_load_dwordx4 %0, %4, %5, %6 offen sc1\n\tbuffer_load_dwordx4 %1, %4, %5, %6 offen offset:256 sc1\n\t"
                     "buffer_load_dwordx4 %2, %4, %5, %6 offen offset:2048 sc1\n\tbuffer_load_dwordx4 %3, %4, %5, %6 offen offset:2304 sc1"
                     : "=&v"(d2[0]), "=&v"(d2[1]), "=&v"(d2[2]), "=&v"(d2[3]) : "v"(vo2), "s"(ctx.rs), "s"(b2) : "memory");
      }
#pragma unroll
      for (int r = 0; r < 4; ++r) A_MUL(f[t].ep[r], f[t].ep[r], f[t].m);
#pragma unroll
      for (int r = 0; r < 4; ++r) A_MUL(f[t].en[r], f[t].en[r], f[t].m2);
      unsigned x0, x1, x2, x3;
      A_CVT(x0, f[t].ep[0], f[t].ep[1]); A_CVT(x1, f[t].ep[2], f[t].ep[3]); A_CVT(x2, f[t].en[0], f[t].en[1]); A_CVT(x3, f[t].en[2], f[t].en[3]);
      phq[t] = v4u{x0, x1, x2, x3};
    }
  }
  STAMP(50 + lp * 4);
  {
    int ovo = ((g >> 1) * 16 + tok) * 16 + (g & 1) * 8;
    const int osb = WS_O + (4 * c * 16 + 2 * m + (w >> 1)) * 1024 + (w & 1) * 512;
    if (lp == 0) { Ctx c2 = ctx; fresh(c2); fr_load<40, 48>(c2); WAIT_VM(8); } else DRAIN();
    asm volatile("" : "+v"(d2[0]), "+v"(d2[1]), "+v"(d2[2]), "+v"(d2[3]), "+v"(ovo) :: "memory");
    for (;;) {
      unsigned bad;
      asm volatile("v_sad_u32 %0, %1, %9, 0 clamp\n\tv_sad_u32 %0, %2, %9, %0 clamp\n\tv_sad_u32 %0, %3, %9, %0 clamp\n\tv_sad_u32 %0, %4, %9, %0 clamp\n\t"
                   "v_sad_u32 %0, %5, %9, %0 clamp\n\tv_sad_u32 %0, %6, %9, %0 clamp\n\tv_sad_u32 %0, %7, %9, %0 clamp\n\tv_sad_u32 %0, %8, %9, %0 clamp"
                   : "=&v"(bad) : "v"(d2[0].y), "v"(d2[0].w), "v"(d2[1].y), "v"(d2[1].w), "v"(d2[2].y), "v"(d2[2].w), "v"(d2[3].y), "v"(d2[3].w), "s"(T));
      if (__builtin_expect(__all(bad == 0), 1) || --ctx.budget < 0) break;
      bld128x4<256, 2048, 2304>(ctx, d2, vo2, b2);
    }
    STAMP(L * 8 + 4);
    v4u akv = {d2[0].x, d2[1].x, d2[2].x, d2[3].x}, az = {d2[0].z, d2[1].z, d2[2].z, d2[3].z};
    f4 num[4], den[4];
    asm volatile("s_nop 1" : "+v"(akv), "+v"(az));
#pragma unroll
    for (int t = 0; t < 4; ++t) { mf32z(den[t], az, phq[t]); mf32z(num[t], akv, phq[t]); }
#pragma unroll
    for (int t = 0; t < 4; ++t) {
      float r, q0, q1, q2, q3; unsigned o0, o1;
      asm volatile("v_add_f32 %2, %12, %7\n\tv_rcp_f32 %2, %2\n\ts_nop 0\n\t"
                   "v_mul_f32 %3, %8, %2\n\tv_mul_f32 %4, %9, %2\n\tv_mul_f32 %5, %10, %2\n\tv_mul_f32 %6, %11, %2\n\t"
                   "v_cvt_pk_bf16_f32 %0, %3, %4\n\tv_cvt_pk_bf16_f32 %1, %5, %6"
                   : "=&v"(o0), "=&v"(o1), "=&v"(r), "=&v"(q0), "=&v"(q1), "=&v"(q2), "=&v"(q3)
                   : "v"(den[t][0]), "v"(num[t][0]), "v"(num[t][1]), "v"(num[t][2]), "v"(num[t][3]), "s"(1e-6f), "v"(den[t]));
      st64(ctx, v2u{o0, o1}, ovo, osb + t * 16384);
    }
  }
  DRAIN();
  BAR();
  flag_store(ctx, WS_FO + c * 128);
}

template <bool ODD>
DEV void out_proj(Ctx& ctx, int lp, f4 (&xacc)[4], rsrc_t out, rsrc_t rsb, rsrc_t rwn, rsrc_t rsx) {
  fresh(ctx);
  const int L = 2 * lp + (ODD ? 1 : 0);
  const bool last = ODD && lp == 1, first = !ODD && lp == 0;
  const int c = ctx.c, m = ctx.m, w = ctx.w, lane = ctx.lane, g = ctx.g, tok = ctx.tok;
  if (first) {
    v2u xr[4];
    asm volatile("s_nop 4\n\tbuffer_load_dwordx2 %0, %4, %5, %6 offen\n\tbuffer_load_dwordx2 %1, %4, %5, %6 offen offset:32\n\t"
                 "buffer_load_dwordx2 %2, %4, %5, %6 offen offset:64\n\tbuffer_load_dwordx2 %3, %4, %5, %6 offen offset:96"
                 : "=&v"(xr[0]), "=&v"(xr[1]), "=&v"(xr[2]), "=&v"(xr[3]) : "v"(tok * 1024 + g * 8), "s"(rsx), "s"((64 * c + 16 * w) * 1024 + 128 * m) : "memory");
    fr_load<0, 8>(ctx);
    part_cs<P_BK>(ctx, L_CS + (w * 6 + 4) * 64);
    asm volatile("s_waitcnt vmcnt(8)" : "+v"(xr[0]), "+v"(xr[1]), "+v"(xr[2]), "+v"(xr[3]) :: "memory");
#pragma unroll
    for (int nt = 0; nt < 4; ++nt)
      xacc[nt] = f4{asf(xr[nt].x << 16), asf(xr[nt].x & 0xffff0000u), asf(xr[nt].y << 16), asf(xr[nt].y & 0xffff0000u)};
  }
  lds_t* tb[4];
#pragma unroll
  for (int nt = 0; nt < 4; ++nt) tb[nt] = ctx.L + opq(L_XT + tr_base(lane, nt));
  v4u wf[4][4];
#pragma unroll
  for (int i = 0; i < 4; ++i)
#pragma unroll
    for (int nt = 0; nt < 4; ++nt) wf[i][nt] = tr_frag(tb[nt], i * 4096);
  const int G = ((g & 1) << 1) | (g >> 1);
  int xvo = (G * 16 + tok) * 16;
  u64 m1 = __ballot(tok & 1), m2 = __ballot(tok & 2);
  const int osb = WS_O + (4 * c + w) * 16384;
  const int xsb = WS_X + (ODD ? ACT_L : 0) + ((4 * c + w) * 16 + 2 * m) * 1024;
  const int ssb = WS_STAT + (ODD ? STAT_L : 0) + ((c * 8 + m) * 64 + 16 * w) * 16;
  asm volatile("" : "+v"(xvo), "+v"(xacc[0]), "+v"(xacc[1]), "+v"(xacc[2]), "+v"(xacc[3]));
#pragma unroll
  for (int nt = 0; nt < 4; ++nt) asm volatile("" : "+v"(wf[0][nt]), "+v"(wf[1][nt]), "+v"(wf[2][nt]), "+a"(wf[3][nt]));
  wait_flags(ctx, WS_FO + ((ODD ? 32 : 0) + c) * 128, 0, false);
  if (ODD) ctx.T += 1u;
  STAMP(L * 8 + 5);
  v4u of[16];
  constexpr int PRE = OPRE;
#pragma unroll
  for (int ks = 0; ks < PRE; ++ks) of[ks] = ld128(ctx, lane * 16 + (ks & 3) * 1024, osb + (ks & ~3) * 1024);
  SB();
  sfor<0, 16>([&](auto ks) {
    if constexpr (ks + PRE < 16) { of[ks + PRE] = ld128(ctx, lane * 16 + ((ks + PRE) & 3) * 1024, osb + ((ks + PRE) & ~3) * 1024); SB(); }
#pragma unroll
    for (int nt = 0; nt < 4; ++nt) {
      if constexpr ((ks & 3) < 3) mf_va(xacc[nt], wf[ks & 3][nt], of[ks]); else mf_aa(xacc[nt], wf[ks & 3][nt], of[ks]);
      if constexpr (ks + 4 < 16) wf[ks & 3][nt] = tr_frag(tb[nt], (ks + 4) * 4096);
      SB();
    }
  });
  NOP(4);
  STAMP(L * 8 + 6);
  v4u fr[2];
#pragma unroll
  for (int u = 0; u < 2; ++u) {
    unsigned a0, a1, b0, b1;
    const f4 a = xacc[2 * u], b = xacc[2 * u + 1];
    asm volatile("v_cvt_pk_bf16_f32 %0, %4, %5\n\tv_cvt_pk_bf16_f32 %1, %6, %7\n\tv_cvt_pk_bf16_f32 %2, %8, %9\n\tv_cvt_pk_bf16_f32 %3, %10, %11\n\t"
                 "s_nop 0\n\tv_permlane16_swap_b32 %0, %2\n\tv_permlane16_swap_b32 %1, %3"
                 : "=&v"(a0), "=&v"(a1), "=&v"(b0), "=&v"(b1)
                 : "v"(a[0]), "v"(a[1]), "v"(a[2]), "v"(a[3]), "v"(b[0]), "v"(b[1]), "v"(b[2]), "v"(b[3]));
    fr[u] = v4u{a0, a1, b0, b1};
    if (!last) st128(ctx, fr[u], xvo + u * 1024, xsb);
  }
  if (last) {
#pragma unroll
    for (int u = 0; u < 2; ++u)
      __builtin_amdgcn_raw_buffer_store_b128(fr[u], out, tok * 1024 + G * 16 + u * 64, (64 * c + 16 * w) * 1024 + 128 * m, 0);
  } else {
    f4 ssum, gram;
    v4u ones = {mk1(), mk1(), mk1(), mk1()};
    asm volatile("s_nop 1" : "+v"(ones));
    mf32z(gram, fr[0], fr[0]); mf32z(ssum, ones, fr[0]);
    mf32c(gram, fr[1], fr[1]); mf32c(ssum, ones, fr[1]);
    asm volatile("s_nop 7" : "+v"(gram), "+v"(ssum));
    float q, t;
    asm volatile("v_cndmask_b32 %0, %2, %3, %6\n\tv_cndmask_b32 %1, %4, %5, %6\n\tv_cndmask_b32 %0, %0, %1, %7"
                 : "=&v"(q), "=&v"(t) : "v"(gram[0]), "v"(gram[1]), "v"(gram[2]), "v"(gram[3]), "s"(m1), "s"(m2));
    if (g == (tok >> 2)) st128(ctx, v4u{asu(ssum[0]), ctx.T, asu(q), ctx.T}, tok * 16, ssb);
    WAIT_VM(1);
    BAR();
    flag_store(ctx, WS_FX + ((ODD ? 32 : 0) + c) * 128);
    if (first) { fr_load<8, 16>(ctx); part_cs<P_BV>(ctx, L_CS + (w * 6 + 5) * 64); DRAIN(); part_cs<P_BQ>(ctx, L_CS + (w * 6 + 3) * 64); }
  }
  asm volatile("" : "+a"(xacc[0]), "+a"(xacc[1]), "+a"(xacc[2]), "+a"(xacc[3]));
  STAMP(L * 8 + 7);
}

__global__ void __launch_bounds__(NT, 1) __attribute__((amdgpu_num_vgpr(88)))
fused_kernel(const unsigned char* __restrict__ x_, const unsigned char* __restrict__ wa_, const unsigned char* __restrict__ wphi_,
             const unsigned char* __restrict__ woa_, const unsigned char* __restrict__ wb_, const unsigned char* __restrict__ wob_,
             unsigned char* out_, unsigned char* ws_) {
  extern __shared__ __attribute__((aligned(16))) unsigned char smem[];
  asm volatile("" ::: "v255", "a255");
  Ctx ctx;
  ctx.ws = (GAS unsigned char*)ws_;
  ctx.rs = __builtin_amdgcn_make_buffer_rsrc((void*)ws_, 0, WS_END, 0x00020000);
  ctx.L = (lds_t*)smem;
  ctx.Lb = (unsigned)(size_t)ctx.L;
  const int b = blockIdx.x, idx = b >> 3;
  ctx.c = (b & 7) * 4 + (idx >> 3);
  ctx.m = idx & 7;
  ctx.w = __builtin_amdgcn_readfirstlane(threadIdx.x >> 6);
  ctx.budget = SPIN_CAP;
  fresh(ctx);
  const int c = ctx.c, m = ctx.m, w = ctx.w, lane = ctx.lane, g = ctx.g, tok = ctx.tok;
  STAMP(40); CSTAMP(0);
  const rsrc_t rsa = __builtin_amdgcn_make_buffer_rsrc((void*)wa_, 0, 512 * 3072, 0x00020000);
  const rsrc_t rsb = __builtin_amdgcn_make_buffer_rsrc((void*)wb_, 0, 512 * 3072, 0x00020000);
  const rsrc_t rwa = __builtin_amdgcn_make_buffer_rsrc((void*)woa_, 0, 512 * 1024, 0x00020000);
  const rsrc_t rwb = __builtin_amdgcn_make_buffer_rsrc((void*)wob_, 0, 512 * 1024, 0x00020000);
  {
    u64 pc; unsigned len;
    asm volatile("s_getpc_b64 %0\n.Lfk_here:\n\ts_mov_b32 %1, .Lfk_end-.Lfk_here" : "=s"(pc), "=s"(len));
    const rsrc_t rc = __builtin_amdgcn_make_buffer_rsrc((void*)pc, 0, len, 0x00020000);
    asm volatile("s_nop 4" ::: "memory");
    pf1<0>(lane * 16, rc, (idx * 4 + w) * 1024);
  }
  unsigned epoch, ph0, ph1, ph2, ph3;
  {
    const GAS unsigned char* pp = (const GAS unsigned char*)wphi_ + (4 * m + w) * 512;
    const unsigned pv = (unsigned)(4 * g * 16 + tok) * 2, zero = 0;
    asm volatile("s_nop 4\n\tglobal_load_dword %0, %5, %6 sc1\n\t"
                 "global_load_ushort %1, %7, %8\n\tglobal_load_ushort %2, %7, %8 offset:32\n\t"
                 "global_load_ushort %3, %7, %8 offset:64\n\tglobal_load_ushort %4, %7, %8 offset:96"
                 : "=&v"(epoch), "=&v"(ph0), "=&v"(ph1), "=&v"(ph2), "=&v"(ph3)
                 : "v"(zero), "s"(ctx.ws), "v"(pv), "s"(pp) : "memory");
  }
  const rsrc_t rsx = __builtin_amdgcn_make_buffer_rsrc((void*)x_, 0, 2048 * 1024, 0x00020000);
  first_pieces<0, 12>(ctx, rsx, rsa);
  {
    const int u = b * 4 + w;
    pfw((lane & 7) * 128, rsa, (u >> 1) * 3072 + 1024 + (u & 1) * 1024);
  }
  first_pieces<12, PRE0>(ctx, rsx, rsa);
  asm volatile("s_waitcnt vmcnt(%5)" : "+v"(epoch), "+v"(ph0), "+v"(ph1), "+v"(ph2), "+v"(ph3) : "n"(PRE0 + 1) : "memory");
  ctx.T = __builtin_amdgcn_readfirstlane(epoch) + 1u;
  v2u wphi = v2u{ph0 | (ph1 << 16), ph2 | (ph3 << 16)};
  f4 xacc[4] = {{0.f, 0.f, 0.f, 0.f}, {0.f, 0.f, 0.f, 0.f}, {0.f, 0.f, 0.f, 0.f}, {0.f, 0.f, 0.f, 0.f}};
  asm volatile("" : "+a"(xacc[0]), "+a"(xacc[1]), "+a"(xacc[2]), "+a"(xacc[3]), "+a"(wphi));
  const rsrc_t out = __builtin_amdgcn_make_buffer_rsrc((void*)out_, 0, 2048 * 1024, 0x00020000);

#pragma nounroll
  for (int lp = 0; lp < 2; ++lp) {
    asm volatile("" : "+a"(xacc[0]), "+a"(xacc[1]), "+a"(xacc[2]), "+a"(xacc[3]), "+a"(wphi));
    layer_A(ctx, lp, wphi, rwa, rsa, rsb, rsx, rwb);
    out_proj<false>(ctx, lp, xacc, out, rsb, rwb, rsx);
    layer_B(ctx, 2 * lp + 1, rwb);
    out_proj<true>(ctx, lp, xacc, out, rsb, rwb, rsx);
  }
  asm volatile(".Lfk_end:");
  if (b == 0) { ctx.m = 0; flag_store(ctx, WS_EPOCH); }
  STAMP(41); CSTAMP(1);
  STAMP_FLUSH();
}

extern "C" void kernel_launch(void* const* d_in, const int* in_sizes, int n_in, void* d_out, int out_size, void* d_ws,
                              size_t ws_size, hipStream_t stream) {
  (void)in_sizes; (void)n_in; (void)out_size; (void)ws_size;
  (void)hipFuncSetAttribute((const void*)fused_kernel, hipFuncAttributeMaxDynamicSharedMemorySize, LDS_BYTES);
  hipLaunchKernelGGL(fused_kernel, dim3(NWG), dim3(NT), LDS_BYTES, stream,
                     (const unsigned char*)d_in[0], (const unsigned char*)d_in[1], (const unsigned char*)d_in[2],
                     (const unsigned char*)d_in[3], (const unsigned char*)d_in[5], (const unsigned char*)d_in[6],
                     (unsigned char*)d_out, (unsigned char*)d_ws);
}
```

```cpp
#include <hip/hip_runtime.h>
#include <hip/hip_bf16.h>
#include <type_traits>

#define GAS __attribute__((address_space(1)))
#define LAS __attribute__((address_space(3)))
#define DEV __device__ __forceinline__
typedef short s4 __attribute__((ext_vector_type(4)));
typedef float f4 __attribute__((ext_vector_type(4)));
typedef float f2 __attribute__((ext_vector_type(2)));
typedef __bf16 bf2 __attribute__((ext_vector_type(2)));
typedef unsigned v4u __attribute__((ext_vector_type(4)));
typedef unsigned v2u __attribute__((ext_vector_type(2)));
typedef unsigned long long u64;
typedef GAS unsigned gu32;
typedef LAS unsigned char lds_t;
typedef __amdgpu_buffer_rsrc_t rsrc_t;
#define RLX_AGENT __ATOMIC_RELAXED, __HIP_MEMORY_SCOPE_AGENT

namespace {
constexpr int NWG = 256, NT = 256;
constexpr int WS_EPOCH = 0;
constexpr int WS_FX = 4096;
constexpr int WS_FO = WS_FX + 2 * 32 * 128;
constexpr int WS_FW = WS_FO + 2 * 32 * 128;
constexpr int WS_KV2 = 1 << 16;
constexpr int WS_KVZ = 1 << 18;
constexpr int WS_STAT = 2 << 18;
constexpr int STAT_L = 32 * 8 * 64 * 16;
constexpr int WS_STAMP = 4 << 18;
constexpr int WS_X = 2 << 20;
constexpr int ACT_L = 2 << 20;
constexpr int WS_O = WS_X + 2 * ACT_L;
constexpr int WS_KV1 = WS_O + ACT_L;
constexpr int WS_FR = WS_KV1 + 8 * 64 * 32 * 256;
constexpr int WS_END = WS_FR + 8 * 4 * 48 * 1024;
constexpr int L_XT = 0;
constexpr int L_RQ = 81920;
constexpr int L_REC = 147456 + 256;
constexpr int L_NM = L_REC + 1280;
constexpr int L_RS = L_NM + 320;
constexpr int L_CS = L_RS + 320;
constexpr int L_END = L_CS + 4 * 2 * 3 * 64;
constexpr int L_IMG = 151552;
constexpr int LDS_BYTES = 160 * 1024;
static_assert(L_END <= L_IMG, "lds");
constexpr int SPIN_CAP = 1 << 18;
constexpr float LOG2E = 1.4426950408889634f;
#define RB 0
#define ST_DLY 4
#define UPOS 12
#define PF_U 1
#define PF_B 0
#define PF_KV 0
#define PF_LATE 1
#define PRE0 16
#define QPOLL 6
#define S2POLL 0
}

DEV float asf(unsigned u) { return __builtin_bit_cast(float, u); }
DEV unsigned asu(float f) { return __builtin_bit_cast(unsigned, f); }
DEV unsigned pk2(float a, float b) { unsigned r; asm("v_cvt_pk_bf16_f32 %0, %1, %2" : "=v"(r) : "v"(a), "v"(b)); return r; }
DEV float xg_sum(float v) {
  auto r = __builtin_amdgcn_permlane16_swap(asu(v), asu(v), false, false);
  v = asf(r[0]) + asf(r[1]);
  auto q = __builtin_amdgcn_permlane32_swap(asu(v), asu(v), false, false);
  return asf(q[0]) + asf(q[1]);
}
DEV float row_sum(float v) {
  v += asf(__builtin_amdgcn_update_dpp(0u, asu(v), 0x128, 0xf, 0xf, false));
  v += asf(__builtin_amdgcn_update_dpp(0u, asu(v), 0x124, 0xf, 0xf, false));
  v += asf(__builtin_amdgcn_update_dpp(0u, asu(v), 0x122, 0xf, 0xf, false));
  v += asf(__builtin_amdgcn_update_dpp(0u, asu(v), 0x121, 0xf, 0xf, false));
  return v;
}
DEV float sum32(float v) {
  v = row_sum(v);
  auto r = __builtin_amdgcn_permlane16_swap(asu(v), asu(v), false, false);
  return asf(r[0]) + asf(r[1]);
}
#define BAR() asm volatile("s_waitcnt lgkmcnt(0)\n\ts_barrier" ::: "memory")
#define RAWBAR() do { asm volatile("" ::: "memory"); __builtin_amdgcn_s_barrier(); asm volatile("" ::: "memory"); } while (0)
#define DRAIN() asm volatile("s_waitcnt vmcnt(0)" ::: "memory")
#define WAIT_VM(n) asm volatile("s_waitcnt vmcnt(%0)" :: "n"(n) : "memory")
#define WAIT_LG(n) __builtin_amdgcn_s_waitcnt(0xC07F | ((n) << 8))
#define WAIT_VMV(n) __builtin_amdgcn_s_waitcnt(0x0F70 | ((n) & 15) | (((n) >> 4) << 14))
#define VNOP() __builtin_amdgcn_s_setprio(0)
#define SB() __builtin_amdgcn_sched_barrier(0)
#define NOP(n) asm volatile("s_nop %0" :: "n"(n))
#define SC1 16
#define ONE2 0x3f803f80u
template <int N> using IC = std::integral_constant<int, N>;

DEV unsigned mk1() { unsigned r; asm volatile("v_mov_b32 %0, 0x3f803f80" : "=v"(r)); return r; }
DEV unsigned mk0() { unsigned r; asm volatile("v_mov_b32 %0, 0" : "=v"(r)); return r; }
DEV int opq(int v) { asm volatile("" : "+v"(v)); return v; }
DEV v4u lds_ld128(lds_t* L, int off) { return *(LAS v4u*)(L + off); }
DEV f4 lds_ldf4(lds_t* L, int off) { return *(LAS f4*)(L + off); }
DEV float lds_ldf(lds_t* L, int off) { return *(LAS float*)(L + off); }
DEV v4u tr_frag(lds_t* L, int off) {
  s4 a = __builtin_amdgcn_ds_read_tr16_b64_v4i16((LAS s4*)(L + off));
  s4 b = __builtin_amdgcn_ds_read_tr16_b64_v4i16((LAS s4*)(L + off + 512));
  v2u ua = __builtin_bit_cast(v2u, a), ub = __builtin_bit_cast(v2u, b);
  return v4u{ua.x, ua.y, ub.x, ub.y};
}
template <int I, int N, class F> DEV void sfor(F&& f) { if constexpr (I < N) { f(IC<I>{}); sfor<I + 1, N>(f); } }

constexpr int P_AK = 0, P_AV = 16, P_BQ = 32, P_BK = 48, P_BV = 64;
#define MF32 "v_mfma_f32_16x16x32_bf16 "
#define MF16 "v_mfma_f32_16x16x16_bf16 "
#define MM_CASE(WC, XC)                                                                                                         \
  if constexpr (WA) {                                                                                                           \
    if constexpr (FIRST) asm volatile(MF32 "%0, " WC "[%c1:%c2], " XC "[%c3:%c4], 0" : "=v"(acc) : "n"(R), "n"(R + 3), "n"(X), "n"(X + 3)); \
    else asm volatile(MF32 "%0, " WC "[%c1:%c2], " XC "[%c3:%c4], %0" : "+v"(acc) : "n"(R), "n"(R + 3), "n"(X), "n"(X + 3));     \
  } else {                                                                                                                      \
    if constexpr (FIRST) asm volatile(MF32 "%0, " XC "[%c3:%c4], " WC "[%c1:%c2], 0" : "=v"(acc) : "n"(R), "n"(R + 3), "n"(X), "n"(X + 3)); \
    else asm volatile(MF32 "%0, " XC "[%c3:%c4], " WC "[%c1:%c2], %0" : "+v"(acc) : "n"(R), "n"(R + 3), "n"(X), "n"(X + 3));     \
  }
template <int F, int RS, bool WA, bool FIRST>
DEV void mm(f4& acc) {
  constexpr int R = 96 + 4 * (F < 40 ? F : F - 40), X = 88 + 4 * (RS & 1);
  if constexpr (F < 40) { if constexpr (RS < 2) { MM_CASE("v", "v") } else { MM_CASE("v", "a") } }
  else { if constexpr (RS < 2) { MM_CASE("a", "v") } else { MM_CASE("a", "a") } }
}
template <int RS, int OFF>
DEV void ring_rd(unsigned addr) {
  constexpr int X = 88 + 4 * (RS & 1);
  if constexpr (RS < 2) asm volatile("ds_read_b128 v[%c1:%c2], %0 offset:%c3" :: "v"(addr), "n"(X), "n"(X + 3), "n"(OFF) : "memory");
  else asm volatile("ds_read_b128 a[%c1:%c2], %0 offset:%c3" :: "v"(addr), "n"(X), "n"(X + 3), "n"(OFF) : "memory");
}
template <int F, bool FIRST>
DEV void wcs(f4& acc, v4u x) {
  constexpr int R = 96 + 4 * (F < 40 ? F : F - 40);
  if constexpr (F < 40) {
    if constexpr (FIRST) asm volatile(MF32 "%0, %1, v[%c2:%c3], 0" : "=&v"(acc) : "v"(x), "n"(R), "n"(R + 3));
    else asm volatile(MF32 "%0, %1, v[%c2:%c3], %0" : "+v"(acc) : "v"(x), "n"(R), "n"(R + 3));
  } else {
    if constexpr (FIRST) asm volatile(MF32 "%0, %1, a[%c2:%c3], 0" : "=&v"(acc) : "v"(x), "n"(R), "n"(R + 3));
    else asm volatile(MF32 "%0, %1, a[%c2:%c3], %0" : "+v"(acc) : "v"(x), "n"(R), "n"(R + 3));
  }
}
template <int F, bool FIRST>
DEV void wxa(f4& acc, v4u x) {
  constexpr int R = 96 + 4 * (F < 40 ? F : F - 40);
  static_assert(F < 40, "");
  if constexpr (FIRST) asm volatile(MF32 "%0, v[%c2:%c3], %1, 0" : "=v"(acc) : "a"(x), "n"(R), "n"(R + 3));
  else asm volatile(MF32 "%0, v[%c2:%c3], %1, %0" : "+v"(acc) : "a"(x), "n"(R), "n"(R + 3));
}
template <int F, bool FIRST>
DEV void wxb(f4& acc, v4u x) {
  constexpr int R = 96 + 4 * (F < 40 ? F : F - 40);
  static_assert(F < 40, "");
  if constexpr (FIRST) asm volatile(MF32 "%0, %1, v[%c2:%c3], 0" : "=v"(acc) : "a"(x), "n"(R), "n"(R + 3));
  else asm volatile(MF32 "%0, %1, v[%c2:%c3], %0" : "+v"(acc) : "a"(x), "n"(R), "n"(R + 3));
}
template <bool FIRST>
DEV void mf_aaz(f4& acc, v4u a, v4u b) {
  if constexpr (FIRST) asm volatile(MF32 "%0, %1, %2, 0" : "=v"(acc) : "a"(a), "a"(b));
  else asm volatile(MF32 "%0, %1, %2, %0" : "+v"(acc) : "a"(a), "a"(b));
}
template <int RS, bool FIRST>
DEV void mmq(f4& acc, v4u a) {
  constexpr int X = 88 + 4 * (RS & 1);
  if constexpr (RS < 2) {
    if constexpr (FIRST) asm volatile(MF32 "%0, %1, v[%c2:%c3], 0" : "=v"(acc) : "a"(a), "n"(X), "n"(X + 3));
    else asm volatile(MF32 "%0, %1, v[%c2:%c3], %0" : "+v"(acc) : "a"(a), "n"(X), "n"(X + 3));
  } else {
    if constexpr (FIRST) asm volatile(MF32 "%0, %1, a[%c2:%c3], 0" : "=v"(acc) : "a"(a), "n"(X), "n"(X + 3));
    else asm volatile(MF32 "%0, %1, a[%c2:%c3], %0" : "+v"(acc) : "a"(a), "n"(X), "n"(X + 3));
  }
}
template <bool FIRST>
DEV void mf_av(f4& acc, v4u a, v4u b) {
  if constexpr (FIRST) asm volatile(MF32 "%0, %1, %2, 0" : "=&v"(acc) : "a"(a), "v"(b));
  else asm volatile(MF32 "%0, %1, %2, %0" : "+v"(acc) : "a"(a), "v"(b));
}
DEV void mf_va(f4& acc, v4u a, v4u b) { asm volatile(MF32 "%0, %1, %2, %0" : "+v"(acc) : "v"(a), "a"(b)); }
DEV void mf_aa(f4& acc, v4u a, v4u b) { asm volatile(MF32 "%0, %1, %2, %0" : "+v"(acc) : "a"(a), "a"(b)); }
DEV void mf32z(f4& d, v4u a, v4u b) { asm volatile(MF32 "%0, %1, %2, 0" : "=&v"(d) : "v"(a), "v"(b)); VNOP(); }
DEV void mf32c(f4& d, v4u a, v4u b) { asm volatile(MF32 "%0, %1, %2, %0" : "+v"(d) : "v"(a), "v"(b)); VNOP(); }
DEV void mf16z(f4& d, v2u a, v2u b) { asm volatile(MF16 "%0, %1, %2, 0" : "=&v"(d) : "v"(a), "v"(b)); VNOP(); }
DEV void mf16c(f4& d, v2u a, v2u b) { asm volatile(MF16 "%0, %1, %2, %0" : "+v"(d) : "v"(a), "v"(b)); VNOP(); }
DEV void mf16z_a(f4& d, v2u a, v2u b) { asm volatile(MF16 "%0, %1, %2, 0" : "=&v"(d) : "a"(a), "v"(b)); VNOP(); }
DEV void mf16c_a(f4& d, v2u a, v2u b) { asm volatile(MF16 "%0, %1, %2, %0" : "+v"(d) : "a"(a), "v"(b)); VNOP(); }
DEV void mf16z_b(f4& d, v2u a, v2u b) { asm volatile(MF16 "%0, %1, %2, 0" : "=&v"(d) : "v"(a), "a"(b)); VNOP(); }
template <int F, int OFF>
DEV void wtr(unsigned addr) {
  constexpr int R = 96 + 4 * (F < 40 ? F : F - 40);
  if constexpr (F < 40)
    asm volatile("ds_read_b64_tr_b16 v[%c1:%c2], %0 offset:%c5\n\tds_read_b64_tr_b16 v[%c3:%c4], %0 offset:%c6"
                 :: "v"(addr), "n"(R), "n"(R + 1), "n"(R + 2), "n"(R + 3), "n"(OFF), "n"(OFF + 512) : "memory");
  else
    asm volatile("ds_read_b64_tr_b16 a[%c1:%c2], %0 offset:%c5\n\tds_read_b64_tr_b16 a[%c3:%c4], %0 offset:%c6"
                 :: "v"(addr), "n"(R), "n"(R + 1), "n"(R + 2), "n"(R + 3), "n"(OFF), "n"(OFF + 512) : "memory");
}
#define A_FMA(d, a, b, c) asm volatile("v_fma_f32 %0, %1, %2, %3" : "=v"(d) : "v"(a), "v"(b), "v"(c))
#define A_FMAN(d, a, b, c) asm volatile("v_fma_f32 %0, %1, -%2, %3" : "=v"(d) : "v"(a), "v"(b), "v"(c))
#define A_MUL(d, a, b) asm volatile("v_mul_f32 %0, %1, %2" : "=v"(d) : "v"(a), "v"(b))
#define A_MULS(d, k, b) asm volatile("v_mul_f32 %0, %1, %2" : "=v"(d) : "s"(k), "v"(b))
#define A_MAX(d, a, b) asm volatile("v_max_f32 %0, %1, %2" : "=v"(d) : "v"(a), "v"(b))
#define A_SUB(d, a, b) asm volatile("v_sub_f32 %0, %1, %2" : "=v"(d) : "v"(a), "v"(b))
#define A_CVT(d, a, b) asm volatile("v_cvt_pk_bf16_f32 %0, %1, %2" : "=v"(d) : "v"(a), "v"(b))
#define A_EXP(d, a) asm volatile("v_exp_f32 %0, %1" : "=v"(d) : "v"(a))
#define A_RCP(d, x4) asm volatile("v_rcp_f32 %0, %1" : "=&v"(d) : "v"(x4[0]), "v"(x4))
#define A_SEL(d, f, t, m) asm volatile("v_cndmask_b32 %0, %1, %2, %3" : "=v"(d) : "v"(f), "v"(t), "s"(m))
#define A_PERM(d, x, sel) asm volatile("v_perm_b32 %0, %1, %1, %2" : "=v"(d) : "v"(x), "v"(sel))
#define A_SW16(a, b) asm volatile("v_permlane16_swap_b32 %0, %1" : "+v"(a), "+v"(b))
#define A_SW32(a, b) asm volatile("v_permlane32_swap_b32 %0, %1" : "+v"(a), "+v"(b))
#define A_LD64(d, addr, off) asm volatile("ds_read_b64 %0, %1 offset:%2" : "=v"(d) : "v"(addr), "n"(off) : "memory")
#define A_LD128A(d, addr, off) asm volatile("ds_read_b128 %0, %1 offset:%2" : "=a"(d) : "v"(addr), "n"(off) : "memory")
#define A_LD128(d, addr, off) asm volatile("ds_read_b128 %0, %1 offset:%2" : "=v"(d) : "v"(addr), "n"(off) : "memory")

#define STAMP(i) do {} while (0)
#define CSTAMP(i) do {} while (0)
#define STAMP_FLUSH() do {} while (0)
#define TS(k) do {} while (0)
#define TS_DECL
#define TS_FLUSH(base, n) do {} while (0)

struct Ctx {
  GAS unsigned char* ws;
  rsrc_t rs;
  lds_t* L;
  unsigned Lb, Lw;
  int c, m, w;
  int lane, tok, g;
  unsigned T;
  int budget;
};
DEV void fresh(Ctx& ctx) {
  int l;
  asm volatile("v_mbcnt_lo_u32_b32 %0, -1, 0\n\tv_mbcnt_hi_u32_b32 %0, -1, %0" : "=v"(l));
  ctx.lane = l; ctx.tok = l & 15; ctx.g = l >> 4;
  asm volatile("" : "+s"(ctx.c), "+s"(ctx.m), "+s"(ctx.w), "+s"(ctx.Lb));
  ctx.Lw = ctx.Lb + 4096 * ctx.w;
}
DEV v4u ld128(Ctx& ctx, int voff, int soff) { return __builtin_amdgcn_raw_buffer_load_b128(ctx.rs, voff, soff, SC1); }
DEV void st128(Ctx& ctx, v4u v, int voff, int soff) { __builtin_amdgcn_raw_buffer_store_b128(v, ctx.rs, voff, soff, SC1); }
DEV void st64(Ctx& ctx, v2u v, int voff, int soff) { __builtin_amdgcn_raw_buffer_store_b64(v, ctx.rs, voff, soff, SC1); }

DEV unsigned bld32(Ctx& ctx, int voff, int soff) {
  unsigned r;
  asm volatile("s_nop 4\n\tbuffer_load_dword %0, %1, %2, %3 offen sc1\n\ts_waitcnt vmcnt(0)" : "=&v"(r) : "v"(voff), "s"(ctx.rs), "s"(soff) : "memory");
  return r;
}
template <int O1, int O2, int O3>
DEV void bld128x4(Ctx& ctx, v4u (&d)[4], int voff, int soff) {
  asm volatile("s_nop 4\n\tbuffer_load_dwordx4 %0, %4, %5, %6 offen sc1\n\tbuffer_load_dwordx4 %1, %4, %5, %6 offen offset:%7 sc1\n\t"
               "buffer_load_dwordx4 %2, %4, %5, %6 offen offset:%8 sc1\n\tbuffer_load_dwordx4 %3, %4, %5, %6 offen offset:%9 sc1\n\ts_waitcnt vmcnt(0)"
               : "=&v"(d[0]), "=&v"(d[1]), "=&v"(d[2]), "=&v"(d[3]) : "v"(voff), "s"(ctx.rs), "s"(soff), "n"(O1), "n"(O2), "n"(O3) : "memory");
}
DEV void wait_flags(Ctx& ctx, int off0, int off1, bool two) {
  if (ctx.w == 0) {
    const int l = ctx.lane & (two ? 15 : 7);
    const int vo = (l < 8 ? off0 : off1 - 128) + l * 16;
    for (;;) {
      const unsigned v = bld32(ctx, vo, 0);
      if (__all(v == ctx.T) || --ctx.budget < 0) break;
    }
    __builtin_amdgcn_fence(__ATOMIC_ACQUIRE, "wavefront");
  }
  BAR();
}
DEV void flag_store(Ctx& ctx, int off) {
  if (ctx.w == 0) {
    unsigned tv, zv;
    asm volatile("v_mov_b32 %0, %2\n\tv_mov_b32 %1, 0" : "=v"(tv), "=v"(zv) : "s"(ctx.T));
    __builtin_amdgcn_raw_buffer_store_b32(tv, ctx.rs, zv, off + ctx.m * 16, SC1);
  }
}

DEV int x0_lane(int tok, int g) { return tok * 64 + ((g ^ ((4 - (tok >> 2)) & 3)) << 4); }
DEV int tr_base(int lane, int j) {
  const int g = lane >> 4, q = (lane & 15) >> 2, p = lane & 3;
  const int s = ((q >> 1) & 1) | ((g & 1) << 1);
  return (8 * g + q) * 128 + ((j ^ s) << 5) + p * 8;
}
template <int F>
DEV void raw_ld1(unsigned voff, rsrc_t rs, int soff) {
  constexpr int R = 96 + 4 * (F < 40 ? F : F - 40);
  if constexpr (F < 40) asm volatile("buffer_load_dwordx4 v[%c3:%c4], %0, %1, %2 offen" :: "v"(voff), "s"(rs), "s"(soff), "n"(R), "n"(R + 3) : "memory");
  else asm volatile("buffer_load_dwordx4 a[%c3:%c4], %0, %1, %2 offen" :: "v"(voff), "s"(rs), "s"(soff), "n"(R), "n"(R + 3) : "memory");
}
template <int P, int I0, int I1>
DEV void raw_load(Ctx& ctx, rsrc_t rs, int colbyte) {
  const unsigned vo = (unsigned)((ctx.lane >> 3) * 3072 + (ctx.lane & 7) * 16);
  const int sb = colbyte + ctx.m * 128 + 32 * ctx.w * 3072;
  sfor<I0, I1>([&](auto i) { raw_ld1<P + i>(vo, rs, sb + (128 * (i >> 2) + 8 * (i & 3)) * 3072); });
}
template <int F, int OFF>
DEV void raw_st1(unsigned addr) {
  constexpr int R = 96 + 4 * (F < 40 ? F : F - 40);
  if constexpr (F < 40) asm volatile("ds_write_b128 %0, v[%c1:%c2] offset:%c3" :: "v"(addr), "n"(R), "n"(R + 3), "n"(OFF) : "memory");
  else asm volatile("ds_write_b128 %0, a[%c1:%c2] offset:%c3" :: "v"(addr), "n"(R), "n"(R + 3), "n"(OFF) : "memory");
}
template <int P, int I0, int I1>
DEV void raw_store(Ctx& ctx) {
  const int lane = ctx.lane, ch = lane & 7, rb1 = (lane >> 4) & 1;
  const unsigned base = ctx.Lb + L_RQ + (32 * ctx.w + (lane >> 3)) * 128 + (ch & 1) * 16;
  const unsigned p0 = base + (((ch >> 1) ^ rb1) << 5), p1 = base + (((ch >> 1) ^ (rb1 | 2)) << 5);
  sfor<I0, I1>([&](auto i) { raw_st1<P + i, (i >> 2) * 16384 + (i & 3) * 1024>((i & 1) ? p1 : p0); });
}
template <int P, int K0, int K1>
DEV void part_tr(Ctx& ctx) {
  const unsigned tb = ctx.Lb + L_RQ + tr_base(ctx.lane, ctx.w);
  sfor<K0, K1>([&](auto ks) { wtr<P + ks, ks * 4096>(tb); });
}
template <int P>
DEV void part_cs(Ctx& ctx, int csoff) {
  v4u ones = {mk1(), mk1(), mk1(), mk1()};
  asm volatile("s_waitcnt lgkmcnt(0)\n\ts_nop 1" : "+v"(ones) :: "memory");
  f4 c0, c1;
  sfor<0, 8>([&](auto k) { wcs<P + 2 * k, k == 0>(c0, ones); wcs<P + 2 * k + 1, k == 0>(c1, ones); });
  asm volatile("s_nop 7" : "+v"(c0), "+v"(c1));
  if (ctx.g == 0) *(LAS float*)(ctx.L + csoff + ctx.tok * 4) = c0[0] + c1[0];
}

template <int P>
DEV void part_step(Ctx& ctx, int csoff) {
  raw_store<P, 0, 8>(ctx);
  BAR();
  part_tr<P, 0, 8>(ctx);
  raw_store<P, 8, 16>(ctx);
  BAR();
  part_tr<P, 8, 16>(ctx);
  part_cs<P>(ctx, csoff);
}

template <int P, int PN>
DEV void part_step_ld(Ctx& ctx, int csoff, rsrc_t rs, int colbyte) {
  raw_load<PN, 0, 4>(ctx, rs, colbyte);
  raw_store<P, 0, 8>(ctx);
  raw_load<PN, 4, 8>(ctx, rs, colbyte);
  BAR();
  part_tr<P, 0, 8>(ctx);
  raw_load<PN, 8, 12>(ctx, rs, colbyte);
  raw_store<P, 8, 16>(ctx);
  raw_load<PN, 12, 16>(ctx, rs, colbyte);
  BAR();
  part_tr<P, 8, 16>(ctx);
  part_cs<P>(ctx, csoff);
}
template <int IM>
DEV void fr_ld(unsigned lv, rsrc_t rs, int so) {
  constexpr int F = P_BQ + IM, R = 96 + 4 * (F < 40 ? F : F - 40);
  if constexpr (F < 40) asm volatile("buffer_load_dwordx4 v[%c3:%c4], %0, %1, %2 offen offset:%5 sc1" :: "v"(lv), "s"(rs), "s"(so), "n"(R), "n"(R + 3), "n"((IM & 3) * 1024) : "memory");
  else asm volatile("buffer_load_dwordx4 a[%c3:%c4], %0, %1, %2 offen offset:%5 sc1" :: "v"(lv), "s"(rs), "s"(so), "n"(R), "n"(R + 3), "n"((IM & 3) * 1024) : "memory");
}
template <int I0, int I1>
DEV void fr_load(Ctx& ctx) {
  const int fb = WS_FR + (ctx.m * 4 + ctx.w) * 49152;
  const unsigned lv = ctx.lane * 16;
  sfor<I0, I1>([&](auto i) { fr_ld<i>(lv, ctx.rs, fb + (i & ~3) * 1024); });
}
template <int OFF>
DEV void pf1(unsigned lv, rsrc_t rs, int so) {
  asm volatile("buffer_load_dwordx4 v[224:227], %0, %1, %2 offen offset:%3" :: "v"(lv), "s"(rs), "s"(so), "n"(OFF) : "memory");
}

template <int SINK = 224>
DEV void pfw(unsigned vo, rsrc_t rs, int so) {
  so = __builtin_amdgcn_readfirstlane(so);
  asm volatile("s_nop 4\n\t" "buffer_load_dword v%c3, %0, %1, %2 offen" :: "v"(vo), "s"(rs), "s"(so), "n"(SINK) : "memory");
}
DEV void readback(Ctx& ctx, int vo, int so) {
  asm volatile("buffer_load_dword v88, %0, %1, %2 offen sc1" :: "v"(vo), "s"(ctx.rs), "s"(so) : "memory");
}
template <int ROWB, int SINK = 224>
DEV void pf32(Ctx& ctx, rsrc_t rs, int col) {
  const int row = 16 * (int)(blockIdx.x >> 3) + 4 * ctx.w;
  pfw<SINK>((unsigned)(((ctx.lane >> 3) & 3) * ROWB + (ctx.lane & 7) * 128), rs, col + row * ROWB);
}
template <int ROWB0, int ROWB1>
DEV void pf_pair(Ctx& ctx, rsrc_t rs0, int col0, rsrc_t rs1, int col1) {
  const int row = 16 * (int)(blockIdx.x >> 3) + 8 * (ctx.w & 1);
  if (ctx.w < 2) pfw((unsigned)((ctx.lane >> 3) * ROWB0 + (ctx.lane & 7) * 128), rs0, col0 + row * ROWB0);
  else pfw((unsigned)((ctx.lane >> 3) * ROWB1 + (ctx.lane & 7) * 128), rs1, col1 + row * ROWB1);
}

struct ImgDma { unsigned v[4]; int sb; };
template <int ROWB>
DEV ImgDma img_prep(Ctx& ctx) {
  const int lane = ctx.lane, cp = lane & 7, rb1 = (lane >> 4) & 1;
  const unsigned vb = (unsigned)((lane >> 3) * ROWB + (cp & 1) * 16);
  const unsigned v0 = vb + (((cp >> 1) ^ rb1) << 5), v1 = vb + (((cp >> 1) ^ (rb1 | 2)) << 5);
  ImgDma d;
  d.v[0] = v0; d.v[1] = v1 + (8 * ROWB - 1024); d.v[2] = v0 + 2 * (8 * ROWB - 1024); d.v[3] = v1 + 3 * (8 * ROWB - 1024);
  d.sb = ctx.m * 128 + 32 * ctx.w * ROWB;
  return d;
}
template <int LOFF> DEV void dma_m0(unsigned lbase) { asm volatile("s_add_u32 m0, %0, %1" :: "s"(lbase), "n"(LOFF) : "scc"); }
template <int SOFF> DEV void dma_so(int& t, int sbase) { asm volatile("s_add_u32 %0, %1, %2" : "=s"(t) : "s"(sbase), "n"(SOFF) : "scc"); }
template <int I> DEV void dma_go(unsigned voff, rsrc_t rs, int so) {
  asm volatile("buffer_load_dwordx4 %0, %1, %2 offen offset:%3 lds" :: "v"(voff), "s"(rs), "s"(so), "n"(I * 1024) : "memory");
}
template <int I> DEV void dma_go_sc1(unsigned voff, rsrc_t rs, int so) {
  asm volatile("buffer_load_dwordx4 %0, %1, %2 offen offset:%3 sc1 lds" :: "v"(voff), "s"(rs), "s"(so), "n"(I * 1024) : "memory");
}
template <int SOFF, int LOFF>
DEV void dma1(unsigned voff, rsrc_t rs, int sbase, unsigned lbase) {
  int t;
  asm volatile("s_add_u32 m0, %4, %6\n\ts_add_u32 %0, %3, %5\n\tbuffer_load_dwordx4 %1, %2, %0 offen lds"
               : "=&s"(t) : "v"(voff), "s"(rs), "s"(sbase), "s"(lbase), "n"(SOFF), "n"(LOFF) : "memory", "scc");
}
template <int ROWB, int COLB, int LDSOFF, int J>
DEV void img_chunk(Ctx& ctx, const ImgDma& d, rsrc_t rs) {
  int so;
  dma_m0<LDSOFF + J * 16384>(ctx.Lw); dma_so<COLB + 128 * J * ROWB>(so, d.sb);
  sfor<0, 4>([&](auto i) { dma_go<i>(d.v[i], rs, so); });
}
template <int TT>
DEV void x_chunk(Ctx& ctx, unsigned lv, int xs) {
  int so;
  dma_m0<L_XT + (TT + 1) * 16384>(ctx.Lw); dma_so<(TT + 1) * 16384>(so, xs);
  sfor<0, 4>([&](auto i) { dma_go_sc1<i>(lv, ctx.rs, so); });
}

struct ST {
  v4u d[4];
  float s, q, t, bias;
  unsigned bad, sv, wrec, wnm;
  int so;
};
template <bool HALO>
DEV void st_prep(Ctx& ctx, ST& st, int L) {
  (void)L;
  const int c = ctx.c, w = ctx.w, r32 = ctx.lane & 31, hf = ctx.lane >> 5;
  const bool halo = HALO && w == 3;
  const int cc = (halo && c > 0) ? c - 1 : c;
  const int row0 = halo ? (c > 0 ? 48 : 0) : 32 * (w & 1);
  const int r = halo ? (r32 & 15) : r32;
  st.so = WS_STAT + (HALO ? 0 : STAT_L) + (cc * 8 * 64 + row0) * 16;
  st.sv = hf * 4096 + r * 16;
  const int lrow = halo ? r : 16 + row0 + r;
  st.wrec = ctx.Lb + L_REC + lrow * 16;
  st.wnm = ctx.Lb + L_NM + lrow * 4;
  st.bias = (halo && c == 0) ? -__builtin_inff() : 0.f;
  asm volatile("" : "+v"(st.sv), "+v"(st.wrec), "+v"(st.wnm), "+v"(st.bias));
}
DEV void st_issue(Ctx& ctx, ST& st) {
  asm volatile("buffer_load_dwordx4 %0, %4, %5, %6 offen sc1\n\t"
               "buffer_load_dwordx4 %1, %4, %5, %6 offen offset:1024 sc1\n\t"
               "buffer_load_dwordx4 %2, %4, %5, %6 offen offset:2048 sc1\n\t"
               "buffer_load_dwordx4 %3, %4, %5, %6 offen offset:3072 sc1"
               : "=&v"(st.d[0]), "=&v"(st.d[1]), "=&v"(st.d[2]), "=&v"(st.d[3]) : "v"(st.sv), "s"(ctx.rs), "s"(st.so) : "memory");
}
DEV void st_retry(Ctx& ctx, ST& st) {
  for (;;) {
    unsigned bad = 0;
    bld128x4<1024, 2048, 3072>(ctx, st.d, st.sv, st.so);
#pragma unroll
    for (int i = 0; i < 4; ++i) bad |= (st.d[i].y ^ ctx.T) | (st.d[i].w ^ ctx.T);
    if (__all(bad == 0) || --ctx.budget < 0) break;
  }
}
DEV void st_check(Ctx& ctx, ST& st) {
  asm volatile("" : "+v"(st.d[0]), "+v"(st.d[1]), "+v"(st.d[2]), "+v"(st.d[3]));
  const unsigned T = ctx.T;
  const bool ok = (st.d[0].y == T) & (st.d[0].w == T) & (st.d[1].y == T) & (st.d[1].w == T) &
                  (st.d[2].y == T) & (st.d[2].w == T) & (st.d[3].y == T) & (st.d[3].w == T);
  if (__builtin_expect(!__all(ok), 0)) st_retry(ctx, st);
}
template <int K>
DEV void st_step(ST& st) {
  if constexpr (K == 0) asm volatile("v_add_f32 %0, %2, %3\n\tv_add_f32 %1, %4, %5" : "=&v"(st.s), "=&v"(st.q) : "v"(st.d[0].x), "v"(st.d[1].x), "v"(st.d[0].z), "v"(st.d[1].z));
  if constexpr (K == 1) asm volatile("v_add_f32 %0, %0, %2\n\tv_add_f32 %1, %1, %3" : "+v"(st.s), "+v"(st.q) : "v"(st.d[2].x), "v"(st.d[2].z));
  if constexpr (K == 2) asm volatile("v_add_f32 %0, %0, %2\n\tv_add_f32 %1, %1, %3" : "+v"(st.s), "+v"(st.q) : "v"(st.d[3].x), "v"(st.d[3].z));
  if constexpr (K == 3) A_SW32(st.s, st.q);
  if constexpr (K == 4) { float x = st.s, y = st.q; asm volatile("v_add_f32 %0, %2, %3\n\tv_add_f32 %1, %2, %3" : "=&v"(st.s), "=&v"(st.q) : "v"(x), "v"(y)); }
  if constexpr (K == 5) A_SW32(st.s, st.q);
  if constexpr (K == 6) asm volatile("v_mul_f32 %0, %2, %0\n\tv_mul_f32 %1, %3, %1" : "+v"(st.s), "+v"(st.q) : "s"(-1.0f / 512.0f), "s"(1.0f / 512.0f));
  if constexpr (K == 7) asm volatile("v_fma_f32 %0, -%1, %1, %0\n\tv_max_f32 %0, 0, %0" : "+v"(st.q) : "v"(st.s));
  if constexpr (K == 8) asm volatile("v_add_f32 %0, %1, %0" : "+v"(st.q) : "s"(1e-5f));
  if constexpr (K == 9) asm volatile("v_rsq_f32 %0, %0" : "+v"(st.q));
  if constexpr (K == 10) asm volatile("v_mov_b32 %0, %1" : "=v"(st.t) : "v"(st.q));
  if constexpr (K == 11) asm volatile("ds_write_b128 %0, %1" :: "v"(st.wrec), "v"(f4{st.s, st.q, st.t, st.bias}) : "memory");
  if constexpr (K == 12) asm volatile("ds_write2_b32 %0, %1, %2 offset1:80" :: "v"(st.wnm), "v"(st.s), "v"(st.q) : "memory");
}

template <bool HALO>
DEV void st_begin(Ctx& ctx, ST& st) {
  st_prep<HALO>(ctx, st, 0);
  if (ctx.w != 0) st_issue(ctx, st);
}
template <bool HALO>
DEV void gather_start(Ctx& ctx, ST& st, int L, unsigned lv, int xs) {
  const int c = ctx.c;
  const int cprev = c > 0 ? c - 1 : 0;
  constexpr int T0 = HALO ? -1 : 0;
  const int xs0 = (HALO && c == 0) ? xs + 16384 : xs;
  if (ctx.w != 0) {
    DRAIN();
    st_check(ctx, st);
    st_step<0>(st); st_step<1>(st); st_step<2>(st); NOP(1); st_step<3>(st); st_step<4>(st); NOP(1); st_step<5>(st);
    st_step<6>(st); st_step<7>(st); st_step<8>(st); st_step<9>(st); NOP(0); st_step<10>(st); st_step<12>(st); st_step<11>(st);
  }
  wait_flags(ctx, WS_FX + ((HALO ? 0 : 32) + c) * 128, WS_FX + ((HALO ? 0 : 32) + cprev) * 128, HALO);
  STAMP(L * 8 + 0);
  x_chunk<T0>(ctx, lv, xs0);
  x_chunk<T0 + 1>(ctx, lv, xs);
  WAIT_VM(4);
  RAWBAR();
  STAMP(L * 8 + 1);
}

template <int OFF> DEV void ring_prime(unsigned ab) { ring_rd<0, OFF>(ab); ring_rd<1, OFF + 1024>(ab); ring_rd<2, OFF + 2048>(ab); WAIT_LG(2); }
template <int PQ, int PK, int PV, int OFF, bool CINIT, class XL, class SH, class GP>
DEV void gemm_tile(unsigned ab, f4& aq, f4& ak, f4& av, XL&& xl, SH&& sh, GP&& gp) {
  sfor<0, 16>([&](auto ks) {
    constexpr bool F = ks == 0 && !CINIT;
    if constexpr (PQ >= 0) {
      mm<(PQ >= 0 ? PQ : 0) + ks, ks & 3, true, F>(aq);
      if (!sh(IC<4 * ks>{})) VNOP();
      ring_rd<(ks + 3) & 3, OFF + (ks + 3) * 1024>(ab);
      gp(IC<4 * ks>{});
      mm<PK + ks, ks & 3, true, F>(ak);
      if (!sh(IC<4 * ks + 1>{})) VNOP();
      gp(IC<4 * ks + 1>{});
    } else {
      mm<PK + ks, ks & 3, true, F>(ak);
      if (!sh(IC<4 * ks>{})) VNOP();
      ring_rd<(ks + 3) & 3, OFF + (ks + 3) * 1024>(ab);
      gp(IC<4 * ks>{});
    }
    mm<PV + ks, ks & 3, false, F>(av);
    WAIT_LG(2 + decltype(xl(ks))::value);
    gp(IC<4 * ks + 2>{});
    SB();
  });
}
#define GA(k) (4 * (k))
#define GB(k) (4 * (k) + 1)
#define GC(k) (4 * (k) + 2)

struct BE {
  f4 aq, ak, av;
  f2 st, ka, kb2; f4 nm4;
  f2 nst, nka, nkb; f4 nnm4;
  f4 csq, csk; float csv;
  float q[4], k[4], v[4];
  unsigned qb[2], kb[2], vb[2], vb2[2], kprev[2], vprev[2], P[4], pk0, pk, ob[2];
  f4 sp, sc, num, den;
  float a, b, t0, t1, m, m2, ft, fa, fb, pa, pb;
  v2u ones;
  unsigned sPA, sPB, sCA, sCB;
  u64 modd, mA, mB;
  unsigned aT, aF, aKa, aKb;
  int ovo;
};

DEV void layer_B(Ctx& ctx, int L, rsrc_t rwo) {
  fresh(ctx);
  ST st;
  st_begin<true>(ctx, st);
  const int c = ctx.c, w = ctx.w, g = ctx.g, tok = ctx.tok;
  const int csb = L_CS + (w * 6 + 3) * 64;
  const unsigned lv = ctx.lane * 16;
  BE e;
  TS_DECL;
  unsigned ab0, ab1;
  ImgDma wd = img_prep<1024>(ctx);
  {
    const bool odd = tok & 1;
    const int ja = 4 * g + (odd ? 1 : 0), jb = ja + 2;
    const bool curA = ja <= tok, curB = jb <= tok;
    const unsigned selA = odd ? 0x01000c0cu : 0x0c0c0100u, selB = odd ? 0x03020c0cu : 0x0c0c0302u, selZ = 0x0c0c0c0cu;
    e.sPA = curA ? selZ : selA; e.sPB = curB ? selZ : selB; e.sCA = curA ? selA : selZ; e.sCB = curB ? selB : selZ;
    e.modd = __ballot(odd); e.mA = __ballot(curA); e.mB = __ballot(curB);
    e.aT = ctx.Lb + L_REC + tok * 16;
    e.aF = ctx.Lb + L_NM + g * 16;
    e.aKa = ctx.Lb + L_REC + (ja - (curA ? 0 : 16)) * 16 + 8;
    e.aKb = ctx.Lb + L_REC + (jb - (curB ? 0 : 16)) * 16 + 8;
    e.csq = lds_ldf4(ctx.L, csb + g * 16); e.csk = lds_ldf4(ctx.L, csb + 64 + g * 16); e.csv = lds_ldf(ctx.L, csb + 128 + tok * 4);
    e.ones = v2u{mk1(), mk0()};
    e.ovo = ((g >> 1) * 16 + tok) * 16 + (g & 1) * 8;
    ab0 = ctx.Lb + L_XT + lv; ab1 = ab0 + 49152;
    asm volatile("" : "+v"(e.sPA), "+v"(e.sPB), "+v"(e.sCA), "+v"(e.sCB), "+v"(e.csq), "+v"(e.csk), "+v"(e.csv), "+a"(e.ones), "+v"(e.ovo),
                      "+v"(e.aT), "+v"(e.aF), "+v"(e.aKa), "+v"(e.aKb), "+v"(ab0), "+v"(ab1), "+v"(wd.v[0]), "+v"(wd.v[1]), "+v"(wd.v[2]), "+v"(wd.v[3]));
  }
  const int xs = WS_X + c * 65536 + w * 4096 - 16384;
  const int osb = WS_O + (4 * c * 16 + 2 * ctx.m + (w >> 1)) * 1024 + (w & 1) * 512;
  gather_start<true>(ctx, st, L, lv, xs);
  ring_prime<0>(ab0);
  f4 xq, xk, xv, yq, yk, yv;
  int so, oso;
  auto stat_rd = [&](auto S, auto h) {
    (void)e;
    if constexpr (h == GC(6)) { A_LD64(e.nst, e.aT, S * 256); A_LD128(e.nnm4, e.aF, S * 64); }
    if constexpr (h == GA(7)) A_LD64(e.nka, e.aKa, S * 256);
    if constexpr (h == GA(8)) A_LD64(e.nkb, e.aKb, S * 256);
  };
  auto xl = [](auto ks) { return IC<(ks == 7 ? 3 : ks == 8 ? 4 : ks == 9 ? 2 : ks == 10 ? 1 : 0)>{}; };
  auto next = [&](f4& q, f4& k, f4& v) { e.aq = q; e.ak = k; e.av = v; e.st = e.nst; e.ka = e.nka; e.kb2 = e.nkb; e.nm4 = e.nnm4; };
  auto epi = [&](auto h) {
    (void)e;
    if constexpr (h == GA(0)) A_FMA(e.q[0], e.st[0], e.csq[0], e.aq[0]);
    if constexpr (h == GB(0)) { A_FMA(e.q[1], e.st[0], e.csq[1], e.aq[1]); A_FMA(e.q[2], e.st[0], e.csq[2], e.aq[2]); }
    if constexpr (h == GC(0)) { A_FMA(e.q[3], e.st[0], e.csq[3], e.aq[3]); A_FMA(e.k[0], e.st[0], e.csk[0], e.ak[0]); }
    if constexpr (h == GA(1)) A_FMA(e.k[1], e.st[0], e.csk[1], e.ak[1]);
    if constexpr (h == GB(1)) { A_FMA(e.k[2], e.st[0], e.csk[2], e.ak[2]); A_FMA(e.k[3], e.st[0], e.csk[3], e.ak[3]); }
    if constexpr (h == GC(1)) { A_CVT(e.qb[0], e.q[0], e.q[1]); A_CVT(e.qb[1], e.q[2], e.q[3]); }
    if constexpr (h == GB(2)) { A_CVT(e.kb[0], e.k[0], e.k[1]); A_CVT(e.kb[1], e.k[2], e.k[3]); }
    if constexpr (h == GC(2)) {
      A_FMA(e.v[0], e.nm4[0], e.csv, e.av[0]); A_FMA(e.v[1], e.nm4[1], e.csv, e.av[1]);
      mf16z(e.sp, v2u{e.kprev[0], e.kprev[1]}, v2u{e.qb[0], e.qb[1]});
      A_FMA(e.v[2], e.nm4[2], e.csv, e.av[2]); A_FMA(e.v[3], e.nm4[3], e.csv, e.av[3]);
      mf16z(e.sc, v2u{e.kb[0], e.kb[1]}, v2u{e.qb[0], e.qb[1]});
      A_CVT(e.vb[0], e.v[0], e.v[1]); A_CVT(e.vb[1], e.v[2], e.v[3]);
    }
    if constexpr (h == GB(3)) { A_CVT(e.vb2[0], e.v[0], e.v[1]); A_CVT(e.vb2[1], e.v[2], e.v[3]); }
    if constexpr (h == GC(3)) A_MULS(e.ft, 0.25f * LOG2E, e.st[1]);
    if constexpr (h == GA(4)) A_MUL(e.fa, e.ka[0], e.ft);
    if constexpr (h == GB(4)) { A_SEL(e.a, e.sc[0], e.sc[1], e.modd); A_SEL(e.t0, e.sp[0], e.sp[1], e.modd); }
    if constexpr (h == GC(4)) { A_SEL(e.b, e.sc[2], e.sc[3], e.modd); A_SEL(e.t1, e.sp[2], e.sp[3], e.modd); }
    if constexpr (h == GB(5)) { A_SEL(e.a, e.t0, e.a, e.mA); A_SEL(e.b, e.t1, e.b, e.mB); }
    if constexpr (h == GC(5)) { A_MUL(e.fb, e.kb2[0], e.ft); A_FMA(e.a, e.a, e.fa, e.ka[1]); }
    if constexpr (h == GB(6)) {
      asm volatile("v_fma_f32 %0, %0, %3, %4\n\tv_max_f32 %1, %2, %0" : "+v"(e.b), "=&v"(e.m) : "v"(e.a), "v"(e.fb), "v"(e.kb2[1]));
    }
    if constexpr (h == GA(7)) A_MAX(e.m2, e.a, e.b);
    if constexpr (h == GC(7)) A_SW16(e.m, e.m2);
    if constexpr (h == GB(8)) { float x = e.m, y = e.m2; A_MAX(e.m, x, y); A_MAX(e.m2, x, y); }
    if constexpr (h == GA(9)) A_SW32(e.m, e.m2);
    if constexpr (h == GB(9)) asm volatile("v_max_f32 %0, %0, %2\n\tv_sub_f32 %1, %1, %0" : "+v"(e.m), "+v"(e.a) : "v"(e.m2));
    if constexpr (h == GC(9)) { A_SUB(e.b, e.b, e.m); }
    if constexpr (h == GB(10)) A_EXP(e.a, e.a);
    if constexpr (h == GC(10)) A_EXP(e.b, e.b);
    if constexpr (h == GB(11)) { A_MUL(e.pa, e.a, e.ka[0]); A_MUL(e.pb, e.b, e.kb2[0]); }
    if constexpr (h == GC(11)) { A_CVT(e.pk, e.pa, e.pb); A_CVT(e.pk0, e.a, e.b); }
    if constexpr (h == GB(12)) { A_PERM(e.P[0], e.pk, e.sPA); A_PERM(e.P[1], e.pk, e.sPB); }
    if constexpr (h == GC(12)) { A_PERM(e.P[2], e.pk, e.sCA); A_PERM(e.P[3], e.pk, e.sCB); }
    if constexpr (h == GA(13)) {
      mf16z_a(e.den, e.ones, v2u{e.pk0, e.pk});
      mf32z(e.num, v4u{e.vprev[0], e.vprev[1], e.vb[0], e.vb[1]}, v4u{e.P[0], e.P[1], e.P[2], e.P[3]});
      e.kprev[0] = e.kb[0]; e.kprev[1] = e.kb[1]; e.vprev[0] = e.vb2[0]; e.vprev[1] = e.vb2[1];
    }
    if constexpr (h == GB(14)) A_RCP(e.m, e.den);
    if constexpr (h == GC(14)) { A_MUL(e.q[0], e.num[0], e.m); A_MUL(e.q[1], e.num[1], e.m); }
    if constexpr (h == GA(15)) A_MUL(e.q[2], e.num[2], e.m);
    if constexpr (h == GB(15)) { A_MUL(e.q[3], e.num[3], e.m); A_CVT(e.ob[0], e.q[0], e.q[1]); }
    if constexpr (h == GC(15)) { A_CVT(e.ob[1], e.q[2], e.q[3]); st64(ctx, v2u{e.ob[0], e.ob[1]}, e.ovo, oso); }
  };
  auto x_sh = [&](auto TT, auto h) {
    if constexpr (h == GB(1)) dma_so<(TT + 1) * 16384>(so, xs);
    if constexpr (h == GA(2)) dma_m0<L_XT + (TT + 1) * 16384>(ctx.Lw);
    return h == GB(1) || h == GA(2);
  };
  auto x_gp = [&](auto h) {
    if constexpr (h == GA(2)) dma_go_sc1<0>(lv, ctx.rs, so);
    if constexpr (h == GA(3)) dma_go_sc1<1>(lv, ctx.rs, so);
    if constexpr (h == GA(5)) dma_go_sc1<2>(lv, ctx.rs, so);
    if constexpr (h == GA(6)) dma_go_sc1<3>(lv, ctx.rs, so);
  };
  auto w_sh = [&](auto J, auto N, auto h) {
    if constexpr (h == GA(7)) WAIT_VMV(N.value);
    if constexpr (h == GB(7)) RAWBAR();
    if constexpr (h == GB(9)) dma_so<128 * J * 1024>(so, wd.sb);
    if constexpr (h == GA(10)) dma_m0<L_XT + J * 16384>(ctx.Lw);
    if constexpr (h == GB(13) && J > 0) dma_so<(J - 1) * 16384>(oso, osb);
    return h == GA(7) || h == GB(7) || h == GB(9) || h == GA(10) || (h == GB(13) && J > 0);
  };
  auto w_gp = [&](auto h) {
    if constexpr (h == GA(10)) dma_go<0>(wd.v[0], rwo, so);
    if constexpr (h == GA(11)) dma_go<1>(wd.v[1], rwo, so);
    if constexpr (h == GA(12)) dma_go<2>(wd.v[2], rwo, so);
    if constexpr (h == GA(14)) dma_go<3>(wd.v[3], rwo, so);
  };
  TS(0);
  gemm_tile<-1, P_BK, P_BV, 0, false>(ab0, xq, xk, xv,
    [](auto ks) { return IC<((ks == 12 || ks == 13 || ks == 14) ? 2 : 0)>{}; },
    [&](auto h) {
      if constexpr (h == GA(0)) dma_so<2 * 16384>(so, xs);
      if constexpr (h == GA(1)) dma_m0<L_XT + 2 * 16384>(ctx.Lw);
      if constexpr (h == GA(11)) WAIT_VMV(4);
      if constexpr (h == GA(12)) RAWBAR();
      return h == GA(0) || h == GA(1) || h == GA(11) || h == GA(12);
    },
    [&](auto h) {
      (void)e;
      if constexpr (h == GA(1)) dma_go_sc1<0>(lv, ctx.rs, so);
      if constexpr (h == GA(4)) dma_go_sc1<1>(lv, ctx.rs, so);
      if constexpr (h == GA(7)) dma_go_sc1<2>(lv, ctx.rs, so);
      if constexpr (h == GC(10)) dma_go_sc1<3>(lv, ctx.rs, so);
      if constexpr (h == GA(12)) { A_LD64(e.nst, e.aT, 0); A_LD128(e.nnm4, e.aF, 0); }
    });
  TS(1);
  gemm_tile<P_BQ, P_BK, P_BV, 16384, false>(ab0, yq, yk, yv, xl,
    [&](auto h) { return x_sh(IC<2>{}, h) | w_sh(IC<0>{}, IC<4>{}, h); },
    [&](auto h) {
      (void)e; (void)xk; (void)xv;
      if constexpr (h == GB(1)) { A_FMA(e.k[0], e.nst[0], e.csk[0], xk[0]); A_FMA(e.k[1], e.nst[0], e.csk[1], xk[1]); }
      if constexpr (h == GC(1)) { A_FMA(e.k[2], e.nst[0], e.csk[2], xk[2]); A_FMA(e.k[3], e.nst[0], e.csk[3], xk[3]); }
      if constexpr (h == GB(2)) { A_FMA(e.v[0], e.nnm4[0], e.csv, xv[0]); A_FMA(e.v[1], e.nnm4[1], e.csv, xv[1]); }
      if constexpr (h == GC(2)) { A_FMA(e.v[2], e.nnm4[2], e.csv, xv[2]); A_FMA(e.v[3], e.nnm4[3], e.csv, xv[3]); }
      if constexpr (h == GB(3)) { A_CVT(e.kprev[0], e.k[0], e.k[1]); A_CVT(e.kprev[1], e.k[2], e.k[3]); }
      if constexpr (h == GC(3)) { A_CVT(e.vprev[0], e.v[0], e.v[1]); A_CVT(e.vprev[1], e.v[2], e.v[3]); }
      if constexpr (h == GC(15)) NOP(1);
      x_gp(h); stat_rd(IC<1>{}, h); w_gp(h);
    });
  next(yq, yk, yv);
  TS(2);
  gemm_tile<P_BQ, P_BK, P_BV, 32768, false>(ab0, xq, xk, xv, xl,
    [&](auto h) { return x_sh(IC<3>{}, h) | w_sh(IC<1>{}, IC<8>{}, h); },
    [&](auto h) { x_gp(h); stat_rd(IC<2>{}, h); w_gp(h); epi(h); });
  next(xq, xk, xv);
  TS(3);
  gemm_tile<P_BQ, P_BK, P_BV, 0, false>(ab1, yq, yk, yv, xl,
    [&](auto h) { return w_sh(IC<2>{}, IC<5>{}, h); },
    [&](auto h) { stat_rd(IC<3>{}, h); w_gp(h); epi(h); });
  next(yq, yk, yv);
  TS(4);
  gemm_tile<P_BQ, P_BK, P_BV, 16384, false>(ab1, xq, xk, xv, xl,
    [&](auto h) { return w_sh(IC<3>{}, IC<63>{}, h); },
    [&](auto h) { stat_rd(IC<4>{}, h); w_gp(h); epi(h); });
  next(xq, xk, xv);
  TS(5);
  {
    dma_so<3 * 16384>(oso, osb);
    A_MULS(e.ft, 0.25f * LOG2E, e.st[1]);
    A_FMA(e.q[0], e.st[0], e.csq[0], e.aq[0]); A_FMA(e.q[1], e.st[0], e.csq[1], e.aq[1]);
    A_FMA(e.q[2], e.st[0], e.csq[2], e.aq[2]); A_FMA(e.q[3], e.st[0], e.csq[3], e.aq[3]);
    A_FMA(e.k[0], e.st[0], e.csk[0], e.ak[0]); A_FMA(e.k[1], e.st[0], e.csk[1], e.ak[1]);
    A_CVT(e.qb[0], e.q[0], e.q[1]); A_CVT(e.qb[1], e.q[2], e.q[3]);
    A_FMA(e.k[2], e.st[0], e.csk[2], e.ak[2]); A_FMA(e.k[3], e.st[0], e.csk[3], e.ak[3]);
    mf16z(e.sp, v2u{e.kprev[0], e.kprev[1]}, v2u{e.qb[0], e.qb[1]});
    A_CVT(e.kb[0], e.k[0], e.k[1]); A_CVT(e.kb[1], e.k[2], e.k[3]);
    A_MUL(e.fa, e.ka[0], e.ft); A_MUL(e.fb, e.kb2[0], e.ft);
    mf16z(e.sc, v2u{e.kb[0], e.kb[1]}, v2u{e.qb[0], e.qb[1]});
    A_FMA(e.v[0], e.nm4[0], e.csv, e.av[0]); A_FMA(e.v[1], e.nm4[1], e.csv, e.av[1]);
    A_FMA(e.v[2], e.nm4[2], e.csv, e.av[2]); A_FMA(e.v[3], e.nm4[3], e.csv, e.av[3]);
    A_SEL(e.t0, e.sp[0], e.sp[1], e.modd); A_SEL(e.t1, e.sp[2], e.sp[3], e.modd);
    A_CVT(e.vb[0], e.v[0], e.v[1]); A_CVT(e.vb[1], e.v[2], e.v[3]);
    asm volatile("v_cndmask_b32 %0, %4, %5, %10\n\tv_cndmask_b32 %1, %6, %7, %10\n\t"
                 "v_cndmask_b32 %0, %8, %0, %11\n\tv_cndmask_b32 %1, %9, %1, %12\n\t"
                 "v_fma_f32 %0, %0, %13, %15\n\tv_fma_f32 %1, %1, %14, %16\n\t"
                 "v_max_f32 %2, %0, %1\n\tv_max_f32 %3, %0, %1\n\ts_nop 1\n\t"
                 "v_permlane16_swap_b32 %2, %3\n\tv_max_f32 %2, %2, %3\n\tv_mov_b32 %3, %2\n\ts_nop 1\n\t"
                 "v_permlane32_swap_b32 %2, %3\n\tv_max_f32 %2, %2, %3\n\tv_sub_f32 %0, %0, %2\n\tv_sub_f32 %1, %1, %2\n\t"
                 "v_exp_f32 %0, %0\n\tv_exp_f32 %1, %1"
                 : "=&v"(e.a), "=&v"(e.b), "=&v"(e.m), "=&v"(e.m2)
                 : "v"(e.sc[0]), "v"(e.sc[1]), "v"(e.sc[2]), "v"(e.sc[3]), "v"(e.t0), "v"(e.t1), "s"(e.modd), "s"(e.mA), "s"(e.mB),
                   "v"(e.fa), "v"(e.fb), "v"(e.ka[1]), "v"(e.kb2[1]));
    asm volatile("v_mul_f32 %4, %7, %9\n\tv_mul_f32 %5, %8, %10\n\tv_cvt_pk_bf16_f32 %6, %7, %8\n\tv_cvt_pk_bf16_f32 %4, %4, %5\n\t"
                 "v_perm_b32 %0, %4, %4, %11\n\tv_perm_b32 %1, %4, %4, %12\n\tv_perm_b32 %2, %4, %4, %13\n\tv_perm_b32 %3, %4, %4, %14"
                 : "=&v"(e.P[0]), "=&v"(e.P[1]), "=&v"(e.P[2]), "=&v"(e.P[3]), "=&v"(e.pk), "=&v"(e.pb), "=&v"(e.pk0)
                 : "v"(e.a), "v"(e.b), "v"(e.ka[0]), "v"(e.kb2[0]), "v"(e.sPA), "v"(e.sPB), "v"(e.sCA), "v"(e.sCB));
    mf16z_a(e.den, e.ones, v2u{e.pk0, e.pk});
    NOP(0);
    mf32z(e.num, v4u{e.vprev[0], e.vprev[1], e.vb[0], e.vb[1]}, v4u{e.P[0], e.P[1], e.P[2], e.P[3]});
    NOP(5);
    asm volatile("v_rcp_f32 %2, %7\n\ts_nop 0\n\t"
                 "v_mul_f32 %3, %8, %2\n\tv_mul_f32 %4, %9, %2\n\tv_mul_f32 %5, %10, %2\n\tv_mul_f32 %6, %11, %2\n\t"
                 "v_cvt_pk_bf16_f32 %0, %3, %4\n\tv_cvt_pk_bf16_f32 %1, %5, %6"
                 : "=&v"(e.ob[0]), "=&v"(e.ob[1]), "=&v"(e.m), "=&v"(e.q[0]), "=&v"(e.q[1]), "=&v"(e.q[2]), "=&v"(e.q[3])
                 : "v"(e.den[0]), "v"(e.num[0]), "v"(e.num[1]), "v"(e.num[2]), "v"(e.num[3]), "v"(e.den));
    st64(ctx, v2u{e.ob[0], e.ob[1]}, e.ovo, oso);
  }
  TS(6);
  STAMP(L * 8 + 4);
  TS_FLUSH(L * 8 + 2, 7);
  DRAIN();
  BAR();
  flag_store(ctx, WS_FO + (32 + c) * 128);
}

constexpr int KLOC[4] = {L_RQ, L_RQ + 32768, L_XT, L_RQ + 16384}, VLOC[4] = {L_RQ + 16384, L_RQ + 49152, L_RQ, L_RQ + 32768};
template <int P0, int P1>
DEV void first_pieces(Ctx& ctx, rsrc_t rsx, rsrc_t rsa) {
  const int lane = ctx.lane, w = ctx.w;
  const ImgDma kd = img_prep<3072>(ctx);
  const unsigned xvo = (unsigned)((lane >> 2) * 1024 + (((lane & 3) ^ ((4 - (lane >> 4)) & 3)) << 4));
  const int xso = (64 * ctx.c + 16 * w) * 1024;
  const unsigned xlb = ctx.Lb + L_XT + (1 + w) * 16384;
  const unsigned rvo = (unsigned)((lane >> 3) * 3072 + (lane & 7) * 16);
  sfor<P0, P1>([&](auto P) {
    constexpr int p = P, q = p & 3, blk = p >> 2, kind = blk % 3, j = blk / 3;
    if constexpr (kind == 0) dma1<(4 * j + q) * 64, (4 * j + q) * 1024>(xvo, rsx, xso, xlb);
    else if constexpr (blk >= 8) raw_ld1<P_BK + (blk == 8 ? 0 : blk == 10 ? 4 : 8) + q>(rvo, rsa, kd.sb + 1024 * kind + (128 * j + 8 * q) * 3072);
    else dma1<1024 * kind + 128 * j * 3072 + q * 1024, (kind == 1 ? KLOC[j] : VLOC[j]) + q * 1024>(kd.v[q], rsa, kd.sb, ctx.Lw);
  });
}
DEV void first_stage(Ctx& ctx, rsrc_t rsx, rsrc_t rsa) {
  fresh(ctx);
  const int lane = ctx.lane, w = ctx.w, g = ctx.g, tok = ctx.tok;
  unsigned wp0, wp1;
  {
    const int ch = lane & 7, rb1 = (lane >> 4) & 1;
    const unsigned base = ctx.Lb + L_RQ + (32 * w + (lane >> 3)) * 128 + (ch & 1) * 16;
    wp0 = base + (((ch >> 1) ^ rb1) << 5); wp1 = base + (((ch >> 1) ^ (rb1 | 2)) << 5);
  }
  unsigned abo = ctx.Lb + L_XT + 16384 + x0_lane(tok, g) + w * 16384;
  unsigned trq = ctx.Lb + L_RQ + tr_base(lane, w), tr0 = ctx.Lb + L_XT + tr_base(lane, w);
  v4u ones = {mk1(), mk1(), mk1(), mk1()};
  unsigned wrec = ctx.Lb + L_REC + ((1 + w) * 16 + tok) * 16, wnm = ctx.Lb + L_NM + ((1 + w) * 16 + tok) * 4;
  unsigned wck = ctx.Lb + L_CS + (w * 6) * 64 + 64 + g * 16, wcv = ctx.Lb + L_CS + (w * 6) * 64 + 128 + tok * 4;
  const u64 m1 = __ballot(tok & 1), m2 = __ballot(tok & 2), mdiag = __ballot(g == (tok >> 2));
  asm volatile("" : "+v"(abo), "+v"(trq), "+v"(tr0), "+a"(ones), "+v"(wrec), "+v"(wnm), "+v"(wck), "+v"(wcv), "+v"(wp0), "+v"(wp1));
  first_pieces<PRE0, 24>(ctx, rsx, rsa);
  STAMP(42);
  f4 csk, csv, gram, ssum;
  v4u xf[4];
  sfor<0, 4>([&](auto j) {
    (void)xf; (void)abo;
    if constexpr (j == 0) WAIT_VM(13);
    if constexpr (j == 1) WAIT_VM(12);
    RAWBAR();
    sfor<0, 4>([&](auto i) {
      constexpr int ks = 4 * j + i;
      (void)xf; (void)abo;
      A_LD128A(xf[i], abo, ks * 1024);
      if constexpr (j == 2) wtr<P_AK + ks, i * 4096>(tr0); else wtr<P_AK + ks, KLOC[j] - L_RQ + i * 4096>(trq);
      wtr<P_AV + ks, VLOC[j] - L_RQ + i * 4096>(trq);
    });
    if constexpr (j < 2) first_pieces<24 + 12 * j, 30 + 12 * j>(ctx, rsx, rsa);
    if constexpr (j == 2) {
      WAIT_VM(6);
      sfor<0, 4>([&](auto q) { raw_st1<P_BK + 4 + q, KLOC[3] - L_RQ + q * 1024>((q & 1) ? wp1 : wp0); });
      WAIT_VM(2);
      sfor<0, 4>([&](auto q) { raw_st1<P_BK + 8 + q, VLOC[3] - L_RQ + q * 1024>((q & 1) ? wp1 : wp0); });
      pf32<3072>(ctx, rsa, 0);
      WAIT_LG(8);
    } else {
      WAIT_LG(0);
    }
    sfor<0, 4>([&](auto i) {
      constexpr int ks = 4 * j + i;
      mf_aaz<ks == 0>(gram, xf[i], xf[i]); VNOP();
      mf_aaz<ks == 0>(ssum, ones, xf[i]); VNOP();
    });
    sfor<0, 4>([&](auto i) {
      constexpr int ks = 4 * j + i;
      wxa<P_AK + ks, ks == 0>(csk, ones); VNOP();
      wxb<P_AV + ks, ks == 0>(csv, ones); VNOP();
    });
    if constexpr (j < 2) first_pieces<30 + 12 * j, 36 + 12 * j>(ctx, rsx, rsa);
    if constexpr (j == 1) {
      {
        lds_t* ib = ctx.L + (L_IMG + tr_base(lane, w));
        const v4u f0 = tr_frag(ib, 0), f1 = tr_frag(ib, 4096);
        const int fb = WS_FR + (ctx.m * 4 + w) * 49152;
        st128(ctx, f0, lane * 16, fb + ctx.c * 1024);
        st128(ctx, f1, lane * 16, fb + (32 + (ctx.c & 15)) * 1024);
        SB();
      }
      WAIT_VM(14);
      sfor<0, 4>([&](auto q) { raw_st1<P_BK + q, VLOC[2] - L_RQ + q * 1024>((q & 1) ? wp1 : wp0); });
    }
    WAIT_LG(0);
    SB();
  });
  STAMP(43);
  float q, t, nm, z0;
  asm volatile("v_cndmask_b32 %0, %4, %5, %9\n\tv_cndmask_b32 %1, %6, %7, %9\n\tv_mul_f32 %2, %11, %8\n\tv_cndmask_b32 %0, %0, %1, %10\n\t"
               "v_mul_f32 %0, %12, %0\n\tv_fma_f32 %0, -%2, %2, %0\n\tv_max_f32 %0, 0, %0\n\tv_add_f32 %0, %13, %0\n\tv_rsq_f32 %0, %0\n\tv_mov_b32 %3, 0\n\tv_mov_b32 %1, %0"
               : "=&v"(q), "=&v"(t), "=&v"(nm), "=&v"(z0)
               : "v"(gram[0]), "v"(gram[1]), "v"(gram[2]), "v"(gram[3]), "v"(ssum[0]), "s"(m1), "s"(m2), "s"(-1.0f / 512.0f), "s"(1.0f / 512.0f), "s"(1e-5f), "v"(ssum));
  u64 sv;
  asm volatile("s_mov_b64 %0, exec\n\ts_mov_b64 exec, %6\n\tds_write_b128 %1, %2\n\tds_write2_b32 %3, %4, %5 offset1:80\n\ts_mov_b64 exec, %0"
               : "=&s"(sv) : "v"(wrec), "v"(f4{nm, q, t, z0}), "v"(wnm), "v"(nm), "v"(q), "s"(mdiag) : "memory");
  asm volatile("ds_write_b128 %0, %1\n\tds_write_b32 %2, %3" :: "v"(wck), "v"(csk), "v"(wcv), "v"(csv[0]), "v"(csv) : "memory");
}

struct FM {
  f4 p, sp, sn;
  float ep[4], en[4], m, m2, c;
  unsigned eb[2], nb[2];
};
#define FM_MM1(f) asm volatile("v_max3_f32 %0, %2, %3, %4\n\tv_max3_f32 %1, -%2, -%3, -%4" : "=&v"(f.m), "=&v"(f.m2) : "v"(f.p[0]), "v"(f.p[1]), "v"(f.p[2]))
#define FM_MM2(f) asm volatile("v_max_f32 %0, %0, %2\n\tv_max_f32 %1, %1, -%2" : "+v"(f.m), "+v"(f.m2) : "v"(f.p[3]))
#define FM_MX2(f) do { float x_ = f.m, y_ = f.m2; A_MAX(f.m, x_, y_); A_MAX(f.m2, x_, y_); } while (0)
#define FM_MXC(f) asm volatile("v_max_f32 %0, %0, %1\n\tv_mul_f32 %1, %0, -%2\n\tv_mul_f32 %0, %0, -%2" : "+v"(f.m), "+v"(f.m2) : "v"(f.c))

struct AE {
  f4 ak, av;
  f2 st, nst; f4 nm4, rs4, nnm4, nrs4;
  f4 csk; float csv;
  float k[4], v[4];
  unsigned kb[2], vb[2], vbo[2], php[2], phm[2];
  v2u wphi, ident, ones, lo1, hi1;
  f4 tp, tm, kvp, kvm, zz;
  FM f;
  unsigned aT, aF;
};

DEV void layer_A(Ctx& ctx, int lp, v2u wphi, rsrc_t rwo, rsrc_t rsa, rsrc_t rsb, rsrc_t rsx) {
  fresh(ctx);
  const int L = 2 * lp;
  const int c = ctx.c, m = ctx.m, w = ctx.w, lane = ctx.lane, g = ctx.g, tok = ctx.tok;
  const int csb = L_CS + (w * 6 + 0) * 64;
  const unsigned lv = lane * 16;
  const unsigned T = ctx.T;
  AE e;
  TS_DECL;
  unsigned ab0, ab1;
  ImgDma wd = img_prep<1024>(ctx), qd = img_prep<3072>(ctx);
  {
    const int d = tok - 4 * g;
    e.ident = v2u{d == 0 ? 0x3f80u : d == 1 ? 0x3f800000u : 0u, d == 2 ? 0x3f80u : d == 3 ? 0x3f800000u : 0u};
    const unsigned lo = tok < 8 ? ONE2 : 0u, hi = tok < 8 ? 0u : ONE2;
    e.lo1 = v2u{lo, lo}; e.hi1 = v2u{hi, hi}; e.ones = v2u{mk1(), mk1()}; e.wphi = wphi;
    e.aT = ctx.Lb + L_REC + tok * 16;
    e.aF = ctx.Lb + L_NM + g * 16;
    ab0 = ctx.Lb + L_XT + (lp == 0 ? x0_lane(tok, g) : lv); ab1 = ab0 + 49152;
    asm volatile("" : "+a"(e.ident), "+a"(e.lo1), "+a"(e.hi1), "+a"(e.ones), "+a"(e.wphi), "+v"(e.aT), "+v"(e.aF),
                      "+v"(ab0), "+v"(ab1), "+v"(qd.v[0]), "+v"(qd.v[1]), "+v"(qd.v[2]), "+v"(qd.v[3]));
  }
  const int xs = WS_X + ACT_L + c * 65536 + w * 4096 - 16384;
  f4 xk, xv, yk, yv;
  int so;
  auto stat_rd = [&](auto S, auto h) {
    (void)e;
    if constexpr (h == GA(12)) A_LD64(e.nst, e.aT, S * 256);
    if constexpr (h == GC(12)) { A_LD128(e.nnm4, e.aF, S * 64); A_LD128(e.nrs4, e.aF, S * 64 + 320); }
  };
  auto xl = [](auto ks) { return IC<(ks == 12 ? 1 : (ks == 13 || ks == 14) ? 3 : 0)>{}; };
  auto next = [&](f4& k, f4& v) { e.ak = k; e.av = v; e.st = e.nst; e.nm4 = e.nnm4; e.rs4 = e.nrs4; };
  auto epi = [&](auto h) {
    (void)e;
    if constexpr (h == GC(0)) { A_FMA(e.k[0], e.st[0], e.csk[0], e.ak[0]); A_FMA(e.k[1], e.st[0], e.csk[1], e.ak[1]); }
    if constexpr (h == GA(1)) { A_FMA(e.k[2], e.st[0], e.csk[2], e.ak[2]); A_FMA(e.k[3], e.st[0], e.csk[3], e.ak[3]); }
    if constexpr (h == GC(1)) { A_CVT(e.kb[0], e.k[0], e.k[1]); A_CVT(e.kb[1], e.k[2], e.k[3]); A_FMA(e.v[0], e.nm4[0], e.csv, e.av[0]); }
    if constexpr (h == GA(2)) {
      A_FMA(e.v[1], e.nm4[1], e.csv, e.av[1]);
      mf16z_a(e.f.p, e.wphi, v2u{e.kb[0], e.kb[1]});
      A_FMA(e.v[2], e.nm4[2], e.csv, e.av[2]); A_FMA(e.v[3], e.nm4[3], e.csv, e.av[3]);
    }
    if constexpr (h == GC(2)) { A_MUL(e.v[0], e.v[0], e.rs4[0]); A_MUL(e.v[1], e.v[1], e.rs4[1]); A_MUL(e.v[2], e.v[2], e.rs4[2]); }
    if constexpr (h == GA(3)) { A_MUL(e.v[3], e.v[3], e.rs4[3]); A_MULS(e.f.c, LOG2E, e.st[1]); }
    if constexpr (h == GC(3)) { FM_MM1(e.f); A_CVT(e.vb[0], e.v[0], e.v[1]); }
    if constexpr (h == GA(4)) FM_MM2(e.f);
    if constexpr (h == GC(4)) { A_CVT(e.vb[1], e.v[2], e.v[3]); A_SW16(e.f.m, e.f.m2); }
    if constexpr (h == GA(5)) FM_MX2(e.f);
    if constexpr (h == GC(5)) A_SW32(e.f.m, e.f.m2);
    if constexpr (h == GA(6)) FM_MXC(e.f);
    if constexpr (h == GC(6)) A_SW16(e.f.m, e.f.m2);
    if constexpr (h == GA(7)) { A_FMA(e.f.ep[0], e.f.p[0], e.f.c, e.f.m); A_FMA(e.f.ep[1], e.f.p[1], e.f.c, e.f.m); }
    if constexpr (h == GC(7)) { A_FMA(e.f.ep[2], e.f.p[2], e.f.c, e.f.m); A_FMA(e.f.ep[3], e.f.p[3], e.f.c, e.f.m); A_FMAN(e.f.en[0], e.f.p[0], e.f.c, e.f.m2); }
    if constexpr (h == GA(8)) { A_FMAN(e.f.en[1], e.f.p[1], e.f.c, e.f.m2); A_FMAN(e.f.en[2], e.f.p[2], e.f.c, e.f.m2); }
    if constexpr (h == GC(8)) { A_FMAN(e.f.en[3], e.f.p[3], e.f.c, e.f.m2); A_EXP(e.f.ep[0], e.f.ep[0]); A_EXP(e.f.ep[1], e.f.ep[1]); }
    if constexpr (h == GA(9)) A_EXP(e.f.ep[2], e.f.ep[2]);
    if constexpr (h == GC(9)) { A_EXP(e.f.ep[3], e.f.ep[3]); A_EXP(e.f.en[0], e.f.en[0]); }
    if constexpr (h == GA(10)) A_EXP(e.f.en[1], e.f.en[1]);
    if constexpr (h == GC(10)) { A_EXP(e.f.en[2], e.f.en[2]); A_EXP(e.f.en[3], e.f.en[3]); }
    if constexpr (h == GA(11)) { A_CVT(e.f.eb[0], e.f.ep[0], e.f.ep[1]); A_CVT(e.f.eb[1], e.f.ep[2], e.f.ep[3]); }
    if constexpr (h == GC(11)) {
      A_CVT(e.f.nb[0], e.f.en[0], e.f.en[1]); A_CVT(e.f.nb[1], e.f.en[2], e.f.en[3]);
      mf16z_a(e.f.sp, e.ones, v2u{e.f.eb[0], e.f.eb[1]});
    }
    if constexpr (h == GA(12)) mf16z_a(e.f.sn, e.ones, v2u{e.f.nb[0], e.f.nb[1]});
    if constexpr (h == GA(13)) A_RCP(e.f.m, e.f.sp);
    if constexpr (h == GC(13)) { A_MUL(e.f.ep[0], e.f.ep[0], e.f.m); A_RCP(e.f.m2, e.f.sn); A_MUL(e.f.ep[1], e.f.ep[1], e.f.m); }
    if constexpr (h == GA(14)) { A_MUL(e.f.ep[2], e.f.ep[2], e.f.m); A_MUL(e.f.ep[3], e.f.ep[3], e.f.m); }
    if constexpr (h == GC(14)) { A_MUL(e.f.en[0], e.f.en[0], e.f.m2); A_MUL(e.f.en[1], e.f.en[1], e.f.m2); A_CVT(e.f.eb[0], e.f.ep[0], e.f.ep[1]); }
    if constexpr (h == GA(15)) { A_CVT(e.f.eb[1], e.f.ep[2], e.f.ep[3]); A_MUL(e.f.en[2], e.f.en[2], e.f.m2); }
    if constexpr (h == GC(15)) {
      A_MUL(e.f.en[3], e.f.en[3], e.f.m2); A_CVT(e.f.nb[0], e.f.en[0], e.f.en[1]);
      mf16z_b(e.tp, v2u{e.f.eb[0], e.f.eb[1]}, e.ident);
      A_CVT(e.f.nb[1], e.f.en[2], e.f.en[3]);
      e.vbo[0] = e.vb[0]; e.vbo[1] = e.vb[1];
    }
  };
  auto epi2 = [&](auto FT, auto h) {
    (void)e;
    if constexpr (h == GA(0)) mf16z_b(e.tm, v2u{e.f.nb[0], e.f.nb[1]}, e.ident);
    if constexpr (h == GC(5)) { A_CVT(e.php[0], e.tp[0], e.tp[1]); }
    if constexpr (h == GC(6)) { A_CVT(e.php[1], e.tp[2], e.tp[3]); }
    if constexpr (h == GA(9)) {
      if constexpr (FT) mf16z(e.kvp, v2u{e.php[0], e.php[1]}, v2u{e.vbo[0], e.vbo[1]}); else mf16c(e.kvp, v2u{e.php[0], e.php[1]}, v2u{e.vbo[0], e.vbo[1]});
      A_CVT(e.phm[0], e.tm[0], e.tm[1]); A_CVT(e.phm[1], e.tm[2], e.tm[3]);
      if constexpr (FT) mf16z_a(e.zz, e.lo1, v2u{e.php[0], e.php[1]}); else mf16c_a(e.zz, e.lo1, v2u{e.php[0], e.php[1]});
    }
    if constexpr (h == GA(10)) {
      if constexpr (FT) mf16z(e.kvm, v2u{e.phm[0], e.phm[1]}, v2u{e.vbo[0], e.vbo[1]}); else mf16c(e.kvm, v2u{e.phm[0], e.phm[1]}, v2u{e.vbo[0], e.vbo[1]});
    }
    if constexpr (h == GA(13)) mf16c_a(e.zz, e.hi1, v2u{e.phm[0], e.phm[1]});
  };
  auto c_sh = [&](auto K0, auto SOFF, auto LOFF, int sb, auto h) {
    if constexpr (h == GA(K0 - 1)) dma_so<SOFF>(so, sb);
    if constexpr (h == GA(K0)) dma_m0<LOFF>(ctx.Lw);
    return h == GA(K0 - 1) || h == GA(K0);
  };
  auto c_gp = [&](auto K0, const ImgDma& d, rsrc_t rs, auto h) {
    if constexpr (h == GC(K0)) dma_go<0>(d.v[0], rs, so);
    if constexpr (h == GC(K0 + 2)) dma_go<1>(d.v[1], rs, so);
    if constexpr (h == GC(K0 + 4)) dma_go<2>(d.v[2], rs, so);
    if constexpr (h == GC(K0 + 6)) dma_go<3>(d.v[3], rs, so);
  };
  constexpr int QS = 128 * 3072;
  constexpr int SL = 32 * 256;
  const int kblk = WS_KV1 + ((m * 64 + w * 16) * 32 + c) * 256;
  const int zblk = WS_KVZ + ((m * 4 + w) * 32 + c) * 256;
  int kvo1 = g * 2 * SL + tok * 16, zvo1 = ((g >> 1) * 16 + tok) * 8;
  asm volatile("" : "+v"(kvo1), "+v"(zvo1));
  {
    const rsrc_t rnull = __builtin_amdgcn_make_buffer_rsrc((void*)ctx.ws, 0, 0, 0x00020000);
    const rsrc_t rx = lp ? ctx.rs : rnull, pw = lp ? rnull : rwo;
    const unsigned lb3 = lp ? ctx.Lw + 3 * 16384 : ctx.Lw, lb4 = lp ? ctx.Lw + 4 * 16384 : ctx.Lw;
    unsigned pv1 = (unsigned)((lane & 31) * 128), pv3 = (unsigned)((lane & 31) * 4), sink = 0, fw;
    const int prow = 16 * (int)(blockIdx.x >> 3) + 4 * w;
    const int ps1 = __builtin_amdgcn_readfirstlane(prow * 1024);
    int fs[4], fwo = WS_FW + m * 128;
#pragma unroll
    for (int i = 0; i < 4; ++i) fs[i] = WS_FR + (m * 4 + w) * 49152 + (16 + 4 * i) * 1024;
    asm volatile("" : "+s"(fs[0]), "+s"(fs[1]), "+s"(fs[2]), "+s"(fs[3]), "+s"(fwo));
    unsigned ack = ctx.Lb + csb + 64 + g * 16, acv = ctx.Lb + csb + 128 + tok * 4;
    asm volatile("" : "+v"(pv3), "+v"(pv1), "+v"(sink), "+v"(ack), "+v"(acv));
    auto pfs = [&](unsigned vo, rsrc_t rs, int so_, auto OFF) {
      (void)sink;
      asm volatile("buffer_load_dword %0, %1, %2, %3 offen offset:%4" : "+v"(sink) : "v"(vo), "s"(rs), "s"(so_), "n"(OFF.value) : "memory");
    };
    if (lp > 0) {
      ST st;
      st_begin<false>(ctx, st);
      gather_start<false>(ctx, st, L, lv, xs);
    } else {
      Ctx c2 = ctx;
      first_stage(c2, rsx, rsa);
      STAMP(1);
    }
    A_LD128(e.csk, ack, 0);
    asm volatile("ds_read_b32 %0, %1" : "=v"(e.csv) : "v"(acv) : "memory");
    ring_prime<16384>(ab0);
    TS(0);
    gemm_tile<-1, P_AK, P_AV, 16384, false>(ab0, xk, xk, xv, xl,
      [&](auto h) {
        if constexpr (h == GA(0)) dma_so<3 * 16384>(so, xs);
        if constexpr (h == GA(1)) dma_m0<L_XT>(lb3);
        if constexpr (h == GA(11)) WAIT_VMV(4);
        if constexpr (h == GA(12)) RAWBAR();
        if constexpr (h == GA(13)) dma_so<4 * 16384>(so, xs);
        if constexpr (h == GA(14)) dma_m0<L_XT>(lb4);
        return h == GA(0) || h == GA(1) || (h >= GA(11) && h <= GA(14));
      },
      [&](auto h) {
        if constexpr (h == GA(1)) dma_go_sc1<0>(lv, rx, so);
        if constexpr (h == GA(4)) dma_go_sc1<1>(lv, rx, so);
        if constexpr (h == GA(7)) dma_go_sc1<2>(lv, rx, so);
        if constexpr (h == GC(10)) dma_go_sc1<3>(lv, rx, so);
        if constexpr (h == GC(14)) dma_go_sc1<0>(lv, rx, so);
        if constexpr (h == GC(15)) NOP(1);
        stat_rd(IC<1>{}, h);
      });
    next(xk, xv);
    if (lp == 0 && w == 0) {
      unsigned tv, zv;
      asm volatile("v_mov_b32 %0, %2\n\tv_mov_b32 %1, 0" : "=v"(tv), "=v"(zv) : "s"(T));
      __builtin_amdgcn_raw_buffer_store_b32(tv, ctx.rs, zv, fwo + c * 4, SC1);
    }
    TS(1);
    gemm_tile<-1, P_AK, P_AV, 32768, false>(ab0, yk, yk, yv, xl,
      [&](auto h) {
        if constexpr (h == GA(11)) WAIT_VMV(7);
        if constexpr (h == GA(12)) RAWBAR();
        return c_sh(IC<7>{}, IC<0>{}, IC<L_RQ>{}, qd.sb, h) | (h == GA(11) || h == GA(12));
      },
      [&](auto h) {
        if constexpr (h == GA(0)) dma_go_sc1<1>(lv, rx, so);
        if constexpr (h == GC(3)) dma_go_sc1<2>(lv, rx, so);
        if constexpr (h == GC(5)) dma_go_sc1<3>(lv, rx, so);
        c_gp(IC<7>{}, qd, rsa, h);
        if constexpr (h == GC(1)) pfs(pv1, pw, ps1, IC<0>{});
        stat_rd(IC<2>{}, h); epi(h);
      });
    next(yk, yv);
    TS(2);
    gemm_tile<-1, P_AK, P_AV, 0, false>(ab1, xk, xk, xv, xl,
      [&](auto h) {
        if constexpr (h == GA(11)) WAIT_VMV(10);
        if constexpr (h == GA(12)) RAWBAR();
        return c_sh(IC<1>{}, IC<QS>{}, IC<L_RQ + 16384>{}, qd.sb, h) | c_sh(IC<9>{}, IC<2 * QS>{}, IC<L_RQ + 32768>{}, qd.sb, h) | (h == GA(11) || h == GA(12));
      },
      [&](auto h) {
        c_gp(IC<1>{}, qd, rsa, h); c_gp(IC<9>{}, qd, rsa, h);
        if constexpr (h == GC(8)) {
          (void)fw;
          asm volatile("buffer_load_dword %0, %1, %2, %3 offen sc1" : "=v"(fw) : "v"(pv3), "s"(ctx.rs), "s"(fwo) : "memory");
        }
        stat_rd(IC<3>{}, h); epi2(IC<1>{}, h); epi(h);
      });
    next(xk, xv);
    if (lp == 0) {
      unsigned f2;
      asm volatile("s_waitcnt vmcnt(4)\n\tv_mov_b32 %0, %1" : "=v"(f2) : "v"(fw) : "memory");
      while (!__all(f2 == T) && --ctx.budget >= 0) {
        f2 = bld32(ctx, pv3, fwo);
      }
      __builtin_amdgcn_fence(__ATOMIC_ACQUIRE, "wavefront");
      sfor<16, 20>([&](auto i) { fr_ld<i>(lv, ctx.rs, fs[0]); });
    }
    TS(3);
    gemm_tile<-1, P_AK, P_AV, 16384, false>(ab1, yk, yk, yv, xl,
      [&](auto h) {
        return c_sh(IC<1>{}, IC<3 * QS>{}, IC<L_RQ + 49152>{}, qd.sb, h) | c_sh(IC<9>{}, IC<0>{}, IC<L_XT>{}, wd.sb, h);
      },
      [&](auto h) {
        c_gp(IC<1>{}, qd, rsa, h);
        if constexpr (h == GC(8)) { if (lp == 0) sfor<20, 24>([&](auto i) { fr_ld<i>(lv, ctx.rs, fs[1]); }); }
        c_gp(IC<9>{}, wd, rwo, h); stat_rd(IC<4>{}, h); epi2(IC<0>{}, h); epi(h);
      });
    next(yk, yv);
  TS(4);
  {
    FM& f = e.f;
    A_MULS(f.c, LOG2E, e.st[1]);
    NOP(0);
    mf16z_b(e.tm, v2u{f.nb[0], f.nb[1]}, e.ident);
    A_FMA(e.k[0], e.st[0], e.csk[0], e.ak[0]); A_FMA(e.k[1], e.st[0], e.csk[1], e.ak[1]);
    A_FMA(e.k[2], e.st[0], e.csk[2], e.ak[2]); A_FMA(e.k[3], e.st[0], e.csk[3], e.ak[3]);
    A_CVT(e.kb[0], e.k[0], e.k[1]); A_CVT(e.php[0], e.tp[0], e.tp[1]); A_CVT(e.kb[1], e.k[2], e.k[3]); A_CVT(e.php[1], e.tp[2], e.tp[3]);
    A_CVT(e.phm[0], e.tm[0], e.tm[1]);
    mf16z_a(f.p, e.wphi, v2u{e.kb[0], e.kb[1]});
    A_CVT(e.phm[1], e.tm[2], e.tm[3]); A_FMA(e.v[0], e.nm4[0], e.csv, e.av[0]);
    mf16c(e.kvp, v2u{e.php[0], e.php[1]}, v2u{e.vbo[0], e.vbo[1]});
    A_FMA(e.v[1], e.nm4[1], e.csv, e.av[1]); A_FMA(e.v[2], e.nm4[2], e.csv, e.av[2]);
    mf16c(e.kvm, v2u{e.phm[0], e.phm[1]}, v2u{e.vbo[0], e.vbo[1]});
    A_FMA(e.v[3], e.nm4[3], e.csv, e.av[3]); A_MUL(e.v[0], e.v[0], e.rs4[0]);
    FM_MM1(f);
    mf16c_a(e.zz, e.lo1, v2u{e.php[0], e.php[1]});
    FM_MM2(f);
    A_MUL(e.v[1], e.v[1], e.rs4[1]); A_MUL(e.v[2], e.v[2], e.rs4[2]);
    A_SW16(f.m, f.m2);
    mf16c_a(e.zz, e.hi1, v2u{e.phm[0], e.phm[1]});
    FM_MX2(f);
    A_MUL(e.v[3], e.v[3], e.rs4[3]); A_CVT(e.vb[0], e.v[0], e.v[1]);
    A_SW32(f.m, f.m2);
    FM_MXC(f);
    A_CVT(e.vb[1], e.v[2], e.v[3]);
    NOP(0);
    A_SW16(f.m, f.m2);
#pragma unroll
    for (int r = 0; r < 4; ++r) A_FMA(f.ep[r], f.p[r], f.c, f.m);
#pragma unroll
    for (int r = 0; r < 4; ++r) A_FMAN(f.en[r], f.p[r], f.c, f.m2);
#pragma unroll
    for (int r = 0; r < 4; ++r) A_EXP(f.ep[r], f.ep[r]);
    A_EXP(f.en[0], f.en[0]);
    A_CVT(f.eb[0], f.ep[0], f.ep[1]); A_CVT(f.eb[1], f.ep[2], f.ep[3]);
    A_EXP(f.en[1], f.en[1]); A_EXP(f.en[2], f.en[2]);
    mf16z_a(f.sp, e.ones, v2u{f.eb[0], f.eb[1]});
    A_EXP(f.en[3], f.en[3]);
    A_CVT(f.nb[0], f.en[0], f.en[1]); A_CVT(f.nb[1], f.en[2], f.en[3]);
    NOP(1);
    mf16z_a(f.sn, e.ones, v2u{f.nb[0], f.nb[1]});
    NOP(1);
    A_RCP(f.m, f.sp);
    NOP(0);
#pragma unroll
    for (int r = 0; r < 4; ++r) A_MUL(f.ep[r], f.ep[r], f.m);
    A_RCP(f.m2, f.sn);
    A_CVT(f.eb[0], f.ep[0], f.ep[1]); A_CVT(f.eb[1], f.ep[2], f.ep[3]);
    A_MUL(f.en[0], f.en[0], f.m2); A_MUL(f.en[1], f.en[1], f.m2);
    mf16z_b(e.tp, v2u{f.eb[0], f.eb[1]}, e.ident);
    A_MUL(f.en[2], f.en[2], f.m2); A_MUL(f.en[3], f.en[3], f.m2);
    A_CVT(f.nb[0], f.en[0], f.en[1]); A_CVT(f.nb[1], f.en[2], f.en[3]);
    NOP(1);
    mf16z_b(e.tm, v2u{f.nb[0], f.nb[1]}, e.ident);
    NOP(0);
    A_CVT(e.php[0], e.tp[0], e.tp[1]); A_CVT(e.php[1], e.tp[2], e.tp[3]);
    NOP(1);
    mf16c(e.kvp, v2u{e.php[0], e.php[1]}, v2u{e.vb[0], e.vb[1]});
    mf16c_a(e.zz, e.lo1, v2u{e.php[0], e.php[1]});
    NOP(0);
    A_CVT(e.phm[0], e.tm[0], e.tm[1]); A_CVT(e.phm[1], e.tm[2], e.tm[3]);
    NOP(1);
    mf16c(e.kvm, v2u{e.phm[0], e.phm[1]}, v2u{e.vb[0], e.vb[1]});
    mf16c_a(e.zz, e.hi1, v2u{e.phm[0], e.phm[1]});
    asm volatile("s_nop 0" : "+v"(e.kvp));
  }
  TS(5);
  {
    const int blk = kblk, vo = kvo1;
    st128(ctx, v4u{asu(e.kvp[0]), T, asu(e.kvp[1]), T}, vo, blk);
    st128(ctx, v4u{asu(e.kvp[2]), T, asu(e.kvp[3]), T}, vo, blk + SL);
    asm volatile("s_nop 3" : "+v"(e.kvm), "+v"(e.zz));
    st128(ctx, v4u{asu(e.kvm[0]), T, asu(e.kvm[1]), T}, vo, blk + 8 * SL);
    st128(ctx, v4u{asu(e.kvm[2]), T, asu(e.kvm[3]), T}, vo, blk + 9 * SL);
    st64(ctx, v2u{asu(e.zz[0]), T}, zvo1, zblk);
  }
    asm volatile("s_waitcnt vmcnt(9)" : "+v"(sink), "+v"(fw) :: "memory");
  }
  STAMP(L * 8 + 2);
  TS_FLUSH(L * 8 + 2, 6);
  f4 aq[4];
  v4u dk[4], dz;
  const int S64 = 2 * c + (w >> 1);
  const int rblk = WS_KV1 + ((m * 64 + S64) * 32) * 256 + (w & 1) * 128;
  const int rzb = WS_KVZ + ((m * 4 + (S64 >> 4)) * 32) * 256 + (S64 & 15) * 16;
  int kvo = (lane >> 3) * 256 + (lane & 7) * 16, zvo = (lane & 31) * 256;
  {
    lds_t* qt = ctx.L + opq(L_RQ + tr_base(lane, w));
    asm volatile("" : "+v"(kvo), "+v"(zvo));
    TS(0);
    RAWBAR();
    TS(1);
    v4u wq[16];
#pragma unroll
    for (int ks = 0; ks < 16; ++ks) wq[ks] = tr_frag(qt, ks * 4096);
    if (lp == 0) {
      { Ctx c2 = ctx; fresh(c2); fr_load<24, 34>(c2); }
      f4 c0, c1;
      v4u ones = {mk1(), mk1(), mk1(), mk1()};
      asm volatile("s_nop 1" : "+v"(ones));
      sfor<0, 8>([&](auto k) { mf_av<k == 0>(c0, wq[2 * k], ones); mf_av<k == 0>(c1, wq[2 * k + 1], ones); });
      asm volatile("s_nop 7" : "+v"(c0), "+v"(c1));
      if (tok == 0) *(LAS f4*)(ctx.L + csb + g * 16) = c0 + c1;
    }
    v4u a1[4];
    auto rd = [&](auto k) {
      (void)a1;
      if constexpr (k < 16) { ring_rd<k & 3, 16384 + k * 1024>(ab0); } else if constexpr (k < 32) { ring_rd<k & 3, (k - 16) * 1024>(ab1); }
    };
    auto rd1 = [&](auto k) {
      (void)a1; (void)ab0; (void)ab1;
      if constexpr (k < 16) { A_LD128(a1[k & 3], ab0, 32768 + k * 1024); } else if constexpr (k < 32) { A_LD128(a1[k & 3], ab1, 16384 + (k - 16) * 1024); }
    };
    sfor<0, 3>([&](auto k) { rd(k); rd1(k); });
#pragma unroll
    for (int ks = 0; ks < 16; ++ks) asm volatile("" : "+a"(wq[ks]));
    WAIT_LG(4);
    TS(2);
    sfor<0, 32>([&](auto k) {
      constexpr int ks = k & 15, u = k >> 4;
      (void)dk; (void)dz; (void)kvo; (void)zvo; (void)rblk; (void)rzb;
      mmq<k & 3, ks == 0>(aq[2 * u], wq[ks]);
      VNOP();
      rd(IC<k + 3>{});
      if constexpr (k >= QPOLL && k < QPOLL + 4) {
        constexpr int i = k - QPOLL;
        asm volatile("buffer_load_dwordx4 %0, %1, %2, %3 offen offset:%4 sc1" : "=v"(dk[i]) : "v"(kvo), "s"(ctx.rs), "s"(i < 2 ? rblk : rblk + 4096), "n"((i & 1) * 2048) : "memory");
      }
      if constexpr (k == QPOLL + 4) asm volatile("buffer_load_dwordx4 %0, %1, %2, %3 offen sc1" : "=v"(dz) : "v"(zvo), "s"(ctx.rs), "s"(rzb) : "memory");
      mf_av<ks == 0>(aq[2 * u + 1], wq[ks], a1[k & 3]);
      WAIT_LG(k + 3 < 32 ? 3 : k == 29 ? 2 : 0);
      rd1(IC<k + 3>{});
      SB();
    });
  }
  TS(6);
  TS_FLUSH(34 + lp * 8, 7);
  STAMP(48 + lp * 4);
  v4u phq[4], d2[4];
  const int b2 = WS_KV2 + (m * 64 + w * 16) * 256;
  int vo2 = g * 512 + tok * 16;
  {
    const f4 csq = lds_ldf4(ctx.L, csb + g * 16);
    f2 st[4];
#pragma unroll
    for (int t = 0; t < 4; ++t) st[t] = *(LAS f2*)(ctx.L + L_REC + ((1 + t) * 16 + tok) * 16);
    int svo = (lane & 7) * 16 + ((lane >> 4) & 1) * 8;
    const u64 mhi = __ballot(lane >= 32);
    const int sblk = WS_KV2 + (m * 64 + S64) * 256 + (w & 1) * 128;
    asm volatile("s_nop 3" : "+v"(aq[0]), "+v"(aq[1]), "+v"(aq[2]), "+v"(aq[3]), "+v"(svo), "+v"(vo2));
    {
      asm volatile("s_waitcnt vmcnt(0)" : "+v"(dk[0]), "+v"(dk[1]), "+v"(dk[2]), "+v"(dk[3]), "+v"(dz) :: "memory");
      STAMP(49 + lp * 4);
      for (;;) {
        unsigned bad;
        asm volatile("v_sad_u32 %0, %1, %11, 0 clamp\n\tv_sad_u32 %0, %2, %11, %0 clamp\n\tv_sad_u32 %0, %3, %11, %0 clamp\n\tv_sad_u32 %0, %4, %11, %0 clamp\n\t"
                     "v_sad_u32 %0, %5, %11, %0 clamp\n\tv_sad_u32 %0, %6, %11, %0 clamp\n\tv_sad_u32 %0, %7, %11, %0 clamp\n\tv_sad_u32 %0, %8, %11, %0 clamp\n\t"
                     "v_sad_u32 %0, %9, %11, %0 clamp\n\tv_sad_u32 %0, %10, %11, %0 clamp"
                     : "=&v"(bad) : "v"(dk[0].y), "v"(dk[0].w), "v"(dk[1].y), "v"(dk[1].w), "v"(dk[2].y), "v"(dk[2].w), "v"(dk[3].y), "v"(dk[3].w),
                       "v"(dz.y), "v"(dz.w), "s"(T));
        if (__builtin_expect(__all(bad == 0), 1) || --ctx.budget < 0) break;
        asm volatile("s_nop 4\n\tbuffer_load_dwordx4 %0, %5, %7, %8 offen sc1\n\tbuffer_load_dwordx4 %1, %5, %7, %8 offen offset:2048 sc1\n\t"
                     "buffer_load_dwordx4 %2, %5, %7, %9 offen sc1\n\tbuffer_load_dwordx4 %3, %5, %7, %9 offen offset:2048 sc1\n\t"
                     "buffer_load_dwordx4 %4, %6, %7, %10 offen sc1\n\ts_waitcnt vmcnt(0)"
                     : "=&v"(dk[0]), "=&v"(dk[1]), "=&v"(dk[2]), "=&v"(dk[3]), "=&v"(dz)
                     : "v"(kvo), "v"(zvo), "s"(ctx.rs), "s"(rblk), "s"(rblk + 4096), "s"(rzb) : "memory");
      }
      float K, K1, Z; unsigned pk, tg;
      asm volatile("v_add_f32 %0, %5, %6\n\tv_add_f32 %1, %9, %10\n\tv_add_f32 %2, %7, %8\n\tv_add_f32 %3, %11, %12\n\t"
                   "v_add_f32 %0, %0, %2\n\tv_add_f32 %1, %1, %3\n\tv_cndmask_b32 %2, %13, %14, %15\n\tv_mov_b32 %4, %16\n\t"
                   "v_permlane32_swap_b32 %0, %1\n\tv_add_f32 %0, %0, %1\n\ts_nop 1\n\t"
                   "v_permlane16_swap_b32 %0, %2\n\tv_add_f32 %0, %0, %2\n\ts_nop 1\n\t"
                   "v_add_f32_dpp %0, %0, %0 row_ror:8 row_mask:0xf bank_mask:0xf\n\ts_nop 1\n\t"
                   "v_add_f32_dpp %0, %0, %0 row_ror:4 row_mask:0xa bank_mask:0xf\n\ts_nop 1\n\t"
                   "v_add_f32_dpp %0, %0, %0 row_ror:2 row_mask:0xa bank_mask:0xf\n\ts_nop 1\n\t"
                   "v_add_f32_dpp %0, %0, %0 row_ror:1 row_mask:0xa bank_mask:0xf\n\t"
                   "v_mov_b32 %1, %0\n\ts_nop 1\n\tv_permlane32_swap_b32 %0, %1\n\tv_cvt_pk_bf16_f32 %3, %0, %1"
                   : "=&v"(K), "=&v"(K1), "=&v"(Z), "=&v"(pk), "=&v"(tg)
                   : "v"(dk[0].x), "v"(dk[1].x), "v"(dk[2].x), "v"(dk[3].x), "v"(dk[0].z), "v"(dk[1].z), "v"(dk[2].z), "v"(dk[3].z),
                     "v"(dz.x), "v"(dz.z), "s"(mhi), "s"(T));
      st64(ctx, v2u{pk, tg}, svo, sblk);
      STAMP(L * 8 + 3);
    }
    FM f[4];
    unsigned qb[4][2];
#pragma unroll
    for (int t = 0; t < 4; ++t) {
      float q[4];
#pragma unroll
      for (int r = 0; r < 4; ++r) A_FMA(q[r], st[t][0], csq[r], aq[t][r]);
      A_MULS(f[t].c, LOG2E, st[t][1]);
      VNOP();
      A_CVT(qb[t][0], q[0], q[1]); A_CVT(qb[t][1], q[2], q[3]);
      if (t > 0) mf16z_a(f[t - 1].p, e.wphi, v2u{qb[t - 1][0], qb[t - 1][1]});
    }
    FM_MM1(f[0]);
    mf16z_a(f[3].p, e.wphi, v2u{qb[3][0], qb[3][1]});
    FM_MM1(f[1]); FM_MM2(f[0]); FM_MM2(f[1]); FM_MM1(f[2]);
    A_SW16(f[0].m, f[0].m2); FM_MM2(f[2]); FM_MM1(f[3]); A_SW16(f[1].m, f[1].m2); FM_MM2(f[3]);
    FM_MX2(f[0]); A_SW16(f[2].m, f[2].m2); FM_MX2(f[1]); A_SW16(f[3].m, f[3].m2);
    A_SW32(f[0].m, f[0].m2); FM_MX2(f[2]); A_SW32(f[1].m, f[1].m2); FM_MX2(f[3]);
    FM_MXC(f[0]); A_SW32(f[2].m, f[2].m2); FM_MXC(f[1]); A_SW32(f[3].m, f[3].m2);
    A_SW16(f[0].m, f[0].m2); FM_MXC(f[2]); A_SW16(f[1].m, f[1].m2); FM_MXC(f[3]);
    if (lp == 0) { Ctx c2 = ctx; fresh(c2); fr_load<34, 40>(c2); }
    RAWBAR();
    sfor<0, 4>([&](auto t) {
      (void)f; (void)e;
      auto piece = [&](auto p) {
        if constexpr ((p & 3) == 0) { dma_so<128 * 1024 * (1 + (p >> 2))>(so, wd.sb); dma_m0<L_XT + 16384 * (1 + (p >> 2))>(ctx.Lw); VNOP(); }
        dma_go<p & 3>(wd.v[p & 3], rwo, so);
      };
      piece(IC<3 * t>{});
#pragma unroll
      for (int r = 0; r < 4; ++r) A_FMA(f[t].ep[r], f[t].p[r], f[t].c, f[t].m);
      if constexpr (t < 2) A_SW16(f[t + 2].m, f[t + 2].m2);
#pragma unroll
      for (int r = 0; r < 4; ++r) A_FMAN(f[t].en[r], f[t].p[r], f[t].c, f[t].m2);
      if constexpr (t > 0) mf16z_a(f[t - 1].sn, e.ones, v2u{f[t - 1].nb[0], f[t - 1].nb[1]}); else VNOP();
      piece(IC<3 * t + 1>{});
#pragma unroll
      for (int r = 0; r < 4; ++r) A_EXP(f[t].ep[r], f[t].ep[r]);
      A_EXP(f[t].en[0], f[t].en[0]);
      piece(IC<3 * t + 2>{});
      A_CVT(f[t].eb[0], f[t].ep[0], f[t].ep[1]); A_CVT(f[t].eb[1], f[t].ep[2], f[t].ep[3]);
      A_EXP(f[t].en[1], f[t].en[1]); A_EXP(f[t].en[2], f[t].en[2]);
      mf16z_a(f[t].sp, e.ones, v2u{f[t].eb[0], f[t].eb[1]});
      A_EXP(f[t].en[3], f[t].en[3]);
      A_CVT(f[t].nb[0], f[t].en[0], f[t].en[1]); A_CVT(f[t].nb[1], f[t].en[2], f[t].en[3]);
    });
#pragma unroll
    for (int t = 0; t < 4; ++t) {
      A_RCP(f[t].m, f[t].sp); A_RCP(f[t].m2, f[t].sn);
      if (t == 0) mf16z_a(f[3].sn, e.ones, v2u{f[3].nb[0], f[3].nb[1]});
      if (t == S2POLL) {
        asm volatile("buffer_load_dwordx4 %0, %4, %5, %6 offen sc1\n\tbuffer_load_dwordx4 %1, %4, %5, %6 offen offset:256 sc1\n\t"
                     "buffer_load_dwordx4 %2, %4, %5, %6 offen offset:2048 sc1\n\tbuffer_load_dwordx4 %3, %4, %5, %6 offen offset:2304 sc1"
                     : "=&v"(d2[0]), "=&v"(d2[1]), "=&v"(d2[2]), "=&v"(d2[3]) : "v"(vo2), "s"(ctx.rs), "s"(b2) : "memory");
      }
#pragma unroll
      for (int r = 0; r < 4; ++r) A_MUL(f[t].ep[r], f[t].ep[r], f[t].m);
#pragma unroll
      for (int r = 0; r < 4; ++r) A_MUL(f[t].en[r], f[t].en[r], f[t].m2);
      unsigned x0, x1, x2, x3;
      A_CVT(x0, f[t].ep[0], f[t].ep[1]); A_CVT(x1, f[t].ep[2], f[t].ep[3]); A_CVT(x2, f[t].en[0], f[t].en[1]); A_CVT(x3, f[t].en[2], f[t].en[3]);
      phq[t] = v4u{x0, x1, x2, x3};
    }
  }
  STAMP(50 + lp * 4);
  {
    int ovo = ((g >> 1) * 16 + tok) * 16 + (g & 1) * 8;
    const int osb = WS_O + (4 * c * 16 + 2 * m + (w >> 1)) * 1024 + (w & 1) * 512;
    if (lp == 0) { Ctx c2 = ctx; fresh(c2); fr_load<40, 48>(c2); WAIT_VM(8); } else DRAIN();
    asm volatile("" : "+v"(d2[0]), "+v"(d2[1]), "+v"(d2[2]), "+v"(d2[3]), "+v"(ovo) :: "memory");
    for (;;) {
      unsigned bad;
      asm volatile("v_sad_u32 %0, %1, %9, 0 clamp\n\tv_sad_u32 %0, %2, %9, %0 clamp\n\tv_sad_u32 %0, %3, %9, %0 clamp\n\tv_sad_u32 %0, %4, %9, %0 clamp\n\t"
                   "v_sad_u32 %0, %5, %9, %0 clamp\n\tv_sad_u32 %0, %6, %9, %0 clamp\n\tv_sad_u32 %0, %7, %9, %0 clamp\n\tv_sad_u32 %0, %8, %9, %0 clamp"
                   : "=&v"(bad) : "v"(d2[0].y), "v"(d2[0].w), "v"(d2[1].y), "v"(d2[1].w), "v"(d2[2].y), "v"(d2[2].w), "v"(d2[3].y), "v"(d2[3].w), "s"(T));
      if (__builtin_expect(__all(bad == 0), 1) || --ctx.budget < 0) break;
      bld128x4<256, 2048, 2304>(ctx, d2, vo2, b2);
    }
    STAMP(L * 8 + 4);
    v4u akv = {d2[0].x, d2[1].x, d2[2].x, d2[3].x}, az = {d2[0].z, d2[1].z, d2[2].z, d2[3].z};
    f4 num[4], den[4];
    asm volatile("s_nop 1" : "+v"(akv), "+v"(az));
#pragma unroll
    for (int t = 0; t < 4; ++t) { mf32z(den[t], az, phq[t]); mf32z(num[t], akv, phq[t]); }
#pragma unroll
    for (int t = 0; t < 4; ++t) {
      float r, q0, q1, q2, q3; unsigned o0, o1;
      asm volatile("v_add_f32 %2, %12, %7\n\tv_rcp_f32 %2, %2\n\ts_nop 0\n\t"
                   "v_mul_f32 %3, %8, %2\n\tv_mul_f32 %4, %9, %2\n\tv_mul_f32 %5, %10, %2\n\tv_mul_f32 %6, %11, %2\n\t"
                   "v_cvt_pk_bf16_f32 %0, %3, %4\n\tv_cvt_pk_bf16_f32 %1, %5, %6"
                   : "=&v"(o0), "=&v"(o1), "=&v"(r), "=&v"(q0), "=&v"(q1), "=&v"(q2), "=&v"(q3)
                   : "v"(den[t][0]), "v"(num[t][0]), "v"(num[t][1]), "v"(num[t][2]), "v"(num[t][3]), "s"(1e-6f), "v"(den[t]));
      st64(ctx, v2u{o0, o1}, ovo, osb + t * 16384);
    }
  }
  DRAIN();
  BAR();
  flag_store(ctx, WS_FO + c * 128);
}

template <bool ODD>
DEV void out_proj(Ctx& ctx, int lp, f4 (&xacc)[4], rsrc_t out, rsrc_t rsb, rsrc_t rwn, rsrc_t rsx) {
  fresh(ctx);
  const int L = 2 * lp + (ODD ? 1 : 0);
  const bool last = ODD && lp == 1, first = !ODD && lp == 0;
  const int c = ctx.c, m = ctx.m, w = ctx.w, lane = ctx.lane, g = ctx.g, tok = ctx.tok;
  if (first) {
    v2u xr[4];
    asm volatile("s_nop 4\n\tbuffer_load_dwordx2 %0, %4, %5, %6 offen\n\tbuffer_load_dwordx2 %1, %4, %5, %6 offen offset:32\n\t"
                 "buffer_load_dwordx2 %2, %4, %5, %6 offen offset:64\n\tbuffer_load_dwordx2 %3, %4, %5, %6 offen offset:96"
                 : "=&v"(xr[0]), "=&v"(xr[1]), "=&v"(xr[2]), "=&v"(xr[3]) : "v"(tok * 1024 + g * 8), "s"(rsx), "s"((64 * c + 16 * w) * 1024 + 128 * m) : "memory");
    fr_load<0, 8>(ctx);
    part_cs<P_BK>(ctx, L_CS + (w * 6 + 4) * 64);
    asm volatile("s_waitcnt vmcnt(8)" : "+v"(xr[0]), "+v"(xr[1]), "+v"(xr[2]), "+v"(xr[3]) :: "memory");
#pragma unroll
    for (int nt = 0; nt < 4; ++nt)
      xacc[nt] = f4{asf(xr[nt].x << 16), asf(xr[nt].x & 0xffff0000u), asf(xr[nt].y << 16), asf(xr[nt].y & 0xffff0000u)};
  }
  lds_t* tb[4];
#pragma unroll
  for (int nt = 0; nt < 4; ++nt) tb[nt] = ctx.L + opq(L_XT + tr_base(lane, nt));
  v4u wf[4][4];
#pragma unroll
  for (int i = 0; i < 4; ++i)
#pragma unroll
    for (int nt = 0; nt < 4; ++nt) wf[i][nt] = tr_frag(tb[nt], i * 4096);
  const int G = ((g & 1) << 1) | (g >> 1);
  int xvo = (G * 16 + tok) * 16;
  u64 m1 = __ballot(tok & 1), m2 = __ballot(tok & 2);
  const int osb = WS_O + (4 * c + w) * 16384;
  const int xsb = WS_X + (ODD ? ACT_L : 0) + ((4 * c + w) * 16 + 2 * m) * 1024;
  const int ssb = WS_STAT + (ODD ? STAT_L : 0) + ((c * 8 + m) * 64 + 16 * w) * 16;
  asm volatile("" : "+v"(xvo), "+v"(xacc[0]), "+v"(xacc[1]), "+v"(xacc[2]), "+v"(xacc[3]));
#pragma unroll
  for (int nt = 0; nt < 4; ++nt) asm volatile("" : "+v"(wf[0][nt]), "+v"(wf[1][nt]), "+v"(wf[2][nt]), "+a"(wf[3][nt]));
  wait_flags(ctx, WS_FO + ((ODD ? 32 : 0) + c) * 128, 0, false);
  if (ODD) ctx.T += 1u;
  STAMP(L * 8 + 5);
  v4u of[16];
  constexpr int PRE = 8;
#pragma unroll
  for (int ks = 0; ks < PRE; ++ks) of[ks] = ld128(ctx, lane * 16 + (ks & 3) * 1024, osb + (ks & ~3) * 1024);
  SB();
  sfor<0, 16>([&](auto ks) {
    if constexpr (ks + PRE < 16) { of[ks + PRE] = ld128(ctx, lane * 16 + ((ks + PRE) & 3) * 1024, osb + ((ks + PRE) & ~3) * 1024); SB(); }
#pragma unroll
    for (int nt = 0; nt < 4; ++nt) {
      if constexpr ((ks & 3) < 3) mf_va(xacc[nt], wf[ks & 3][nt], of[ks]); else mf_aa(xacc[nt], wf[ks & 3][nt], of[ks]);
      if constexpr (ks + 4 < 16) wf[ks & 3][nt] = tr_frag(tb[nt], (ks + 4) * 4096);
      SB();
    }
  });
  NOP(4);
  STAMP(L * 8 + 6);
  v4u fr[2];
#pragma unroll
  for (int u = 0; u < 2; ++u) {
    unsigned a0, a1, b0, b1;
    const f4 a = xacc[2 * u], b = xacc[2 * u + 1];
    asm volatile("v_cvt_pk_bf16_f32 %0, %4, %5\n\tv_cvt_pk_bf16_f32 %1, %6, %7\n\tv_cvt_pk_bf16_f32 %2, %8, %9\n\tv_cvt_pk_bf16_f32 %3, %10, %11\n\t"
                 "s_nop 0\n\tv_permlane16_swap_b32 %0, %2\n\tv_permlane16_swap_b32 %1, %3"
                 : "=&v"(a0), "=&v"(a1), "=&v"(b0), "=&v"(b1)
                 : "v"(a[0]), "v"(a[1]), "v"(a[2]), "v"(a[3]), "v"(b[0]), "v"(b[1]), "v"(b[2]), "v"(b[3]));
    fr[u] = v4u{a0, a1, b0, b1};
    if (!last) st128(ctx, fr[u], xvo + u * 1024, xsb);
  }
  if (last) {
#pragma unroll
    for (int u = 0; u < 2; ++u)
      __builtin_amdgcn_raw_buffer_store_b128(fr[u], out, tok * 1024 + G * 16 + u * 64, (64 * c + 16 * w) * 1024 + 128 * m, 0);
  } else {
    f4 ssum, gram;
    v4u ones = {mk1(), mk1(), mk1(), mk1()};
    asm volatile("s_nop 1" : "+v"(ones));
    mf32z(gram, fr[0], fr[0]); mf32z(ssum, ones, fr[0]);
    mf32c(gram, fr[1], fr[1]); mf32c(ssum, ones, fr[1]);
    asm volatile("s_nop 7" : "+v"(gram), "+v"(ssum));
    float q, t;
    asm volatile("v_cndmask_b32 %0, %2, %3, %6\n\tv_cndmask_b32 %1, %4, %5, %6\n\tv_cndmask_b32 %0, %0, %1, %7"
                 : "=&v"(q), "=&v"(t) : "v"(gram[0]), "v"(gram[1]), "v"(gram[2]), "v"(gram[3]), "s"(m1), "s"(m2));
    if (g == (tok >> 2)) st128(ctx, v4u{asu(ssum[0]), ctx.T, asu(q), ctx.T}, tok * 16, ssb);
    WAIT_VM(1);
    BAR();
    flag_store(ctx, WS_FX + ((ODD ? 32 : 0) + c) * 128);
    if (first) { fr_load<8, 16>(ctx); part_cs<P_BV>(ctx, L_CS + (w * 6 + 5) * 64); DRAIN(); part_cs<P_BQ>(ctx, L_CS + (w * 6 + 3) * 64); }
  }
  asm volatile("" : "+a"(xacc[0]), "+a"(xacc[1]), "+a"(xacc[2]), "+a"(xacc[3]));
  STAMP(L * 8 + 7);
}

__global__ void __launch_bounds__(NT, 1) __attribute__((amdgpu_num_vgpr(88)))
fused_kernel(const unsigned char* __restrict__ x_, const unsigned char* __restrict__ wa_, const unsigned char* __restrict__ wphi_,
             const unsigned char* __restrict__ woa_, const unsigned char* __restrict__ wb_, const unsigned char* __restrict__ wob_,
             unsigned char* out_, unsigned char* ws_) {
  extern __shared__ __attribute__((aligned(16))) unsigned char smem[];
  asm volatile("" ::: "v255", "a255");
  Ctx ctx;
  ctx.ws = (GAS unsigned char*)ws_;
  ctx.rs = __builtin_amdgcn_make_buffer_rsrc((void*)ws_, 0, WS_END, 0x00020000);
  ctx.L = (lds_t*)smem;
  ctx.Lb = (unsigned)(size_t)ctx.L;
  const int b = blockIdx.x, idx = b >> 3;
  ctx.c = (b & 7) * 4 + (idx >> 3);
  ctx.m = idx & 7;
  ctx.w = __builtin_amdgcn_readfirstlane(threadIdx.x >> 6);
  ctx.budget = SPIN_CAP;
  fresh(ctx);
  const int c = ctx.c, m = ctx.m, w = ctx.w, lane = ctx.lane, g = ctx.g, tok = ctx.tok;
  STAMP(40); CSTAMP(0);
  const rsrc_t rsa = __builtin_amdgcn_make_buffer_rsrc((void*)wa_, 0, 512 * 3072, 0x00020000);
  const rsrc_t rsb = __builtin_amdgcn_make_buffer_rsrc((void*)wb_, 0, 512 * 3072, 0x00020000);
  const rsrc_t rwa = __builtin_amdgcn_make_buffer_rsrc((void*)woa_, 0, 512 * 1024, 0x00020000);
  const rsrc_t rwb = __builtin_amdgcn_make_buffer_rsrc((void*)wob_, 0, 512 * 1024, 0x00020000);
  {
    u64 pc; unsigned len;
    asm volatile("s_getpc_b64 %0\n.Lfk_here:\n\ts_mov_b32 %1, .Lfk_end-.Lfk_here" : "=s"(pc), "=s"(len));
    const rsrc_t rc = __builtin_amdgcn_make_buffer_rsrc((void*)pc, 0, len, 0x00020000);
    asm volatile("s_nop 4" ::: "memory");
    pf1<0>(lane * 16, rc, (idx * 4 + w) * 1024);
  }
  unsigned epoch, ph0, ph1, ph2, ph3;
  {
    const GAS unsigned char* pp = (const GAS unsigned char*)wphi_ + (4 * m + w) * 512;
    const unsigned pv = (unsigned)(4 * g * 16 + tok) * 2, zero = 0;
    asm volatile("s_nop 4\n\tglobal_load_dword %0, %5, %6 sc1\n\t"
                 "global_load_ushort %1, %7, %8\n\tglobal_load_ushort %2, %7, %8 offset:32\n\t"
                 "global_load_ushort %3, %7, %8 offset:64\n\tglobal_load_ushort %4, %7, %8 offset:96"
                 : "=&v"(epoch), "=&v"(ph0), "=&v"(ph1), "=&v"(ph2), "=&v"(ph3)
                 : "v"(zero), "s"(ctx.ws), "v"(pv), "s"(pp) : "memory");
  }
  const rsrc_t rsx = __builtin_amdgcn_make_buffer_rsrc((void*)x_, 0, 2048 * 1024, 0x00020000);
  {
    const int cp = lane & 7, sw = ((lane >> 4) & 1) | ((w & 1) << 1);
    const unsigned vo = (unsigned)((lane >> 3) * 3072 + (cp & 1) * 16 + (((cp >> 1) ^ sw) << 5));
    const int s0 = (c >> 4) * 1024 + m * 128 + (32 * (c & 15) + 8 * w) * 3072, s1 = 2048 + m * 128 + (32 * (c & 15) + 8 * w) * 3072;
    dma1<0, L_IMG>(vo, rsb, s0, ctx.Lb + w * 1024); dma1<0, L_IMG + 4096>(vo, rsb, s1, ctx.Lb + w * 1024);
  }
  first_pieces<0, 12>(ctx, rsx, rsa);
  {
    const int u = b * 4 + w;
    const unsigned l32 = lane & 31;
    if (u < 384) pfw(l32 * 128, rsa, u * 4096);
    else if (u < 512) pfw(l32 * 128, rwa, (u - 384) * 4096);
    else pfw(l32 * 128, rwb, ((u - 512) & 127) * 4096);
  }
  first_pieces<12, PRE0>(ctx, rsx, rsa);
  asm volatile("s_waitcnt vmcnt(%5)" : "+v"(epoch), "+v"(ph0), "+v"(ph1), "+v"(ph2), "+v"(ph3) : "n"(PRE0 + 3) : "memory");
  ctx.T = __builtin_amdgcn_readfirstlane(epoch) + 1u;
  v2u wphi = v2u{ph0 | (ph1 << 16), ph2 | (ph3 << 16)};
  f4 xacc[4] = {{0.f, 0.f, 0.f, 0.f}, {0.f, 0.f, 0.f, 0.f}, {0.f, 0.f, 0.f, 0.f}, {0.f, 0.f, 0.f, 0.f}};
  asm volatile("" : "+a"(xacc[0]), "+a"(xacc[1]), "+a"(xacc[2]), "+a"(xacc[3]), "+a"(wphi));
  const rsrc_t out = __builtin_amdgcn_make_buffer_rsrc((void*)out_, 0, 2048 * 1024, 0x00020000);

#pragma nounroll
  for (int lp = 0; lp < 2; ++lp) {
    asm volatile("" : "+a"(xacc[0]), "+a"(xacc[1]), "+a"(xacc[2]), "+a"(xacc[3]), "+a"(wphi));
    layer_A(ctx, lp, wphi, rwa, rsa, rsb, rsx);
    out_proj<false>(ctx, lp, xacc, out, rsb, rwb, rsx);
    layer_B(ctx, 2 * lp + 1, rwb);
    out_proj<true>(ctx, lp, xacc, out, rsb, rwb, rsx);
  }
  asm volatile(".Lfk_end:");
  if (b == 0) { ctx.m = 0; flag_store(ctx, WS_EPOCH); }
  STAMP(41); CSTAMP(1);
  STAMP_FLUSH();
}

extern "C" void kernel_launch(void* const* d_in, const int* in_sizes, int n_in, void* d_out, int out_size, void* d_ws,
                              size_t ws_size, hipStream_t stream) {
  (void)in_sizes; (void)n_in; (void)out_size; (void)ws_size;
  (void)hipFuncSetAttribute((const void*)fused_kernel, hipFuncAttributeMaxDynamicSharedMemorySize, LDS_BYTES);
  hipLaunchKernelGGL(fused_kernel, dim3(NWG), dim3(NT), LDS_BYTES, stream,
                     (const unsigned char*)d_in[0], (const unsigned char*)d_in[1], (const unsigned char*)d_in[2],
                     (const unsigned char*)d_in[3], (const unsigned char*)d_in[5], (const unsigned char*)d_in[6],
                     (unsigned char*)d_out, (unsigned char*)d_ws);
}
```
